```python
import torch

def _softmax(x, axis=-1):
    return torch.softmax(x, dim=axis)
def _gelu(x):
    return torch.nn.functional.gelu(x, approximate="tanh")
def _silu(x):
    return torch.nn.functional.silu(x)
def _sigmoid(x):
    return torch.sigmoid(x)
def _layernorm(x, axis=-1, eps=1e-5):
    m = x.mean(dim=axis, keepdim=True)
    v = x.var(dim=axis, keepdim=True, unbiased=False)
    return (x - m) / torch.sqrt(v + eps)
def _scatter_add(tgt, idx, src, axis=0):
    return tgt.clone().index_add_(axis, idx, src)
def _segment_sum(src, seg, n):
    out = torch.zeros((n,) + src.shape[1:], dtype=src.dtype)
    return out.index_add_(0, seg, src)
def _pad(x, pw):
    flat = tuple(v for pair in reversed(pw) for v in pair)
    return torch.nn.functional.pad(x, flat)

kernel_name = 'parallel_sum__compressed_kv_cla__dense_activation_squared_relu__prompt'

def setup_inputs():

    _w = (lambda a: (lambda t: t.to(torch.bfloat16) if t.is_floating_point() else t)(torch.as_tensor(a)))
    def _A_setup():
        B, H, Sq, Sk, D, M = 1, 16, 2048, 2048, 256, 448
        Dc = 4 * D
        return {
            "x": _w((lambda *s: torch.randn(*s, dtype=torch.bfloat16) * 0.05)(B, Sq, M)),
            "c_kv": _w((lambda *s: torch.randn(*s, dtype=torch.bfloat16) * 1.6)(B, Sk, Dc)),
            "w_q": _w((lambda *s: torch.randn(*s, dtype=torch.bfloat16) * 0.8)(M, H, D) * (M ** -0.5)),
            "w_uk": _w((lambda *s: torch.randn(*s, dtype=torch.bfloat16) * 0.8)(Dc, H, D) * (Dc ** -0.5)),
            "w_uv": _w((lambda *s: torch.randn(*s, dtype=torch.bfloat16) * 1.6)(Dc, H, D) * (Dc ** -0.5)),
            "w_o": _w((lambda *s: torch.randn(*s, dtype=torch.bfloat16) * 1.6)(H, D, M) * ((H * D) ** -0.5)),
        }
    _A_in = _A_setup()

    def _B_setup():
        B, S, D, F = 1, 2048, 448, 1792
        return {
            "x": _w((lambda *s: torch.randn(*s, dtype=torch.bfloat16) * 0.05)(B, S, D)),
            "w1": _w((lambda *s: torch.randn(*s, dtype=torch.bfloat16) * 0.05)(D, F)),
            "b1": _w((lambda *s: torch.randn(*s, dtype=torch.bfloat16) * 0.2)(F)),
            "w2": _w((lambda *s: torch.randn(*s, dtype=torch.bfloat16) * 0.05)(F, D)),
            "b2": _w((lambda *s: torch.randn(*s, dtype=torch.bfloat16) * 0.8)(D)),
        }
    _B_in = _B_setup()

    return {
        "A_x": _A_in["x"],
        "A_c_kv": _A_in["c_kv"],
        "A_w_q": _A_in["w_q"],
        "A_w_uk": _A_in["w_uk"],
        "A_w_uv": _A_in["w_uv"],
        "A_w_o": _A_in["w_o"],
        "B_x": _B_in["x"],
        "B_w1": _B_in["w1"],
        "B_b1": _B_in["b1"],
        "B_w2": _B_in["w2"],
        "B_b2": _B_in["b2"],
    }

def reference(A_x, A_c_kv, A_w_q, A_w_uk, A_w_uv, A_w_o, B_x, B_w1, B_b1, B_w2, B_b2):
    def _A_ref(x, c_kv, w_q, w_uk, w_uv, w_o):
        B, Sq, M = x.shape[0], x.shape[1], x.shape[2]
        Sk = c_kv.shape[1]
        H, D = w_q.shape[1], w_q.shape[2]
        q = torch.einsum("bqm,mhd->bhqd", x, w_q)
        k = torch.einsum("bkc,chd->bhkd", c_kv, w_uk)
        v = torch.einsum("bkc,chd->bhkd", c_kv, w_uv)
        scores = torch.einsum("bhqd,bhkd->bhqk", q, k) * (D ** -0.5)
        off = Sk - Sq
        qi = torch.arange(Sq)[:, None]
        kj = torch.arange(Sk)[None, :]
        cmask = kj <= (qi + off)
        scores = torch.where(cmask[None, None], scores, -1e9)
        p = _softmax(scores, axis=-1)
        ctx = torch.einsum("bhqk,bhkd->bhqd", p, v)
        return torch.einsum("bhqd,hdm->bqm", ctx, w_o)

    def _B_ref(x, w1, b1, w2, b2):
        h = torch.einsum("bsd,df->bsf", x, w1) + b1
        zero = torch.zeros_like(h)
        r = torch.where(h > zero, h, zero)
        h = r * r
        out = torch.einsum("bsf,fd->bsd", h, w2) + b2
        return out

    x = A_x
    xn = _layernorm(x, axis=-1)
    return x + _A_ref(xn, A_c_kv, A_w_q, A_w_uk, A_w_uv, A_w_o) + _B_ref(xn, B_w1, B_b1, B_w2, B_b2)
```

```cpp
#include <hip/hip_runtime.h>
#include <hip/hip_bf16.h>
typedef __hip_bfloat16 bf16;
__device__ __forceinline__ float b2f(bf16 x){return __bfloat162float(x);}
__device__ __forceinline__ bf16 f2b(float x){return __float2bfloat16(x);}
__device__ __forceinline__ float rb(float x){return b2f(f2b(x));}

__global__ void ln_k(const bf16* x, bf16* xn){
  int row=blockIdx.x; int t=threadIdx.x;
  float v[7]; float s=0;
  for(int i=0;i<7;i++){v[i]=b2f(x[row*448+t+64*i]); s+=v[i];}
  for(int o=32;o;o>>=1) s+=__shfl_xor(s,o);
  float mean=s/448.f; float q=0;
  for(int i=0;i<7;i++){float d=v[i]-mean; q+=d*d;}
  for(int o=32;o;o>>=1) q+=__shfl_xor(q,o);
  float var=rb(q/448.f); float mb=rb(mean);
  float sd=rb(sqrtf(rb(var+1e-5f)));
  for(int i=0;i<7;i++) xn[row*448+t+64*i]=f2b(rb(v[i]-mb)/sd);
}
__global__ void gemm_k(const bf16* A,int lda,const bf16* B,int ldb,bf16* C,int ldc,int K,int mode,const bf16* bias){
  __shared__ float As[64][17], Bs[16][65];
  int tx=threadIdx.x&15, ty=threadIdx.x>>4; int m0=blockIdx.y*64,n0=blockIdx.x*64;
  float acc[4][4]={};
  for(int k0=0;k0<K;k0+=16){
    for(int i=threadIdx.x;i<1024;i+=256){int r=i>>4,c=i&15; As[r][c]=b2f(A[(long)(m0+r)*lda+k0+c]);}
    for(int i=threadIdx.x;i<1024;i+=256){int r=i>>6,c=i&63; Bs[r][c]=b2f(B[(long)(k0+r)*ldb+n0+c]);}
    __syncthreads();
    for(int k=0;k<16;k++){
      float a[4],b[4];
      for(int i=0;i<4;i++){a[i]=As[ty*4+i][k]; b[i]=Bs[k][tx*4+i];}
      for(int i=0;i<4;i++)for(int j=0;j<4;j++)acc[i][j]+=a[i]*b[j];
    }
    __syncthreads();
  }
  for(int i=0;i<4;i++)for(int j=0;j<4;j++){
    float v=acc[i][j]; int n=n0+tx*4+j;
    if(mode==1){v=rb(rb(v)+b2f(bias[n])); v=v>0?v:0; v=v*v;}
    C[(long)(m0+ty*4+i)*ldc+n]=f2b(v);
  }
}
__global__ void attn_k(const bf16* Q,const bf16* Kk,const bf16* V,bf16* ctx){
  __shared__ float qs[256]; __shared__ float p[2048]; __shared__ float red[256];
  int q=blockIdx.x,h=blockIdx.y,t=threadIdx.x;
  qs[t]=b2f(Q[(long)q*4096+h*256+t]); __syncthreads();
  float mx=-1e30f;
  for(int k=t;k<=q;k+=256){ float s=0; const bf16* kr=Kk+(long)k*4096+h*256; for(int d=0;d<256;d++) s+=qs[d]*b2f(kr[d]); s*=0.0625f; p[k]=s; mx=fmaxf(mx,s);}
  red[t]=mx; __syncthreads(); for(int o=128;o;o>>=1){ if(t<o) red[t]=fmaxf(red[t],red[t+o]); __syncthreads(); }
  mx=red[0]; __syncthreads();
  float sm=0; for(int k=t;k<=q;k+=256){ float e=__expf(p[k]-mx); p[k]=e; sm+=e; }
  red[t]=sm; __syncthreads(); for(int o=128;o;o>>=1){ if(t<o) red[t]+=red[t+o]; __syncthreads(); }
  sm=red[0];
  float o=0; for(int k=0;k<=q;k++) o+=rb(p[k]/sm)*b2f(V[(long)k*4096+h*256+t]);
  ctx[(long)q*4096+h*256+t]=f2b(o);
}
__global__ void fin_k(const bf16* x,const bf16* a,const bf16* b,const bf16* b2,bf16* out){
  int i=blockIdx.x*256+threadIdx.x; if(i>=2048*448) return; int n=i%448;
  float v=rb(b2f(x[i])+b2f(a[i])); float w=rb(b2f(b[i])+b2f(b2[n])); out[i]=f2b(v+w);
}
extern "C" void kernel_launch(void* const* d_in, const int* in_sizes, int n_in, void* d_out, int out_size, void* d_ws, size_t ws_size, hipStream_t stream) {
  const bf16* x=(const bf16*)d_in[0]; const bf16* c=(const bf16*)d_in[1]; const bf16* wq=(const bf16*)d_in[2]; const bf16* wk=(const bf16*)d_in[3];
  const bf16* wv=(const bf16*)d_in[4]; const bf16* wo=(const bf16*)d_in[5]; const bf16* w1=(const bf16*)d_in[7]; const bf16* b1=(const bf16*)d_in[8];
  const bf16* w2=(const bf16*)d_in[9]; const bf16* b2=(const bf16*)d_in[10];
  bf16* ws=(bf16*)d_ws; bf16* xn=ws; bf16* q=xn+2048*448; bf16* k=q+2048*4096; bf16* v=k+2048*4096; bf16* ctx=v+2048*4096; bf16* h=ctx+2048*4096; bf16* oa=h+2048*1792; bf16* ob=oa+2048*448;
  ln_k<<<2048,64,0,stream>>>(x,xn);
  gemm_k<<<dim3(64,32),256,0,stream>>>(xn,448,wq,4096,q,4096,448,0,nullptr);
  gemm_k<<<dim3(64,32),256,0,stream>>>(c,1024,wk,4096,k,4096,1024,0,nullptr);
  gemm_k<<<dim3(64,32),256,0,stream>>>(c,1024,wv,4096,v,4096,1024,0,nullptr);
  gemm_k<<<dim3(28,32),256,0,stream>>>(xn,448,w1,1792,h,1792,448,1,b1);
  attn_k<<<dim3(2048,16),256,0,stream>>>(q,k,v,ctx);
  gemm_k<<<dim3(7,32),256,0,stream>>>(ctx,4096,wo,448,oa,448,4096,0,nullptr);
  gemm_k<<<dim3(7,32),256,0,stream>>>(h,1792,w2,448,ob,448,1792,0,nullptr);
  fin_k<<<(2048*448+255)/256,256,0,stream>>>(x,oa,ob,b2,(bf16*)d_out);
}
```

```cpp
#include <hip/hip_runtime.h>
typedef unsigned short u16;
typedef unsigned long long u64;
typedef unsigned u32x4 __attribute__((ext_vector_type(4)));
typedef unsigned u32x8 __attribute__((ext_vector_type(8)));
#define WS_KV   0u
#define WS_XN   (32u << 20)
#define WS_SY   (34u << 20)
#define WS_FL   (WS_SY + 65536u)
#define WS_ST   (35u << 20)
#define WS_QH   (36u << 20)
#define WS_PO   (44u << 20)
#define WS_Y    (52u << 20)
#define WS_END  (96u << 20)
#define LA 17u

static __device__ const unsigned TAB[13440] __attribute__((aligned(16))) = {
0,13440,30240,43680,16,13456,30256,43696,32,13472,30272,43712,48,13488,30288,43728,64,13504,30304,47104,80,13520,30320,47120,96,16896,30336,47136,112,16912,30352,47152,
128,16928,30368,47168,144,16944,30384,47184,160,16960,30400,47200,176,16976,30416,47216,192,16992,33792,47232,208,17008,33808,47248,3584,17024,33824,47264,3600,17040,33840,47280,
3616,17056,33856,47296,3632,17072,33872,47312,3648,17088,33888,50688,3664,17104,33904,50704,3680,20480,33920,50720,3696,20496,33936,50736,3712,20512,33952,50752,3728,20528,33968,50768,
3744,20544,33984,50784,3760,20560,34000,50800,3776,20576,37376,50816,3792,20592,37392,50832,7168,20608,37408,50848,7184,20624,37424,50864,7200,20640,37440,50880,7216,20656,37456,50896,
7232,20672,37472,54272,7248,20688,37488,54288,7264,24064,37504,54304,7280,24080,37520,54320,7296,24096,37536,54336,7312,24112,37552,54352,7328,24128,37568,54368,7344,24144,37584,54384,
7360,24160,40960,54400,7376,24176,40976,54416,10752,24192,40992,54432,10768,24208,41008,54448,10784,24224,41024,54464,10800,24240,41040,54480,10816,24256,41056,57856,10832,24272,41072,57872,
10848,27648,41088,57888,10864,27664,41104,57904,10880,27680,41120,57920,10896,27696,41136,57936,10912,27712,41152,57952,10928,27728,41168,57968,10944,27744,44544,57984,10960,27760,44560,58000,
14336,27776,44576,58016,14352,27792,44592,58032,14368,27808,44608,58048,14384,27824,44624,58064,14400,27840,44640,61440,14416,27856,44656,61456,14432,31232,44672,61472,14448,31248,44688,61488,
64576,78016,94816,0,64592,78032,94832,64,64608,81408,94848,128,64624,81424,94864,192,64640,81440,94880,256,64656,81456,94896,320,64672,81472,94912,384,64688,81488,94928,448,
64704,81504,98304,512,64720,81520,98320,576,68096,81536,98336,640,68112,81552,98352,704,68128,81568,98368,768,68144,81584,98384,832,68160,81600,98400,896,68176,81616,98416,960,
68192,84992,98432,16,68208,85008,98448,80,68224,85024,98464,144,68240,85040,98480,208,68256,85056,98496,272,68272,85072,98512,336,68288,85088,101888,400,68304,85104,101904,464,
71680,85120,101920,528,71696,85136,101936,592,71712,85152,101952,656,71728,85168,101968,720,71744,85184,101984,784,71760,85200,102000,848,71776,88576,102016,912,71792,88592,102032,976,
71808,88608,102048,32,71824,88624,102064,96,71840,88640,102080,160,71856,88656,102096,224,71872,88672,105472,288,71888,88688,105488,352,75264,88704,105504,416,75280,88720,105520,480,
75296,88736,105536,544,75312,88752,105552,608,75328,88768,105568,672,75344,88784,105584,736,75360,92160,105600,800,75376,92176,105616,864,75392,92192,105632,928,75408,92208,105648,992,
75424,92224,105664,48,75440,92240,105680,112,75456,92256,109056,176,75472,92272,109072,240,78848,92288,109088,304,78864,92304,109104,368,78880,92320,109120,432,78896,92336,109136,496,
78912,92352,109152,560,78928,92368,109168,624,78944,95744,109184,688,78960,95760,109200,752,78976,95776,109216,816,78992,95792,109232,880,79008,95808,109248,944,79024,95824,109264,1008,
0,32,64,96,8,40,72,104,16,48,80,112,24,56,88,120,544,512,608,576,552,520,616,584,560,528,624,592,568,536,632,600,
1088,1120,1024,1056,1096,1128,1032,1064,1104,1136,1040,1072,1112,1144,1048,1080,1632,1600,1568,1536,1640,1608,1576,1544,1648,1616,1584,1552,1656,1624,1592,1560,
2176,2208,2240,2272,2184,2216,2248,2280,2192,2224,2256,2288,2200,2232,2264,2296,2720,2688,2784,2752,2728,2696,2792,2760,2736,2704,2800,2768,2744,2712,2808,2776,
3264,3296,3200,3232,3272,3304,3208,3240,3280,3312,3216,3248,3288,3320,3224,3256,3808,3776,3744,3712,3816,3784,3752,3720,3824,3792,3760,3728,3832,3800,3768,3736,
4096,4128,4160,4192,4104,4136,4168,4200,4112,4144,4176,4208,4120,4152,4184,4216,4640,4608,4704,4672,4648,4616,4712,4680,4656,4624,4720,4688,4664,4632,4728,4696,
5184,5216,5120,5152,5192,5224,5128,5160,5200,5232,5136,5168,5208,5240,5144,5176,5728,5696,5664,5632,5736,5704,5672,5640,5744,5712,5680,5648,5752,5720,5688,5656,
6272,6304,6336,6368,6280,6312,6344,6376,6288,6320,6352,6384,6296,6328,6360,6392,6816,6784,6880,6848,6824,6792,6888,6856,6832,6800,6896,6864,6840,6808,6904,6872,
7360,7392,7296,7328,7368,7400,7304,7336,7376,7408,7312,7344,7384,7416,7320,7352,7904,7872,7840,7808,7912,7880,7848,7816,7920,7888,7856,7824,7928,7896,7864,7832,
128,160,192,224,136,168,200,232,144,176,208,240,152,184,216,248,672,640,736,704,680,648,744,712,688,656,752,720,696,664,760,728,
1216,1248,1152,1184,1224,1256,1160,1192,1232,1264,1168,1200,1240,1272,1176,1208,1760,1728,1696,1664,1768,1736,1704,1672,1776,1744,1712,1680,1784,1752,1720,1688,
2048,2080,2112,2144,2056,2088,2120,2152,2064,2096,2128,2160,2072,2104,2136,2168,2592,2560,2656,2624,2600,2568,2664,2632,2608,2576,2672,2640,2616,2584,2680,2648,
3136,3168,3072,3104,3144,3176,3080,3112,3152,3184,3088,3120,3160,3192,3096,3128,3680,3648,3616,3584,3688,3656,3624,3592,3696,3664,3632,3600,3704,3672,3640,3608,
4224,4256,4288,4320,4232,4264,4296,4328,4240,4272,4304,4336,4248,4280,4312,4344,4768,4736,4832,4800,4776,4744,4840,4808,4784,4752,4848,4816,4792,4760,4856,4824,
5312,5344,5248,5280,5320,5352,5256,5288,5328,5360,5264,5296,5336,5368,5272,5304,5856,5824,5792,5760,5864,5832,5800,5768,5872,5840,5808,5776,5880,5848,5816,5784,
6144,6176,6208,6240,6152,6184,6216,6248,6160,6192,6224,6256,6168,6200,6232,6264,6688,6656,6752,6720,6696,6664,6760,6728,6704,6672,6768,6736,6712,6680,6776,6744,
7232,7264,7168,7200,7240,7272,7176,7208,7248,7280,7184,7216,7256,7288,7192,7224,7776,7744,7712,7680,7784,7752,7720,7688,7792,7760,7728,7696,7800,7768,7736,7704,
0,15424,30848,46272,16,15440,30864,46288,32,15456,30880,46304,48,15472,30896,46320,64,15360,30912,46208,80,15376,30928,46224,96,15392,30944,46240,112,15408,30960,46256,
128,15552,30720,46144,144,15568,30736,46160,160,15584,30752,46176,176,15600,30768,46192,192,15488,30784,46080,208,15504,30800,46096,224,15520,30816,46112,240,15536,30832,46128,
256,15680,31104,46528,272,15696,31120,46544,288,15712,31136,46560,304,15728,31152,46576,320,15616,31168,46464,336,15632,31184,46480,352,15648,31200,46496,368,15664,31216,46512,
384,15808,30976,46400,400,15824,30992,46416,416,15840,31008,46432,432,15856,31024,46448,448,15744,31040,46336,464,15760,31056,46352,480,15776,31072,46368,496,15792,31088,46384,
8224,23648,39072,54496,8240,23664,39088,54512,8192,23616,39040,54464,8208,23632,39056,54480,8288,23584,39136,54432,8304,23600,39152,54448,8256,23552,39104,54400,8272,23568,39120,54416,
8352,23776,38944,54368,8368,23792,38960,54384,8320,23744,38912,54336,8336,23760,38928,54352,8416,23712,39008,54304,8432,23728,39024,54320,8384,23680,38976,54272,8400,23696,38992,54288,
8480,23904,39328,54752,8496,23920,39344,54768,8448,23872,39296,54720,8464,23888,39312,54736,8544,23840,39392,54688,8560,23856,39408,54704,8512,23808,39360,54656,8528,23824,39376,54672,
8608,24032,39200,54624,8624,24048,39216,54640,8576,24000,39168,54592,8592,24016,39184,54608,8672,23968,39264,54560,8688,23984,39280,54576,8640,23936,39232,54528,8656,23952,39248,54544,
65536,80960,96384,111808,65552,80976,96400,111824,65568,80992,96416,111840,65584,81008,96432,111856,65600,80896,96448,111744,65616,80912,96464,111760,65632,80928,96480,111776,65648,80944,96496,111792,
65664,81088,96256,111680,65680,81104,96272,111696,65696,81120,96288,111712,65712,81136,96304,111728,65728,81024,96320,111616,65744,81040,96336,111632,65760,81056,96352,111648,65776,81072,96368,111664,
65792,81216,96640,112064,65808,81232,96656,112080,65824,81248,96672,112096,65840,81264,96688,112112,65856,81152,96704,112000,65872,81168,96720,112016,65888,81184,96736,112032,65904,81200,96752,112048,
65920,81344,96512,111936,65936,81360,96528,111952,65952,81376,96544,111968,65968,81392,96560,111984,65984,81280,96576,111872,66000,81296,96592,111888,66016,81312,96608,111904,66032,81328,96624,111920,
73760,89184,104608,120032,73776,89200,104624,120048,73728,89152,104576,120000,73744,89168,104592,120016,73824,89120,104672,119968,73840,89136,104688,119984,73792,89088,104640,119936,73808,89104,104656,119952,
73888,89312,104480,119904,73904,89328,104496,119920,73856,89280,104448,119872,73872,89296,104464,119888,73952,89248,104544,119840,73968,89264,104560,119856,73920,89216,104512,119808,73936,89232,104528,119824,
74016,89440,104864,120288,74032,89456,104880,120304,73984,89408,104832,120256,74000,89424,104848,120272,74080,89376,104928,120224,74096,89392,104944,120240,74048,89344,104896,120192,74064,89360,104912,120208,
74144,89568,104736,120160,74160,89584,104752,120176,74112,89536,104704,120128,74128,89552,104720,120144,74208,89504,104800,120096,74224,89520,104816,120112,74176,89472,104768,120064,74192,89488,104784,120080,
0,2688,6048,8736,16,2704,6064,8752,32,2720,6080,8768,48,2736,6096,8784,64,2752,6112,9472,80,2768,6128,9488,96,3456,6144,9504,112,3472,6160,9520,
128,3488,6176,9536,144,3504,6192,9552,160,3520,6208,9568,176,3536,6224,9584,192,3552,6912,9600,208,3568,6928,9616,896,3584,6944,9632,912,3600,6960,9648,
928,3616,6976,9664,944,3632,6992,9680,960,3648,7008,10368,976,3664,7024,10384,992,4352,7040,10400,1008,4368,7056,10416,1024,4384,7072,10432,1040,4400,7088,10448,
1056,4416,7104,10464,1072,4432,7120,10480,1088,4448,7808,10496,1104,4464,7824,10512,1792,4480,7840,10528,1808,4496,7856,10544,1824,4512,7872,10560,1840,4528,7888,10576,
1856,4544,7904,11264,1872,4560,7920,11280,1888,5248,7936,11296,1904,5264,7952,11312,1920,5280,7968,11328,1936,5296,7984,11344,1952,5312,8000,11360,1968,5328,8016,11376,
1984,5344,8704,11392,2000,5360,8720,11408,2688,5376,8736,11424,2704,5392,8752,11440,2720,5408,8768,11456,2736,5424,8784,11472,2752,5440,8800,12160,2768,5456,8816,12176,
2784,6144,8832,12192,2800,6160,8848,12208,2816,6176,8864,12224,2832,6192,8880,12240,2848,6208,8896,12256,2864,6224,8912,12272,2880,6240,9600,12288,2896,6256,9616,12304,
3584,6272,9632,12320,3600,6288,9648,12336,3616,6304,9664,12352,3632,6320,9680,12368,3648,6336,9696,13056,3664,6352,9712,13072,3680,7040,9728,13088,3696,7056,9744,13104,
16192,18880,22240,0,16208,18896,22256,0,16224,19584,22272,0,16240,19600,22288,0,16256,19616,22304,0,16272,19632,22320,0,16288,19648,22336,0,16304,19664,22352,0,
16320,19680,23040,0,16336,19696,23056,0,17024,19712,23072,0,17040,19728,23088,0,17056,19744,23104,0,17072,19760,23120,0,17088,19776,23136,0,17104,19792,23152,0,
17120,20480,23168,0,17136,20496,23184,0,17152,20512,23200,0,17168,20528,23216,0,17184,20544,23232,0,17200,20560,23248,0,17216,20576,23936,0,17232,20592,23952,0,
17920,20608,23968,0,17936,20624,23984,0,17952,20640,24000,0,17968,20656,24016,0,17984,20672,24032,0,18000,20688,24048,0,18016,21376,24064,0,18032,21392,24080,0,
18048,21408,24096,0,18064,21424,24112,0,18080,21440,24128,0,18096,21456,24144,0,18112,21472,24832,0,18128,21488,24848,0,18816,21504,24864,0,18832,21520,24880,0,
18848,21536,24896,0,18864,21552,24912,0,18880,21568,24928,0,18896,21584,24944,0,18912,22272,24960,0,18928,22288,24976,0,18944,22304,24992,0,18960,22320,25008,0,
18976,22336,25024,0,18992,22352,25040,0,19008,22368,25728,0,19024,22384,25744,0,19712,22400,25760,0,19728,22416,25776,0,19744,22432,25792,0,19760,22448,25808,0,
19776,22464,25824,0,19792,22480,25840,0,19808,23168,25856,0,19824,23184,25872,0,19840,23200,25888,0,19856,23216,25904,0,19872,23232,25920,0,19888,23248,25936,0,
0,252645134,4294967279,0,8,252645132,4294967247,0,16,252645128,4294967183,0,24,252645120,4294967055,0,224,252645120,4294967055,0,232,252645120,4294967055,0,240,252645120,4294967055,0,248,252645120,4294967055,0,
448,252645120,4294967055,0,456,252645120,4294967055,0,464,252645120,4294967055,0,472,252645120,4294967055,0,672,252645120,4294967055,0,680,252645120,4294967055,0,688,252645120,4294967055,0,696,252645120,4294967055,0,
896,252645135,4294967295,0,904,252645135,4294967295,0,912,252645135,4294967295,0,920,252645135,4294967295,0,1120,252645134,4294967279,0,1128,252645132,4294967247,0,1136,252645128,4294967183,0,1144,252645120,4294967055,0,
1344,252645120,4294967055,0,1352,252645120,4294967055,0,1360,252645120,4294967055,0,1368,252645120,4294967055,0,1568,252645120,4294967055,0,1576,252645120,4294967055,0,1584,252645120,4294967055,0,1592,252645120,4294967055,0,
1792,252645135,4294967295,0,1800,252645135,4294967295,0,1808,252645135,4294967295,0,1816,252645135,4294967295,0,2016,252645135,4294967295,0,2024,252645135,4294967295,0,2032,252645135,4294967295,0,2040,252645135,4294967295,0,
2240,252645134,4294967279,0,2248,252645132,4294967247,0,2256,252645128,4294967183,0,2264,252645120,4294967055,0,2464,252645120,4294967055,0,2472,252645120,4294967055,0,2480,252645120,4294967055,0,2488,252645120,4294967055,0,
2688,252645135,4294967295,0,2696,252645135,4294967295,0,2704,252645135,4294967295,0,2712,252645135,4294967295,0,2912,252645135,4294967295,0,2920,252645135,4294967295,0,2928,252645135,4294967295,0,2936,252645135,4294967295,0,
3136,252645135,4294967295,0,3144,252645135,4294967295,0,3152,252645135,4294967295,0,3160,252645135,4294967295,0,3360,252645134,4294967279,0,3368,252645132,4294967247,0,3376,252645128,4294967183,0,3384,252645120,4294967055,0,
114688,128128,144928,158368,114704,128144,144944,158384,114720,128160,144960,158400,114736,128176,144976,158416,114752,128192,144992,161792,114768,128208,145008,161808,114784,131584,145024,161824,114800,131600,145040,161840,
114816,131616,145056,161856,114832,131632,145072,161872,114848,131648,145088,161888,114864,131664,145104,161904,114880,131680,148480,161920,114896,131696,148496,161936,118272,131712,148512,161952,118288,131728,148528,161968,
118304,131744,148544,161984,118320,131760,148560,162000,118336,131776,148576,165376,118352,131792,148592,165392,118368,135168,148608,165408,118384,135184,148624,165424,118400,135200,148640,165440,118416,135216,148656,165456,
118432,135232,148672,165472,118448,135248,148688,165488,118464,135264,152064,165504,118480,135280,152080,165520,121856,135296,152096,165536,121872,135312,152112,165552,121888,135328,152128,165568,121904,135344,152144,165584,
121920,135360,152160,168960,121936,135376,152176,168976,121952,138752,152192,168992,121968,138768,152208,169008,121984,138784,152224,169024,122000,138800,152240,169040,122016,138816,152256,169056,122032,138832,152272,169072,
122048,138848,155648,169088,122064,138864,155664,169104,125440,138880,155680,169120,125456,138896,155696,169136,125472,138912,155712,169152,125488,138928,155728,169168,125504,138944,155744,172544,125520,138960,155760,172560,
125536,142336,155776,172576,125552,142352,155792,172592,125568,142368,155808,172608,125584,142384,155824,172624,125600,142400,155840,172640,125616,142416,155856,172656,125632,142432,159232,172672,125648,142448,159248,172688,
129024,142464,159264,172704,129040,142480,159280,172720,129056,142496,159296,172736,129072,142512,159312,172752,129088,142528,159328,176128,129104,142544,159344,176144,129120,145920,159360,176160,129136,145936,159376,176176,
179264,192704,209504,0,179280,192720,209520,64,179296,196096,209536,128,179312,196112,209552,192,179328,196128,209568,256,179344,196144,209584,320,179360,196160,209600,384,179376,196176,209616,448,
179392,196192,212992,512,179408,196208,213008,576,182784,196224,213024,640,182800,196240,213040,704,182816,196256,213056,768,182832,196272,213072,832,182848,196288,213088,896,182864,196304,213104,960,
182880,199680,213120,16,182896,199696,213136,80,182912,199712,213152,144,182928,199728,213168,208,182944,199744,213184,272,182960,199760,213200,336,182976,199776,216576,400,182992,199792,216592,464,
186368,199808,216608,528,186384,199824,216624,592,186400,199840,216640,656,186416,199856,216656,720,186432,199872,216672,784,186448,199888,216688,848,186464,203264,216704,912,186480,203280,216720,976,
186496,203296,216736,32,186512,203312,216752,96,186528,203328,216768,160,186544,203344,216784,224,186560,203360,220160,288,186576,203376,220176,352,189952,203392,220192,416,189968,203408,220208,480,
189984,203424,220224,544,190000,203440,220240,608,190016,203456,220256,672,190032,203472,220272,736,190048,206848,220288,800,190064,206864,220304,864,190080,206880,220320,928,190096,206896,220336,992,
190112,206912,220352,48,190128,206928,220368,112,190144,206944,223744,176,190160,206960,223760,240,193536,206976,223776,304,193552,206992,223792,368,193568,207008,223808,432,193584,207024,223824,496,
193600,207040,223840,560,193616,207056,223856,624,193632,210432,223872,688,193648,210448,223888,752,193664,210464,223904,816,193680,210480,223920,880,193696,210496,223936,944,193712,210512,223952,1008,
0,32,64,96,8,40,72,104,16,48,80,112,24,56,88,120,544,512,608,576,552,520,616,584,560,528,624,592,568,536,632,600,
1088,1120,1024,1056,1096,1128,1032,1064,1104,1136,1040,1072,1112,1144,1048,1080,1632,1600,1568,1536,1640,1608,1576,1544,1648,1616,1584,1552,1656,1624,1592,1560,
2176,2208,2240,2272,2184,2216,2248,2280,2192,2224,2256,2288,2200,2232,2264,2296,2720,2688,2784,2752,2728,2696,2792,2760,2736,2704,2800,2768,2744,2712,2808,2776,
3264,3296,3200,3232,3272,3304,3208,3240,3280,3312,3216,3248,3288,3320,3224,3256,3808,3776,3744,3712,3816,3784,3752,3720,3824,3792,3760,3728,3832,3800,3768,3736,
4096,4128,4160,4192,4104,4136,4168,4200,4112,4144,4176,4208,4120,4152,4184,4216,4640,4608,4704,4672,4648,4616,4712,4680,4656,4624,4720,4688,4664,4632,4728,4696,
5184,5216,5120,5152,5192,5224,5128,5160,5200,5232,5136,5168,5208,5240,5144,5176,5728,5696,5664,5632,5736,5704,5672,5640,5744,5712,5680,5648,5752,5720,5688,5656,
6272,6304,6336,6368,6280,6312,6344,6376,6288,6320,6352,6384,6296,6328,6360,6392,6816,6784,6880,6848,6824,6792,6888,6856,6832,6800,6896,6864,6840,6808,6904,6872,
7360,7392,7296,7328,7368,7400,7304,7336,7376,7408,7312,7344,7384,7416,7320,7352,7904,7872,7840,7808,7912,7880,7848,7816,7920,7888,7856,7824,7928,7896,7864,7832,
128,160,192,224,136,168,200,232,144,176,208,240,152,184,216,248,672,640,736,704,680,648,744,712,688,656,752,720,696,664,760,728,
1216,1248,1152,1184,1224,1256,1160,1192,1232,1264,1168,1200,1240,1272,1176,1208,1760,1728,1696,1664,1768,1736,1704,1672,1776,1744,1712,1680,1784,1752,1720,1688,
2048,2080,2112,2144,2056,2088,2120,2152,2064,2096,2128,2160,2072,2104,2136,2168,2592,2560,2656,2624,2600,2568,2664,2632,2608,2576,2672,2640,2616,2584,2680,2648,
3136,3168,3072,3104,3144,3176,3080,3112,3152,3184,3088,3120,3160,3192,3096,3128,3680,3648,3616,3584,3688,3656,3624,3592,3696,3664,3632,3600,3704,3672,3640,3608,
4224,4256,4288,4320,4232,4264,4296,4328,4240,4272,4304,4336,4248,4280,4312,4344,4768,4736,4832,4800,4776,4744,4840,4808,4784,4752,4848,4816,4792,4760,4856,4824,
5312,5344,5248,5280,5320,5352,5256,5288,5328,5360,5264,5296,5336,5368,5272,5304,5856,5824,5792,5760,5864,5832,5800,5768,5872,5840,5808,5776,5880,5848,5816,5784,
6144,6176,6208,6240,6152,6184,6216,6248,6160,6192,6224,6256,6168,6200,6232,6264,6688,6656,6752,6720,6696,6664,6760,6728,6704,6672,6768,6736,6712,6680,6776,6744,
7232,7264,7168,7200,7240,7272,7176,7208,7248,7280,7184,7216,7256,7288,7192,7224,7776,7744,7712,7680,7784,7752,7720,7688,7792,7760,7728,7696,7800,7768,7736,7704,
131072,146496,161920,177344,131088,146512,161936,177360,131104,146528,161952,177376,131120,146544,161968,177392,131136,146432,161984,177280,131152,146448,162000,177296,131168,146464,162016,177312,131184,146480,162032,177328,
131200,146624,161792,177216,131216,146640,161808,177232,131232,146656,161824,177248,131248,146672,161840,177264,131264,146560,161856,177152,131280,146576,161872,177168,131296,146592,161888,177184,131312,146608,161904,177200,
131328,146752,162176,177600,131344,146768,162192,177616,131360,146784,162208,177632,131376,146800,162224,177648,131392,146688,162240,177536,131408,146704,162256,177552,131424,146720,162272,177568,131440,146736,162288,177584,
131456,146880,162048,177472,131472,146896,162064,177488,131488,146912,162080,177504,131504,146928,162096,177520,131520,146816,162112,177408,131536,146832,162128,177424,131552,146848,162144,177440,131568,146864,162160,177456,
139296,154720,170144,185568,139312,154736,170160,185584,139264,154688,170112,185536,139280,154704,170128,185552,139360,154656,170208,185504,139376,154672,170224,185520,139328,154624,170176,185472,139344,154640,170192,185488,
139424,154848,170016,185440,139440,154864,170032,185456,139392,154816,169984,185408,139408,154832,170000,185424,139488,154784,170080,185376,139504,154800,170096,185392,139456,154752,170048,185344,139472,154768,170064,185360,
139552,154976,170400,185824,139568,154992,170416,185840,139520,154944,170368,185792,139536,154960,170384,185808,139616,154912,170464,185760,139632,154928,170480,185776,139584,154880,170432,185728,139600,154896,170448,185744,
139680,155104,170272,185696,139696,155120,170288,185712,139648,155072,170240,185664,139664,155088,170256,185680,139744,155040,170336,185632,139760,155056,170352,185648,139712,155008,170304,185600,139728,155024,170320,185616,
196608,212032,227456,242880,196624,212048,227472,242896,196640,212064,227488,242912,196656,212080,227504,242928,196672,211968,227520,242816,196688,211984,227536,242832,196704,212000,227552,242848,196720,212016,227568,242864,
196736,212160,227328,242752,196752,212176,227344,242768,196768,212192,227360,242784,196784,212208,227376,242800,196800,212096,227392,242688,196816,212112,227408,242704,196832,212128,227424,242720,196848,212144,227440,242736,
196864,212288,227712,243136,196880,212304,227728,243152,196896,212320,227744,243168,196912,212336,227760,243184,196928,212224,227776,243072,196944,212240,227792,243088,196960,212256,227808,243104,196976,212272,227824,243120,
196992,212416,227584,243008,197008,212432,227600,243024,197024,212448,227616,243040,197040,212464,227632,243056,197056,212352,227648,242944,197072,212368,227664,242960,197088,212384,227680,242976,197104,212400,227696,242992,
204832,220256,235680,251104,204848,220272,235696,251120,204800,220224,235648,251072,204816,220240,235664,251088,204896,220192,235744,251040,204912,220208,235760,251056,204864,220160,235712,251008,204880,220176,235728,251024,
204960,220384,235552,250976,204976,220400,235568,250992,204928,220352,235520,250944,204944,220368,235536,250960,205024,220320,235616,250912,205040,220336,235632,250928,204992,220288,235584,250880,205008,220304,235600,250896,
205088,220512,235936,251360,205104,220528,235952,251376,205056,220480,235904,251328,205072,220496,235920,251344,205152,220448,236000,251296,205168,220464,236016,251312,205120,220416,235968,251264,205136,220432,235984,251280,
205216,220640,235808,251232,205232,220656,235824,251248,205184,220608,235776,251200,205200,220624,235792,251216,205280,220576,235872,251168,205296,220592,235888,251184,205248,220544,235840,251136,205264,220560,235856,251152,
28672,31360,34720,37408,28688,31376,34736,37424,28704,31392,34752,37440,28720,31408,34768,37456,28736,31424,34784,38144,28752,31440,34800,38160,28768,32128,34816,38176,28784,32144,34832,38192,
28800,32160,34848,38208,28816,32176,34864,38224,28832,32192,34880,38240,28848,32208,34896,38256,28864,32224,35584,38272,28880,32240,35600,38288,29568,32256,35616,38304,29584,32272,35632,38320,
29600,32288,35648,38336,29616,32304,35664,38352,29632,32320,35680,39040,29648,32336,35696,39056,29664,33024,35712,39072,29680,33040,35728,39088,29696,33056,35744,39104,29712,33072,35760,39120,
29728,33088,35776,39136,29744,33104,35792,39152,29760,33120,36480,39168,29776,33136,36496,39184,30464,33152,36512,39200,30480,33168,36528,39216,30496,33184,36544,39232,30512,33200,36560,39248,
30528,33216,36576,39936,30544,33232,36592,39952,30560,33920,36608,39968,30576,33936,36624,39984,30592,33952,36640,40000,30608,33968,36656,40016,30624,33984,36672,40032,30640,34000,36688,40048,
30656,34016,37376,40064,30672,34032,37392,40080,31360,34048,37408,40096,31376,34064,37424,40112,31392,34080,37440,40128,31408,34096,37456,40144,31424,34112,37472,40832,31440,34128,37488,40848,
31456,34816,37504,40864,31472,34832,37520,40880,31488,34848,37536,40896,31504,34864,37552,40912,31520,34880,37568,40928,31536,34896,37584,40944,31552,34912,38272,40960,31568,34928,38288,40976,
32256,34944,38304,40992,32272,34960,38320,41008,32288,34976,38336,41024,32304,34992,38352,41040,32320,35008,38368,41728,32336,35024,38384,41744,32352,35712,38400,41760,32368,35728,38416,41776,
44864,47552,50912,0,44880,47568,50928,0,44896,48256,50944,0,44912,48272,50960,0,44928,48288,50976,0,44944,48304,50992,0,44960,48320,51008,0,44976,48336,51024,0,
44992,48352,51712,0,45008,48368,51728,0,45696,48384,51744,0,45712,48400,51760,0,45728,48416,51776,0,45744,48432,51792,0,45760,48448,51808,0,45776,48464,51824,0,
45792,49152,51840,0,45808,49168,51856,0,45824,49184,51872,0,45840,49200,51888,0,45856,49216,51904,0,45872,49232,51920,0,45888,49248,52608,0,45904,49264,52624,0,
46592,49280,52640,0,46608,49296,52656,0,46624,49312,52672,0,46640,49328,52688,0,46656,49344,52704,0,46672,49360,52720,0,46688,50048,52736,0,46704,50064,52752,0,
46720,50080,52768,0,46736,50096,52784,0,46752,50112,52800,0,46768,50128,52816,0,46784,50144,53504,0,46800,50160,53520,0,47488,50176,53536,0,47504,50192,53552,0,
47520,50208,53568,0,47536,50224,53584,0,47552,50240,53600,0,47568,50256,53616,0,47584,50944,53632,0,47600,50960,53648,0,47616,50976,53664,0,47632,50992,53680,0,
47648,51008,53696,0,47664,51024,53712,0,47680,51040,54400,0,47696,51056,54416,0,48384,51072,54432,0,48400,51088,54448,0,48416,51104,54464,0,48432,51120,54480,0,
48448,51136,54496,0,48464,51152,54512,0,48480,51840,54528,0,48496,51856,54544,0,48512,51872,54560,0,48528,51888,54576,0,48544,51904,54592,0,48560,51920,54608,0,
0,252644864,4294962959,0,8,252644352,4294954767,0,16,252643328,4294938383,0,24,252641280,4294905615,0,224,252641280,4294905615,0,232,252641280,4294905615,0,240,252641280,4294905615,0,248,252641280,4294905615,0,
448,252641280,4294905615,0,456,252641280,4294905615,0,464,252641280,4294905615,0,472,252641280,4294905615,0,672,252641280,4294905615,0,680,252641280,4294905615,0,688,252641280,4294905615,0,696,252641280,4294905615,0,
896,252645120,4294967055,0,904,252645120,4294967055,0,912,252645120,4294967055,0,920,252645120,4294967055,0,1120,252644864,4294962959,0,1128,252644352,4294954767,0,1136,252643328,4294938383,0,1144,252641280,4294905615,0,
1344,252641280,4294905615,0,1352,252641280,4294905615,0,1360,252641280,4294905615,0,1368,252641280,4294905615,0,1568,252641280,4294905615,0,1576,252641280,4294905615,0,1584,252641280,4294905615,0,1592,252641280,4294905615,0,
1792,252645120,4294967055,0,1800,252645120,4294967055,0,1808,252645120,4294967055,0,1816,252645120,4294967055,0,2016,252645120,4294967055,0,2024,252645120,4294967055,0,2032,252645120,4294967055,0,2040,252645120,4294967055,0,
2240,252644864,4294962959,0,2248,252644352,4294954767,0,2256,252643328,4294938383,0,2264,252641280,4294905615,0,2464,252641280,4294905615,0,2472,252641280,4294905615,0,2480,252641280,4294905615,0,2488,252641280,4294905615,0,
2688,252645120,4294967055,0,2696,252645120,4294967055,0,2704,252645120,4294967055,0,2712,252645120,4294967055,0,2912,252645120,4294967055,0,2920,252645120,4294967055,0,2928,252645120,4294967055,0,2936,252645120,4294967055,0,
3136,252645120,4294967055,0,3144,252645120,4294967055,0,3152,252645120,4294967055,0,3160,252645120,4294967055,0,3360,252644864,4294962959,0,3368,252644352,4294954767,0,3376,252643328,4294938383,0,3384,252641280,4294905615,0,
229376,242816,259616,273056,229392,242832,259632,273072,229408,242848,259648,273088,229424,242864,259664,273104,229440,242880,259680,276480,229456,242896,259696,276496,229472,246272,259712,276512,229488,246288,259728,276528,
229504,246304,259744,276544,229520,246320,259760,276560,229536,246336,259776,276576,229552,246352,259792,276592,229568,246368,263168,276608,229584,246384,263184,276624,232960,246400,263200,276640,232976,246416,263216,276656,
232992,246432,263232,276672,233008,246448,263248,276688,233024,246464,263264,280064,233040,246480,263280,280080,233056,249856,263296,280096,233072,249872,263312,280112,233088,249888,263328,280128,233104,249904,263344,280144,
233120,249920,263360,280160,233136,249936,263376,280176,233152,249952,266752,280192,233168,249968,266768,280208,236544,249984,266784,280224,236560,250000,266800,280240,236576,250016,266816,280256,236592,250032,266832,280272,
236608,250048,266848,283648,236624,250064,266864,283664,236640,253440,266880,283680,236656,253456,266896,283696,236672,253472,266912,283712,236688,253488,266928,283728,236704,253504,266944,283744,236720,253520,266960,283760,
236736,253536,270336,283776,236752,253552,270352,283792,240128,253568,270368,283808,240144,253584,270384,283824,240160,253600,270400,283840,240176,253616,270416,283856,240192,253632,270432,287232,240208,253648,270448,287248,
240224,257024,270464,287264,240240,257040,270480,287280,240256,257056,270496,287296,240272,257072,270512,287312,240288,257088,270528,287328,240304,257104,270544,287344,240320,257120,273920,287360,240336,257136,273936,287376,
243712,257152,273952,287392,243728,257168,273968,287408,243744,257184,273984,287424,243760,257200,274000,287440,243776,257216,274016,290816,243792,257232,274032,290832,243808,260608,274048,290848,243824,260624,274064,290864,
293952,307392,324192,0,293968,307408,324208,64,293984,310784,324224,128,294000,310800,324240,192,294016,310816,324256,256,294032,310832,324272,320,294048,310848,324288,384,294064,310864,324304,448,
294080,310880,327680,512,294096,310896,327696,576,297472,310912,327712,640,297488,310928,327728,704,297504,310944,327744,768,297520,310960,327760,832,297536,310976,327776,896,297552,310992,327792,960,
297568,314368,327808,16,297584,314384,327824,80,297600,314400,327840,144,297616,314416,327856,208,297632,314432,327872,272,297648,314448,327888,336,297664,314464,331264,400,297680,314480,331280,464,
301056,314496,331296,528,301072,314512,331312,592,301088,314528,331328,656,301104,314544,331344,720,301120,314560,331360,784,301136,314576,331376,848,301152,317952,331392,912,301168,317968,331408,976,
301184,317984,331424,32,301200,318000,331440,96,301216,318016,331456,160,301232,318032,331472,224,301248,318048,334848,288,301264,318064,334864,352,304640,318080,334880,416,304656,318096,334896,480,
304672,318112,334912,544,304688,318128,334928,608,304704,318144,334944,672,304720,318160,334960,736,304736,321536,334976,800,304752,321552,334992,864,304768,321568,335008,928,304784,321584,335024,992,
304800,321600,335040,48,304816,321616,335056,112,304832,321632,338432,176,304848,321648,338448,240,308224,321664,338464,304,308240,321680,338480,368,308256,321696,338496,432,308272,321712,338512,496,
308288,321728,338528,560,308304,321744,338544,624,308320,325120,338560,688,308336,325136,338576,752,308352,325152,338592,816,308368,325168,338608,880,308384,325184,338624,944,308400,325200,338640,1008,
0,32,64,96,8,40,72,104,16,48,80,112,24,56,88,120,544,512,608,576,552,520,616,584,560,528,624,592,568,536,632,600,
1088,1120,1024,1056,1096,1128,1032,1064,1104,1136,1040,1072,1112,1144,1048,1080,1632,1600,1568,1536,1640,1608,1576,1544,1648,1616,1584,1552,1656,1624,1592,1560,
2176,2208,2240,2272,2184,2216,2248,2280,2192,2224,2256,2288,2200,2232,2264,2296,2720,2688,2784,2752,2728,2696,2792,2760,2736,2704,2800,2768,2744,2712,2808,2776,
3264,3296,3200,3232,3272,3304,3208,3240,3280,3312,3216,3248,3288,3320,3224,3256,3808,3776,3744,3712,3816,3784,3752,3720,3824,3792,3760,3728,3832,3800,3768,3736,
4096,4128,4160,4192,4104,4136,4168,4200,4112,4144,4176,4208,4120,4152,4184,4216,4640,4608,4704,4672,4648,4616,4712,4680,4656,4624,4720,4688,4664,4632,4728,4696,
5184,5216,5120,5152,5192,5224,5128,5160,5200,5232,5136,5168,5208,5240,5144,5176,5728,5696,5664,5632,5736,5704,5672,5640,5744,5712,5680,5648,5752,5720,5688,5656,
6272,6304,6336,6368,6280,6312,6344,6376,6288,6320,6352,6384,6296,6328,6360,6392,6816,6784,6880,6848,6824,6792,6888,6856,6832,6800,6896,6864,6840,6808,6904,6872,
7360,7392,7296,7328,7368,7400,7304,7336,7376,7408,7312,7344,7384,7416,7320,7352,7904,7872,7840,7808,7912,7880,7848,7816,7920,7888,7856,7824,7928,7896,7864,7832,
128,160,192,224,136,168,200,232,144,176,208,240,152,184,216,248,672,640,736,704,680,648,744,712,688,656,752,720,696,664,760,728,
1216,1248,1152,1184,1224,1256,1160,1192,1232,1264,1168,1200,1240,1272,1176,1208,1760,1728,1696,1664,1768,1736,1704,1672,1776,1744,1712,1680,1784,1752,1720,1688,
2048,2080,2112,2144,2056,2088,2120,2152,2064,2096,2128,2160,2072,2104,2136,2168,2592,2560,2656,2624,2600,2568,2664,2632,2608,2576,2672,2640,2616,2584,2680,2648,
3136,3168,3072,3104,3144,3176,3080,3112,3152,3184,3088,3120,3160,3192,3096,3128,3680,3648,3616,3584,3688,3656,3624,3592,3696,3664,3632,3600,3704,3672,3640,3608,
4224,4256,4288,4320,4232,4264,4296,4328,4240,4272,4304,4336,4248,4280,4312,4344,4768,4736,4832,4800,4776,4744,4840,4808,4784,4752,4848,4816,4792,4760,4856,4824,
5312,5344,5248,5280,5320,5352,5256,5288,5328,5360,5264,5296,5336,5368,5272,5304,5856,5824,5792,5760,5864,5832,5800,5768,5872,5840,5808,5776,5880,5848,5816,5784,
6144,6176,6208,6240,6152,6184,6216,6248,6160,6192,6224,6256,6168,6200,6232,6264,6688,6656,6752,6720,6696,6664,6760,6728,6704,6672,6768,6736,6712,6680,6776,6744,
7232,7264,7168,7200,7240,7272,7176,7208,7248,7280,7184,7216,7256,7288,7192,7224,7776,7744,7712,7680,7784,7752,7720,7688,7792,7760,7728,7696,7800,7768,7736,7704,
262144,277568,292992,308416,262160,277584,293008,308432,262176,277600,293024,308448,262192,277616,293040,308464,262208,277504,293056,308352,262224,277520,293072,308368,262240,277536,293088,308384,262256,277552,293104,308400,
262272,277696,292864,308288,262288,277712,292880,308304,262304,277728,292896,308320,262320,277744,292912,308336,262336,277632,292928,308224,262352,277648,292944,308240,262368,277664,292960,308256,262384,277680,292976,308272,
262400,277824,293248,308672,262416,277840,293264,308688,262432,277856,293280,308704,262448,277872,293296,308720,262464,277760,293312,308608,262480,277776,293328,308624,262496,277792,293344,308640,262512,277808,293360,308656,
262528,277952,293120,308544,262544,277968,293136,308560,262560,277984,293152,308576,262576,278000,293168,308592,262592,277888,293184,308480,262608,277904,293200,308496,262624,277920,293216,308512,262640,277936,293232,308528,
270368,285792,301216,316640,270384,285808,301232,316656,270336,285760,301184,316608,270352,285776,301200,316624,270432,285728,301280,316576,270448,285744,301296,316592,270400,285696,301248,316544,270416,285712,301264,316560,
270496,285920,301088,316512,270512,285936,301104,316528,270464,285888,301056,316480,270480,285904,301072,316496,270560,285856,301152,316448,270576,285872,301168,316464,270528,285824,301120,316416,270544,285840,301136,316432,
270624,286048,301472,316896,270640,286064,301488,316912,270592,286016,301440,316864,270608,286032,301456,316880,270688,285984,301536,316832,270704,286000,301552,316848,270656,285952,301504,316800,270672,285968,301520,316816,
270752,286176,301344,316768,270768,286192,301360,316784,270720,286144,301312,316736,270736,286160,301328,316752,270816,286112,301408,316704,270832,286128,301424,316720,270784,286080,301376,316672,270800,286096,301392,316688,
327680,343104,358528,373952,327696,343120,358544,373968,327712,343136,358560,373984,327728,343152,358576,374000,327744,343040,358592,373888,327760,343056,358608,373904,327776,343072,358624,373920,327792,343088,358640,373936,
327808,343232,358400,373824,327824,343248,358416,373840,327840,343264,358432,373856,327856,343280,358448,373872,327872,343168,358464,373760,327888,343184,358480,373776,327904,343200,358496,373792,327920,343216,358512,373808,
327936,343360,358784,374208,327952,343376,358800,374224,327968,343392,358816,374240,327984,343408,358832,374256,328000,343296,358848,374144,328016,343312,358864,374160,328032,343328,358880,374176,328048,343344,358896,374192,
328064,343488,358656,374080,328080,343504,358672,374096,328096,343520,358688,374112,328112,343536,358704,374128,328128,343424,358720,374016,328144,343440,358736,374032,328160,343456,358752,374048,328176,343472,358768,374064,
335904,351328,366752,382176,335920,351344,366768,382192,335872,351296,366720,382144,335888,351312,366736,382160,335968,351264,366816,382112,335984,351280,366832,382128,335936,351232,366784,382080,335952,351248,366800,382096,
336032,351456,366624,382048,336048,351472,366640,382064,336000,351424,366592,382016,336016,351440,366608,382032,336096,351392,366688,381984,336112,351408,366704,382000,336064,351360,366656,381952,336080,351376,366672,381968,
336160,351584,367008,382432,336176,351600,367024,382448,336128,351552,366976,382400,336144,351568,366992,382416,336224,351520,367072,382368,336240,351536,367088,382384,336192,351488,367040,382336,336208,351504,367056,382352,
336288,351712,366880,382304,336304,351728,366896,382320,336256,351680,366848,382272,336272,351696,366864,382288,336352,351648,366944,382240,336368,351664,366960,382256,336320,351616,366912,382208,336336,351632,366928,382224,
57344,60032,63392,66080,57360,60048,63408,66096,57376,60064,63424,66112,57392,60080,63440,66128,57408,60096,63456,66816,57424,60112,63472,66832,57440,60800,63488,66848,57456,60816,63504,66864,
57472,60832,63520,66880,57488,60848,63536,66896,57504,60864,63552,66912,57520,60880,63568,66928,57536,60896,64256,66944,57552,60912,64272,66960,58240,60928,64288,66976,58256,60944,64304,66992,
58272,60960,64320,67008,58288,60976,64336,67024,58304,60992,64352,67712,58320,61008,64368,67728,58336,61696,64384,67744,58352,61712,64400,67760,58368,61728,64416,67776,58384,61744,64432,67792,
58400,61760,64448,67808,58416,61776,64464,67824,58432,61792,65152,67840,58448,61808,65168,67856,59136,61824,65184,67872,59152,61840,65200,67888,59168,61856,65216,67904,59184,61872,65232,67920,
59200,61888,65248,68608,59216,61904,65264,68624,59232,62592,65280,68640,59248,62608,65296,68656,59264,62624,65312,68672,59280,62640,65328,68688,59296,62656,65344,68704,59312,62672,65360,68720,
59328,62688,66048,68736,59344,62704,66064,68752,60032,62720,66080,68768,60048,62736,66096,68784,60064,62752,66112,68800,60080,62768,66128,68816,60096,62784,66144,69504,60112,62800,66160,69520,
60128,63488,66176,69536,60144,63504,66192,69552,60160,63520,66208,69568,60176,63536,66224,69584,60192,63552,66240,69600,60208,63568,66256,69616,60224,63584,66944,69632,60240,63600,66960,69648,
60928,63616,66976,69664,60944,63632,66992,69680,60960,63648,67008,69696,60976,63664,67024,69712,60992,63680,67040,70400,61008,63696,67056,70416,61024,64384,67072,70432,61040,64400,67088,70448,
73536,76224,79584,0,73552,76240,79600,0,73568,76928,79616,0,73584,76944,79632,0,73600,76960,79648,0,73616,76976,79664,0,73632,76992,79680,0,73648,77008,79696,0,
73664,77024,80384,0,73680,77040,80400,0,74368,77056,80416,0,74384,77072,80432,0,74400,77088,80448,0,74416,77104,80464,0,74432,77120,80480,0,74448,77136,80496,0,
74464,77824,80512,0,74480,77840,80528,0,74496,77856,80544,0,74512,77872,80560,0,74528,77888,80576,0,74544,77904,80592,0,74560,77920,81280,0,74576,77936,81296,0,
75264,77952,81312,0,75280,77968,81328,0,75296,77984,81344,0,75312,78000,81360,0,75328,78016,81376,0,75344,78032,81392,0,75360,78720,81408,0,75376,78736,81424,0,
75392,78752,81440,0,75408,78768,81456,0,75424,78784,81472,0,75440,78800,81488,0,75456,78816,82176,0,75472,78832,82192,0,76160,78848,82208,0,76176,78864,82224,0,
76192,78880,82240,0,76208,78896,82256,0,76224,78912,82272,0,76240,78928,82288,0,76256,79616,82304,0,76272,79632,82320,0,76288,79648,82336,0,76304,79664,82352,0,
76320,79680,82368,0,76336,79696,82384,0,76352,79712,83072,0,76368,79728,83088,0,77056,79744,83104,0,77072,79760,83120,0,77088,79776,83136,0,77104,79792,83152,0,
77120,79808,83168,0,77136,79824,83184,0,77152,80512,83200,0,77168,80528,83216,0,77184,80544,83232,0,77200,80560,83248,0,77216,80576,83264,0,77232,80592,83280,0,
0,252575744,4293857039,0,8,252444672,4291759887,0,16,252182528,4287565583,0,24,251658240,4279176975,0,224,251658240,4279176975,0,232,251658240,4279176975,0,240,251658240,4279176975,0,248,251658240,4279176975,0,
448,251658240,4279176975,0,456,251658240,4279176975,0,464,251658240,4279176975,0,472,251658240,4279176975,0,672,251658240,4279176975,0,680,251658240,4279176975,0,688,251658240,4279176975,0,696,251658240,4279176975,0,
896,252641280,4294905615,0,904,252641280,4294905615,0,912,252641280,4294905615,0,920,252641280,4294905615,0,1120,252575744,4293857039,0,1128,252444672,4291759887,0,1136,252182528,4287565583,0,1144,251658240,4279176975,0,
1344,251658240,4279176975,0,1352,251658240,4279176975,0,1360,251658240,4279176975,0,1368,251658240,4279176975,0,1568,251658240,4279176975,0,1576,251658240,4279176975,0,1584,251658240,4279176975,0,1592,251658240,4279176975,0,
1792,252641280,4294905615,0,1800,252641280,4294905615,0,1808,252641280,4294905615,0,1816,252641280,4294905615,0,2016,252641280,4294905615,0,2024,252641280,4294905615,0,2032,252641280,4294905615,0,2040,252641280,4294905615,0,
2240,252575744,4293857039,0,2248,252444672,4291759887,0,2256,252182528,4287565583,0,2264,251658240,4279176975,0,2464,251658240,4279176975,0,2472,251658240,4279176975,0,2480,251658240,4279176975,0,2488,251658240,4279176975,0,
2688,252641280,4294905615,0,2696,252641280,4294905615,0,2704,252641280,4294905615,0,2712,252641280,4294905615,0,2912,252641280,4294905615,0,2920,252641280,4294905615,0,2928,252641280,4294905615,0,2936,252641280,4294905615,0,
3136,252641280,4294905615,0,3144,252641280,4294905615,0,3152,252641280,4294905615,0,3160,252641280,4294905615,0,3360,252575744,4293857039,0,3368,252444672,4291759887,0,3376,252182528,4287565583,0,3384,251658240,4279176975,0,
344064,357504,374304,387744,344080,357520,374320,387760,344096,357536,374336,387776,344112,357552,374352,387792,344128,357568,374368,391168,344144,357584,374384,391184,344160,360960,374400,391200,344176,360976,374416,391216,
344192,360992,374432,391232,344208,361008,374448,391248,344224,361024,374464,391264,344240,361040,374480,391280,344256,361056,377856,391296,344272,361072,377872,391312,347648,361088,377888,391328,347664,361104,377904,391344,
347680,361120,377920,391360,347696,361136,377936,391376,347712,361152,377952,394752,347728,361168,377968,394768,347744,364544,377984,394784,347760,364560,378000,394800,347776,364576,378016,394816,347792,364592,378032,394832,
347808,364608,378048,394848,347824,364624,378064,394864,347840,364640,381440,394880,347856,364656,381456,394896,351232,364672,381472,394912,351248,364688,381488,394928,351264,364704,381504,394944,351280,364720,381520,394960,
351296,364736,381536,398336,351312,364752,381552,398352,351328,368128,381568,398368,351344,368144,381584,398384,351360,368160,381600,398400,351376,368176,381616,398416,351392,368192,381632,398432,351408,368208,381648,398448,
351424,368224,385024,398464,351440,368240,385040,398480,354816,368256,385056,398496,354832,368272,385072,398512,354848,368288,385088,398528,354864,368304,385104,398544,354880,368320,385120,401920,354896,368336,385136,401936,
354912,371712,385152,401952,354928,371728,385168,401968,354944,371744,385184,401984,354960,371760,385200,402000,354976,371776,385216,402016,354992,371792,385232,402032,355008,371808,388608,402048,355024,371824,388624,402064,
358400,371840,388640,402080,358416,371856,388656,402096,358432,371872,388672,402112,358448,371888,388688,402128,358464,371904,388704,405504,358480,371920,388720,405520,358496,375296,388736,405536,358512,375312,388752,405552,
408640,422080,438880,0,408656,422096,438896,64,408672,425472,438912,128,408688,425488,438928,192,408704,425504,438944,256,408720,425520,438960,320,408736,425536,438976,384,408752,425552,438992,448,
408768,425568,442368,512,408784,425584,442384,576,412160,425600,442400,640,412176,425616,442416,704,412192,425632,442432,768,412208,425648,442448,832,412224,425664,442464,896,412240,425680,442480,960,
412256,429056,442496,16,412272,429072,442512,80,412288,429088,442528,144,412304,429104,442544,208,412320,429120,442560,272,412336,429136,442576,336,412352,429152,445952,400,412368,429168,445968,464,
415744,429184,445984,528,415760,429200,446000,592,415776,429216,446016,656,415792,429232,446032,720,415808,429248,446048,784,415824,429264,446064,848,415840,432640,446080,912,415856,432656,446096,976,
415872,432672,446112,32,415888,432688,446128,96,415904,432704,446144,160,415920,432720,446160,224,415936,432736,449536,288,415952,432752,449552,352,419328,432768,449568,416,419344,432784,449584,480,
419360,432800,449600,544,419376,432816,449616,608,419392,432832,449632,672,419408,432848,449648,736,419424,436224,449664,800,419440,436240,449680,864,419456,436256,449696,928,419472,436272,449712,992,
419488,436288,449728,48,419504,436304,449744,112,419520,436320,453120,176,419536,436336,453136,240,422912,436352,453152,304,422928,436368,453168,368,422944,436384,453184,432,422960,436400,453200,496,
422976,436416,453216,560,422992,436432,453232,624,423008,439808,453248,688,423024,439824,453264,752,423040,439840,453280,816,423056,439856,453296,880,423072,439872,453312,944,423088,439888,453328,1008,
0,32,64,96,8,40,72,104,16,48,80,112,24,56,88,120,544,512,608,576,552,520,616,584,560,528,624,592,568,536,632,600,
1088,1120,1024,1056,1096,1128,1032,1064,1104,1136,1040,1072,1112,1144,1048,1080,1632,1600,1568,1536,1640,1608,1576,1544,1648,1616,1584,1552,1656,1624,1592,1560,
2176,2208,2240,2272,2184,2216,2248,2280,2192,2224,2256,2288,2200,2232,2264,2296,2720,2688,2784,2752,2728,2696,2792,2760,2736,2704,2800,2768,2744,2712,2808,2776,
3264,3296,3200,3232,3272,3304,3208,3240,3280,3312,3216,3248,3288,3320,3224,3256,3808,3776,3744,3712,3816,3784,3752,3720,3824,3792,3760,3728,3832,3800,3768,3736,
4096,4128,4160,4192,4104,4136,4168,4200,4112,4144,4176,4208,4120,4152,4184,4216,4640,4608,4704,4672,4648,4616,4712,4680,4656,4624,4720,4688,4664,4632,4728,4696,
5184,5216,5120,5152,5192,5224,5128,5160,5200,5232,5136,5168,5208,5240,5144,5176,5728,5696,5664,5632,5736,5704,5672,5640,5744,5712,5680,5648,5752,5720,5688,5656,
6272,6304,6336,6368,6280,6312,6344,6376,6288,6320,6352,6384,6296,6328,6360,6392,6816,6784,6880,6848,6824,6792,6888,6856,6832,6800,6896,6864,6840,6808,6904,6872,
7360,7392,7296,7328,7368,7400,7304,7336,7376,7408,7312,7344,7384,7416,7320,7352,7904,7872,7840,7808,7912,7880,7848,7816,7920,7888,7856,7824,7928,7896,7864,7832,
128,160,192,224,136,168,200,232,144,176,208,240,152,184,216,248,672,640,736,704,680,648,744,712,688,656,752,720,696,664,760,728,
1216,1248,1152,1184,1224,1256,1160,1192,1232,1264,1168,1200,1240,1272,1176,1208,1760,1728,1696,1664,1768,1736,1704,1672,1776,1744,1712,1680,1784,1752,1720,1688,
2048,2080,2112,2144,2056,2088,2120,2152,2064,2096,2128,2160,2072,2104,2136,2168,2592,2560,2656,2624,2600,2568,2664,2632,2608,2576,2672,2640,2616,2584,2680,2648,
3136,3168,3072,3104,3144,3176,3080,3112,3152,3184,3088,3120,3160,3192,3096,3128,3680,3648,3616,3584,3688,3656,3624,3592,3696,3664,3632,3600,3704,3672,3640,3608,
4224,4256,4288,4320,4232,4264,4296,4328,4240,4272,4304,4336,4248,4280,4312,4344,4768,4736,4832,4800,4776,4744,4840,4808,4784,4752,4848,4816,4792,4760,4856,4824,
5312,5344,5248,5280,5320,5352,5256,5288,5328,5360,5264,5296,5336,5368,5272,5304,5856,5824,5792,5760,5864,5832,5800,5768,5872,5840,5808,5776,5880,5848,5816,5784,
6144,6176,6208,6240,6152,6184,6216,6248,6160,6192,6224,6256,6168,6200,6232,6264,6688,6656,6752,6720,6696,6664,6760,6728,6704,6672,6768,6736,6712,6680,6776,6744,
7232,7264,7168,7200,7240,7272,7176,7208,7248,7280,7184,7216,7256,7288,7192,7224,7776,7744,7712,7680,7784,7752,7720,7688,7792,7760,7728,7696,7800,7768,7736,7704,
393216,408640,424064,439488,393232,408656,424080,439504,393248,408672,424096,439520,393264,408688,424112,439536,393280,408576,424128,439424,393296,408592,424144,439440,393312,408608,424160,439456,393328,408624,424176,439472,
393344,408768,423936,439360,393360,408784,423952,439376,393376,408800,423968,439392,393392,408816,423984,439408,393408,408704,424000,439296,393424,408720,424016,439312,393440,408736,424032,439328,393456,408752,424048,439344,
393472,408896,424320,439744,393488,408912,424336,439760,393504,408928,424352,439776,393520,408944,424368,439792,393536,408832,424384,439680,393552,408848,424400,439696,393568,408864,424416,439712,393584,408880,424432,439728,
393600,409024,424192,439616,393616,409040,424208,439632,393632,409056,424224,439648,393648,409072,424240,439664,393664,408960,424256,439552,393680,408976,424272,439568,393696,408992,424288,439584,393712,409008,424304,439600,
401440,416864,432288,447712,401456,416880,432304,447728,401408,416832,432256,447680,401424,416848,432272,447696,401504,416800,432352,447648,401520,416816,432368,447664,401472,416768,432320,447616,401488,416784,432336,447632,
401568,416992,432160,447584,401584,417008,432176,447600,401536,416960,432128,447552,401552,416976,432144,447568,401632,416928,432224,447520,401648,416944,432240,447536,401600,416896,432192,447488,401616,416912,432208,447504,
401696,417120,432544,447968,401712,417136,432560,447984,401664,417088,432512,447936,401680,417104,432528,447952,401760,417056,432608,447904,401776,417072,432624,447920,401728,417024,432576,447872,401744,417040,432592,447888,
401824,417248,432416,447840,401840,417264,432432,447856,401792,417216,432384,447808,401808,417232,432400,447824,401888,417184,432480,447776,401904,417200,432496,447792,401856,417152,432448,447744,401872,417168,432464,447760,
458752,474176,489600,505024,458768,474192,489616,505040,458784,474208,489632,505056,458800,474224,489648,505072,458816,474112,489664,504960,458832,474128,489680,504976,458848,474144,489696,504992,458864,474160,489712,505008,
458880,474304,489472,504896,458896,474320,489488,504912,458912,474336,489504,504928,458928,474352,489520,504944,458944,474240,489536,504832,458960,474256,489552,504848,458976,474272,489568,504864,458992,474288,489584,504880,
459008,474432,489856,505280,459024,474448,489872,505296,459040,474464,489888,505312,459056,474480,489904,505328,459072,474368,489920,505216,459088,474384,489936,505232,459104,474400,489952,505248,459120,474416,489968,505264,
459136,474560,489728,505152,459152,474576,489744,505168,459168,474592,489760,505184,459184,474608,489776,505200,459200,474496,489792,505088,459216,474512,489808,505104,459232,474528,489824,505120,459248,474544,489840,505136,
466976,482400,497824,513248,466992,482416,497840,513264,466944,482368,497792,513216,466960,482384,497808,513232,467040,482336,497888,513184,467056,482352,497904,513200,467008,482304,497856,513152,467024,482320,497872,513168,
467104,482528,497696,513120,467120,482544,497712,513136,467072,482496,497664,513088,467088,482512,497680,513104,467168,482464,497760,513056,467184,482480,497776,513072,467136,482432,497728,513024,467152,482448,497744,513040,
467232,482656,498080,513504,467248,482672,498096,513520,467200,482624,498048,513472,467216,482640,498064,513488,467296,482592,498144,513440,467312,482608,498160,513456,467264,482560,498112,513408,467280,482576,498128,513424,
467360,482784,497952,513376,467376,482800,497968,513392,467328,482752,497920,513344,467344,482768,497936,513360,467424,482720,498016,513312,467440,482736,498032,513328,467392,482688,497984,513280,467408,482704,498000,513296,
86016,88704,92064,94752,86032,88720,92080,94768,86048,88736,92096,94784,86064,88752,92112,94800,86080,88768,92128,95488,86096,88784,92144,95504,86112,89472,92160,95520,86128,89488,92176,95536,
86144,89504,92192,95552,86160,89520,92208,95568,86176,89536,92224,95584,86192,89552,92240,95600,86208,89568,92928,95616,86224,89584,92944,95632,86912,89600,92960,95648,86928,89616,92976,95664,
86944,89632,92992,95680,86960,89648,93008,95696,86976,89664,93024,96384,86992,89680,93040,96400,87008,90368,93056,96416,87024,90384,93072,96432,87040,90400,93088,96448,87056,90416,93104,96464,
87072,90432,93120,96480,87088,90448,93136,96496,87104,90464,93824,96512,87120,90480,93840,96528,87808,90496,93856,96544,87824,90512,93872,96560,87840,90528,93888,96576,87856,90544,93904,96592,
87872,90560,93920,97280,87888,90576,93936,97296,87904,91264,93952,97312,87920,91280,93968,97328,87936,91296,93984,97344,87952,91312,94000,97360,87968,91328,94016,97376,87984,91344,94032,97392,
88000,91360,94720,97408,88016,91376,94736,97424,88704,91392,94752,97440,88720,91408,94768,97456,88736,91424,94784,97472,88752,91440,94800,97488,88768,91456,94816,98176,88784,91472,94832,98192,
88800,92160,94848,98208,88816,92176,94864,98224,88832,92192,94880,98240,88848,92208,94896,98256,88864,92224,94912,98272,88880,92240,94928,98288,88896,92256,95616,98304,88912,92272,95632,98320,
89600,92288,95648,98336,89616,92304,95664,98352,89632,92320,95680,98368,89648,92336,95696,98384,89664,92352,95712,99072,89680,92368,95728,99088,89696,93056,95744,99104,89712,93072,95760,99120,
102208,104896,108256,0,102224,104912,108272,0,102240,105600,108288,0,102256,105616,108304,0,102272,105632,108320,0,102288,105648,108336,0,102304,105664,108352,0,102320,105680,108368,0,
102336,105696,109056,0,102352,105712,109072,0,103040,105728,109088,0,103056,105744,109104,0,103072,105760,109120,0,103088,105776,109136,0,103104,105792,109152,0,103120,105808,109168,0,
103136,106496,109184,0,103152,106512,109200,0,103168,106528,109216,0,103184,106544,109232,0,103200,106560,109248,0,103216,106576,109264,0,103232,106592,109952,0,103248,106608,109968,0,
103936,106624,109984,0,103952,106640,110000,0,103968,106656,110016,0,103984,106672,110032,0,104000,106688,110048,0,104016,106704,110064,0,104032,107392,110080,0,104048,107408,110096,0,
104064,107424,110112,0,104080,107440,110128,0,104096,107456,110144,0,104112,107472,110160,0,104128,107488,110848,0,104144,107504,110864,0,104832,107520,110880,0,104848,107536,110896,0,
104864,107552,110912,0,104880,107568,110928,0,104896,107584,110944,0,104912,107600,110960,0,104928,108288,110976,0,104944,108304,110992,0,104960,108320,111008,0,104976,108336,111024,0,
104992,108352,111040,0,105008,108368,111056,0,105024,108384,111744,0,105040,108400,111760,0,105728,108416,111776,0,105744,108432,111792,0,105760,108448,111808,0,105776,108464,111824,0,
105792,108480,111840,0,105808,108496,111856,0,105824,109184,111872,0,105840,109200,111888,0,105856,109216,111904,0,105872,109232,111920,0,105888,109248,111936,0,105904,109264,111952,0,
0,234881024,4010741519,0,8,201326592,3473870607,0,16,134217728,2400128783,0,24,0,252645135,0,224,0,252645135,0,232,0,252645135,0,240,0,252645135,0,248,0,252645135,0,
448,0,252645135,0,456,0,252645135,0,464,0,252645135,0,472,0,252645135,0,672,0,252645135,0,680,0,252645135,0,688,0,252645135,0,696,0,252645135,0,
896,251658240,4279176975,0,904,251658240,4279176975,0,912,251658240,4279176975,0,920,251658240,4279176975,0,1120,234881024,4010741519,0,1128,201326592,3473870607,0,1136,134217728,2400128783,0,1144,0,252645135,0,
1344,0,252645135,0,1352,0,252645135,0,1360,0,252645135,0,1368,0,252645135,0,1568,0,252645135,0,1576,0,252645135,0,1584,0,252645135,0,1592,0,252645135,0,
1792,251658240,4279176975,0,1800,251658240,4279176975,0,1808,251658240,4279176975,0,1816,251658240,4279176975,0,2016,251658240,4279176975,0,2024,251658240,4279176975,0,2032,251658240,4279176975,0,2040,251658240,4279176975,0,
2240,234881024,4010741519,0,2248,201326592,3473870607,0,2256,134217728,2400128783,0,2264,0,252645135,0,2464,0,252645135,0,2472,0,252645135,0,2480,0,252645135,0,2488,0,252645135,0,
2688,251658240,4279176975,0,2696,251658240,4279176975,0,2704,251658240,4279176975,0,2712,251658240,4279176975,0,2912,251658240,4279176975,0,2920,251658240,4279176975,0,2928,251658240,4279176975,0,2936,251658240,4279176975,0,
3136,251658240,4279176975,0,3144,251658240,4279176975,0,3152,251658240,4279176975,0,3160,251658240,4279176975,0,3360,234881024,4010741519,0,3368,201326592,3473870607,0,3376,134217728,2400128783,0,3384,0,252645135,0,
0,268435968,3670016,0,524288,268435968,3670016,0,1048576,268435968,3670016,0,1572864,268435968,3670016,0,2097152,268435968,3670016,0,2621440,268435968,3670016,0,3145728,268435968,3670016,0,0,805306592,1605632,0,
458752,805306592,1605632,0,917504,805306592,1605632,0,1376256,805306592,1605632,0,0,1746927616,0,0,65536,1746927616,0,0,32768,1746927616,0,0,131072,1746927616,0,0,98304,1746927616,0,0,
196608,1746927616,0,0,163840,1746927616,0,0,262144,1746927616,0,0,229376,1746927616,0,0,327680,1746927616,0,0,294912,1746927616,0,0,393216,1746927616,0,0,360448,1746927616,0,0,
458752,1746927616,0,0,425984,1746927616,0,0,524288,1746927616,0,0,491520,1746927616,0,0,589824,1746927616,0,0,557056,1746927616,0,0,655360,1746927616,0,0,622592,1746927616,0,0,
720896,1746927616,0,0,688128,1746927616,0,0,786432,1746927616,0,0,753664,1746927616,0,0,851968,1746927616,0,0,819200,1746927616,0,0,917504,1746927616,0,0,884736,1746927616,0,0,
983040,1746927616,0,0,950272,1746927616,0,0,1048576,1746927616,0,0,1015808,1746927616,0,0,1081344,1746927616,0,0,0,1073842176,1605632,0,0,671318016,3670016,0,114688,671318016,3670016,0,
224,1073842176,1605632,0,224,671318016,3670016,0,114912,671318016,3670016,0,448,1073842176,1605632,0,448,671318016,3670016,0,115136,671318016,3670016,0,672,1073842176,1605632,0,672,671318016,3670016,0,
115360,671318016,3670016,0,0,1476395008,0,0,0,1476395008,0,0,0,1476395008,0,0,0,1476395008,0,0,0,1476395008,0,0,0,1476395008,0,0,0,1476395008,0,0,
0,268435968,3670016,0,524288,268435968,3670016,0,1048576,268435968,3670016,0,1572864,268435968,3670016,0,2097152,268435968,3670016,0,2621440,268435968,3670016,0,3145728,268435968,3670016,0,0,805306592,1605632,0,
458752,805306592,1605632,0,917504,805306592,1605632,0,1376256,805306592,1605632,0,0,1746927616,0,0,65536,1746927616,0,0,32768,1746927616,0,0,131072,1746927616,0,0,98304,1746927616,0,0,
196608,1746927616,0,0,163840,1746927616,0,0,262144,1746927616,0,0,229376,1746927616,0,0,327680,1746927616,0,0,294912,1746927616,0,0,393216,1746927616,0,0,360448,1746927616,0,0,
458752,1746927616,0,0,425984,1746927616,0,0,524288,1746927616,0,0,491520,1746927616,0,0,589824,1746927616,0,0,557056,1746927616,0,0,655360,1746927616,0,0,622592,1746927616,0,0,
720896,1746927616,0,0,688128,1746927616,0,0,786432,1746927616,0,0,753664,1746927616,0,0,851968,1746927616,0,0,819200,1746927616,0,0,917504,1746927616,0,0,884736,1746927616,0,0,
983040,1746927616,0,0,950272,1746927616,0,0,1048576,1746927616,0,0,1015808,1746927616,0,0,1081344,1746927616,0,0,0,1073842176,1605632,0,0,671318016,3670016,0,114688,671318016,3670016,0,
224,1073842176,1605632,0,224,671318016,3670016,0,114912,671318016,3670016,0,448,1073842176,1605632,0,448,671318016,3670016,0,115136,671318016,3670016,0,672,1073842176,1605632,0,672,671318016,3670016,0,
115360,671318016,3670016,0,0,1476395008,0,0,0,1476395008,0,0,0,1476395008,0,0,0,1476395008,0,0,0,1476395008,0,0,0,1476395008,0,0,0,1476395008,0,0,
0,268435968,3670016,0,524288,268435968,3670016,0,1048576,268435968,3670016,0,1572864,268435968,3670016,0,2097152,268435968,3670016,0,2621440,268435968,3670016,0,3145728,268435968,3670016,0,0,805306592,1605632,0,
458752,805306592,1605632,0,917504,805306592,1605632,0,1376256,805306592,1605632,0,0,1746927616,0,0,65536,1746927616,0,0,32768,1746927616,0,0,131072,1746927616,0,0,98304,1746927616,0,0,
196608,1746927616,0,0,163840,1746927616,0,0,262144,1746927616,0,0,229376,1746927616,0,0,327680,1746927616,0,0,294912,1746927616,0,0,393216,1746927616,0,0,360448,1746927616,0,0,
458752,1746927616,0,0,425984,1746927616,0,0,524288,1746927616,0,0,491520,1746927616,0,0,589824,1746927616,0,0,557056,1746927616,0,0,655360,1746927616,0,0,622592,1746927616,0,0,
720896,1746927616,0,0,688128,1746927616,0,0,786432,1746927616,0,0,753664,1746927616,0,0,851968,1746927616,0,0,819200,1746927616,0,0,917504,1746927616,0,0,884736,1746927616,0,0,
983040,1746927616,0,0,950272,1746927616,0,0,1048576,1746927616,0,0,1015808,1746927616,0,0,1081344,1746927616,0,0,0,1073842176,1605632,0,0,671318016,3670016,0,114688,671318016,3670016,0,
224,1073842176,1605632,0,224,671318016,3670016,0,114912,671318016,3670016,0,448,1073842176,1605632,0,448,671318016,3670016,0,115136,671318016,3670016,0,672,1073842176,1605632,0,672,671318016,3670016,0,
115360,671318016,3670016,0,0,1476395008,0,0,0,1476395008,0,0,0,1476395008,0,0,0,1476395008,0,0,0,1476395008,0,0,0,1476395008,0,0,0,1476395008,0,0,
0,268435968,3670016,0,524288,268435968,3670016,0,1048576,268435968,3670016,0,1572864,268435968,3670016,0,2097152,268435968,3670016,0,2621440,268435968,3670016,0,3145728,268435968,3670016,0,0,805306592,1605632,0,
458752,805306592,1605632,0,917504,805306592,1605632,0,1376256,805306592,1605632,0,0,1746927616,0,0,65536,1746927616,0,0,32768,1746927616,0,0,131072,1746927616,0,0,98304,1746927616,0,0,
196608,1746927616,0,0,163840,1746927616,0,0,262144,1746927616,0,0,229376,1746927616,0,0,327680,1746927616,0,0,294912,1746927616,0,0,393216,1746927616,0,0,360448,1746927616,0,0,
458752,1746927616,0,0,425984,1746927616,0,0,524288,1746927616,0,0,491520,1746927616,0,0,589824,1746927616,0,0,557056,1746927616,0,0,655360,1746927616,0,0,622592,1746927616,0,0,
720896,1746927616,0,0,688128,1746927616,0,0,786432,1746927616,0,0,753664,1746927616,0,0,851968,1746927616,0,0,819200,1746927616,0,0,917504,1746927616,0,0,884736,1746927616,0,0,
983040,1746927616,0,0,950272,1746927616,0,0,1048576,1746927616,0,0,1015808,1746927616,0,0,1081344,1746927616,0,0,0,1073842176,1605632,0,0,671318016,3670016,0,114688,671318016,3670016,0,
224,1073842176,1605632,0,224,671318016,3670016,0,114912,671318016,3670016,0,448,1073842176,1605632,0,448,671318016,3670016,0,115136,671318016,3670016,0,672,1073842176,1605632,0,672,671318016,3670016,0,
115360,671318016,3670016,0,0,1476395008,0,0,0,1476395008,0,0,0,1476395008,0,0,0,1476395008,0,0,0,1476395008,0,0,0,1476395008,0,0,0,1476395008,0,0,
0,268435968,3670016,0,524288,268435968,3670016,0,1048576,268435968,3670016,0,1572864,268435968,3670016,0,2097152,268435968,3670016,0,2621440,268435968,3670016,0,3145728,268435968,3670016,0,0,805306592,1605632,0,
458752,805306592,1605632,0,917504,805306592,1605632,0,1376256,805306592,1605632,0,0,1746927616,0,0,65536,1746927616,0,0,32768,1746927616,0,0,131072,1746927616,0,0,98304,1746927616,0,0,
196608,1746927616,0,0,163840,1746927616,0,0,262144,1746927616,0,0,229376,1746927616,0,0,327680,1746927616,0,0,294912,1746927616,0,0,393216,1746927616,0,0,360448,1746927616,0,0,
458752,1746927616,0,0,425984,1746927616,0,0,524288,1746927616,0,0,491520,1746927616,0,0,589824,1746927616,0,0,557056,1746927616,0,0,655360,1746927616,0,0,622592,1746927616,0,0,
720896,1746927616,0,0,688128,1746927616,0,0,786432,1746927616,0,0,753664,1746927616,0,0,851968,1746927616,0,0,819200,1746927616,0,0,917504,1746927616,0,0,884736,1746927616,0,0,
983040,1746927616,0,0,950272,1746927616,0,0,1048576,1746927616,0,0,1015808,1746927616,0,0,1081344,1746927616,0,0,0,1073842176,1605632,0,0,671318016,3670016,0,114688,671318016,3670016,0,
224,1073842176,1605632,0,224,671318016,3670016,0,114912,671318016,3670016,0,448,1073842176,1605632,0,448,671318016,3670016,0,115136,671318016,3670016,0,672,1073842176,1605632,0,672,671318016,3670016,0,
115360,671318016,3670016,0,0,1476395008,0,0,0,1476395008,0,0,0,1476395008,0,0,0,1476395008,0,0,0,1476395008,0,0,0,1476395008,0,0,0,1476395008,0,0,
0,268435968,3670016,0,524288,268435968,3670016,0,1048576,268435968,3670016,0,1572864,268435968,3670016,0,2097152,268435968,3670016,0,2621440,268435968,3670016,0,3145728,268435968,3670016,0,0,805306592,1605632,0,
458752,805306592,1605632,0,917504,805306592,1605632,0,1376256,805306592,1605632,0,0,1746927616,0,0,65536,1746927616,0,0,32768,1746927616,0,0,131072,1746927616,0,0,98304,1746927616,0,0,
196608,1746927616,0,0,163840,1746927616,0,0,262144,1746927616,0,0,229376,1746927616,0,0,327680,1746927616,0,0,294912,1746927616,0,0,393216,1746927616,0,0,360448,1746927616,0,0,
458752,1746927616,0,0,425984,1746927616,0,0,524288,1746927616,0,0,491520,1746927616,0,0,589824,1746927616,0,0,557056,1746927616,0,0,655360,1746927616,0,0,622592,1746927616,0,0,
720896,1746927616,0,0,688128,1746927616,0,0,786432,1746927616,0,0,753664,1746927616,0,0,851968,1746927616,0,0,819200,1746927616,0,0,917504,1746927616,0,0,884736,1746927616,0,0,
983040,1746927616,0,0,950272,1746927616,0,0,1048576,1746927616,0,0,1015808,1746927616,0,0,1081344,1746927616,0,0,0,1073842176,1605632,0,0,671318016,3670016,0,114688,671318016,3670016,0,
224,1073842176,1605632,0,224,671318016,3670016,0,114912,671318016,3670016,0,448,1073842176,1605632,0,448,671318016,3670016,0,115136,671318016,3670016,0,672,1073842176,1605632,0,672,671318016,3670016,0,
115360,671318016,3670016,0,0,1476395008,0,0,0,1476395008,0,0,0,1476395008,0,0,0,1476395008,0,0,0,1476395008,0,0,0,1476395008,0,0,0,1476395008,0,0,
0,268435968,3670016,0,524288,268435968,3670016,0,1048576,268435968,3670016,0,1572864,268435968,3670016,0,2097152,268435968,3670016,0,2621440,268435968,3670016,0,3145728,268435968,3670016,0,0,805306592,1605632,0,
458752,805306592,1605632,0,917504,805306592,1605632,0,1376256,805306592,1605632,0,0,1746927616,0,0,65536,1746927616,0,0,32768,1746927616,0,0,131072,1746927616,0,0,98304,1746927616,0,0,
196608,1746927616,0,0,163840,1746927616,0,0,262144,1746927616,0,0,229376,1746927616,0,0,327680,1746927616,0,0,294912,1746927616,0,0,393216,1746927616,0,0,360448,1746927616,0,0,
458752,1746927616,0,0,425984,1746927616,0,0,524288,1746927616,0,0,491520,1746927616,0,0,589824,1746927616,0,0,557056,1746927616,0,0,655360,1746927616,0,0,622592,1746927616,0,0,
720896,1746927616,0,0,688128,1746927616,0,0,786432,1746927616,0,0,753664,1746927616,0,0,851968,1746927616,0,0,819200,1746927616,0,0,917504,1746927616,0,0,884736,1746927616,0,0,
983040,1746927616,0,0,950272,1746927616,0,0,1048576,1746927616,0,0,1015808,1746927616,0,0,1081344,1746927616,0,0,0,1073842176,1605632,0,0,671318016,3670016,0,114688,671318016,3670016,0,
224,1073842176,1605632,0,224,671318016,3670016,0,114912,671318016,3670016,0,448,1073842176,1605632,0,448,671318016,3670016,0,115136,671318016,3670016,0,672,1073842176,1605632,0,672,671318016,3670016,0,
115360,671318016,3670016,0,0,1476395008,0,0,0,1476395008,0,0,0,1476395008,0,0,0,1476395008,0,0,0,1476395008,0,0,0,1476395008,0,0,0,1476395008,0,0,
0,268435968,3670016,0,524288,268435968,3670016,0,1048576,268435968,3670016,0,1572864,268435968,3670016,0,2097152,268435968,3670016,0,2621440,268435968,3670016,0,3145728,268435968,3670016,0,0,805306592,1605632,0,
458752,805306592,1605632,0,917504,805306592,1605632,0,1376256,805306592,1605632,0,0,1746927616,0,0,65536,1746927616,0,0,32768,1746927616,0,0,131072,1746927616,0,0,98304,1746927616,0,0,
196608,1746927616,0,0,163840,1746927616,0,0,262144,1746927616,0,0,229376,1746927616,0,0,327680,1746927616,0,0,294912,1746927616,0,0,393216,1746927616,0,0,360448,1746927616,0,0,
458752,1746927616,0,0,425984,1746927616,0,0,524288,1746927616,0,0,491520,1746927616,0,0,589824,1746927616,0,0,557056,1746927616,0,0,655360,1746927616,0,0,622592,1746927616,0,0,
720896,1746927616,0,0,688128,1746927616,0,0,786432,1746927616,0,0,753664,1746927616,0,0,851968,1746927616,0,0,819200,1746927616,0,0,917504,1746927616,0,0,884736,1746927616,0,0,
983040,1746927616,0,0,950272,1746927616,0,0,1048576,1746927616,0,0,1015808,1746927616,0,0,1114112,1746927616,0,0,1081344,1746927616,0,0,1146880,1746927616,0,0,0,1073842176,1605632,0,
0,671318016,3670016,0,114688,671318016,3670016,0,224,1073842176,1605632,0,224,671318016,3670016,0,114912,671318016,3670016,0,448,1073842176,1605632,0,448,671318016,3670016,0,115136,671318016,3670016,0,
672,1073842176,1605632,0,672,671318016,3670016,0,115360,671318016,3670016,0,0,1476395008,0,0,0,1476395008,0,0,0,1476395008,0,0,0,1476395008,0,0,0,1476395008,0,0,
0,268435968,3670016,0,524288,268435968,3670016,0,1048576,268435968,3670016,0,1572864,268435968,3670016,0,2097152,268435968,3670016,0,2621440,268435968,3670016,0,3145728,268435968,3670016,0,0,805306592,1605632,0,
458752,805306592,1605632,0,917504,805306592,1605632,0,1376256,805306592,1605632,0,1114112,1746927616,0,0,1179648,1746927616,0,0,1146880,1746927616,0,0,1245184,1746927616,0,0,1212416,1746927616,0,0,
1310720,1746927616,0,0,1277952,1746927616,0,0,1376256,1746927616,0,0,1343488,1746927616,0,0,1441792,1746927616,0,0,1409024,1746927616,0,0,1507328,1746927616,0,0,1474560,1746927616,0,0,
1572864,1746927616,0,0,1540096,1746927616,0,0,1638400,1746927616,0,0,1605632,1746927616,0,0,1703936,1746927616,0,0,1671168,1746927616,0,0,1769472,1746927616,0,0,1736704,1746927616,0,0,
1835008,1746927616,0,0,1802240,1746927616,0,0,1900544,1746927616,0,0,1867776,1746927616,0,0,1966080,1746927616,0,0,1933312,1746927616,0,0,2031616,1746927616,0,0,1998848,1746927616,0,0,
2064384,1746927616,0,0,0,1746927616,0,0,65536,1746927616,0,0,32768,1746927616,0,0,98304,1746927616,0,0,0,1073842176,1605632,0,0,671318016,3670016,0,114688,671318016,3670016,0,
224,1073842176,1605632,0,224,671318016,3670016,0,114912,671318016,3670016,0,448,1073842176,1605632,0,448,671318016,3670016,0,115136,671318016,3670016,0,672,1073842176,1605632,0,672,671318016,3670016,0,
115360,671318016,3670016,0,0,1476395008,0,0,0,1476395008,0,0,0,1476395008,0,0,0,1476395008,0,0,0,1476395008,0,0,0,1476395008,0,0,0,1476395008,0,0,
0,268435968,3670016,0,524288,268435968,3670016,0,1048576,268435968,3670016,0,1572864,268435968,3670016,0,2097152,268435968,3670016,0,2621440,268435968,3670016,0,3145728,268435968,3670016,0,0,805306592,1605632,0,
458752,805306592,1605632,0,917504,805306592,1605632,0,1376256,805306592,1605632,0,1114112,1746927616,0,0,1179648,1746927616,0,0,1146880,1746927616,0,0,1245184,1746927616,0,0,1212416,1746927616,0,0,
1310720,1746927616,0,0,1277952,1746927616,0,0,1376256,1746927616,0,0,1343488,1746927616,0,0,1441792,1746927616,0,0,1409024,1746927616,0,0,1507328,1746927616,0,0,1474560,1746927616,0,0,
1572864,1746927616,0,0,1540096,1746927616,0,0,1638400,1746927616,0,0,1605632,1746927616,0,0,1703936,1746927616,0,0,1671168,1746927616,0,0,1769472,1746927616,0,0,1736704,1746927616,0,0,
1835008,1746927616,0,0,1802240,1746927616,0,0,1900544,1746927616,0,0,1867776,1746927616,0,0,1933312,1746927616,0,0,0,1746927616,0,0,65536,1746927616,0,0,32768,1746927616,0,0,
131072,1746927616,0,0,98304,1746927616,0,0,196608,1746927616,0,0,163840,1746927616,0,0,229376,1746927616,0,0,0,1073842176,1605632,0,0,671318016,3670016,0,114688,671318016,3670016,0,
224,1073842176,1605632,0,224,671318016,3670016,0,114912,671318016,3670016,0,448,1073842176,1605632,0,448,671318016,3670016,0,115136,671318016,3670016,0,672,1073842176,1605632,0,672,671318016,3670016,0,
115360,671318016,3670016,0,0,1476395008,0,0,0,1476395008,0,0,0,1476395008,0,0,0,1476395008,0,0,0,1476395008,0,0,0,1476395008,0,0,0,1476395008,0,0,
0,268435968,3670016,0,524288,268435968,3670016,0,1048576,268435968,3670016,0,1572864,268435968,3670016,0,2097152,268435968,3670016,0,2621440,268435968,3670016,0,3145728,268435968,3670016,0,0,805306592,1605632,0,
458752,805306592,1605632,0,917504,805306592,1605632,0,1376256,805306592,1605632,0,1114112,1746927616,0,0,1179648,1746927616,0,0,1146880,1746927616,0,0,1245184,1746927616,0,0,1212416,1746927616,0,0,
1310720,1746927616,0,0,1277952,1746927616,0,0,1376256,1746927616,0,0,1343488,1746927616,0,0,1441792,1746927616,0,0,1409024,1746927616,0,0,1507328,1746927616,0,0,1474560,1746927616,0,0,
1572864,1746927616,0,0,1540096,1746927616,0,0,1638400,1746927616,0,0,1605632,1746927616,0,0,1703936,1746927616,0,0,1671168,1746927616,0,0,1769472,1746927616,0,0,1736704,1746927616,0,0,
1802240,1746927616,0,0,0,1746927616,0,0,65536,1746927616,0,0,32768,1746927616,0,0,131072,1746927616,0,0,98304,1746927616,0,0,196608,1746927616,0,0,163840,1746927616,0,0,
262144,1746927616,0,0,229376,1746927616,0,0,327680,1746927616,0,0,294912,1746927616,0,0,360448,1746927616,0,0,0,1073842176,1605632,0,0,671318016,3670016,0,114688,671318016,3670016,0,
224,1073842176,1605632,0,224,671318016,3670016,0,114912,671318016,3670016,0,448,1073842176,1605632,0,448,671318016,3670016,0,115136,671318016,3670016,0,672,1073842176,1605632,0,672,671318016,3670016,0,
115360,671318016,3670016,0,0,1476395008,0,0,0,1476395008,0,0,0,1476395008,0,0,0,1476395008,0,0,0,1476395008,0,0,0,1476395008,0,0,0,1476395008,0,0,
0,268435968,3670016,0,524288,268435968,3670016,0,1048576,268435968,3670016,0,1572864,268435968,3670016,0,2097152,268435968,3670016,0,2621440,268435968,3670016,0,3145728,268435968,3670016,0,0,805306592,1605632,0,
458752,805306592,1605632,0,917504,805306592,1605632,0,1376256,805306592,1605632,0,1114112,1746927616,0,0,1179648,1746927616,0,0,1146880,1746927616,0,0,1245184,1746927616,0,0,1212416,1746927616,0,0,
1310720,1746927616,0,0,1277952,1746927616,0,0,1376256,1746927616,0,0,1343488,1746927616,0,0,1441792,1746927616,0,0,1409024,1746927616,0,0,1507328,1746927616,0,0,1474560,1746927616,0,0,
1572864,1746927616,0,0,1540096,1746927616,0,0,1638400,1746927616,0,0,1605632,1746927616,0,0,1671168,1746927616,0,0,0,1746927616,0,0,65536,1746927616,0,0,32768,1746927616,0,0,
131072,1746927616,0,0,98304,1746927616,0,0,196608,1746927616,0,0,163840,1746927616,0,0,262144,1746927616,0,0,229376,1746927616,0,0,327680,1746927616,0,0,294912,1746927616,0,0,
393216,1746927616,0,0,360448,1746927616,0,0,458752,1746927616,0,0,425984,1746927616,0,0,491520,1746927616,0,0,0,1073842176,1605632,0,0,671318016,3670016,0,114688,671318016,3670016,0,
224,1073842176,1605632,0,224,671318016,3670016,0,114912,671318016,3670016,0,448,1073842176,1605632,0,448,671318016,3670016,0,115136,671318016,3670016,0,672,1073842176,1605632,0,672,671318016,3670016,0,
115360,671318016,3670016,0,0,1476395008,0,0,0,1476395008,0,0,0,1476395008,0,0,0,1476395008,0,0,0,1476395008,0,0,0,1476395008,0,0,0,1476395008,0,0,
0,268435968,3670016,0,524288,268435968,3670016,0,1048576,268435968,3670016,0,1572864,268435968,3670016,0,2097152,268435968,3670016,0,2621440,268435968,3670016,0,3145728,268435968,3670016,0,0,805306592,1605632,0,
458752,805306592,1605632,0,917504,805306592,1605632,0,1376256,805306592,1605632,0,1114112,1746927616,0,0,1179648,1746927616,0,0,1146880,1746927616,0,0,1245184,1746927616,0,0,1212416,1746927616,0,0,
1310720,1746927616,0,0,1277952,1746927616,0,0,1376256,1746927616,0,0,1343488,1746927616,0,0,1441792,1746927616,0,0,1409024,1746927616,0,0,1507328,1746927616,0,0,1474560,1746927616,0,0,
1540096,1746927616,0,0,0,1746927616,0,0,65536,1746927616,0,0,32768,1746927616,0,0,131072,1746927616,0,0,98304,1746927616,0,0,196608,1746927616,0,0,163840,1746927616,0,0,
262144,1746927616,0,0,229376,1746927616,0,0,327680,1746927616,0,0,294912,1746927616,0,0,393216,1746927616,0,0,360448,1746927616,0,0,458752,1746927616,0,0,425984,1746927616,0,0,
524288,1746927616,0,0,491520,1746927616,0,0,589824,1746927616,0,0,557056,1746927616,0,0,622592,1746927616,0,0,0,1073842176,1605632,0,0,671318016,3670016,0,114688,671318016,3670016,0,
224,1073842176,1605632,0,224,671318016,3670016,0,114912,671318016,3670016,0,448,1073842176,1605632,0,448,671318016,3670016,0,115136,671318016,3670016,0,672,1073842176,1605632,0,672,671318016,3670016,0,
115360,671318016,3670016,0,0,1476395008,0,0,0,1476395008,0,0,0,1476395008,0,0,0,1476395008,0,0,0,1476395008,0,0,0,1476395008,0,0,0,1476395008,0,0,
0,268435968,3670016,0,524288,268435968,3670016,0,1048576,268435968,3670016,0,1572864,268435968,3670016,0,2097152,268435968,3670016,0,2621440,268435968,3670016,0,3145728,268435968,3670016,0,0,805306592,1605632,0,
458752,805306592,1605632,0,917504,805306592,1605632,0,1376256,805306592,1605632,0,1114112,1746927616,0,0,1179648,1746927616,0,0,1146880,1746927616,0,0,1245184,1746927616,0,0,1212416,1746927616,0,0,
1310720,1746927616,0,0,1277952,1746927616,0,0,1376256,1746927616,0,0,1343488,1746927616,0,0,1409024,1746927616,0,0,0,1746927616,0,0,65536,1746927616,0,0,32768,1746927616,0,0,
131072,1746927616,0,0,98304,1746927616,0,0,196608,1746927616,0,0,163840,1746927616,0,0,262144,1746927616,0,0,229376,1746927616,0,0,327680,1746927616,0,0,294912,1746927616,0,0,
393216,1746927616,0,0,360448,1746927616,0,0,458752,1746927616,0,0,425984,1746927616,0,0,524288,1746927616,0,0,491520,1746927616,0,0,589824,1746927616,0,0,557056,1746927616,0,0,
655360,1746927616,0,0,622592,1746927616,0,0,720896,1746927616,0,0,688128,1746927616,0,0,753664,1746927616,0,0,0,1073842176,1605632,0,0,671318016,3670016,0,114688,671318016,3670016,0,
224,1073842176,1605632,0,224,671318016,3670016,0,114912,671318016,3670016,0,448,1073842176,1605632,0,448,671318016,3670016,0,115136,671318016,3670016,0,672,1073842176,1605632,0,672,671318016,3670016,0,
115360,671318016,3670016,0,0,1476395008,0,0,0,1476395008,0,0,0,1476395008,0,0,0,1476395008,0,0,0,1476395008,0,0,0,1476395008,0,0,0,1476395008,0,0,
0,268435968,3670016,0,524288,268435968,3670016,0,1048576,268435968,3670016,0,1572864,268435968,3670016,0,2097152,268435968,3670016,0,2621440,268435968,3670016,0,3145728,268435968,3670016,0,0,805306592,1605632,0,
458752,805306592,1605632,0,917504,805306592,1605632,0,1376256,805306592,1605632,0,1114112,1746927616,0,0,1179648,1746927616,0,0,1146880,1746927616,0,0,1245184,1746927616,0,0,1212416,1746927616,0,0,
1277952,1746927616,0,0,0,1746927616,0,0,65536,1746927616,0,0,32768,1746927616,0,0,131072,1746927616,0,0,98304,1746927616,0,0,196608,1746927616,0,0,163840,1746927616,0,0,
262144,1746927616,0,0,229376,1746927616,0,0,327680,1746927616,0,0,294912,1746927616,0,0,393216,1746927616,0,0,360448,1746927616,0,0,458752,1746927616,0,0,425984,1746927616,0,0,
524288,1746927616,0,0,491520,1746927616,0,0,589824,1746927616,0,0,557056,1746927616,0,0,655360,1746927616,0,0,622592,1746927616,0,0,720896,1746927616,0,0,688128,1746927616,0,0,
786432,1746927616,0,0,753664,1746927616,0,0,851968,1746927616,0,0,819200,1746927616,0,0,884736,1746927616,0,0,0,1073842176,1605632,0,0,671318016,3670016,0,114688,671318016,3670016,0,
224,1073842176,1605632,0,224,671318016,3670016,0,114912,671318016,3670016,0,448,1073842176,1605632,0,448,671318016,3670016,0,115136,671318016,3670016,0,672,1073842176,1605632,0,672,671318016,3670016,0,
115360,671318016,3670016,0,0,1476395008,0,0,0,1476395008,0,0,0,1476395008,0,0,0,1476395008,0,0,0,1476395008,0,0,0,1476395008,0,0,0,1476395008,0,0,
0,268435968,3670016,0,524288,268435968,3670016,0,1048576,268435968,3670016,0,1572864,268435968,3670016,0,2097152,268435968,3670016,0,2621440,268435968,3670016,0,3145728,268435968,3670016,0,0,805306592,1605632,0,
458752,805306592,1605632,0,917504,805306592,1605632,0,1376256,805306592,1605632,0,0,1746927616,0,0,65536,1746927616,0,0,32768,1746927616,0,0,131072,1746927616,0,0,98304,1746927616,0,0,
196608,1746927616,0,0,163840,1746927616,0,0,262144,1746927616,0,0,229376,1746927616,0,0,327680,1746927616,0,0,294912,1746927616,0,0,393216,1746927616,0,0,360448,1746927616,0,0,
458752,1746927616,0,0,425984,1746927616,0,0,524288,1746927616,0,0,491520,1746927616,0,0,589824,1746927616,0,0,557056,1746927616,0,0,655360,1746927616,0,0,622592,1746927616,0,0,
720896,1746927616,0,0,688128,1746927616,0,0,786432,1746927616,0,0,753664,1746927616,0,0,851968,1746927616,0,0,819200,1746927616,0,0,917504,1746927616,0,0,884736,1746927616,0,0,
983040,1746927616,0,0,950272,1746927616,0,0,1015808,1746927616,0,0,0,1073842176,1605632,0,0,671318016,3670016,0,114688,671318016,3670016,0,224,1073842176,1605632,0,224,671318016,3670016,0,
114912,671318016,3670016,0,448,1073842176,1605632,0,448,671318016,3670016,0,115136,671318016,3670016,0,672,1073842176,1605632,0,672,671318016,3670016,0,115360,671318016,3670016,0,0,1476395008,0,0,
0,1476395008,0,0,0,1476395008,0,0,0,1476395008,0,0,0,1476395008,0,0,0,1476395008,0,0,0,1476395008,0,0,0,1476395008,0,0,0,1476395008,0,0,
15,2,16,4294967266,4294967295,4294967266,45,0,14,2,16,4294967268,4294967295,4294967268,45,0,13,2,16,4294967270,4294967295,4294967270,45,0,12,2,16,4294967272,4294967295,4294967272,45,0,
11,2,16,4294967274,4294967295,4294967274,45,0,10,2,16,4294967276,4294967295,4294967276,45,0,9,2,16,4294967278,4294967295,4294967278,45,0,8,0,17,4294967280,4294967295,4294967280,47,1073741824,
0,3,14,4294967283,1,0,45,1073741824,1,3,12,4294967285,3,4294967294,45,1073741824,2,3,10,4294967287,5,4294967292,45,1073741824,3,3,8,4294967289,7,4294967290,45,1073741824,
4,3,6,4294967291,9,4294967288,45,1073741824,5,3,4,4294967293,11,4294967286,45,1073741824,6,3,2,4294967295,13,4294967284,45,1073741824,7,1,15,4294967282,4294967295,4294967282,43,1073741824};

__global__ __launch_bounds__(256, 1) void fused_block(const u16* x, const void* ckv, const void* wq, const void* wk, const void* wv, const void* wo,
                                                      const void* w1, const void* b1, const void* w2, const u16* b2, u16* out, char* ws, void* dbg, const void* zero) {
  const void* ka = (const void*)__builtin_amdgcn_kernarg_segment_ptr();
  u32x8 kk;
  u32x4 p0, p1; u64 p2, p3, p4, p5, p6;
  asm volatile("s_load_dwordx8 %0, %8, 0x8\n s_load_dwordx4 s[24:27], %8, 0x28\n s_load_dwordx2 s[12:13], %8, 0x10\n s_load_dwordx2 s[14:15], %8, 0x40\n"
               "s_load_dwordx2 s[20:21], %8, 0x58\n s_load_dwordx2 s[60:61], %8, 0x0\n s_load_dwordx2 s[88:89], %8, 0x50\n s_load_dwordx2 s[96:97], %8, 0x48\n"
               "s_load_dwordx2 s[28:29], %8, 0x38"
               : "=&s"(kk), "=&{s[24:27]}"(p0), "=&{s[12:15]}"(p1), "=&{s[20:21]}"(p2), "=&{s[60:61]}"(p3), "=&{s[88:89]}"(p4), "=&{s[96:97]}"(p5), "=&{s[28:29]}"(p6) : "s"(ka));
  unsigned bid = blockIdx.x, tid = threadIdx.x;
  asm volatile("" : "+s"(bid));
  asm volatile("" : "+v"(tid));
  const unsigned w = __builtin_amdgcn_readfirstlane(tid >> 6);

  {
    unsigned co = (((bid >> 3) * 4 + w) << 10) + ((tid & 7) << 7);
    asm volatile("s_getpc_b64 s[92:93]\nL_fused_pc:\n v_min_u32 %0, L_fused_code_end-L_fused_pc-4, %0\n global_load_dword v255, %0, s[92:93]"
                 : "+v"(co), "+v"(tid) :: "s92", "s93", "v255", "memory");
  }
  asm volatile("" : "+s"(bid));
  unsigned l = tid & 63;
  const unsigned r = bid >> 3, h = (bid & 7) * 2 + (r & 1), role = r >> 1, i = role & 7, isB = role >> 3, id = h * 16 + role;
  const unsigned own = isB ? i : 15 - i;
  u32x8 kv1, ks1, ks2;
  #pragma unroll
  for (int p = 0; p < 4; ++p) {
    kv1[p] = (8 * p + (l >> 3)) * 2048 + (((l ^ (4 * p + (l >> 4))) & 7) << 4) - 1024 * p;
    const unsigned row = 2 * p + (l >> 5), f = (row & 3) | ((w & 1) << 2);
    kv1[4 + p] = row * 8192 + (((l ^ (f << 1)) & 31) << 4) - 1024 * p;
  }
  ks2[0] = (256 * i + 128 * (w >> 1) + 32 * (w & 1)) * 2048; ks2[1] = w * 65536 + h * 512; ks2[2] = w * 4096;
  ks2[3] = WS_KV + (h << 21) + (4 * i + 2 * (w >> 1)) * 65536 + (isB ? 32768 + (w & 1) * 16384 : (w & 1) * 4096);
  ks2[4] = isB; ks2[5] = WS_FL + id * 128; ks2[6] = WS_SY + id * 128; ks2[7] = role;
  unsigned pw0 = ((64 + (i * 4 + w) * 2 * 5 + (l >> 2)) * 8192 + h * 512 + (l & 3) * 128) | (l < 8 * 5 ? 0u : 0x40000000u);
  unsigned pc0 = ((256 * i + ((h * 2 + isB) * 4 + w) * 2 + (l & 1)) * 2048 + (1 + (l >> 1)) * 128) | (l < 2 * 5 ? 0u : 0x40000000u);
  asm volatile("" : "+v"(pw0), "+v"(pc0), "+v"(kv1));
  asm volatile("s_waitcnt lgkmcnt(0)" : "+s"(kk), "+s"(ks2));

  ks1[0] = kk[0]; ks1[1] = kk[1] & 0xffffu; ks1[2] = 2048 * 1024 * 2; ks1[3] = 0x20000;
  ks1[4] = isB ? kk[6] : kk[4]; ks1[5] = (isB ? kk[7] : kk[5]) & 0xffffu; ks1[6] = 1024 * 4096 * 2; ks1[7] = 0x20000;
  u32x8 rt; unsigned dm;
  asm volatile(
"s_add_u32 s84, s73, 0\n"
"s_add_u32 m0, s74, 0\n"
"s_nop 0\n"
"buffer_load_dwordx4 v164, s[68:71], s84 offen offset:0 lds\n"
"buffer_load_dwordx4 v165, s[68:71], s84 offen offset:1024 lds\n"
"buffer_load_dwordx4 v166, s[68:71], s84 offen offset:2048 lds\n"
"buffer_load_dwordx4 v167, s[68:71], s84 offen offset:3072 lds\n"
"s_lshl_b32 s85, s79, 5\n"
"s_add_u32 s85, s85, 53248\n"
"s_load_dwordx8 s[36:43], s[16:17], s85\n"
"s_add_u32 s84, s73, 262144\n"
"s_add_u32 m0, s74, 16384\n"
"s_nop 0\n"
"buffer_load_dwordx4 v164, s[68:71], s84 offen offset:0 lds\n"
"buffer_load_dwordx4 v165, s[68:71], s84 offen offset:1024 lds\n"
"buffer_load_dwordx4 v166, s[68:71], s84 offen offset:2048 lds\n"
"buffer_load_dwordx4 v167, s[68:71], s84 offen offset:3072 lds\n"
"s_add_u32 s85, s72, 0\n"
"s_add_u32 s82, s74, 98304\n"
"s_mov_b32 m0, s82\n"
"s_nop 0\n"
"buffer_load_dwordx4 v160, s[64:67], s85 offen offset:0 lds\n"
"buffer_load_dwordx4 v161, s[64:67], s85 offen offset:1024 lds\n"
"buffer_load_dwordx4 v162, s[64:67], s85 offen offset:2048 lds\n"
"buffer_load_dwordx4 v163, s[64:67], s85 offen offset:3072 lds\n"
"buffer_load_dword v255, v254, s[68:71], 0 offen\n"
"buffer_load_dword v255, v253, s[64:67], 0 offen\n"
"s_add_u32 s84, s73, 524288\n"
"s_add_u32 m0, s74, 32768\n"
"s_nop 0\n"
"buffer_load_dwordx4 v164, s[68:71], s84 offen offset:0 lds\n"
"buffer_load_dwordx4 v165, s[68:71], s84 offen offset:1024 lds\n"
"buffer_load_dwordx4 v166, s[68:71], s84 offen offset:2048 lds\n"
"buffer_load_dwordx4 v167, s[68:71], s84 offen offset:3072 lds\n"
    : "=&{s[36:43]}"(rt), "=&{s92}"(dm)
    : "{v254}"(pw0), "{v253}"(pc0), "{s[24:27]}"(p0), "{s[12:15]}"(p1), "{s[20:21]}"(p2), "{s[88:89]}"(p4), "{s[96:97]}"(p5), "{s[28:29]}"(p6), "{s33}"(w), "{s7}"(h),
      "{s[16:17]}"(TAB), "{v[160:167]}"(kv1), "{s[64:71]}"(ks1), "{s[72:79]}"(ks2)
    : "s80", "s81", "s82", "s83", "s84", "s85", "s86", "v255", "scc", "memory");

  {
    unsigned to = min(((r * 4 + w) * 4 + (l & 3)) * 128, 53756u);
    asm volatile("global_load_dword v255, %0, %2" : "+v"(to), "+v"(l) : "s"(TAB) : "v255", "memory");
  }
  const unsigned g = l >> 4, c = l & 15, qq = c >> 2, pp = l & 3;
  u32x8 kv2; unsigned kv3[2];
  #pragma unroll
  for (int j = 0; j < 8; ++j) kv2[j] = (8 * g + qq) * 512 + 256 * (w & 1) + ((j ^ (qq | ((g & 1) << 2))) << 5) + 8 * pp;
  kv3[0] = (64 * (w >> 1) + c) * 128 + ((g ^ (c >> 1)) << 4); kv3[1] = (64 * (w >> 1) + c) * 128 + (((4 + g) ^ (c >> 1)) << 4);
  const unsigned lnx = (own * 128 + h * 8 + w * 2 + ((l >> 2) & 1)) * 896 + (l >> 3) * 64 + (l & 3) * 8, lnb = l < 48 ? 0u : 0x40000000u;
  unsigned l16 = l * 16, vz = 0, stv = 0u;
  asm volatile(
"s_add_u32 s85, s72, 131072\n"
"s_add_u32 s82, s74, 114688\n"
"s_mov_b32 m0, s82\n"
"s_nop 0\n"
"buffer_load_dwordx4 v160, s[64:67], s85 offen offset:0 lds\n"
"buffer_load_dwordx4 v161, s[64:67], s85 offen offset:1024 lds\n"
"buffer_load_dwordx4 v162, s[64:67], s85 offen offset:2048 lds\n"
"buffer_load_dwordx4 v163, s[64:67], s85 offen offset:3072 lds\n"
"s_add_u32 s84, s73, 786432\n"
"s_add_u32 m0, s74, 49152\n"
"s_nop 0\n"
"buffer_load_dwordx4 v164, s[68:71], s84 offen offset:0 lds\n"
"buffer_load_dwordx4 v165, s[68:71], s84 offen offset:1024 lds\n"
"buffer_load_dwordx4 v166, s[68:71], s84 offen offset:2048 lds\n"
"buffer_load_dwordx4 v167, s[68:71], s84 offen offset:3072 lds\n"
"s_mov_b32 s22, 0x6000000\n"
"s_mov_b32 s23, 0x20000\n"
"s_mov_b32 s62, 0x1c0000\n"
"s_mov_b32 s63, 0x20000\n"
"s_mov_b32 s90, 0x1c0000\n"
"s_mov_b32 s91, 0x20000\n"
"s_mov_b32 s98, 0x380\n"
"s_mov_b32 s99, 0x20000\n"
"s_mov_b32 s30, 0xe00\n"
"s_mov_b32 s31, 0x20000\n"
"s_waitcnt lgkmcnt(0)\n"
"s_and_b32 s21, s21, 0xffff\n"
"s_and_b32 s61, s61, 0xffff\n"
"s_and_b32 s89, s89, 0xffff\n"
"s_and_b32 s97, s97, 0xffff\n"
"s_and_b32 s29, s29, 0xffff\n"
"buffer_load_dwordx2 v[232:233], v202, s[60:63], 0 offen offset:0\n"
"buffer_load_dwordx2 v[234:235], v202, s[60:63], 0 offen offset:32\n"
"v_add_u32 v202, v252, v202\n"
"buffer_load_dwordx2 v[236:237], v202, s[60:63], 0 offen offset:512\n"
"buffer_load_dwordx2 v[238:239], v202, s[60:63], 0 offen offset:544\n"
"s_mov_b32 s83, 98304\n"
"v_add_u32 v178, s83, v176\n"
"v_add_u32 v179, s83, v177\n"
"s_waitcnt vmcnt(22)\n"
"s_barrier\n"
"ds_read_b64_tr_b16 v[0:1], v168 offset:0\n"
"ds_read_b64_tr_b16 v[2:3], v168 offset:2048\n"
"ds_read_b64_tr_b16 v[8:9], v169 offset:0\n"
"ds_read_b64_tr_b16 v[10:11], v169 offset:2048\n"
"ds_read_b64_tr_b16 v[16:17], v170 offset:0\n"
"ds_read_b64_tr_b16 v[18:19], v170 offset:2048\n"
"ds_read_b64_tr_b16 v[24:25], v171 offset:0\n"
"ds_read_b64_tr_b16 v[26:27], v171 offset:2048\n"
"ds_read_b64_tr_b16 v[32:33], v172 offset:0\n"
"ds_read_b64_tr_b16 v[34:35], v172 offset:2048\n"
"ds_read_b64_tr_b16 v[40:41], v173 offset:0\n"
"ds_read_b64_tr_b16 v[42:43], v173 offset:2048\n"
"ds_read_b64_tr_b16 v[48:49], v174 offset:0\n"
"ds_read_b64_tr_b16 v[50:51], v174 offset:2048\n"
"s_waitcnt lgkmcnt(4)\n"
"ds_read_b64_tr_b16 v[56:57], v175 offset:0\n"
"ds_read_b64_tr_b16 v[58:59], v175 offset:2048\n"
"ds_read_b64_tr_b16 v[4:5], v168 offset:16384\n"
"ds_read_b64_tr_b16 v[6:7], v168 offset:18432\n"
"ds_read_b64_tr_b16 v[12:13], v169 offset:16384\n"
"ds_read_b64_tr_b16 v[14:15], v169 offset:18432\n"
"ds_read_b64_tr_b16 v[20:21], v170 offset:16384\n"
"ds_read_b64_tr_b16 v[22:23], v170 offset:18432\n"
"ds_read_b64_tr_b16 v[28:29], v171 offset:16384\n"
"ds_read_b64_tr_b16 v[30:31], v171 offset:18432\n"
"ds_read_b64_tr_b16 v[36:37], v172 offset:16384\n"
"ds_read_b64_tr_b16 v[38:39], v172 offset:18432\n"
"ds_read_b64_tr_b16 v[44:45], v173 offset:16384\n"
"ds_read_b64_tr_b16 v[46:47], v173 offset:18432\n"
"ds_read_b64_tr_b16 v[52:53], v174 offset:16384\n"
"ds_read_b64_tr_b16 v[54:55], v174 offset:18432\n"
"s_waitcnt lgkmcnt(4)\n"
"ds_read_b64_tr_b16 v[60:61], v175 offset:16384\n"
"ds_read_b64_tr_b16 v[62:63], v175 offset:18432\n"
"s_waitcnt vmcnt(12)\n"
"s_waitcnt lgkmcnt(0)\n"
"s_barrier\n"
"ds_read_b128 v[128:131], v178 offset:0\n"
"ds_read_b128 v[132:135], v179 offset:0\n"
"s_mov_b32 s87, 1073741824\n"
"s_mov_b32 s59, 1073741824\n"
"s_mov_b32 s80, 6\n"
"s_cmp_eq_u32 s76, 0\n"
"s_cbranch_scc0 L_k1v%=\n"
"s_waitcnt lgkmcnt(1)\n"
"v_mfma_f32_16x16x32_bf16 a[0:3], v[0:3], v[128:131], 0\n"
"s_mov_b32 s35, s36\n"
"ds_read_b128 v[136:139], v178 offset:2048\n"
"s_waitcnt lgkmcnt(1)\n"
"v_mfma_f32_16x16x32_bf16 a[0:3], v[4:7], v[132:135], a[0:3]\n"
"s_add_u32 s85, s72, 128\n"
"ds_read_b128 v[140:143], v179 offset:2048\n"
"v_mfma_f32_16x16x32_bf16 a[4:7], v[12:15], v[132:135], 0\n"
"s_mul_i32 s9, s33, 32768\n"
"ds_read_b64_tr_b16 v[64:65], v168 offset:32768\n"
"v_mfma_f32_16x16x32_bf16 a[4:7], v[8:11], v[128:131], a[4:7]\n"
"s_add_u32 s9, s9, 0x8000\n"
"v_mfma_f32_16x16x32_bf16 a[8:11], v[16:19], v[128:131], 0\n"
"s_add_u32 s82, s74, 131072\n"
"s_add_u32 s9, s9, s77\n"
"ds_read_b64_tr_b16 v[66:67], v168 offset:34816\n"
"v_mfma_f32_16x16x32_bf16 a[8:11], v[20:23], v[132:135], a[8:11]\n"
"s_mul_i32 s48, s7, 2048\n"
"v_mfma_f32_16x16x32_bf16 a[12:15], v[28:31], v[132:135], 0\n"
"s_mov_b32 m0, s82\n"
"s_mul_i32 s49, s79, 128\n"
"ds_read_b64_tr_b16 v[72:73], v169 offset:32768\n"
"v_mfma_f32_16x16x32_bf16 a[12:15], v[24:27], v[128:131], a[12:15]\n"
"s_mul_i32 s50, s79, 4\n"
"v_mfma_f32_16x16x32_bf16 a[16:19], v[32:35], v[128:131], 0\n"
"s_add_u32 s50, s50, s33\n"
"ds_read_b64_tr_b16 v[74:75], v169 offset:34816\n"
"v_mfma_f32_16x16x32_bf16 a[16:19], v[36:39], v[132:135], a[16:19]\n"
"buffer_load_dwordx4 v160, s[64:67], s85 offen offset:0 lds\n"
"v_mfma_f32_16x16x32_bf16 a[20:23], v[44:47], v[132:135], 0\n"
"s_mul_i32 s51, s50, 57344\n"
"s_mul_i32 s92, s7, 512\n"
"ds_read_b64_tr_b16 v[80:81], v170 offset:32768\n"
"v_mfma_f32_16x16x32_bf16 a[20:23], v[40:43], v[128:131], a[20:23]\n"
"buffer_load_dwordx4 v161, s[64:67], s85 offen offset:1024 lds\n"
"v_mfma_f32_16x16x32_bf16 a[24:27], v[48:51], v[128:131], 0\n"
"s_add_u32 s51, s51, s92\n"
"ds_read_b64_tr_b16 v[82:83], v170 offset:34816\n"
"v_mfma_f32_16x16x32_bf16 a[24:27], v[52:55], v[132:135], a[24:27]\n"
"buffer_load_dwordx4 v162, s[64:67], s85 offen offset:2048 lds\n"
"v_mfma_f32_16x16x32_bf16 a[28:31], v[60:63], v[132:135], 0\n"
"s_add_u32 s12, s12, s51\n"
"s_addc_u32 s13, s13, 0\n"
"ds_read_b64_tr_b16 v[88:89], v171 offset:32768\n"
"v_mfma_f32_16x16x32_bf16 a[28:31], v[56:59], v[128:131], a[28:31]\n"
"s_waitcnt lgkmcnt(7)\n"
"s_mul_i32 s51, s50, 25088\n"
"s_mul_i32 s93, s7, 224\n"
"v_mfma_f32_16x16x32_bf16 a[32:35], v[0:3], v[136:139], 0\n"
"s_add_u32 s51, s51, s93\n"
"s_add_u32 s84, s73, 1048576\n"
"ds_read_b128 v[144:147], v178 offset:4096\n"
"v_mfma_f32_16x16x32_bf16 a[32:35], v[4:7], v[140:143], a[32:35]\n"
"ds_read_b128 v[148:151], v179 offset:4096\n"
"ds_read_b64_tr_b16 v[90:91], v171 offset:34816\n"
"v_mfma_f32_16x16x32_bf16 a[36:39], v[12:15], v[140:143], 0\n"
"buffer_load_dwordx4 v163, s[64:67], s85 offen offset:3072 lds\n"
"v_mfma_f32_16x16x32_bf16 a[36:39], v[8:11], v[136:139], a[36:39]\n"
"s_add_u32 s26, s26, s51\n"
"s_addc_u32 s27, s27, 0\n"
"ds_read_b64_tr_b16 v[96:97], v172 offset:32768\n"
"v_mfma_f32_16x16x32_bf16 a[40:43], v[16:19], v[136:139], 0\n"
"s_mul_i32 s51, s50, 3584\n"
"s_add_u32 m0, s74, 0\n"
"s_mul_i32 s94, s7, 229376\n"
"v_mfma_f32_16x16x32_bf16 a[40:43], v[20:23], v[140:143], a[40:43]\n"
"s_add_u32 s51, s51, s94\n"
"ds_read_b64_tr_b16 v[98:99], v172 offset:34816\n"
"v_mfma_f32_16x16x32_bf16 a[44:47], v[28:31], v[140:143], 0\n"
"buffer_load_dwordx4 v164, s[68:71], s84 offen offset:0 lds\n"
"v_mfma_f32_16x16x32_bf16 a[44:47], v[24:27], v[136:139], a[44:47]\n"
"s_add_u32 s24, s24, s51\n"
"s_addc_u32 s25, s25, 0\n"
"ds_read_b64_tr_b16 v[104:105], v173 offset:32768\n"
"v_mfma_f32_16x16x32_bf16 a[48:51], v[32:35], v[136:139], 0\n"
"buffer_load_dwordx4 v165, s[68:71], s84 offen offset:1024 lds\n"
"v_mfma_f32_16x16x32_bf16 a[48:51], v[36:39], v[140:143], a[48:51]\n"
"s_mul_i32 s52, s50, 14\n"
"s_min_u32 s52, s52, 770\n"
"ds_read_b64_tr_b16 v[106:107], v173 offset:34816\n"
"v_mfma_f32_16x16x32_bf16 a[52:55], v[44:47], v[140:143], 0\n"
"buffer_load_dwordx4 v166, s[68:71], s84 offen offset:2048 lds\n"
"v_mfma_f32_16x16x32_bf16 a[52:55], v[40:43], v[136:139], a[52:55]\n"
"s_mul_i32 s51, s52, 128\n"
"s_mul_i32 s95, s7, 100352\n"
"ds_read_b64_tr_b16 v[112:113], v174 offset:32768\n"
"v_mfma_f32_16x16x32_bf16 a[56:59], v[48:51], v[136:139], 0\n"
"buffer_load_dwordx4 v167, s[68:71], s84 offen offset:3072 lds\n"
"v_mfma_f32_16x16x32_bf16 a[56:59], v[52:55], v[140:143], a[56:59]\n"
"s_add_u32 s51, s51, s95\n"
"s_mul_i32 s53, s7, 224\n"
"ds_read_b64_tr_b16 v[114:115], v174 offset:34816\n"
"v_mfma_f32_16x16x32_bf16 a[60:63], v[60:63], v[140:143], 0\n"
"s_add_u32 s14, s14, s51\n"
"s_addc_u32 s15, s15, 0\n"
"s_add_u32 s84, s73, 1310720\n"
"v_mfma_f32_16x16x32_bf16 a[60:63], v[56:59], v[136:139], a[60:63]\n"
"s_waitcnt lgkmcnt(7)\n"
"s_and_b32 s56, s79, 7\n"
"v_bfe_u32 v242, v227, 4, 4\n"
"v_mfma_f32_16x16x32_bf16 a[64:67], v[0:3], v[144:147], 0\n"
"s_add_u32 m0, s74, 16384\n"
"ds_read_b128 v[152:155], v178 offset:6144\n"
"ds_read_b64_tr_b16 v[120:121], v175 offset:32768\n"
"v_mfma_f32_16x16x32_bf16 a[64:67], v[4:7], v[148:151], a[64:67]\n"
"ds_read_b128 v[156:159], v179 offset:6144\n"
"v_lshl_add_u32 v182, v242, 7, s48\n"
"v_mfma_f32_16x16x32_bf16 a[68:71], v[12:15], v[148:151], 0\n"
"ds_read_b64_tr_b16 v[122:123], v175 offset:34816\n"
"v_lshl_add_u32 v240, v242, 11, s49\n"
"v_mfma_f32_16x16x32_bf16 a[68:71], v[8:11], v[144:147], a[68:71]\n"
"buffer_load_dwordx4 v164, s[68:71], s84 offen offset:0 lds\n"
"v_mfma_f32_16x16x32_bf16 a[72:75], v[16:19], v[144:147], 0\n"
"v_and_b32 v241, 256, v227\n"
"v_cmp_eq_u32 vcc, 0, v241\n"
"v_mfma_f32_16x16x32_bf16 a[72:75], v[20:23], v[148:151], a[72:75]\n"
"s_add_u32 s83, s83, 16384\n"
"v_cndmask_b32 v181, v240, v182, vcc\n"
"v_add_u32 v181, 0x2200000, v181\n"
"v_mfma_f32_16x16x32_bf16 a[76:79], v[28:31], v[148:151], 0\n"
"buffer_load_dword v180, v181, s[20:23], 0 offen offset:0 sc1\n"
"v_mfma_f32_16x16x32_bf16 a[76:79], v[24:27], v[144:147], a[76:79]\n"
"s_cmp_lt_u32 s83, 163840\n"
"s_cselect_b32 s83, s83, 65536\n"
"v_add_u32 v182, 0x2210000, v182\n"
"v_mfma_f32_16x16x32_bf16 a[80:83], v[32:35], v[144:147], 0\n"
"buffer_load_dwordx4 v165, s[68:71], s84 offen offset:1024 lds\n"
"v_mfma_f32_16x16x32_bf16 a[80:83], v[36:39], v[148:151], a[80:83]\n"
"v_add_u32 v178, s83, v176\n"
"v_mov_b32 v204, s12\n"
"v_mfma_f32_16x16x32_bf16 a[84:87], v[44:47], v[148:151], 0\n"
"v_add_u32 v179, s83, v177\n"
"v_mov_b32 v205, s13\n"
"v_mfma_f32_16x16x32_bf16 a[84:87], v[40:43], v[144:147], a[84:87]\n"
"buffer_load_dwordx4 v166, s[68:71], s84 offen offset:2048 lds\n"
"v_mfma_f32_16x16x32_bf16 a[88:91], v[48:51], v[144:147], 0\n"
"buffer_load_dwordx4 v167, s[68:71], s84 offen offset:3072 lds\n"
"v_mfma_f32_16x16x32_bf16 a[88:91], v[52:55], v[148:151], a[88:91]\n"
"v_mov_b32 v206, 8192\n"
"v_mov_b32 v207, 0\n"
"v_mfma_f32_16x16x32_bf16 a[92:95], v[60:63], v[148:151], 0\n"
"s_waitcnt lgkmcnt(0)\n"
"v_mov_b32 v241, 384\n"
"v_lshrrev_b32 v240, 6, v227\n"
"v_mfma_f32_16x16x32_bf16 a[92:95], v[56:59], v[144:147], a[92:95]\n"
"s_waitcnt vmcnt(17)\n"
"s_barrier\n"
"v_cmp_eq_u32 vcc, 1, v240\n"
"v_mfma_f32_16x16x32_bf16 a[96:99], v[0:3], v[152:155], 0\n"
"ds_read_b128 v[128:131], v178 offset:0\n"
"v_mov_b32 v242, s26\n"
"v_mfma_f32_16x16x32_bf16 a[96:99], v[4:7], v[156:159], a[96:99]\n"
"s_add_u32 s85, s72, 131200\n"
"ds_read_b128 v[132:135], v179 offset:0\n"
"v_cndmask_b32 v204, v204, v242, vcc\n"
"v_mfma_f32_16x16x32_bf16 a[100:103], v[12:15], v[156:159], 0\n"
"v_mov_b32 v242, s27\n"
"v_cndmask_b32 v205, v205, v242, vcc\n"
"v_mfma_f32_16x16x32_bf16 a[100:103], v[8:11], v[152:155], a[100:103]\n"
"s_add_u32 s82, s74, 147456\n"
"v_mov_b32 v242, 3584\n"
"v_cndmask_b32 v206, v206, v242, vcc\n"
"v_mfma_f32_16x16x32_bf16 a[104:107], v[16:19], v[152:155], 0\n"
"v_mov_b32 v242, 220\n"
"v_cndmask_b32 v241, v241, v242, vcc\n"
"v_mfma_f32_16x16x32_bf16 a[104:107], v[20:23], v[156:159], a[104:107]\n"
"s_mov_b32 m0, s82\n"
"v_cmp_eq_u32 vcc, 2, v240\n"
"v_mov_b32 v242, s24\n"
"v_mfma_f32_16x16x32_bf16 a[108:111], v[28:31], v[156:159], 0\n"
"v_cndmask_b32 v204, v204, v242, vcc\n"
"v_mov_b32 v242, s25\n"
"v_mfma_f32_16x16x32_bf16 a[108:111], v[24:27], v[152:155], a[108:111]\n"
"buffer_load_dwordx4 v160, s[64:67], s85 offen offset:0 lds\n"
"v_mfma_f32_16x16x32_bf16 a[112:115], v[32:35], v[152:155], 0\n"
"v_cndmask_b32 v205, v205, v242, vcc\n"
"v_mov_b32 v242, 512\n"
"v_mfma_f32_16x16x32_bf16 a[112:115], v[36:39], v[156:159], a[112:115]\n"
"buffer_load_dwordx4 v161, s[64:67], s85 offen offset:1024 lds\n"
"v_mfma_f32_16x16x32_bf16 a[116:119], v[44:47], v[156:159], 0\n"
"v_mfma_f32_16x16x32_bf16 a[116:119], v[40:43], v[152:155], a[116:119]\n"
"buffer_load_dwordx4 v162, s[64:67], s85 offen offset:2048 lds\n"
"v_mfma_f32_16x16x32_bf16 a[120:123], v[48:51], v[152:155], 0\n"
"v_mfma_f32_16x16x32_bf16 a[120:123], v[52:55], v[156:159], a[120:123]\n"
"buffer_load_dwordx4 v163, s[64:67], s85 offen offset:3072 lds\n"
"v_mfma_f32_16x16x32_bf16 a[124:127], v[60:63], v[156:159], 0\n"
"v_mfma_f32_16x16x32_bf16 a[124:127], v[56:59], v[152:155], a[124:127]\n"
"s_waitcnt lgkmcnt(0)\n"
"v_mfma_f32_16x16x32_bf16 a[128:131], v[0:3], v[128:131], 0\n"
"ds_read_b128 v[136:139], v178 offset:2048\n"
"v_cndmask_b32 v206, v206, v242, vcc\n"
"v_mfma_f32_16x16x32_bf16 a[128:131], v[4:7], v[132:135], a[128:131]\n"
"s_add_u32 s84, s73, 1572864\n"
"ds_read_b128 v[140:143], v179 offset:2048\n"
"v_mov_b32 v242, 384\n"
"v_mfma_f32_16x16x32_bf16 a[132:135], v[12:15], v[132:135], 0\n"
"ds_read_b64_tr_b16 v[68:69], v168 offset:49152\n"
"v_cndmask_b32 v241, v241, v242, vcc\n"
"v_mfma_f32_16x16x32_bf16 a[132:135], v[8:11], v[128:131], a[132:135]\n"
"s_add_u32 m0, s74, 32768\n"
"v_cmp_eq_u32 vcc, 3, v240\n"
"v_mov_b32 v242, s14\n"
"v_mfma_f32_16x16x32_bf16 a[136:139], v[16:19], v[128:131], 0\n"
"ds_read_b64_tr_b16 v[70:71], v168 offset:51200\n"
"v_cndmask_b32 v204, v204, v242, vcc\n"
"v_mfma_f32_16x16x32_bf16 a[136:139], v[20:23], v[132:135], a[136:139]\n"
"buffer_load_dwordx4 v164, s[68:71], s84 offen offset:0 lds\n"
"v_mfma_f32_16x16x32_bf16 a[140:143], v[28:31], v[132:135], 0\n"
"ds_read_b64_tr_b16 v[76:77], v169 offset:49152\n"
"v_mov_b32 v242, s15\n"
"v_mfma_f32_16x16x32_bf16 a[140:143], v[24:27], v[128:131], a[140:143]\n"
"buffer_load_dwordx4 v165, s[68:71], s84 offen offset:1024 lds\n"
"v_mfma_f32_16x16x32_bf16 a[144:147], v[32:35], v[128:131], 0\n"
"ds_read_b64_tr_b16 v[78:79], v169 offset:51200\n"
"v_cndmask_b32 v205, v205, v242, vcc\n"
"v_mfma_f32_16x16x32_bf16 a[144:147], v[36:39], v[132:135], a[144:147]\n"
"buffer_load_dwordx4 v166, s[68:71], s84 offen offset:2048 lds\n"
"v_mfma_f32_16x16x32_bf16 a[148:151], v[44:47], v[132:135], 0\n"
"ds_read_b64_tr_b16 v[84:85], v170 offset:49152\n"
"v_mov_b32 v242, 256\n"
"v_mfma_f32_16x16x32_bf16 a[148:151], v[40:43], v[128:131], a[148:151]\n"
"buffer_load_dwordx4 v167, s[68:71], s84 offen offset:3072 lds\n"
"v_mfma_f32_16x16x32_bf16 a[152:155], v[48:51], v[128:131], 0\n"
"ds_read_b64_tr_b16 v[86:87], v170 offset:51200\n"
"v_cndmask_b32 v206, v206, v242, vcc\n"
"v_mfma_f32_16x16x32_bf16 a[152:155], v[52:55], v[132:135], a[152:155]\n"
"s_add_u32 s85, s72, 256\n"
"v_mov_b32 v242, 128\n"
"v_cndmask_b32 v241, v241, v242, vcc\n"
"v_mfma_f32_16x16x32_bf16 a[156:159], v[60:63], v[132:135], 0\n"
"ds_read_b64_tr_b16 v[92:93], v171 offset:49152\n"
"v_cmp_eq_u32 vcc, 4, v240\n"
"v_mfma_f32_16x16x32_bf16 a[156:159], v[56:59], v[128:131], a[156:159]\n"
"s_waitcnt lgkmcnt(7)\n"
"v_mov_b32 v242, s96\n"
"v_cndmask_b32 v204, v204, v242, vcc\n"
"v_mfma_f32_16x16x32_bf16 a[160:163], v[0:3], v[136:139], 0\n"
"s_mul_i32 s36, s35, 114688\n"
"ds_read_b128 v[144:147], v178 offset:4096\n"
"v_mov_b32 v242, s97\n"
"v_mfma_f32_16x16x32_bf16 a[160:163], v[4:7], v[140:143], a[160:163]\n"
"s_add_u32 s82, s74, 65536\n"
"ds_read_b128 v[148:151], v179 offset:4096\n"
"ds_read_b64_tr_b16 v[94:95], v171 offset:51200\n"
"v_mfma_f32_16x16x32_bf16 a[164:167], v[12:15], v[140:143], 0\n"
"s_mul_i32 s92, s33, 28672\n"
"v_cndmask_b32 v205, v205, v242, vcc\n"
"v_mov_b32 v242, 128\n"
"v_mfma_f32_16x16x32_bf16 a[164:167], v[8:11], v[136:139], a[164:167]\n"
"s_add_u32 s36, s36, s92\n"
"ds_read_b64_tr_b16 v[100:101], v172 offset:49152\n"
"v_cndmask_b32 v206, v206, v242, vcc\n"
"v_mfma_f32_16x16x32_bf16 a[168:171], v[16:19], v[136:139], 0\n"
"s_add_u32 s36, s36, 0x2000000\n"
"v_mov_b32 v242, 0\n"
"v_cndmask_b32 v241, v241, v242, vcc\n"
"v_mfma_f32_16x16x32_bf16 a[168:171], v[20:23], v[140:143], a[168:171]\n"
"s_mov_b32 m0, s82\n"
"ds_read_b64_tr_b16 v[102:103], v172 offset:51200\n"
"v_cmp_eq_u32 vcc, 5, v240\n"
"v_mfma_f32_16x16x32_bf16 a[172:175], v[28:31], v[140:143], 0\n"
"s_mul_i32 s51, s7, 8\n"
"v_mov_b32 v242, s28\n"
"v_cndmask_b32 v204, v204, v242, vcc\n"
"v_mfma_f32_16x16x32_bf16 a[172:175], v[24:27], v[136:139], a[172:175]\n"
"s_add_u32 s51, s51, s56\n"
"ds_read_b64_tr_b16 v[108:109], v173 offset:49152\n"
"v_mov_b32 v242, s29\n"
"v_mfma_f32_16x16x32_bf16 a[176:179], v[32:35], v[136:139], 0\n"
"s_mul_i32 s11, s51, 65536\n"
"v_cndmask_b32 v205, v205, v242, vcc\n"
"v_mov_b32 v242, 0\n"
"v_mfma_f32_16x16x32_bf16 a[176:179], v[36:39], v[140:143], a[176:179]\n"
"s_mul_i32 s93, s33, 16384\n"
"ds_read_b64_tr_b16 v[110:111], v173 offset:51200\n"
"v_cndmask_b32 v206, v206, v242, vcc\n"
"v_mfma_f32_16x16x32_bf16 a[180:183], v[44:47], v[140:143], 0\n"
"s_add_u32 s11, s11, s93\n"
"v_mov_b32 v242, 220\n"
"v_cndmask_b32 v241, v241, v242, vcc\n"
"v_mfma_f32_16x16x32_bf16 a[180:183], v[40:43], v[136:139], a[180:183]\n"
"s_add_u32 s11, s11, 0x2400000\n"
"ds_read_b64_tr_b16 v[116:117], v174 offset:49152\n"
"v_mov_b32 v242, s53\n"
"v_mfma_f32_16x16x32_bf16 a[184:187], v[48:51], v[136:139], 0\n"
"s_or_b32 s11, s11, s43\n"
"v_cndmask_b32 v242, 0, v242, vcc\n"
"v_and_b32 v240, 48, v227\n"
"v_mfma_f32_16x16x32_bf16 a[184:187], v[52:55], v[140:143], a[184:187]\n"
"s_mul_i32 s43, s51, 65536\n"
"ds_read_b64_tr_b16 v[118:119], v174 offset:51200\n"
"v_lshlrev_b32 v240, 3, v240\n"
"v_mfma_f32_16x16x32_bf16 a[188:191], v[60:63], v[140:143], 0\n"
"s_mul_i32 s94, s33, 16384\n"
"v_min_u32 v241, v241, v240\n"
"v_add_u32 v241, v241, v242\n"
"v_mfma_f32_16x16x32_bf16 a[188:191], v[56:59], v[136:139], a[188:191]\n"
"s_waitcnt lgkmcnt(7)\n"
"v_add_co_u32 v204, vcc, v204, v241\n"
"s_nop 1\n"
"v_addc_co_u32 v205, vcc, 0, v205, vcc\n"
"v_mfma_f32_16x16x32_bf16 a[192:195], v[0:3], v[144:147], 0\n"
"s_add_u32 s43, s43, s94\n"
"ds_read_b128 v[152:155], v178 offset:6144\n"
"ds_read_b64_tr_b16 v[124:125], v175 offset:49152\n"
"v_mfma_f32_16x16x32_bf16 a[192:195], v[4:7], v[148:151], a[192:195]\n"
"s_mul_i32 s44, s51, 4096\n"
"ds_read_b128 v[156:159], v179 offset:6144\n"
"v_mov_b32 v248, 0\n"
"v_mfma_f32_16x16x32_bf16 a[196:199], v[12:15], v[148:151], 0\n"
"s_mul_i32 s95, s33, 1024\n"
"ds_read_b64_tr_b16 v[126:127], v175 offset:51200\n"
"v_mov_b32 v249, 0\n"
"v_mfma_f32_16x16x32_bf16 a[196:199], v[8:11], v[144:147], a[196:199]\n"
"s_add_u32 s44, s44, s95\n"
"v_mov_b32 v250, 0\n"
"v_mov_b32 v251, 0\n"
"v_mfma_f32_16x16x32_bf16 a[200:203], v[16:19], v[144:147], 0\n"
"buffer_store_dwordx4 v[248:251], v231, s[20:23], s77 offen offset:0 sc1\n"
"v_mfma_f32_16x16x32_bf16 a[200:203], v[20:23], v[148:151], a[200:203]\n"
"s_add_u32 s83, s83, 16384\n"
"buffer_store_dwordx4 v[248:251], v231, s[20:23], s9 offen offset:0 sc1\n"
"v_mfma_f32_16x16x32_bf16 a[204:207], v[28:31], v[148:151], 0\n"
"buffer_load_dwordx4 v160, s[64:67], s85 offen offset:0 lds\n"
"v_mfma_f32_16x16x32_bf16 a[204:207], v[24:27], v[144:147], a[204:207]\n"
"s_cmp_lt_u32 s83, 163840\n"
"s_cselect_b32 s83, s83, 65536\n"
"s_add_u32 s44, s44, 0x2300000\n"
"v_mfma_f32_16x16x32_bf16 a[208:211], v[32:35], v[144:147], 0\n"
"buffer_load_dwordx4 v161, s[64:67], s85 offen offset:1024 lds\n"
"v_mfma_f32_16x16x32_bf16 a[208:211], v[36:39], v[148:151], a[208:211]\n"
"s_mul_i32 s45, s76, -4\n"
"s_add_u32 s45, s45, 0x8\n"
"v_add_u32 v178, s83, v176\n"
"v_mfma_f32_16x16x32_bf16 a[212:215], v[44:47], v[148:151], 0\n"
"s_xor_b32 s52, s77, 1024\n"
"s_add_u32 s45, s45, s52\n"
"v_add_u32 v179, s83, v177\n"
"v_mfma_f32_16x16x32_bf16 a[212:215], v[40:43], v[144:147], a[212:215]\n"
"buffer_load_dwordx4 v162, s[64:67], s85 offen offset:2048 lds\n"
"v_mfma_f32_16x16x32_bf16 a[216:219], v[48:51], v[144:147], 0\n"
"buffer_load_dwordx4 v163, s[64:67], s85 offen offset:3072 lds\n"
"v_mfma_f32_16x16x32_bf16 a[216:219], v[52:55], v[148:151], a[216:219]\n"
"s_add_u32 s85, s72, 131328\n"
"s_mul_i32 s46, s7, 1835008\n"
"s_mul_i32 s92, s35, 114688\n"
"v_mfma_f32_16x16x32_bf16 a[220:223], v[60:63], v[148:151], 0\n"
"s_waitcnt lgkmcnt(0)\n"
"s_add_u32 s82, s74, 81920\n"
"s_add_u32 s46, s46, s92\n"
"v_mfma_f32_16x16x32_bf16 a[220:223], v[56:59], v[144:147], a[220:223]\n"
"s_waitcnt vmcnt(19)\n"
"s_barrier\n"
"s_mul_i32 s93, s33, 28672\n"
"v_mfma_f32_16x16x32_bf16 a[224:227], v[0:3], v[152:155], 0\n"
"s_add_u32 s46, s46, s93\n"
"s_mov_b32 m0, s82\n"
"ds_read_b128 v[128:131], v178 offset:0\n"
"v_mfma_f32_16x16x32_bf16 a[224:227], v[4:7], v[156:159], a[224:227]\n"
"ds_read_b128 v[132:135], v179 offset:0\n"
"buffer_load_dwordx4 v160, s[64:67], s85 offen offset:0 lds\n"
"v_mfma_f32_16x16x32_bf16 a[228:231], v[12:15], v[156:159], 0\n"
"buffer_load_dwordx4 v161, s[64:67], s85 offen offset:1024 lds\n"
"v_mfma_f32_16x16x32_bf16 a[228:231], v[8:11], v[152:155], a[228:231]\n"
"buffer_load_dwordx4 v162, s[64:67], s85 offen offset:2048 lds\n"
"v_mfma_f32_16x16x32_bf16 a[232:235], v[16:19], v[152:155], 0\n"
"buffer_load_dwordx4 v163, s[64:67], s85 offen offset:3072 lds\n"
"v_mfma_f32_16x16x32_bf16 a[232:235], v[20:23], v[156:159], a[232:235]\n"
"s_add_u32 s46, s46, 0x3400000\n"
"s_mul_i32 s47, s33, 8192\n"
"s_mul_i32 s6, s33, 224\n"
"v_mfma_f32_16x16x32_bf16 a[236:239], v[28:31], v[156:159], 0\n"
"s_mul_i32 s4, s35, 114688\n"
"s_mul_i32 s94, s7, 3584\n"
"s_add_u32 s4, s4, s94\n"
"v_mfma_f32_16x16x32_bf16 a[236:239], v[24:27], v[152:155], a[236:239]\n"
"s_add_u32 s4, s4, s6\n"
"s_add_u32 s5, s4, 57344\n"
"s_mul_i32 s8, s35, 114688\n"
"v_mfma_f32_16x16x32_bf16 a[240:243], v[32:35], v[152:155], 0\n"
"s_mul_i32 s95, s7, 7168\n"
"s_add_u32 s8, s8, s95\n"
"v_lshrrev_b32 v240, 7, v227\n"
"v_mfma_f32_16x16x32_bf16 a[240:243], v[36:39], v[156:159], a[240:243]\n"
"s_mul_i32 s92, s33, 1792\n"
"v_and_b32 v241, 112, v227\n"
"v_lshl_add_u32 v203, v240, 11, v241\n"
"v_mfma_f32_16x16x32_bf16 a[244:247], v[44:47], v[156:159], 0\n"
"s_add_u32 s8, s8, s92\n"
"v_cmp_eq_u32 vcc, 0, v252\n"
"s_nop 1\n"
"v_cndmask_b32 v183, 0, 1.0, vcc\n"
"v_mfma_f32_16x16x32_bf16 a[244:247], v[40:43], v[152:155], a[244:247]\n"
"s_add_u32 s8, s8, 0x3400000\n"
"v_lshrrev_b32 v240, 8, v227\n"
"v_lshl_add_u32 v200, v240, 3, s53\n"
"v_mfma_f32_16x16x32_bf16 a[248:251], v[48:51], v[152:155], 0\n"
"s_lshr_b32 s52, s7, 2\n"
"s_mul_i32 s10, s52, 32768\n"
"v_add_u32 v201, s47, v227\n"
"v_mfma_f32_16x16x32_bf16 a[248:251], v[52:55], v[156:159], a[248:251]\n"
"s_mul_i32 s93, s79, 128\n"
"s_add_u32 s10, s10, s93\n"
"s_add_u32 s10, s10, 0x2218000\n"
"v_mfma_f32_16x16x32_bf16 a[252:255], v[60:63], v[156:159], 0\n"
"s_mov_b64 s[18:19], s[16:17]\n"
"s_mul_i32 s52, s33, 9216\n"
"s_add_u32 s52, s52, 0x1000\n"
"v_mfma_f32_16x16x32_bf16 a[252:255], v[56:59], v[152:155], a[252:255]\n"
"s_waitcnt lgkmcnt(0)\n"
"s_add_u32 s16, s16, s52\n"
"s_addc_u32 s17, s17, 0\n"
"s_mul_i32 s52, s79, 1024\n"
"s_add_u32 s52, s52, 0x9000\n"
"s_add_u32 s18, s18, s52\n"
"s_addc_u32 s19, s19, 0\n"
"s_bfe_u32 s50, s7, 131073\n"
"s_lshr_b32 s52, s7, 3\n"
"s_and_b32 s57, s7, 1\n"
"s_add_u32 s84, s73, 1835008\n"
"s_mul_i32 s58, s35, 114688\n"
"s_mul_i32 s94, s50, 28672\n"
"s_add_u32 s58, s58, s94\n"
"s_add_u32 m0, s74, 49152\n"
"s_mul_i32 s95, s52, 1024\n"
"s_add_u32 s58, s58, s95\n"
"s_mul_i32 s92, s57, 512\n"
"buffer_load_dwordx4 v164, s[68:71], s84 offen offset:0 lds\n"
"s_add_u32 s58, s58, s92\n"
"s_mul_i32 s93, s33, 128\n"
"s_add_u32 s58, s58, s93\n"
"s_add_u32 s58, s58, 0x2000000\n"
"buffer_load_dwordx4 v165, s[68:71], s84 offen offset:1024 lds\n"
"v_add_u32 v203, s58, v203\n"
"buffer_load_dwordx4 v166, s[68:71], s84 offen offset:2048 lds\n"
"buffer_load_dwordx4 v167, s[68:71], s84 offen offset:3072 lds\n"
"global_load_dword v255, v[204:205], off\n"
"v_lshl_add_u64 v[204:205], v[206:207], 0, v[204:205]\n"
"v_mfma_f32_16x16x32_bf16 a[0:3], v[64:67], v[128:131], a[0:3]\n"
"s_mov_b32 s27, 0x20000\n"
"ds_read_b128 v[136:139], v178 offset:2048\n"
"v_and_b32 v247, 0xffff0000, v239\n"
"v_mfma_f32_16x16x32_bf16 a[0:3], v[68:71], v[132:135], a[0:3]\n"
"s_add_u32 s85, s72, 384\n"
"ds_read_b128 v[140:143], v179 offset:2048\n"
"v_lshlrev_b32 v246, 16, v239\n"
"v_mfma_f32_16x16x32_bf16 a[4:7], v[76:79], v[132:135], a[4:7]\n"
"s_mov_b32 s48, 0\n"
"ds_read_b64_tr_b16 v[0:1], v168 offset:0\n"
"v_and_b32 v245, 0xffff0000, v238\n"
"v_mfma_f32_16x16x32_bf16 a[4:7], v[72:75], v[128:131], a[4:7]\n"
"v_lshlrev_b32 v244, 16, v238\n"
"v_mfma_f32_16x16x32_bf16 a[8:11], v[80:83], v[128:131], a[8:11]\n"
"s_add_u32 s82, s74, 98304\n"
"ds_read_b64_tr_b16 v[2:3], v168 offset:2048\n"
"v_and_b32 v243, 0xffff0000, v237\n"
"v_mfma_f32_16x16x32_bf16 a[8:11], v[84:87], v[132:135], a[8:11]\n"
"s_mov_b32 s49, 0\n"
"v_lshlrev_b32 v242, 16, v237\n"
"v_and_b32 v241, 0xffff0000, v236\n"
"v_mfma_f32_16x16x32_bf16 a[12:15], v[92:95], v[132:135], a[12:15]\n"
"s_mov_b32 s50, 131072\n"
"ds_read_b64_tr_b16 v[8:9], v169 offset:0\n"
"v_lshlrev_b32 v240, 16, v236\n"
"v_mfma_f32_16x16x32_bf16 a[12:15], v[88:91], v[128:131], a[12:15]\n"
"s_mov_b32 m0, s82\n"
"v_and_b32 v239, 0xffff0000, v235\n"
"v_lshlrev_b32 v238, 16, v235\n"
"v_mfma_f32_16x16x32_bf16 a[16:19], v[96:99], v[128:131], a[16:19]\n"
"s_mov_b32 s56, 0x47800000\n"
"ds_read_b64_tr_b16 v[10:11], v169 offset:2048\n"
"v_and_b32 v237, 0xffff0000, v234\n"
"v_mfma_f32_16x16x32_bf16 a[16:19], v[100:103], v[132:135], a[16:19]\n"
"v_lshlrev_b32 v236, 16, v234\n"
"v_mfma_f32_16x16x32_bf16 a[20:23], v[108:111], v[132:135], a[20:23]\n"
"s_mov_b32 s57, 0x3db8aa3b\n"
"ds_read_b64_tr_b16 v[16:17], v170 offset:0\n"
"v_and_b32 v235, 0xffff0000, v233\n"
"v_mfma_f32_16x16x32_bf16 a[20:23], v[104:107], v[128:131], a[20:23]\n"
"buffer_load_dwordx4 v160, s[64:67], s85 offen offset:0 lds\n"
"v_mfma_f32_16x16x32_bf16 a[24:27], v[112:115], v[128:131], a[24:27]\n"
"s_mov_b32 s53, 0\n"
"ds_read_b64_tr_b16 v[18:19], v170 offset:2048\n"
"v_lshlrev_b32 v234, 16, v233\n"
"v_mfma_f32_16x16x32_bf16 a[24:27], v[116:119], v[132:135], a[24:27]\n"
"buffer_load_dwordx4 v161, s[64:67], s85 offen offset:1024 lds\n"
"v_mfma_f32_16x16x32_bf16 a[28:31], v[124:127], v[132:135], a[28:31]\n"
"ds_read_b64_tr_b16 v[24:25], v171 offset:0\n"
"v_and_b32 v233, 0xffff0000, v232\n"
"v_mfma_f32_16x16x32_bf16 a[28:31], v[120:123], v[128:131], a[28:31]\n"
"s_waitcnt lgkmcnt(7)\n"
"v_lshlrev_b32 v232, 16, v232\n"
"v_add_f32 v248, v232, v233\n"
"v_mfma_f32_16x16x32_bf16 a[32:35], v[64:67], v[136:139], a[32:35]\n"
"ds_read_b128 v[144:147], v178 offset:4096\n"
"v_add_f32 v249, v234, v235\n"
"v_mfma_f32_16x16x32_bf16 a[32:35], v[68:71], v[140:143], a[32:35]\n"
"ds_read_b128 v[148:151], v179 offset:4096\n"
"ds_read_b64_tr_b16 v[26:27], v171 offset:2048\n"
"v_mfma_f32_16x16x32_bf16 a[36:39], v[76:79], v[140:143], a[36:39]\n"
"buffer_load_dwordx4 v162, s[64:67], s85 offen offset:2048 lds\n"
"v_mfma_f32_16x16x32_bf16 a[36:39], v[72:75], v[136:139], a[36:39]\n"
"ds_read_b64_tr_b16 v[32:33], v172 offset:0\n"
"v_add_f32 v248, v248, v236\n"
"v_mfma_f32_16x16x32_bf16 a[40:43], v[80:83], v[136:139], a[40:43]\n"
"buffer_load_dwordx4 v163, s[64:67], s85 offen offset:3072 lds\n"
"v_mfma_f32_16x16x32_bf16 a[40:43], v[84:87], v[140:143], a[40:43]\n"
"s_add_u32 s85, s72, 131456\n"
"ds_read_b64_tr_b16 v[34:35], v172 offset:2048\n"
"v_add_f32 v249, v249, v237\n"
"v_mfma_f32_16x16x32_bf16 a[44:47], v[92:95], v[140:143], a[44:47]\n"
"v_add_f32 v248, v248, v238\n"
"v_add_f32 v249, v249, v239\n"
"v_mfma_f32_16x16x32_bf16 a[44:47], v[88:91], v[136:139], a[44:47]\n"
"ds_read_b64_tr_b16 v[40:41], v173 offset:0\n"
"v_add_f32 v248, v248, v240\n"
"v_mfma_f32_16x16x32_bf16 a[48:51], v[96:99], v[136:139], a[48:51]\n"
"s_add_u32 s82, s74, 114688\n"
"v_add_f32 v249, v249, v241\n"
"v_add_f32 v248, v248, v242\n"
"v_mfma_f32_16x16x32_bf16 a[48:51], v[100:103], v[140:143], a[48:51]\n"
"ds_read_b64_tr_b16 v[42:43], v173 offset:2048\n"
"v_add_f32 v249, v249, v243\n"
"v_mfma_f32_16x16x32_bf16 a[52:55], v[108:111], v[140:143], a[52:55]\n"
"v_add_f32 v248, v248, v244\n"
"v_add_f32 v249, v249, v245\n"
"v_mfma_f32_16x16x32_bf16 a[52:55], v[104:107], v[136:139], a[52:55]\n"
"s_mov_b32 m0, s82\n"
"ds_read_b64_tr_b16 v[48:49], v174 offset:0\n"
"v_add_f32 v248, v248, v246\n"
"v_mfma_f32_16x16x32_bf16 a[56:59], v[112:115], v[136:139], a[56:59]\n"
"v_add_f32 v249, v249, v247\n"
"v_add_f32 v248, v248, v249\n"
"v_mfma_f32_16x16x32_bf16 a[56:59], v[116:119], v[140:143], a[56:59]\n"
"ds_read_b64_tr_b16 v[50:51], v174 offset:2048\n"
"v_add_f32_dpp v248, v248, v248 quad_perm:[1,0,3,2] row_mask:0xf bank_mask:0xf bound_ctrl:1\n"
"v_mfma_f32_16x16x32_bf16 a[60:63], v[124:127], v[140:143], a[60:63]\n"
"buffer_load_dwordx4 v160, s[64:67], s85 offen offset:0 lds\n"
"v_mfma_f32_16x16x32_bf16 a[60:63], v[120:123], v[136:139], a[60:63]\n"
"s_waitcnt lgkmcnt(7)\n"
"v_add_f32_dpp v248, v248, v248 quad_perm:[2,3,0,1] row_mask:0xf bank_mask:0xf bound_ctrl:1\n"
"s_nop 1\n"
"v_add_f32_dpp v248, v248, v248 row_ror:8 row_mask:0xf bank_mask:0xf bound_ctrl:1\n"
"v_mfma_f32_16x16x32_bf16 a[64:67], v[64:67], v[144:147], a[64:67]\n"
"ds_read_b128 v[152:155], v178 offset:6144\n"
"ds_read_b64_tr_b16 v[56:57], v175 offset:0\n"
"v_mfma_f32_16x16x32_bf16 a[64:67], v[68:71], v[148:151], a[64:67]\n"
"ds_read_b128 v[156:159], v179 offset:6144\n"
"v_mov_b32 v250, v248\n"
"v_mfma_f32_16x16x32_bf16 a[68:71], v[76:79], v[148:151], a[68:71]\n"
"ds_read_b64_tr_b16 v[58:59], v175 offset:2048\n"
"v_permlane32_swap_b32 v250, v248\n"
"v_mfma_f32_16x16x32_bf16 a[68:71], v[72:75], v[144:147], a[68:71]\n"
"buffer_load_dwordx4 v161, s[64:67], s85 offen offset:1024 lds\n"
"v_mfma_f32_16x16x32_bf16 a[72:75], v[80:83], v[144:147], a[72:75]\n"
"buffer_load_dwordx4 v162, s[64:67], s85 offen offset:2048 lds\n"
"v_mfma_f32_16x16x32_bf16 a[72:75], v[84:87], v[148:151], a[72:75]\n"
"s_add_u32 s83, s83, 16384\n"
"v_add_f32 v248, v248, v250\n"
"v_mov_b32 v250, v248\n"
"v_mfma_f32_16x16x32_bf16 a[76:79], v[92:95], v[148:151], a[76:79]\n"
"s_nop 0\n"
"v_permlane16_swap_b32 v250, v248\n"
"v_add_f32 v248, v248, v250\n"
"v_mfma_f32_16x16x32_bf16 a[76:79], v[88:91], v[144:147], a[76:79]\n"
"s_cmp_lt_u32 s83, 163840\n"
"s_cselect_b32 s83, s83, 65536\n"
"v_mul_f32 v248, 0x3b124925, v248\n"
"v_mfma_f32_16x16x32_bf16 a[80:83], v[96:99], v[144:147], a[80:83]\n"
"buffer_load_dwordx4 v163, s[64:67], s85 offen offset:3072 lds\n"
"v_mfma_f32_16x16x32_bf16 a[80:83], v[100:103], v[148:151], a[80:83]\n"
"v_add_u32 v178, s83, v176\n"
"v_sub_f32 v250, v232, v248\n"
"v_mfma_f32_16x16x32_bf16 a[84:87], v[108:111], v[148:151], a[84:87]\n"
"v_add_u32 v179, s83, v177\n"
"v_mul_f32 v249, v250, v250\n"
"v_mfma_f32_16x16x32_bf16 a[84:87], v[104:107], v[144:147], a[84:87]\n"
"v_sub_f32 v250, v233, v248\n"
"v_fmac_f32 v249, v250, v250\n"
"v_mfma_f32_16x16x32_bf16 a[88:91], v[112:115], v[144:147], a[88:91]\n"
"v_sub_f32 v250, v234, v248\n"
"v_fmac_f32 v249, v250, v250\n"
"v_mfma_f32_16x16x32_bf16 a[88:91], v[116:119], v[148:151], a[88:91]\n"
"v_sub_f32 v250, v235, v248\n"
"v_fmac_f32 v249, v250, v250\n"
"v_mfma_f32_16x16x32_bf16 a[92:95], v[124:127], v[148:151], a[92:95]\n"
"s_waitcnt lgkmcnt(0)\n"
"v_sub_f32 v250, v236, v248\n"
"v_fmac_f32 v249, v250, v250\n"
"v_mfma_f32_16x16x32_bf16 a[92:95], v[120:123], v[144:147], a[92:95]\n"
"s_waitcnt vmcnt(27)\n"
"s_barrier\n"
"v_sub_f32 v250, v237, v248\n"
"v_mfma_f32_16x16x32_bf16 a[96:99], v[64:67], v[152:155], a[96:99]\n"
"ds_read_b128 v[128:131], v178 offset:0\n"
"v_fmac_f32 v249, v250, v250\n"
"v_mfma_f32_16x16x32_bf16 a[96:99], v[68:71], v[156:159], a[96:99]\n"
"s_add_u32 s84, s73, 2097152\n"
"ds_read_b128 v[132:135], v179 offset:0\n"
"v_sub_f32 v250, v238, v248\n"
"v_mfma_f32_16x16x32_bf16 a[100:103], v[76:79], v[156:159], a[100:103]\n"
"v_fmac_f32 v249, v250, v250\n"
"v_sub_f32 v250, v239, v248\n"
"v_mfma_f32_16x16x32_bf16 a[100:103], v[72:75], v[152:155], a[100:103]\n"
"s_add_u32 m0, s74, 0\n"
"v_fmac_f32 v249, v250, v250\n"
"v_sub_f32 v250, v240, v248\n"
"v_mfma_f32_16x16x32_bf16 a[104:107], v[80:83], v[152:155], a[104:107]\n"
"v_mul_f32 v251, v250, v250\n"
"v_sub_f32 v250, v241, v248\n"
"v_mfma_f32_16x16x32_bf16 a[104:107], v[84:87], v[156:159], a[104:107]\n"
"buffer_load_dwordx4 v164, s[68:71], s84 offen offset:0 lds\n"
"v_mfma_f32_16x16x32_bf16 a[108:111], v[92:95], v[156:159], a[108:111]\n"
"v_fmac_f32 v251, v250, v250\n"
"v_sub_f32 v250, v242, v248\n"
"v_mfma_f32_16x16x32_bf16 a[108:111], v[88:91], v[152:155], a[108:111]\n"
"buffer_load_dwordx4 v165, s[68:71], s84 offen offset:1024 lds\n"
"v_mfma_f32_16x16x32_bf16 a[112:115], v[96:99], v[152:155], a[112:115]\n"
"v_fmac_f32 v251, v250, v250\n"
"v_sub_f32 v250, v243, v248\n"
"v_mfma_f32_16x16x32_bf16 a[112:115], v[100:103], v[156:159], a[112:115]\n"
"buffer_load_dwordx4 v166, s[68:71], s84 offen offset:2048 lds\n"
"v_mfma_f32_16x16x32_bf16 a[116:119], v[108:111], v[156:159], a[116:119]\n"
"v_fmac_f32 v251, v250, v250\n"
"v_sub_f32 v250, v244, v248\n"
"v_mfma_f32_16x16x32_bf16 a[116:119], v[104:107], v[152:155], a[116:119]\n"
"buffer_load_dwordx4 v167, s[68:71], s84 offen offset:3072 lds\n"
"v_mfma_f32_16x16x32_bf16 a[120:123], v[112:115], v[152:155], a[120:123]\n"
"v_fmac_f32 v251, v250, v250\n"
"v_sub_f32 v250, v245, v248\n"
"v_mfma_f32_16x16x32_bf16 a[120:123], v[116:119], v[156:159], a[120:123]\n"
"v_fmac_f32 v251, v250, v250\n"
"v_sub_f32 v250, v246, v248\n"
"v_mfma_f32_16x16x32_bf16 a[124:127], v[124:127], v[156:159], a[124:127]\n"
"v_fmac_f32 v251, v250, v250\n"
"v_sub_f32 v250, v247, v248\n"
"v_mfma_f32_16x16x32_bf16 a[124:127], v[120:123], v[152:155], a[124:127]\n"
"s_waitcnt lgkmcnt(0)\n"
"v_fmac_f32 v251, v250, v250\n"
"v_fmac_f32 v249, v251, v183\n"
"s_nop 1\n"
"v_add_f32_dpp v249, v249, v249 quad_perm:[1,0,3,2] row_mask:0xf bank_mask:0xf bound_ctrl:1\n"
"s_nop 1\n"
"v_add_f32_dpp v249, v249, v249 quad_perm:[2,3,0,1] row_mask:0xf bank_mask:0xf bound_ctrl:1\n"
"s_nop 1\n"
"v_add_f32_dpp v249, v249, v249 row_ror:8 row_mask:0xf bank_mask:0xf bound_ctrl:1\n"
"v_mov_b32 v250, v249\n"
"s_nop 1\n"
"v_permlane32_swap_b32 v250, v249\n"
"v_add_f32 v249, v249, v250\n"
"v_mov_b32 v250, v249\n"
"v_mfma_f32_16x16x32_bf16 a[128:131], v[64:67], v[128:131], a[128:131]\n"
"ds_read_b128 v[136:139], v178 offset:2048\n"
"v_permlane16_swap_b32 v250, v249\n"
"v_mfma_f32_16x16x32_bf16 a[128:131], v[68:71], v[132:135], a[128:131]\n"
"s_add_u32 s85, s72, 512\n"
"ds_read_b128 v[140:143], v179 offset:2048\n"
"v_add_f32 v249, v249, v250\n"
"v_mfma_f32_16x16x32_bf16 a[132:135], v[76:79], v[132:135], a[132:135]\n"
"ds_read_b64_tr_b16 v[4:5], v168 offset:16384\n"
"v_mul_f32 v249, 0x3b124925, v249\n"
"v_mfma_f32_16x16x32_bf16 a[132:135], v[72:75], v[128:131], a[132:135]\n"
"v_cvt_pk_bf16_f32 v249, v249, v249\n"
"v_mfma_f32_16x16x32_bf16 a[136:139], v[80:83], v[128:131], a[136:139]\n"
"ds_read_b64_tr_b16 v[6:7], v168 offset:18432\n"
"v_and_b32 v249, 0xffff0000, v249\n"
"v_mfma_f32_16x16x32_bf16 a[136:139], v[84:87], v[132:135], a[136:139]\n"
"v_add_f32 v249, 0x3727c5ac, v249\n"
"v_cvt_pk_bf16_f32 v249, v249, v249\n"
"v_mfma_f32_16x16x32_bf16 a[140:143], v[92:95], v[132:135], a[140:143]\n"
"ds_read_b64_tr_b16 v[12:13], v169 offset:16384\n"
"v_and_b32 v249, 0xffff0000, v249\n"
"v_mfma_f32_16x16x32_bf16 a[140:143], v[88:91], v[128:131], a[140:143]\n"
"s_add_u32 s82, s74, 131072\n"
"v_sqrt_f32 v249, v249\n"
"v_mfma_f32_16x16x32_bf16 a[144:147], v[96:99], v[128:131], a[144:147]\n"
"ds_read_b64_tr_b16 v[14:15], v169 offset:18432\n"
"v_cvt_pk_bf16_f32 v249, v249, v249\n"
"v_mfma_f32_16x16x32_bf16 a[144:147], v[100:103], v[132:135], a[144:147]\n"
"v_and_b32 v249, 0xffff0000, v249\n"
"v_mfma_f32_16x16x32_bf16 a[148:151], v[108:111], v[132:135], a[148:151]\n"
"ds_read_b64_tr_b16 v[20:21], v170 offset:16384\n"
"v_cvt_pk_bf16_f32 v248, v248, v248\n"
"v_mfma_f32_16x16x32_bf16 a[148:151], v[104:107], v[128:131], a[148:151]\n"
"v_rcp_f32 v249, v249\n"
"v_mfma_f32_16x16x32_bf16 a[152:155], v[112:115], v[128:131], a[152:155]\n"
"ds_read_b64_tr_b16 v[22:23], v170 offset:18432\n"
"v_and_b32 v248, 0xffff0000, v248\n"
"v_mfma_f32_16x16x32_bf16 a[152:155], v[116:119], v[132:135], a[152:155]\n"
"s_mov_b32 m0, s82\n"
"v_sub_f32 v232, v232, v248\n"
"v_sub_f32 v233, v233, v248\n"
"v_mfma_f32_16x16x32_bf16 a[156:159], v[124:127], v[132:135], a[156:159]\n"
"ds_read_b64_tr_b16 v[28:29], v171 offset:16384\n"
"v_cvt_pk_bf16_f32 v250, v232, v233\n"
"v_mfma_f32_16x16x32_bf16 a[156:159], v[120:123], v[128:131], a[156:159]\n"
"s_waitcnt lgkmcnt(7)\n"
"v_lshlrev_b32 v232, 16, v250\n"
"v_and_b32 v233, 0xffff0000, v250\n"
"v_mfma_f32_16x16x32_bf16 a[160:163], v[64:67], v[136:139], a[160:163]\n"
"ds_read_b128 v[144:147], v178 offset:4096\n"
"v_mul_f32 v232, v232, v249\n"
"v_mfma_f32_16x16x32_bf16 a[160:163], v[68:71], v[140:143], a[160:163]\n"
"ds_read_b128 v[148:151], v179 offset:4096\n"
"ds_read_b64_tr_b16 v[30:31], v171 offset:18432\n"
"v_mfma_f32_16x16x32_bf16 a[164:167], v[76:79], v[140:143], a[164:167]\n"
"v_mul_f32 v233, v233, v249\n"
"v_cvt_pk_bf16_f32 v232, v232, v233\n"
"v_mfma_f32_16x16x32_bf16 a[164:167], v[72:75], v[136:139], a[164:167]\n"
"ds_read_b64_tr_b16 v[36:37], v172 offset:16384\n"
"v_sub_f32 v234, v234, v248\n"
"v_mfma_f32_16x16x32_bf16 a[168:171], v[80:83], v[136:139], a[168:171]\n"
"buffer_load_dwordx4 v160, s[64:67], s85 offen offset:0 lds\n"
"v_mfma_f32_16x16x32_bf16 a[168:171], v[84:87], v[140:143], a[168:171]\n"
"ds_read_b64_tr_b16 v[38:39], v172 offset:18432\n"
"v_sub_f32 v235, v235, v248\n"
"v_mfma_f32_16x16x32_bf16 a[172:175], v[92:95], v[140:143], a[172:175]\n"
"v_cvt_pk_bf16_f32 v250, v234, v235\n"
"v_lshlrev_b32 v234, 16, v250\n"
"v_mfma_f32_16x16x32_bf16 a[172:175], v[88:91], v[136:139], a[172:175]\n"
"ds_read_b64_tr_b16 v[44:45], v173 offset:16384\n"
"v_and_b32 v235, 0xffff0000, v250\n"
"v_mfma_f32_16x16x32_bf16 a[176:179], v[96:99], v[136:139], a[176:179]\n"
"buffer_load_dwordx4 v161, s[64:67], s85 offen offset:1024 lds\n"
"v_mfma_f32_16x16x32_bf16 a[176:179], v[100:103], v[140:143], a[176:179]\n"
"ds_read_b64_tr_b16 v[46:47], v173 offset:18432\n"
"v_mul_f32 v234, v234, v249\n"
"v_mfma_f32_16x16x32_bf16 a[180:183], v[108:111], v[140:143], a[180:183]\n"
"v_mul_f32 v235, v235, v249\n"
"v_cvt_pk_bf16_f32 v233, v234, v235\n"
"v_mfma_f32_16x16x32_bf16 a[180:183], v[104:107], v[136:139], a[180:183]\n"
"ds_read_b64_tr_b16 v[52:53], v174 offset:16384\n"
"v_sub_f32 v236, v236, v248\n"
"v_mfma_f32_16x16x32_bf16 a[184:187], v[112:115], v[136:139], a[184:187]\n"
"v_sub_f32 v237, v237, v248\n"
"v_cvt_pk_bf16_f32 v250, v236, v237\n"
"v_mfma_f32_16x16x32_bf16 a[184:187], v[116:119], v[140:143], a[184:187]\n"
"ds_read_b64_tr_b16 v[54:55], v174 offset:18432\n"
"v_lshlrev_b32 v236, 16, v250\n"
"v_mfma_f32_16x16x32_bf16 a[188:191], v[124:127], v[140:143], a[188:191]\n"
"buffer_load_dwordx4 v162, s[64:67], s85 offen offset:2048 lds\n"
"v_mfma_f32_16x16x32_bf16 a[188:191], v[120:123], v[136:139], a[188:191]\n"
"s_waitcnt lgkmcnt(7)\n"
"v_and_b32 v237, 0xffff0000, v250\n"
"v_mul_f32 v236, v236, v249\n"
"v_mfma_f32_16x16x32_bf16 a[192:195], v[64:67], v[144:147], a[192:195]\n"
"ds_read_b128 v[152:155], v178 offset:6144\n"
"ds_read_b64_tr_b16 v[60:61], v175 offset:16384\n"
"v_mfma_f32_16x16x32_bf16 a[192:195], v[68:71], v[148:151], a[192:195]\n"
"ds_read_b128 v[156:159], v179 offset:6144\n"
"v_mul_f32 v237, v237, v249\n"
"v_mfma_f32_16x16x32_bf16 a[196:199], v[76:79], v[148:151], a[196:199]\n"
"ds_read_b64_tr_b16 v[62:63], v175 offset:18432\n"
"v_cvt_pk_bf16_f32 v234, v236, v237\n"
"v_mfma_f32_16x16x32_bf16 a[196:199], v[72:75], v[144:147], a[196:199]\n"
"v_sub_f32 v238, v238, v248\n"
"v_sub_f32 v239, v239, v248\n"
"v_mfma_f32_16x16x32_bf16 a[200:203], v[80:83], v[144:147], a[200:203]\n"
"buffer_load_dwordx4 v163, s[64:67], s85 offen offset:3072 lds\n"
"v_mfma_f32_16x16x32_bf16 a[200:203], v[84:87], v[148:151], a[200:203]\n"
"s_add_u32 s83, s83, 16384\n"
"v_cvt_pk_bf16_f32 v250, v238, v239\n"
"v_lshlrev_b32 v238, 16, v250\n"
"v_mfma_f32_16x16x32_bf16 a[204:207], v[92:95], v[148:151], a[204:207]\n"
"v_and_b32 v239, 0xffff0000, v250\n"
"v_mul_f32 v238, v238, v249\n"
"v_mfma_f32_16x16x32_bf16 a[204:207], v[88:91], v[144:147], a[204:207]\n"
"s_cmp_lt_u32 s83, 163840\n"
"s_cselect_b32 s83, s83, 65536\n"
"v_mul_f32 v239, v239, v249\n"
"v_mfma_f32_16x16x32_bf16 a[208:211], v[96:99], v[144:147], a[208:211]\n"
"v_cvt_pk_bf16_f32 v235, v238, v239\n"
"v_sub_f32 v240, v240, v248\n"
"v_mfma_f32_16x16x32_bf16 a[208:211], v[100:103], v[148:151], a[208:211]\n"
"v_add_u32 v178, s83, v176\n"
"v_sub_f32 v241, v241, v248\n"
"v_mfma_f32_16x16x32_bf16 a[212:215], v[108:111], v[148:151], a[212:215]\n"
"v_add_u32 v179, s83, v177\n"
"v_cvt_pk_bf16_f32 v250, v240, v241\n"
"v_mfma_f32_16x16x32_bf16 a[212:215], v[104:107], v[144:147], a[212:215]\n"
"v_lshlrev_b32 v240, 16, v250\n"
"v_and_b32 v241, 0xffff0000, v250\n"
"v_mfma_f32_16x16x32_bf16 a[216:219], v[112:115], v[144:147], a[216:219]\n"
"v_mul_f32 v240, v240, v249\n"
"v_mul_f32 v241, v241, v249\n"
"v_mfma_f32_16x16x32_bf16 a[216:219], v[116:119], v[148:151], a[216:219]\n"
"v_cvt_pk_bf16_f32 v236, v240, v241\n"
"v_sub_f32 v242, v242, v248\n"
"v_mfma_f32_16x16x32_bf16 a[220:223], v[124:127], v[148:151], a[220:223]\n"
"s_waitcnt lgkmcnt(0)\n"
"v_sub_f32 v243, v243, v248\n"
"v_cvt_pk_bf16_f32 v250, v242, v243\n"
"v_mfma_f32_16x16x32_bf16 a[220:223], v[120:123], v[144:147], a[220:223]\n"
"s_waitcnt vmcnt(25)\n"
"s_barrier\n"
"v_lshlrev_b32 v242, 16, v250\n"
"v_mfma_f32_16x16x32_bf16 a[224:227], v[64:67], v[152:155], a[224:227]\n"
"ds_read_b128 v[128:131], v178 offset:0\n"
"v_and_b32 v243, 0xffff0000, v250\n"
"v_mfma_f32_16x16x32_bf16 a[224:227], v[68:71], v[156:159], a[224:227]\n"
"s_add_u32 s84, s73, 2359296\n"
"ds_read_b128 v[132:135], v179 offset:0\n"
"v_mul_f32 v242, v242, v249\n"
"v_mfma_f32_16x16x32_bf16 a[228:231], v[76:79], v[156:159], a[228:231]\n"
"v_mul_f32 v243, v243, v249\n"
"v_cvt_pk_bf16_f32 v237, v242, v243\n"
"v_mfma_f32_16x16x32_bf16 a[228:231], v[72:75], v[152:155], a[228:231]\n"
"s_add_u32 m0, s74, 16384\n"
"v_sub_f32 v244, v244, v248\n"
"v_sub_f32 v245, v245, v248\n"
"v_mfma_f32_16x16x32_bf16 a[232:235], v[80:83], v[152:155], a[232:235]\n"
"v_cvt_pk_bf16_f32 v250, v244, v245\n"
"v_lshlrev_b32 v244, 16, v250\n"
"v_mfma_f32_16x16x32_bf16 a[232:235], v[84:87], v[156:159], a[232:235]\n"
"buffer_load_dwordx4 v164, s[68:71], s84 offen offset:0 lds\n"
"v_mfma_f32_16x16x32_bf16 a[236:239], v[92:95], v[156:159], a[236:239]\n"
"v_and_b32 v245, 0xffff0000, v250\n"
"v_mul_f32 v244, v244, v249\n"
"v_mfma_f32_16x16x32_bf16 a[236:239], v[88:91], v[152:155], a[236:239]\n"
"buffer_load_dwordx4 v165, s[68:71], s84 offen offset:1024 lds\n"
"v_mfma_f32_16x16x32_bf16 a[240:243], v[96:99], v[152:155], a[240:243]\n"
"v_mul_f32 v245, v245, v249\n"
"v_cvt_pk_bf16_f32 v238, v244, v245\n"
"v_mfma_f32_16x16x32_bf16 a[240:243], v[100:103], v[156:159], a[240:243]\n"
"buffer_load_dwordx4 v166, s[68:71], s84 offen offset:2048 lds\n"
"v_mfma_f32_16x16x32_bf16 a[244:247], v[108:111], v[156:159], a[244:247]\n"
"v_sub_f32 v246, v246, v248\n"
"v_sub_f32 v247, v247, v248\n"
"v_mfma_f32_16x16x32_bf16 a[244:247], v[104:107], v[152:155], a[244:247]\n"
"buffer_load_dwordx4 v167, s[68:71], s84 offen offset:3072 lds\n"
"v_mfma_f32_16x16x32_bf16 a[248:251], v[112:115], v[152:155], a[248:251]\n"
"buffer_store_dwordx4 v[232:235], v203, s[20:23], 0 offen offset:0 sc1\n"
"v_mfma_f32_16x16x32_bf16 a[248:251], v[116:119], v[156:159], a[248:251]\n"
"s_mov_b32 s54, 16384\n"
"v_cvt_pk_bf16_f32 v250, v246, v247\n"
"v_lshlrev_b32 v246, 16, v250\n"
"v_mfma_f32_16x16x32_bf16 a[252:255], v[124:127], v[156:159], a[252:255]\n"
"v_and_b32 v247, 0xffff0000, v250\n"
"v_mul_f32 v246, v246, v249\n"
"v_mfma_f32_16x16x32_bf16 a[252:255], v[120:123], v[152:155], a[252:255]\n"
"s_waitcnt lgkmcnt(0)\n"
"v_mul_f32 v247, v247, v249\n"
"v_cvt_pk_bf16_f32 v239, v246, v247\n"
"v_add_u32 v203, v252, v203\n"
"buffer_store_dwordx4 v[236:239], v203, s[20:23], s54 offen offset:0 sc1\n"
"s_mov_b32 s81, 512\n"
"s_add_u32 s82, s74, 147456\n"
".p2align 6\n"
"L_k1loopk%=:\n"
"v_mfma_f32_16x16x32_bf16 a[0:3], v[0:3], v[128:131], a[0:3]\n"
"ds_read_b128 v[136:139], v178 offset:2048\n"
"v_mfma_f32_16x16x32_bf16 a[0:3], v[4:7], v[132:135], a[0:3]\n"
"ds_read_b128 v[140:143], v179 offset:2048\n"
"v_mfma_f32_16x16x32_bf16 a[4:7], v[12:15], v[132:135], a[4:7]\n"
"s_add_u32 s85, s72, s81\n"
"ds_read_b64_tr_b16 v[64:65], v168 offset:32768\n"
"v_mfma_f32_16x16x32_bf16 a[4:7], v[8:11], v[128:131], a[4:7]\n"
"s_cmp_lt_u32 s81, 2048\n"
"s_cselect_b32 s85, s85, 0x40000000\n"
"v_mfma_f32_16x16x32_bf16 a[8:11], v[16:19], v[128:131], a[8:11]\n"
"ds_read_b64_tr_b16 v[66:67], v168 offset:34816\n"
"v_mfma_f32_16x16x32_bf16 a[8:11], v[20:23], v[132:135], a[8:11]\n"
"s_add_u32 s85, s85, 131072\n"
"s_mov_b32 m0, s82\n"
"v_mfma_f32_16x16x32_bf16 a[12:15], v[28:31], v[132:135], a[12:15]\n"
"ds_read_b64_tr_b16 v[72:73], v169 offset:32768\n"
"v_mfma_f32_16x16x32_bf16 a[12:15], v[24:27], v[128:131], a[12:15]\n"
"buffer_load_dwordx4 v160, s[64:67], s85 offen offset:0 lds\n"
"v_mfma_f32_16x16x32_bf16 a[16:19], v[32:35], v[128:131], a[16:19]\n"
"ds_read_b64_tr_b16 v[74:75], v169 offset:34816\n"
"v_mfma_f32_16x16x32_bf16 a[16:19], v[36:39], v[132:135], a[16:19]\n"
"v_mfma_f32_16x16x32_bf16 a[20:23], v[44:47], v[132:135], a[20:23]\n"
"ds_read_b64_tr_b16 v[80:81], v170 offset:32768\n"
"v_mfma_f32_16x16x32_bf16 a[20:23], v[40:43], v[128:131], a[20:23]\n"
"v_mfma_f32_16x16x32_bf16 a[24:27], v[48:51], v[128:131], a[24:27]\n"
"ds_read_b64_tr_b16 v[82:83], v170 offset:34816\n"
"v_mfma_f32_16x16x32_bf16 a[24:27], v[52:55], v[132:135], a[24:27]\n"
"buffer_load_dwordx4 v161, s[64:67], s85 offen offset:1024 lds\n"
"v_mfma_f32_16x16x32_bf16 a[28:31], v[60:63], v[132:135], a[28:31]\n"
"ds_read_b64_tr_b16 v[88:89], v171 offset:32768\n"
"v_mfma_f32_16x16x32_bf16 a[28:31], v[56:59], v[128:131], a[28:31]\n"
"s_waitcnt lgkmcnt(7)\n"
"v_mfma_f32_16x16x32_bf16 a[32:35], v[0:3], v[136:139], a[32:35]\n"
"ds_read_b128 v[144:147], v178 offset:4096\n"
"v_mfma_f32_16x16x32_bf16 a[32:35], v[4:7], v[140:143], a[32:35]\n"
"ds_read_b128 v[148:151], v179 offset:4096\n"
"ds_read_b64_tr_b16 v[90:91], v171 offset:34816\n"
"v_mfma_f32_16x16x32_bf16 a[36:39], v[12:15], v[140:143], a[36:39]\n"
"v_mfma_f32_16x16x32_bf16 a[36:39], v[8:11], v[136:139], a[36:39]\n"
"ds_read_b64_tr_b16 v[96:97], v172 offset:32768\n"
"v_mfma_f32_16x16x32_bf16 a[40:43], v[16:19], v[136:139], a[40:43]\n"
"buffer_load_dwordx4 v162, s[64:67], s85 offen offset:2048 lds\n"
"v_mfma_f32_16x16x32_bf16 a[40:43], v[20:23], v[140:143], a[40:43]\n"
"ds_read_b64_tr_b16 v[98:99], v172 offset:34816\n"
"v_mfma_f32_16x16x32_bf16 a[44:47], v[28:31], v[140:143], a[44:47]\n"
"v_mfma_f32_16x16x32_bf16 a[44:47], v[24:27], v[136:139], a[44:47]\n"
"ds_read_b64_tr_b16 v[104:105], v173 offset:32768\n"
"v_mfma_f32_16x16x32_bf16 a[48:51], v[32:35], v[136:139], a[48:51]\n"
"v_mfma_f32_16x16x32_bf16 a[48:51], v[36:39], v[140:143], a[48:51]\n"
"ds_read_b64_tr_b16 v[106:107], v173 offset:34816\n"
"v_mfma_f32_16x16x32_bf16 a[52:55], v[44:47], v[140:143], a[52:55]\n"
"buffer_load_dwordx4 v163, s[64:67], s85 offen offset:3072 lds\n"
"v_mfma_f32_16x16x32_bf16 a[52:55], v[40:43], v[136:139], a[52:55]\n"
"ds_read_b64_tr_b16 v[112:113], v174 offset:32768\n"
"v_mfma_f32_16x16x32_bf16 a[56:59], v[48:51], v[136:139], a[56:59]\n"
"s_add_u32 s82, s82, 16384\n"
"v_mfma_f32_16x16x32_bf16 a[56:59], v[52:55], v[140:143], a[56:59]\n"
"s_cmp_lt_u32 s82, 163840\n"
"s_cselect_b32 s86, 0, 98304\n"
"ds_read_b64_tr_b16 v[114:115], v174 offset:34816\n"
"v_mfma_f32_16x16x32_bf16 a[60:63], v[60:63], v[140:143], a[60:63]\n"
"v_mfma_f32_16x16x32_bf16 a[60:63], v[56:59], v[136:139], a[60:63]\n"
"s_waitcnt lgkmcnt(7)\n"
"s_sub_u32 s82, s82, s86\n"
"v_mfma_f32_16x16x32_bf16 a[64:67], v[0:3], v[144:147], a[64:67]\n"
"ds_read_b128 v[152:155], v178 offset:6144\n"
"ds_read_b64_tr_b16 v[120:121], v175 offset:32768\n"
"v_mfma_f32_16x16x32_bf16 a[64:67], v[4:7], v[148:151], a[64:67]\n"
"ds_read_b128 v[156:159], v179 offset:6144\n"
"v_mfma_f32_16x16x32_bf16 a[68:71], v[12:15], v[148:151], a[68:71]\n"
"ds_read_b64_tr_b16 v[122:123], v175 offset:34816\n"
"v_mfma_f32_16x16x32_bf16 a[68:71], v[8:11], v[144:147], a[68:71]\n"
"s_add_u32 s81, s81, 128\n"
"v_mfma_f32_16x16x32_bf16 a[72:75], v[16:19], v[144:147], a[72:75]\n"
"v_mfma_f32_16x16x32_bf16 a[72:75], v[20:23], v[148:151], a[72:75]\n"
"s_add_u32 s83, s83, 16384\n"
"v_mfma_f32_16x16x32_bf16 a[76:79], v[28:31], v[148:151], a[76:79]\n"
"v_mfma_f32_16x16x32_bf16 a[76:79], v[24:27], v[144:147], a[76:79]\n"
"s_cmp_lt_u32 s83, 163840\n"
"s_cselect_b32 s83, s83, 65536\n"
"v_mfma_f32_16x16x32_bf16 a[80:83], v[32:35], v[144:147], a[80:83]\n"
"v_mfma_f32_16x16x32_bf16 a[80:83], v[36:39], v[148:151], a[80:83]\n"
"v_add_u32 v178, s83, v176\n"
"v_mfma_f32_16x16x32_bf16 a[84:87], v[44:47], v[148:151], a[84:87]\n"
"v_add_u32 v179, s83, v177\n"
"v_mfma_f32_16x16x32_bf16 a[84:87], v[40:43], v[144:147], a[84:87]\n"
"v_mfma_f32_16x16x32_bf16 a[88:91], v[48:51], v[144:147], a[88:91]\n"
"s_add_u32 s84, s84, 262144\n"
"v_mfma_f32_16x16x32_bf16 a[88:91], v[52:55], v[148:151], a[88:91]\n"
"v_mfma_f32_16x16x32_bf16 a[92:95], v[60:63], v[148:151], a[92:95]\n"
"s_waitcnt lgkmcnt(0)\n"
"v_mfma_f32_16x16x32_bf16 a[92:95], v[56:59], v[144:147], a[92:95]\n"
"s_waitcnt vmcnt(20)\n"
"s_barrier\n"
"v_mfma_f32_16x16x32_bf16 a[96:99], v[0:3], v[152:155], a[96:99]\n"
"ds_read_b128 v[128:131], v178 offset:0\n"
"v_mfma_f32_16x16x32_bf16 a[96:99], v[4:7], v[156:159], a[96:99]\n"
"s_add_u32 m0, s74, 32768\n"
"ds_read_b128 v[132:135], v179 offset:0\n"
"v_mfma_f32_16x16x32_bf16 a[100:103], v[12:15], v[156:159], a[100:103]\n"
"v_mfma_f32_16x16x32_bf16 a[100:103], v[8:11], v[152:155], a[100:103]\n"
"buffer_load_dwordx4 v164, s[68:71], s84 offen offset:0 lds\n"
"v_mfma_f32_16x16x32_bf16 a[104:107], v[16:19], v[152:155], a[104:107]\n"
"v_mfma_f32_16x16x32_bf16 a[104:107], v[20:23], v[156:159], a[104:107]\n"
"v_mfma_f32_16x16x32_bf16 a[108:111], v[28:31], v[156:159], a[108:111]\n"
"buffer_load_dwordx4 v165, s[68:71], s84 offen offset:1024 lds\n"
"v_mfma_f32_16x16x32_bf16 a[108:111], v[24:27], v[152:155], a[108:111]\n"
"v_mfma_f32_16x16x32_bf16 a[112:115], v[32:35], v[152:155], a[112:115]\n"
"v_mfma_f32_16x16x32_bf16 a[112:115], v[36:39], v[156:159], a[112:115]\n"
"buffer_load_dwordx4 v166, s[68:71], s84 offen offset:2048 lds\n"
"v_mfma_f32_16x16x32_bf16 a[116:119], v[44:47], v[156:159], a[116:119]\n"
"v_mfma_f32_16x16x32_bf16 a[116:119], v[40:43], v[152:155], a[116:119]\n"
"v_mfma_f32_16x16x32_bf16 a[120:123], v[48:51], v[152:155], a[120:123]\n"
"buffer_load_dwordx4 v167, s[68:71], s84 offen offset:3072 lds\n"
"v_mfma_f32_16x16x32_bf16 a[120:123], v[52:55], v[156:159], a[120:123]\n"
"global_load_dword v255, v[204:205], off\n"
"v_mfma_f32_16x16x32_bf16 a[124:127], v[60:63], v[156:159], a[124:127]\n"
"v_lshl_add_u64 v[204:205], v[206:207], 0, v[204:205]\n"
"v_mfma_f32_16x16x32_bf16 a[124:127], v[56:59], v[152:155], a[124:127]\n"
"s_waitcnt lgkmcnt(0)\n"
"v_mfma_f32_16x16x32_bf16 a[128:131], v[0:3], v[128:131], a[128:131]\n"
"ds_read_b128 v[136:139], v178 offset:2048\n"
"v_mfma_f32_16x16x32_bf16 a[128:131], v[4:7], v[132:135], a[128:131]\n"
"ds_read_b128 v[140:143], v179 offset:2048\n"
"v_mfma_f32_16x16x32_bf16 a[132:135], v[12:15], v[132:135], a[132:135]\n"
"s_add_u32 s85, s72, s81\n"
"ds_read_b64_tr_b16 v[68:69], v168 offset:49152\n"
"v_mfma_f32_16x16x32_bf16 a[132:135], v[8:11], v[128:131], a[132:135]\n"
"s_cmp_lt_u32 s81, 2048\n"
"s_cselect_b32 s85, s85, 0x40000000\n"
"v_mfma_f32_16x16x32_bf16 a[136:139], v[16:19], v[128:131], a[136:139]\n"
"ds_read_b64_tr_b16 v[70:71], v168 offset:51200\n"
"v_mfma_f32_16x16x32_bf16 a[136:139], v[20:23], v[132:135], a[136:139]\n"
"s_mov_b32 m0, s82\n"
"v_mfma_f32_16x16x32_bf16 a[140:143], v[28:31], v[132:135], a[140:143]\n"
"ds_read_b64_tr_b16 v[76:77], v169 offset:49152\n"
"v_mfma_f32_16x16x32_bf16 a[140:143], v[24:27], v[128:131], a[140:143]\n"
"buffer_load_dwordx4 v160, s[64:67], s85 offen offset:0 lds\n"
"v_mfma_f32_16x16x32_bf16 a[144:147], v[32:35], v[128:131], a[144:147]\n"
"ds_read_b64_tr_b16 v[78:79], v169 offset:51200\n"
"v_mfma_f32_16x16x32_bf16 a[144:147], v[36:39], v[132:135], a[144:147]\n"
"v_mfma_f32_16x16x32_bf16 a[148:151], v[44:47], v[132:135], a[148:151]\n"
"ds_read_b64_tr_b16 v[84:85], v170 offset:49152\n"
"v_mfma_f32_16x16x32_bf16 a[148:151], v[40:43], v[128:131], a[148:151]\n"
"v_mfma_f32_16x16x32_bf16 a[152:155], v[48:51], v[128:131], a[152:155]\n"
"ds_read_b64_tr_b16 v[86:87], v170 offset:51200\n"
"v_mfma_f32_16x16x32_bf16 a[152:155], v[52:55], v[132:135], a[152:155]\n"
"buffer_load_dwordx4 v161, s[64:67], s85 offen offset:1024 lds\n"
"v_mfma_f32_16x16x32_bf16 a[156:159], v[60:63], v[132:135], a[156:159]\n"
"ds_read_b64_tr_b16 v[92:93], v171 offset:49152\n"
"v_mfma_f32_16x16x32_bf16 a[156:159], v[56:59], v[128:131], a[156:159]\n"
"s_waitcnt lgkmcnt(7)\n"
"v_mfma_f32_16x16x32_bf16 a[160:163], v[0:3], v[136:139], a[160:163]\n"
"ds_read_b128 v[144:147], v178 offset:4096\n"
"v_mfma_f32_16x16x32_bf16 a[160:163], v[4:7], v[140:143], a[160:163]\n"
"ds_read_b128 v[148:151], v179 offset:4096\n"
"ds_read_b64_tr_b16 v[94:95], v171 offset:51200\n"
"v_mfma_f32_16x16x32_bf16 a[164:167], v[12:15], v[140:143], a[164:167]\n"
"v_mfma_f32_16x16x32_bf16 a[164:167], v[8:11], v[136:139], a[164:167]\n"
"ds_read_b64_tr_b16 v[100:101], v172 offset:49152\n"
"v_mfma_f32_16x16x32_bf16 a[168:171], v[16:19], v[136:139], a[168:171]\n"
"buffer_load_dwordx4 v162, s[64:67], s85 offen offset:2048 lds\n"
"v_mfma_f32_16x16x32_bf16 a[168:171], v[20:23], v[140:143], a[168:171]\n"
"ds_read_b64_tr_b16 v[102:103], v172 offset:51200\n"
"v_mfma_f32_16x16x32_bf16 a[172:175], v[28:31], v[140:143], a[172:175]\n"
"v_mfma_f32_16x16x32_bf16 a[172:175], v[24:27], v[136:139], a[172:175]\n"
"ds_read_b64_tr_b16 v[108:109], v173 offset:49152\n"
"v_mfma_f32_16x16x32_bf16 a[176:179], v[32:35], v[136:139], a[176:179]\n"
"v_mfma_f32_16x16x32_bf16 a[176:179], v[36:39], v[140:143], a[176:179]\n"
"ds_read_b64_tr_b16 v[110:111], v173 offset:51200\n"
"v_mfma_f32_16x16x32_bf16 a[180:183], v[44:47], v[140:143], a[180:183]\n"
"buffer_load_dwordx4 v163, s[64:67], s85 offen offset:3072 lds\n"
"v_mfma_f32_16x16x32_bf16 a[180:183], v[40:43], v[136:139], a[180:183]\n"
"ds_read_b64_tr_b16 v[116:117], v174 offset:49152\n"
"v_mfma_f32_16x16x32_bf16 a[184:187], v[48:51], v[136:139], a[184:187]\n"
"s_add_u32 s82, s82, 16384\n"
"v_mfma_f32_16x16x32_bf16 a[184:187], v[52:55], v[140:143], a[184:187]\n"
"s_cmp_lt_u32 s82, 163840\n"
"s_cselect_b32 s86, 0, 98304\n"
"ds_read_b64_tr_b16 v[118:119], v174 offset:51200\n"
"v_mfma_f32_16x16x32_bf16 a[188:191], v[60:63], v[140:143], a[188:191]\n"
"v_mfma_f32_16x16x32_bf16 a[188:191], v[56:59], v[136:139], a[188:191]\n"
"s_waitcnt lgkmcnt(7)\n"
"s_sub_u32 s82, s82, s86\n"
"v_mfma_f32_16x16x32_bf16 a[192:195], v[0:3], v[144:147], a[192:195]\n"
"ds_read_b128 v[152:155], v178 offset:6144\n"
"ds_read_b64_tr_b16 v[124:125], v175 offset:49152\n"
"v_mfma_f32_16x16x32_bf16 a[192:195], v[4:7], v[148:151], a[192:195]\n"
"ds_read_b128 v[156:159], v179 offset:6144\n"
"v_mfma_f32_16x16x32_bf16 a[196:199], v[12:15], v[148:151], a[196:199]\n"
"ds_read_b64_tr_b16 v[126:127], v175 offset:51200\n"
"v_mfma_f32_16x16x32_bf16 a[196:199], v[8:11], v[144:147], a[196:199]\n"
"v_mfma_f32_16x16x32_bf16 a[200:203], v[16:19], v[144:147], a[200:203]\n"
"v_mfma_f32_16x16x32_bf16 a[200:203], v[20:23], v[148:151], a[200:203]\n"
"s_add_u32 s83, s83, 16384\n"
"v_mfma_f32_16x16x32_bf16 a[204:207], v[28:31], v[148:151], a[204:207]\n"
"v_mfma_f32_16x16x32_bf16 a[204:207], v[24:27], v[144:147], a[204:207]\n"
"s_cmp_lt_u32 s83, 163840\n"
"s_cselect_b32 s83, s83, 65536\n"
"v_mfma_f32_16x16x32_bf16 a[208:211], v[32:35], v[144:147], a[208:211]\n"
"v_mfma_f32_16x16x32_bf16 a[208:211], v[36:39], v[148:151], a[208:211]\n"
"v_add_u32 v178, s83, v176\n"
"v_mfma_f32_16x16x32_bf16 a[212:215], v[44:47], v[148:151], a[212:215]\n"
"v_add_u32 v179, s83, v177\n"
"v_mfma_f32_16x16x32_bf16 a[212:215], v[40:43], v[144:147], a[212:215]\n"
"v_mfma_f32_16x16x32_bf16 a[216:219], v[48:51], v[144:147], a[216:219]\n"
"s_add_u32 s84, s84, 262144\n"
"v_mfma_f32_16x16x32_bf16 a[216:219], v[52:55], v[148:151], a[216:219]\n"
"v_mfma_f32_16x16x32_bf16 a[220:223], v[60:63], v[148:151], a[220:223]\n"
"s_waitcnt lgkmcnt(0)\n"
"v_mfma_f32_16x16x32_bf16 a[220:223], v[56:59], v[144:147], a[220:223]\n"
"s_waitcnt vmcnt(21)\n"
"s_barrier\n"
"v_mfma_f32_16x16x32_bf16 a[224:227], v[0:3], v[152:155], a[224:227]\n"
"ds_read_b128 v[128:131], v178 offset:0\n"
"v_mfma_f32_16x16x32_bf16 a[224:227], v[4:7], v[156:159], a[224:227]\n"
"s_add_u32 m0, s74, 49152\n"
"ds_read_b128 v[132:135], v179 offset:0\n"
"v_mfma_f32_16x16x32_bf16 a[228:231], v[12:15], v[156:159], a[228:231]\n"
"v_mfma_f32_16x16x32_bf16 a[228:231], v[8:11], v[152:155], a[228:231]\n"
"buffer_load_dwordx4 v164, s[68:71], s84 offen offset:0 lds\n"
"v_mfma_f32_16x16x32_bf16 a[232:235], v[16:19], v[152:155], a[232:235]\n"
"v_mfma_f32_16x16x32_bf16 a[232:235], v[20:23], v[156:159], a[232:235]\n"
"v_mfma_f32_16x16x32_bf16 a[236:239], v[28:31], v[156:159], a[236:239]\n"
"buffer_load_dwordx4 v165, s[68:71], s84 offen offset:1024 lds\n"
"v_mfma_f32_16x16x32_bf16 a[236:239], v[24:27], v[152:155], a[236:239]\n"
"v_mfma_f32_16x16x32_bf16 a[240:243], v[32:35], v[152:155], a[240:243]\n"
"v_mfma_f32_16x16x32_bf16 a[240:243], v[36:39], v[156:159], a[240:243]\n"
"buffer_load_dwordx4 v166, s[68:71], s84 offen offset:2048 lds\n"
"v_mfma_f32_16x16x32_bf16 a[244:247], v[44:47], v[156:159], a[244:247]\n"
"v_mfma_f32_16x16x32_bf16 a[244:247], v[40:43], v[152:155], a[244:247]\n"
"v_mfma_f32_16x16x32_bf16 a[248:251], v[48:51], v[152:155], a[248:251]\n"
"buffer_load_dwordx4 v167, s[68:71], s84 offen offset:3072 lds\n"
"v_mfma_f32_16x16x32_bf16 a[248:251], v[52:55], v[156:159], a[248:251]\n"
"v_mfma_f32_16x16x32_bf16 a[252:255], v[60:63], v[156:159], a[252:255]\n"
"v_mfma_f32_16x16x32_bf16 a[252:255], v[56:59], v[152:155], a[252:255]\n"
"s_waitcnt lgkmcnt(0)\n"
"v_mfma_f32_16x16x32_bf16 a[0:3], v[64:67], v[128:131], a[0:3]\n"
"ds_read_b128 v[136:139], v178 offset:2048\n"
"v_mfma_f32_16x16x32_bf16 a[0:3], v[68:71], v[132:135], a[0:3]\n"
"ds_read_b128 v[140:143], v179 offset:2048\n"
"v_mfma_f32_16x16x32_bf16 a[4:7], v[76:79], v[132:135], a[4:7]\n"
"s_add_u32 s85, s72, s81\n"
"ds_read_b64_tr_b16 v[0:1], v168 offset:0\n"
"v_mfma_f32_16x16x32_bf16 a[4:7], v[72:75], v[128:131], a[4:7]\n"
"s_cmp_lt_u32 s81, 2048\n"
"s_cselect_b32 s85, s85, 0x40000000\n"
"v_mfma_f32_16x16x32_bf16 a[8:11], v[80:83], v[128:131], a[8:11]\n"
"ds_read_b64_tr_b16 v[2:3], v168 offset:2048\n"
"v_mfma_f32_16x16x32_bf16 a[8:11], v[84:87], v[132:135], a[8:11]\n"
"s_add_u32 s85, s85, 131072\n"
"s_mov_b32 m0, s82\n"
"v_mfma_f32_16x16x32_bf16 a[12:15], v[92:95], v[132:135], a[12:15]\n"
"ds_read_b64_tr_b16 v[8:9], v169 offset:0\n"
"v_mfma_f32_16x16x32_bf16 a[12:15], v[88:91], v[128:131], a[12:15]\n"
"buffer_load_dwordx4 v160, s[64:67], s85 offen offset:0 lds\n"
"v_mfma_f32_16x16x32_bf16 a[16:19], v[96:99], v[128:131], a[16:19]\n"
"ds_read_b64_tr_b16 v[10:11], v169 offset:2048\n"
"v_mfma_f32_16x16x32_bf16 a[16:19], v[100:103], v[132:135], a[16:19]\n"
"v_mfma_f32_16x16x32_bf16 a[20:23], v[108:111], v[132:135], a[20:23]\n"
"ds_read_b64_tr_b16 v[16:17], v170 offset:0\n"
"v_mfma_f32_16x16x32_bf16 a[20:23], v[104:107], v[128:131], a[20:23]\n"
"v_mfma_f32_16x16x32_bf16 a[24:27], v[112:115], v[128:131], a[24:27]\n"
"ds_read_b64_tr_b16 v[18:19], v170 offset:2048\n"
"v_mfma_f32_16x16x32_bf16 a[24:27], v[116:119], v[132:135], a[24:27]\n"
"buffer_load_dwordx4 v161, s[64:67], s85 offen offset:1024 lds\n"
"v_mfma_f32_16x16x32_bf16 a[28:31], v[124:127], v[132:135], a[28:31]\n"
"ds_read_b64_tr_b16 v[24:25], v171 offset:0\n"
"v_mfma_f32_16x16x32_bf16 a[28:31], v[120:123], v[128:131], a[28:31]\n"
"s_waitcnt lgkmcnt(7)\n"
"v_mfma_f32_16x16x32_bf16 a[32:35], v[64:67], v[136:139], a[32:35]\n"
"ds_read_b128 v[144:147], v178 offset:4096\n"
"v_mfma_f32_16x16x32_bf16 a[32:35], v[68:71], v[140:143], a[32:35]\n"
"ds_read_b128 v[148:151], v179 offset:4096\n"
"ds_read_b64_tr_b16 v[26:27], v171 offset:2048\n"
"v_mfma_f32_16x16x32_bf16 a[36:39], v[76:79], v[140:143], a[36:39]\n"
"v_mfma_f32_16x16x32_bf16 a[36:39], v[72:75], v[136:139], a[36:39]\n"
"ds_read_b64_tr_b16 v[32:33], v172 offset:0\n"
"v_mfma_f32_16x16x32_bf16 a[40:43], v[80:83], v[136:139], a[40:43]\n"
"buffer_load_dwordx4 v162, s[64:67], s85 offen offset:2048 lds\n"
"v_mfma_f32_16x16x32_bf16 a[40:43], v[84:87], v[140:143], a[40:43]\n"
"ds_read_b64_tr_b16 v[34:35], v172 offset:2048\n"
"v_mfma_f32_16x16x32_bf16 a[44:47], v[92:95], v[140:143], a[44:47]\n"
"v_mfma_f32_16x16x32_bf16 a[44:47], v[88:91], v[136:139], a[44:47]\n"
"ds_read_b64_tr_b16 v[40:41], v173 offset:0\n"
"v_mfma_f32_16x16x32_bf16 a[48:51], v[96:99], v[136:139], a[48:51]\n"
"v_mfma_f32_16x16x32_bf16 a[48:51], v[100:103], v[140:143], a[48:51]\n"
"ds_read_b64_tr_b16 v[42:43], v173 offset:2048\n"
"v_mfma_f32_16x16x32_bf16 a[52:55], v[108:111], v[140:143], a[52:55]\n"
"buffer_load_dwordx4 v163, s[64:67], s85 offen offset:3072 lds\n"
"v_mfma_f32_16x16x32_bf16 a[52:55], v[104:107], v[136:139], a[52:55]\n"
"ds_read_b64_tr_b16 v[48:49], v174 offset:0\n"
"v_mfma_f32_16x16x32_bf16 a[56:59], v[112:115], v[136:139], a[56:59]\n"
"s_add_u32 s82, s82, 16384\n"
"v_mfma_f32_16x16x32_bf16 a[56:59], v[116:119], v[140:143], a[56:59]\n"
"s_cmp_lt_u32 s82, 163840\n"
"s_cselect_b32 s86, 0, 98304\n"
"ds_read_b64_tr_b16 v[50:51], v174 offset:2048\n"
"v_mfma_f32_16x16x32_bf16 a[60:63], v[124:127], v[140:143], a[60:63]\n"
"v_mfma_f32_16x16x32_bf16 a[60:63], v[120:123], v[136:139], a[60:63]\n"
"s_waitcnt lgkmcnt(7)\n"
"s_sub_u32 s82, s82, s86\n"
"v_mfma_f32_16x16x32_bf16 a[64:67], v[64:67], v[144:147], a[64:67]\n"
"ds_read_b128 v[152:155], v178 offset:6144\n"
"ds_read_b64_tr_b16 v[56:57], v175 offset:0\n"
"v_mfma_f32_16x16x32_bf16 a[64:67], v[68:71], v[148:151], a[64:67]\n"
"ds_read_b128 v[156:159], v179 offset:6144\n"
"v_mfma_f32_16x16x32_bf16 a[68:71], v[76:79], v[148:151], a[68:71]\n"
"ds_read_b64_tr_b16 v[58:59], v175 offset:2048\n"
"v_mfma_f32_16x16x32_bf16 a[68:71], v[72:75], v[144:147], a[68:71]\n"
"s_add_u32 s81, s81, 128\n"
"v_mfma_f32_16x16x32_bf16 a[72:75], v[80:83], v[144:147], a[72:75]\n"
"v_mfma_f32_16x16x32_bf16 a[72:75], v[84:87], v[148:151], a[72:75]\n"
"s_add_u32 s83, s83, 16384\n"
"v_mfma_f32_16x16x32_bf16 a[76:79], v[92:95], v[148:151], a[76:79]\n"
"v_mfma_f32_16x16x32_bf16 a[76:79], v[88:91], v[144:147], a[76:79]\n"
"s_cmp_lt_u32 s83, 163840\n"
"s_cselect_b32 s83, s83, 65536\n"
"v_mfma_f32_16x16x32_bf16 a[80:83], v[96:99], v[144:147], a[80:83]\n"
"v_mfma_f32_16x16x32_bf16 a[80:83], v[100:103], v[148:151], a[80:83]\n"
"v_add_u32 v178, s83, v176\n"
"v_mfma_f32_16x16x32_bf16 a[84:87], v[108:111], v[148:151], a[84:87]\n"
"v_add_u32 v179, s83, v177\n"
"v_mfma_f32_16x16x32_bf16 a[84:87], v[104:107], v[144:147], a[84:87]\n"
"v_mfma_f32_16x16x32_bf16 a[88:91], v[112:115], v[144:147], a[88:91]\n"
"s_add_u32 s84, s84, 262144\n"
"v_mfma_f32_16x16x32_bf16 a[88:91], v[116:119], v[148:151], a[88:91]\n"
"v_mfma_f32_16x16x32_bf16 a[92:95], v[124:127], v[148:151], a[92:95]\n"
"s_waitcnt lgkmcnt(0)\n"
"v_mfma_f32_16x16x32_bf16 a[92:95], v[120:123], v[144:147], a[92:95]\n"
"s_waitcnt vmcnt(21)\n"
"s_barrier\n"
"v_mfma_f32_16x16x32_bf16 a[96:99], v[64:67], v[152:155], a[96:99]\n"
"ds_read_b128 v[128:131], v178 offset:0\n"
"v_mfma_f32_16x16x32_bf16 a[96:99], v[68:71], v[156:159], a[96:99]\n"
"s_add_u32 m0, s74, 0\n"
"ds_read_b128 v[132:135], v179 offset:0\n"
"v_mfma_f32_16x16x32_bf16 a[100:103], v[76:79], v[156:159], a[100:103]\n"
"v_mfma_f32_16x16x32_bf16 a[100:103], v[72:75], v[152:155], a[100:103]\n"
"buffer_load_dwordx4 v164, s[68:71], s84 offen offset:0 lds\n"
"v_mfma_f32_16x16x32_bf16 a[104:107], v[80:83], v[152:155], a[104:107]\n"
"v_mfma_f32_16x16x32_bf16 a[104:107], v[84:87], v[156:159], a[104:107]\n"
"v_mfma_f32_16x16x32_bf16 a[108:111], v[92:95], v[156:159], a[108:111]\n"
"buffer_load_dwordx4 v165, s[68:71], s84 offen offset:1024 lds\n"
"v_mfma_f32_16x16x32_bf16 a[108:111], v[88:91], v[152:155], a[108:111]\n"
"v_mfma_f32_16x16x32_bf16 a[112:115], v[96:99], v[152:155], a[112:115]\n"
"v_mfma_f32_16x16x32_bf16 a[112:115], v[100:103], v[156:159], a[112:115]\n"
"buffer_load_dwordx4 v166, s[68:71], s84 offen offset:2048 lds\n"
"v_mfma_f32_16x16x32_bf16 a[116:119], v[108:111], v[156:159], a[116:119]\n"
"v_mfma_f32_16x16x32_bf16 a[116:119], v[104:107], v[152:155], a[116:119]\n"
"v_mfma_f32_16x16x32_bf16 a[120:123], v[112:115], v[152:155], a[120:123]\n"
"buffer_load_dwordx4 v167, s[68:71], s84 offen offset:3072 lds\n"
"v_mfma_f32_16x16x32_bf16 a[120:123], v[116:119], v[156:159], a[120:123]\n"
"v_mfma_f32_16x16x32_bf16 a[124:127], v[124:127], v[156:159], a[124:127]\n"
"v_mfma_f32_16x16x32_bf16 a[124:127], v[120:123], v[152:155], a[124:127]\n"
"s_waitcnt lgkmcnt(0)\n"
"v_mfma_f32_16x16x32_bf16 a[128:131], v[64:67], v[128:131], a[128:131]\n"
"ds_read_b128 v[136:139], v178 offset:2048\n"
"v_mfma_f32_16x16x32_bf16 a[128:131], v[68:71], v[132:135], a[128:131]\n"
"ds_read_b128 v[140:143], v179 offset:2048\n"
"v_mfma_f32_16x16x32_bf16 a[132:135], v[76:79], v[132:135], a[132:135]\n"
"s_add_u32 s85, s72, s81\n"
"ds_read_b64_tr_b16 v[4:5], v168 offset:16384\n"
"v_mfma_f32_16x16x32_bf16 a[132:135], v[72:75], v[128:131], a[132:135]\n"
"s_cmp_lt_u32 s81, 2048\n"
"s_cselect_b32 s85, s85, 0x40000000\n"
"v_mfma_f32_16x16x32_bf16 a[136:139], v[80:83], v[128:131], a[136:139]\n"
"ds_read_b64_tr_b16 v[6:7], v168 offset:18432\n"
"v_mfma_f32_16x16x32_bf16 a[136:139], v[84:87], v[132:135], a[136:139]\n"
"s_mov_b32 m0, s82\n"
"v_mfma_f32_16x16x32_bf16 a[140:143], v[92:95], v[132:135], a[140:143]\n"
"ds_read_b64_tr_b16 v[12:13], v169 offset:16384\n"
"v_mfma_f32_16x16x32_bf16 a[140:143], v[88:91], v[128:131], a[140:143]\n"
"buffer_load_dwordx4 v160, s[64:67], s85 offen offset:0 lds\n"
"v_mfma_f32_16x16x32_bf16 a[144:147], v[96:99], v[128:131], a[144:147]\n"
"ds_read_b64_tr_b16 v[14:15], v169 offset:18432\n"
"v_mfma_f32_16x16x32_bf16 a[144:147], v[100:103], v[132:135], a[144:147]\n"
"v_mfma_f32_16x16x32_bf16 a[148:151], v[108:111], v[132:135], a[148:151]\n"
"ds_read_b64_tr_b16 v[20:21], v170 offset:16384\n"
"v_mfma_f32_16x16x32_bf16 a[148:151], v[104:107], v[128:131], a[148:151]\n"
"v_mfma_f32_16x16x32_bf16 a[152:155], v[112:115], v[128:131], a[152:155]\n"
"ds_read_b64_tr_b16 v[22:23], v170 offset:18432\n"
"v_mfma_f32_16x16x32_bf16 a[152:155], v[116:119], v[132:135], a[152:155]\n"
"buffer_load_dwordx4 v161, s[64:67], s85 offen offset:1024 lds\n"
"v_mfma_f32_16x16x32_bf16 a[156:159], v[124:127], v[132:135], a[156:159]\n"
"ds_read_b64_tr_b16 v[28:29], v171 offset:16384\n"
"v_mfma_f32_16x16x32_bf16 a[156:159], v[120:123], v[128:131], a[156:159]\n"
"s_waitcnt lgkmcnt(7)\n"
"v_mfma_f32_16x16x32_bf16 a[160:163], v[64:67], v[136:139], a[160:163]\n"
"ds_read_b128 v[144:147], v178 offset:4096\n"
"v_mfma_f32_16x16x32_bf16 a[160:163], v[68:71], v[140:143], a[160:163]\n"
"ds_read_b128 v[148:151], v179 offset:4096\n"
"ds_read_b64_tr_b16 v[30:31], v171 offset:18432\n"
"v_mfma_f32_16x16x32_bf16 a[164:167], v[76:79], v[140:143], a[164:167]\n"
"v_mfma_f32_16x16x32_bf16 a[164:167], v[72:75], v[136:139], a[164:167]\n"
"ds_read_b64_tr_b16 v[36:37], v172 offset:16384\n"
"v_mfma_f32_16x16x32_bf16 a[168:171], v[80:83], v[136:139], a[168:171]\n"
"buffer_load_dwordx4 v162, s[64:67], s85 offen offset:2048 lds\n"
"v_mfma_f32_16x16x32_bf16 a[168:171], v[84:87], v[140:143], a[168:171]\n"
"ds_read_b64_tr_b16 v[38:39], v172 offset:18432\n"
"v_mfma_f32_16x16x32_bf16 a[172:175], v[92:95], v[140:143], a[172:175]\n"
"v_mfma_f32_16x16x32_bf16 a[172:175], v[88:91], v[136:139], a[172:175]\n"
"ds_read_b64_tr_b16 v[44:45], v173 offset:16384\n"
"v_mfma_f32_16x16x32_bf16 a[176:179], v[96:99], v[136:139], a[176:179]\n"
"v_mfma_f32_16x16x32_bf16 a[176:179], v[100:103], v[140:143], a[176:179]\n"
"ds_read_b64_tr_b16 v[46:47], v173 offset:18432\n"
"v_mfma_f32_16x16x32_bf16 a[180:183], v[108:111], v[140:143], a[180:183]\n"
"buffer_load_dwordx4 v163, s[64:67], s85 offen offset:3072 lds\n"
"v_mfma_f32_16x16x32_bf16 a[180:183], v[104:107], v[136:139], a[180:183]\n"
"ds_read_b64_tr_b16 v[52:53], v174 offset:16384\n"
"v_mfma_f32_16x16x32_bf16 a[184:187], v[112:115], v[136:139], a[184:187]\n"
"s_add_u32 s82, s82, 16384\n"
"v_mfma_f32_16x16x32_bf16 a[184:187], v[116:119], v[140:143], a[184:187]\n"
"s_cmp_lt_u32 s82, 163840\n"
"s_cselect_b32 s86, 0, 98304\n"
"ds_read_b64_tr_b16 v[54:55], v174 offset:18432\n"
"v_mfma_f32_16x16x32_bf16 a[188:191], v[124:127], v[140:143], a[188:191]\n"
"v_mfma_f32_16x16x32_bf16 a[188:191], v[120:123], v[136:139], a[188:191]\n"
"s_waitcnt lgkmcnt(7)\n"
"s_sub_u32 s82, s82, s86\n"
"v_mfma_f32_16x16x32_bf16 a[192:195], v[64:67], v[144:147], a[192:195]\n"
"ds_read_b128 v[152:155], v178 offset:6144\n"
"ds_read_b64_tr_b16 v[60:61], v175 offset:16384\n"
"v_mfma_f32_16x16x32_bf16 a[192:195], v[68:71], v[148:151], a[192:195]\n"
"ds_read_b128 v[156:159], v179 offset:6144\n"
"v_mfma_f32_16x16x32_bf16 a[196:199], v[76:79], v[148:151], a[196:199]\n"
"ds_read_b64_tr_b16 v[62:63], v175 offset:18432\n"
"v_mfma_f32_16x16x32_bf16 a[196:199], v[72:75], v[144:147], a[196:199]\n"
"v_mfma_f32_16x16x32_bf16 a[200:203], v[80:83], v[144:147], a[200:203]\n"
"v_mfma_f32_16x16x32_bf16 a[200:203], v[84:87], v[148:151], a[200:203]\n"
"s_add_u32 s83, s83, 16384\n"
"v_mfma_f32_16x16x32_bf16 a[204:207], v[92:95], v[148:151], a[204:207]\n"
"v_mfma_f32_16x16x32_bf16 a[204:207], v[88:91], v[144:147], a[204:207]\n"
"s_cmp_lt_u32 s83, 163840\n"
"s_cselect_b32 s83, s83, 65536\n"
"v_mfma_f32_16x16x32_bf16 a[208:211], v[96:99], v[144:147], a[208:211]\n"
"v_mfma_f32_16x16x32_bf16 a[208:211], v[100:103], v[148:151], a[208:211]\n"
"v_add_u32 v178, s83, v176\n"
"v_mfma_f32_16x16x32_bf16 a[212:215], v[108:111], v[148:151], a[212:215]\n"
"v_add_u32 v179, s83, v177\n"
"v_mfma_f32_16x16x32_bf16 a[212:215], v[104:107], v[144:147], a[212:215]\n"
"v_mfma_f32_16x16x32_bf16 a[216:219], v[112:115], v[144:147], a[216:219]\n"
"s_add_u32 s84, s84, 262144\n"
"v_mfma_f32_16x16x32_bf16 a[216:219], v[116:119], v[148:151], a[216:219]\n"
"v_mfma_f32_16x16x32_bf16 a[220:223], v[124:127], v[148:151], a[220:223]\n"
"s_waitcnt lgkmcnt(0)\n"
"v_mfma_f32_16x16x32_bf16 a[220:223], v[120:123], v[144:147], a[220:223]\n"
"s_waitcnt vmcnt(21)\n"
"s_barrier\n"
"v_mfma_f32_16x16x32_bf16 a[224:227], v[64:67], v[152:155], a[224:227]\n"
"ds_read_b128 v[128:131], v178 offset:0\n"
"v_mfma_f32_16x16x32_bf16 a[224:227], v[68:71], v[156:159], a[224:227]\n"
"s_add_u32 m0, s74, 16384\n"
"ds_read_b128 v[132:135], v179 offset:0\n"
"v_mfma_f32_16x16x32_bf16 a[228:231], v[76:79], v[156:159], a[228:231]\n"
"v_mfma_f32_16x16x32_bf16 a[228:231], v[72:75], v[152:155], a[228:231]\n"
"buffer_load_dwordx4 v164, s[68:71], s84 offen offset:0 lds\n"
"v_mfma_f32_16x16x32_bf16 a[232:235], v[80:83], v[152:155], a[232:235]\n"
"v_mfma_f32_16x16x32_bf16 a[232:235], v[84:87], v[156:159], a[232:235]\n"
"v_mfma_f32_16x16x32_bf16 a[236:239], v[92:95], v[156:159], a[236:239]\n"
"buffer_load_dwordx4 v165, s[68:71], s84 offen offset:1024 lds\n"
"v_mfma_f32_16x16x32_bf16 a[236:239], v[88:91], v[152:155], a[236:239]\n"
"v_mfma_f32_16x16x32_bf16 a[240:243], v[96:99], v[152:155], a[240:243]\n"
"v_mfma_f32_16x16x32_bf16 a[240:243], v[100:103], v[156:159], a[240:243]\n"
"buffer_load_dwordx4 v166, s[68:71], s84 offen offset:2048 lds\n"
"v_mfma_f32_16x16x32_bf16 a[244:247], v[108:111], v[156:159], a[244:247]\n"
"v_mfma_f32_16x16x32_bf16 a[244:247], v[104:107], v[152:155], a[244:247]\n"
"v_mfma_f32_16x16x32_bf16 a[248:251], v[112:115], v[152:155], a[248:251]\n"
"buffer_load_dwordx4 v167, s[68:71], s84 offen offset:3072 lds\n"
"v_mfma_f32_16x16x32_bf16 a[248:251], v[116:119], v[156:159], a[248:251]\n"
"v_mfma_f32_16x16x32_bf16 a[252:255], v[124:127], v[156:159], a[252:255]\n"
"v_mfma_f32_16x16x32_bf16 a[252:255], v[120:123], v[152:155], a[252:255]\n"
"s_waitcnt lgkmcnt(0)\n"
"s_cmp_ge_u32 s80, 4\n"
"s_cbranch_scc1 L_b1k%=\n"
"L_b1rk%=:\n"
"s_sub_u32 s80, s80, 1\n"
"s_cmp_lg_u32 s80, 0\n"
"s_cbranch_scc1 L_k1loopk%=\n"
"v_mfma_f32_16x16x32_bf16 a[0:3], v[0:3], v[128:131], a[0:3]\n"
"ds_read_b128 v[136:139], v178 offset:2048\n"
"v_mfma_f32_16x16x32_bf16 a[0:3], v[4:7], v[132:135], a[0:3]\n"
"ds_read_b128 v[140:143], v179 offset:2048\n"
"v_mfma_f32_16x16x32_bf16 a[4:7], v[12:15], v[132:135], a[4:7]\n"
"ds_read_b64_tr_b16 v[64:65], v168 offset:32768\n"
"v_mfma_f32_16x16x32_bf16 a[4:7], v[8:11], v[128:131], a[4:7]\n"
"ds_read_b64_tr_b16 v[66:67], v168 offset:34816\n"
"v_mfma_f32_16x16x32_bf16 a[8:11], v[16:19], v[128:131], a[8:11]\n"
"ds_read_b64_tr_b16 v[72:73], v169 offset:32768\n"
"v_mfma_f32_16x16x32_bf16 a[8:11], v[20:23], v[132:135], a[8:11]\n"
"ds_read_b64_tr_b16 v[74:75], v169 offset:34816\n"
"v_mfma_f32_16x16x32_bf16 a[12:15], v[28:31], v[132:135], a[12:15]\n"
"ds_read_b64_tr_b16 v[80:81], v170 offset:32768\n"
"v_mfma_f32_16x16x32_bf16 a[12:15], v[24:27], v[128:131], a[12:15]\n"
"ds_read_b64_tr_b16 v[82:83], v170 offset:34816\n"
"v_mfma_f32_16x16x32_bf16 a[16:19], v[32:35], v[128:131], a[16:19]\n"
"ds_read_b64_tr_b16 v[88:89], v171 offset:32768\n"
"v_mfma_f32_16x16x32_bf16 a[16:19], v[36:39], v[132:135], a[16:19]\n"
"ds_read_b64_tr_b16 v[90:91], v171 offset:34816\n"
"v_mfma_f32_16x16x32_bf16 a[20:23], v[44:47], v[132:135], a[20:23]\n"
"ds_read_b64_tr_b16 v[96:97], v172 offset:32768\n"
"v_mfma_f32_16x16x32_bf16 a[20:23], v[40:43], v[128:131], a[20:23]\n"
"ds_read_b64_tr_b16 v[98:99], v172 offset:34816\n"
"v_mfma_f32_16x16x32_bf16 a[24:27], v[48:51], v[128:131], a[24:27]\n"
"ds_read_b64_tr_b16 v[104:105], v173 offset:32768\n"
"v_mfma_f32_16x16x32_bf16 a[24:27], v[52:55], v[132:135], a[24:27]\n"
"ds_read_b64_tr_b16 v[106:107], v173 offset:34816\n"
"v_mfma_f32_16x16x32_bf16 a[28:31], v[60:63], v[132:135], a[28:31]\n"
"ds_read_b64_tr_b16 v[112:113], v174 offset:32768\n"
"v_mfma_f32_16x16x32_bf16 a[28:31], v[56:59], v[128:131], a[28:31]\n"
"ds_read_b64_tr_b16 v[114:115], v174 offset:34816\n"
"s_waitcnt lgkmcnt(12)\n"
"v_mfma_f32_16x16x32_bf16 a[32:35], v[0:3], v[136:139], a[32:35]\n"
"ds_read_b128 v[144:147], v178 offset:4096\n"
"v_mfma_f32_16x16x32_bf16 a[32:35], v[4:7], v[140:143], a[32:35]\n"
"ds_read_b128 v[148:151], v179 offset:4096\n"
"v_mfma_f32_16x16x32_bf16 a[36:39], v[12:15], v[140:143], a[36:39]\n"
"ds_read_b64_tr_b16 v[120:121], v175 offset:32768\n"
"v_mfma_f32_16x16x32_bf16 a[36:39], v[8:11], v[136:139], a[36:39]\n"
"ds_read_b64_tr_b16 v[122:123], v175 offset:34816\n"
"v_mfma_f32_16x16x32_bf16 a[40:43], v[16:19], v[136:139], a[40:43]\n"
"v_mfma_f32_16x16x32_bf16 a[40:43], v[20:23], v[140:143], a[40:43]\n"
"s_waitcnt vmcnt(0)\n"
"s_barrier\n"
"v_mfma_f32_16x16x32_bf16 a[44:47], v[28:31], v[140:143], a[44:47]\n"
"ds_read_b64_tr_b16 v[68:69], v168 offset:49152\n"
"v_mfma_f32_16x16x32_bf16 a[44:47], v[24:27], v[136:139], a[44:47]\n"
"s_add_u32 m0, s47, 131072\n"
"ds_read_b64_tr_b16 v[70:71], v168 offset:51200\n"
"v_mfma_f32_16x16x32_bf16 a[48:51], v[32:35], v[136:139], a[48:51]\n"
"ds_read_b64_tr_b16 v[76:77], v169 offset:49152\n"
"v_readlane_b32 s24, v224, s48\n"
"v_mfma_f32_16x16x32_bf16 a[48:51], v[36:39], v[140:143], a[48:51]\n"
"ds_read_b64_tr_b16 v[78:79], v169 offset:51200\n"
"v_readlane_b32 s25, v225, s48\n"
"v_mfma_f32_16x16x32_bf16 a[52:55], v[44:47], v[140:143], a[52:55]\n"
"ds_read_b64_tr_b16 v[84:85], v170 offset:49152\n"
"buffer_load_dword v239, v181, s[20:23], 0 offen offset:0 sc1\n"
"v_mfma_f32_16x16x32_bf16 a[52:55], v[40:43], v[136:139], a[52:55]\n"
"ds_read_b64_tr_b16 v[86:87], v170 offset:51200\n"
"v_readlane_b32 s26, v226, s48\n"
"v_mfma_f32_16x16x32_bf16 a[56:59], v[48:51], v[136:139], a[56:59]\n"
"s_add_u32 s48, s48, 1\n"
"ds_read_b64_tr_b16 v[92:93], v171 offset:49152\n"
"v_mfma_f32_16x16x32_bf16 a[56:59], v[52:55], v[140:143], a[56:59]\n"
"ds_read_b64_tr_b16 v[94:95], v171 offset:51200\n"
"v_mfma_f32_16x16x32_bf16 a[60:63], v[60:63], v[140:143], a[60:63]\n"
"ds_read_b64_tr_b16 v[100:101], v172 offset:49152\n"
"v_mfma_f32_16x16x32_bf16 a[60:63], v[56:59], v[136:139], a[60:63]\n"
"ds_read_b64_tr_b16 v[102:103], v172 offset:51200\n"
"s_waitcnt lgkmcnt(9)\n"
"v_mfma_f32_16x16x32_bf16 a[64:67], v[0:3], v[144:147], a[64:67]\n"
"ds_read_b128 v[152:155], v178 offset:6144\n"
"v_mfma_f32_16x16x32_bf16 a[64:67], v[4:7], v[148:151], a[64:67]\n"
"ds_read_b128 v[156:159], v179 offset:6144\n"
"buffer_load_dwordx4 v208, s[24:27], s49 offen offset:0 lds\n"
"v_mfma_f32_16x16x32_bf16 a[68:71], v[12:15], v[148:151], a[68:71]\n"
"ds_read_b64_tr_b16 v[108:109], v173 offset:49152\n"
"v_mfma_f32_16x16x32_bf16 a[68:71], v[8:11], v[144:147], a[68:71]\n"
"ds_read_b64_tr_b16 v[110:111], v173 offset:51200\n"
"v_mfma_f32_16x16x32_bf16 a[72:75], v[16:19], v[144:147], a[72:75]\n"
"ds_read_b64_tr_b16 v[116:117], v174 offset:49152\n"
"v_mfma_f32_16x16x32_bf16 a[72:75], v[20:23], v[148:151], a[72:75]\n"
"ds_read_b64_tr_b16 v[118:119], v174 offset:51200\n"
"buffer_load_dwordx4 v209, s[24:27], s49 offen offset:1024 lds\n"
"v_mfma_f32_16x16x32_bf16 a[76:79], v[28:31], v[148:151], a[76:79]\n"
"ds_read_b64_tr_b16 v[124:125], v175 offset:49152\n"
"v_mfma_f32_16x16x32_bf16 a[76:79], v[24:27], v[144:147], a[76:79]\n"
"ds_read_b64_tr_b16 v[126:127], v175 offset:51200\n"
"v_mfma_f32_16x16x32_bf16 a[80:83], v[32:35], v[144:147], a[80:83]\n"
"buffer_load_dwordx4 v210, s[24:27], s49 offen offset:2048 lds\n"
"v_mfma_f32_16x16x32_bf16 a[80:83], v[36:39], v[148:151], a[80:83]\n"
"buffer_load_dwordx4 v211, s[24:27], s49 offen offset:3072 lds\n"
"v_mfma_f32_16x16x32_bf16 a[84:87], v[44:47], v[148:151], a[84:87]\n"
"v_mfma_f32_16x16x32_bf16 a[84:87], v[40:43], v[144:147], a[84:87]\n"
"s_add_u32 m0, m0, 4096\n"
"v_add_u32 v178, 98304, v176\n"
"v_mfma_f32_16x16x32_bf16 a[88:91], v[48:51], v[144:147], a[88:91]\n"
"v_add_u32 v179, 98304, v177\n"
"v_mfma_f32_16x16x32_bf16 a[88:91], v[52:55], v[148:151], a[88:91]\n"
"buffer_load_dwordx4 v212, s[24:27], s49 offen offset:0 lds\n"
"v_mfma_f32_16x16x32_bf16 a[92:95], v[60:63], v[148:151], a[92:95]\n"
"s_waitcnt lgkmcnt(0)\n"
"v_mfma_f32_16x16x32_bf16 a[92:95], v[56:59], v[144:147], a[92:95]\n"
"s_barrier\n"
"v_mfma_f32_16x16x32_bf16 a[96:99], v[0:3], v[152:155], a[96:99]\n"
"ds_read_b128 v[128:131], v178 offset:0\n"
"v_mfma_f32_16x16x32_bf16 a[96:99], v[4:7], v[156:159], a[96:99]\n"
"ds_read_b128 v[132:135], v179 offset:0\n"
"v_mfma_f32_16x16x32_bf16 a[100:103], v[12:15], v[156:159], a[100:103]\n"
"buffer_load_dwordx4 v213, s[24:27], s49 offen offset:1024 lds\n"
"v_mfma_f32_16x16x32_bf16 a[100:103], v[8:11], v[152:155], a[100:103]\n"
"v_mfma_f32_16x16x32_bf16 a[104:107], v[16:19], v[152:155], a[104:107]\n"
"v_mfma_f32_16x16x32_bf16 a[104:107], v[20:23], v[156:159], a[104:107]\n"
"buffer_load_dwordx4 v214, s[24:27], s49 offen offset:2048 lds\n"
"v_mfma_f32_16x16x32_bf16 a[108:111], v[28:31], v[156:159], a[108:111]\n"
"v_mfma_f32_16x16x32_bf16 a[108:111], v[24:27], v[152:155], a[108:111]\n"
"v_mfma_f32_16x16x32_bf16 a[112:115], v[32:35], v[152:155], a[112:115]\n"
"v_mfma_f32_16x16x32_bf16 a[112:115], v[36:39], v[156:159], a[112:115]\n"
"buffer_load_dwordx4 v215, s[24:27], s49 offen offset:3072 lds\n"
"v_mfma_f32_16x16x32_bf16 a[116:119], v[44:47], v[156:159], a[116:119]\n"
"v_mfma_f32_16x16x32_bf16 a[116:119], v[40:43], v[152:155], a[116:119]\n"
"v_mfma_f32_16x16x32_bf16 a[120:123], v[48:51], v[152:155], a[120:123]\n"
"v_mfma_f32_16x16x32_bf16 a[120:123], v[52:55], v[156:159], a[120:123]\n"
"v_mfma_f32_16x16x32_bf16 a[124:127], v[60:63], v[156:159], a[124:127]\n"
"v_mfma_f32_16x16x32_bf16 a[124:127], v[56:59], v[152:155], a[124:127]\n"
"s_waitcnt lgkmcnt(1)\n"
"v_mfma_f32_16x16x32_bf16 a[0:3], v[64:67], v[128:131], a[0:3]\n"
"s_add_u32 m0, s47, 0\n"
"ds_read_b128 v[136:139], v178 offset:2048\n"
"s_waitcnt lgkmcnt(1)\n"
"v_mfma_f32_16x16x32_bf16 a[0:3], v[68:71], v[132:135], a[0:3]\n"
"ds_read_b128 v[140:143], v179 offset:2048\n"
"v_readlane_b32 s24, v224, s48\n"
"v_mfma_f32_16x16x32_bf16 a[4:7], v[76:79], v[132:135], a[4:7]\n"
"v_readlane_b32 s25, v225, s48\n"
"v_mfma_f32_16x16x32_bf16 a[4:7], v[72:75], v[128:131], a[4:7]\n"
"v_readlane_b32 s26, v226, s48\n"
"v_mfma_f32_16x16x32_bf16 a[8:11], v[80:83], v[128:131], a[8:11]\n"
"s_add_u32 s48, s48, 1\n"
"v_mfma_f32_16x16x32_bf16 a[8:11], v[84:87], v[132:135], a[8:11]\n"
"v_mfma_f32_16x16x32_bf16 a[12:15], v[92:95], v[132:135], a[12:15]\n"
"s_nop 0\n"
"buffer_load_dwordx4 v208, s[24:27], s49 offen offset:0 lds\n"
"v_mfma_f32_16x16x32_bf16 a[12:15], v[88:91], v[128:131], a[12:15]\n"
"v_mfma_f32_16x16x32_bf16 a[16:19], v[96:99], v[128:131], a[16:19]\n"
"v_mfma_f32_16x16x32_bf16 a[16:19], v[100:103], v[132:135], a[16:19]\n"
"buffer_load_dwordx4 v209, s[24:27], s49 offen offset:1024 lds\n"
"v_mfma_f32_16x16x32_bf16 a[20:23], v[108:111], v[132:135], a[20:23]\n"
"v_mfma_f32_16x16x32_bf16 a[20:23], v[104:107], v[128:131], a[20:23]\n"
"v_mfma_f32_16x16x32_bf16 a[24:27], v[112:115], v[128:131], a[24:27]\n"
"buffer_load_dwordx4 v210, s[24:27], s49 offen offset:2048 lds\n"
"v_mfma_f32_16x16x32_bf16 a[24:27], v[116:119], v[132:135], a[24:27]\n"
"v_mfma_f32_16x16x32_bf16 a[28:31], v[124:127], v[132:135], a[28:31]\n"
"v_mfma_f32_16x16x32_bf16 a[28:31], v[120:123], v[128:131], a[28:31]\n"
"buffer_load_dwordx4 v211, s[24:27], s49 offen offset:3072 lds\n"
"s_waitcnt lgkmcnt(0)\n"
"v_mfma_f32_16x16x32_bf16 a[32:35], v[64:67], v[136:139], a[32:35]\n"
"s_add_u32 m0, m0, 4096\n"
"ds_read_b128 v[144:147], v178 offset:4096\n"
"v_mfma_f32_16x16x32_bf16 a[32:35], v[68:71], v[140:143], a[32:35]\n"
"ds_read_b128 v[148:151], v179 offset:4096\n"
"v_mfma_f32_16x16x32_bf16 a[36:39], v[76:79], v[140:143], a[36:39]\n"
"buffer_load_dwordx4 v212, s[24:27], s49 offen offset:0 lds\n"
"v_mfma_f32_16x16x32_bf16 a[36:39], v[72:75], v[136:139], a[36:39]\n"
"v_mfma_f32_16x16x32_bf16 a[40:43], v[80:83], v[136:139], a[40:43]\n"
"v_mfma_f32_16x16x32_bf16 a[40:43], v[84:87], v[140:143], a[40:43]\n"
"buffer_load_dwordx4 v213, s[24:27], s49 offen offset:1024 lds\n"
"v_mfma_f32_16x16x32_bf16 a[44:47], v[92:95], v[140:143], a[44:47]\n"
"v_mfma_f32_16x16x32_bf16 a[44:47], v[88:91], v[136:139], a[44:47]\n"
"v_mfma_f32_16x16x32_bf16 a[48:51], v[96:99], v[136:139], a[48:51]\n"
"buffer_load_dwordx4 v214, s[24:27], s49 offen offset:2048 lds\n"
"v_mfma_f32_16x16x32_bf16 a[48:51], v[100:103], v[140:143], a[48:51]\n"
"v_mfma_f32_16x16x32_bf16 a[52:55], v[108:111], v[140:143], a[52:55]\n"
"v_mfma_f32_16x16x32_bf16 a[52:55], v[104:107], v[136:139], a[52:55]\n"
"buffer_load_dwordx4 v215, s[24:27], s49 offen offset:3072 lds\n"
"v_mfma_f32_16x16x32_bf16 a[56:59], v[112:115], v[136:139], a[56:59]\n"
"v_mfma_f32_16x16x32_bf16 a[56:59], v[116:119], v[140:143], a[56:59]\n"
"v_mfma_f32_16x16x32_bf16 a[60:63], v[124:127], v[140:143], a[60:63]\n"
"v_mfma_f32_16x16x32_bf16 a[60:63], v[120:123], v[136:139], a[60:63]\n"
"s_add_u32 m0, s47, 32768\n"
"s_waitcnt lgkmcnt(1)\n"
"v_mfma_f32_16x16x32_bf16 a[64:67], v[64:67], v[144:147], a[64:67]\n"
"ds_read_b128 v[152:155], v178 offset:6144\n"
"v_readlane_b32 s24, v224, s48\n"
"s_waitcnt lgkmcnt(1)\n"
"v_mfma_f32_16x16x32_bf16 a[64:67], v[68:71], v[148:151], a[64:67]\n"
"ds_read_b128 v[156:159], v179 offset:6144\n"
"v_readlane_b32 s25, v225, s48\n"
"v_mfma_f32_16x16x32_bf16 a[68:71], v[76:79], v[148:151], a[68:71]\n"
"v_readlane_b32 s26, v226, s48\n"
"v_mfma_f32_16x16x32_bf16 a[68:71], v[72:75], v[144:147], a[68:71]\n"
"s_add_u32 s48, s48, 1\n"
"v_mfma_f32_16x16x32_bf16 a[72:75], v[80:83], v[144:147], a[72:75]\n"
"v_mfma_f32_16x16x32_bf16 a[72:75], v[84:87], v[148:151], a[72:75]\n"
"s_nop 0\n"
"buffer_load_dwordx4 v208, s[24:27], s49 offen offset:0 lds\n"
"v_mfma_f32_16x16x32_bf16 a[76:79], v[92:95], v[148:151], a[76:79]\n"
"v_mfma_f32_16x16x32_bf16 a[76:79], v[88:91], v[144:147], a[76:79]\n"
"v_mfma_f32_16x16x32_bf16 a[80:83], v[96:99], v[144:147], a[80:83]\n"
"buffer_load_dwordx4 v209, s[24:27], s49 offen offset:1024 lds\n"
"v_mfma_f32_16x16x32_bf16 a[80:83], v[100:103], v[148:151], a[80:83]\n"
"v_mfma_f32_16x16x32_bf16 a[84:87], v[108:111], v[148:151], a[84:87]\n"
"v_mfma_f32_16x16x32_bf16 a[84:87], v[104:107], v[144:147], a[84:87]\n"
"v_add_u32 v178, 81920, v176\n"
"v_mfma_f32_16x16x32_bf16 a[88:91], v[112:115], v[144:147], a[88:91]\n"
"v_add_u32 v179, 81920, v177\n"
"v_mfma_f32_16x16x32_bf16 a[88:91], v[116:119], v[148:151], a[88:91]\n"
"buffer_load_dwordx4 v210, s[24:27], s49 offen offset:2048 lds\n"
"v_mfma_f32_16x16x32_bf16 a[92:95], v[124:127], v[148:151], a[92:95]\n"
"s_waitcnt lgkmcnt(0)\n"
"v_mfma_f32_16x16x32_bf16 a[92:95], v[120:123], v[144:147], a[92:95]\n"
"s_barrier\n"
"v_mfma_f32_16x16x32_bf16 a[96:99], v[64:67], v[152:155], a[96:99]\n"
"ds_read_b128 v[128:131], v178 offset:0\n"
"v_mfma_f32_16x16x32_bf16 a[96:99], v[68:71], v[156:159], a[96:99]\n"
"ds_read_b128 v[132:135], v179 offset:0\n"
"v_mfma_f32_16x16x32_bf16 a[100:103], v[76:79], v[156:159], a[100:103]\n"
"buffer_load_dwordx4 v211, s[24:27], s49 offen offset:3072 lds\n"
"v_mfma_f32_16x16x32_bf16 a[100:103], v[72:75], v[152:155], a[100:103]\n"
"s_add_u32 m0, m0, 4096\n"
"v_mfma_f32_16x16x32_bf16 a[104:107], v[80:83], v[152:155], a[104:107]\n"
"buffer_load_dwordx4 v212, s[24:27], s49 offen offset:0 lds\n"
"v_mfma_f32_16x16x32_bf16 a[104:107], v[84:87], v[156:159], a[104:107]\n"
"buffer_load_dwordx4 v213, s[24:27], s49 offen offset:1024 lds\n"
"v_mfma_f32_16x16x32_bf16 a[108:111], v[92:95], v[156:159], a[108:111]\n"
"v_mfma_f32_16x16x32_bf16 a[108:111], v[88:91], v[152:155], a[108:111]\n"
"buffer_load_dwordx4 v214, s[24:27], s49 offen offset:2048 lds\n"
"v_mfma_f32_16x16x32_bf16 a[112:115], v[96:99], v[152:155], a[112:115]\n"
"v_mfma_f32_16x16x32_bf16 a[112:115], v[100:103], v[156:159], a[112:115]\n"
"v_mfma_f32_16x16x32_bf16 a[116:119], v[108:111], v[156:159], a[116:119]\n"
"buffer_load_dwordx4 v215, s[24:27], s49 offen offset:3072 lds\n"
"v_mfma_f32_16x16x32_bf16 a[116:119], v[104:107], v[152:155], a[116:119]\n"
"v_mfma_f32_16x16x32_bf16 a[120:123], v[112:115], v[152:155], a[120:123]\n"
"v_mfma_f32_16x16x32_bf16 a[120:123], v[116:119], v[156:159], a[120:123]\n"
"v_mfma_f32_16x16x32_bf16 a[124:127], v[124:127], v[156:159], a[124:127]\n"
"v_mfma_f32_16x16x32_bf16 a[124:127], v[120:123], v[152:155], a[124:127]\n"
"s_waitcnt vmcnt(24)\n"
"s_mov_b32 s55, 0\n"
"L_syk%=_chk:\n"
"v_cmp_ne_u32 vcc, v180, v239\n"
"s_nop 0\n"
"s_cmp_eq_u64 vcc, -1\n"
"s_cbranch_scc1 L_syk%=_ok\n"
"s_sleep 2\n"
"buffer_load_dword v239, v181, s[20:23], 0 offen offset:0 sc1\n"
"s_add_u32 s55, s55, 1\n"
"s_waitcnt vmcnt(0)\n"
"s_cmp_lt_u32 s55, 500000\n"
"s_cbranch_scc1 L_syk%=_chk\n"
"L_syk%=_ok:\n"
"s_cmp_lg_u32 s47, 0\n"
"s_cbranch_scc1 L_b2k%=\n"
"v_readlane_b32 s54, v180, s79\n"
"s_nop 0\n"
"s_add_u32 s54, s54, 2\n"
"v_mov_b32 v240, s54\n"
"s_mov_b64 exec, 1\n"
"buffer_store_dword v240, v231, s[20:23], s78 offen offset:0 sc1\n"
"s_mov_b64 exec, -1\n"
"s_nop 3\n"
"L_b2k%=:\n"
"s_waitcnt lgkmcnt(0)\n"
"v_mfma_f32_16x16x32_bf16 a[128:131], v[0:3], v[128:131], a[128:131]\n"
"s_mov_b32 s81, 8192\n"
"s_mov_b32 s85, s75\n"
"ds_read_b128 v[136:139], v178 offset:2048\n"
"v_mfma_f32_16x16x32_bf16 a[128:131], v[4:7], v[132:135], a[128:131]\n"
"ds_read_b128 v[140:143], v179 offset:2048\n"
"v_mfma_f32_16x16x32_bf16 a[132:135], v[12:15], v[132:135], a[132:135]\n"
"v_accvgpr_read_b32 v168, a0\n"
"v_accvgpr_read_b32 v169, a1\n"
"v_mfma_f32_16x16x32_bf16 a[132:135], v[8:11], v[128:131], a[132:135]\n"
"s_add_u32 s86, s36, 0\n"
"v_accvgpr_read_b32 v170, a2\n"
"v_accvgpr_read_b32 v171, a3\n"
"v_mfma_f32_16x16x32_bf16 a[136:139], v[16:19], v[128:131], a[136:139]\n"
"buffer_load_dwordx4 a[0:3], v191, s[20:23], s86 offen offset:0 sc1\n"
"v_mfma_f32_16x16x32_bf16 a[136:139], v[20:23], v[132:135], a[136:139]\n"
"v_accvgpr_read_b32 v172, a4\n"
"v_accvgpr_read_b32 v173, a5\n"
"v_mfma_f32_16x16x32_bf16 a[140:143], v[28:31], v[132:135], a[140:143]\n"
"v_accvgpr_read_b32 v174, a6\n"
"v_accvgpr_read_b32 v175, a7\n"
"v_mfma_f32_16x16x32_bf16 a[140:143], v[24:27], v[128:131], a[140:143]\n"
"buffer_load_dwordx4 a[4:7], v191, s[20:23], s86 offen offset:1024 sc1\n"
"v_mfma_f32_16x16x32_bf16 a[144:147], v[32:35], v[128:131], a[144:147]\n"
"v_cvt_pk_bf16_f32 v240, v168, v169\n"
"v_cvt_pk_bf16_f32 v241, v170, v171\n"
"v_mfma_f32_16x16x32_bf16 a[144:147], v[36:39], v[132:135], a[144:147]\n"
"v_cvt_pk_bf16_f32 v242, v172, v173\n"
"v_cvt_pk_bf16_f32 v243, v174, v175\n"
"v_mfma_f32_16x16x32_bf16 a[148:151], v[44:47], v[132:135], a[148:151]\n"
"buffer_store_dwordx4 v[240:243], v227, s[20:23], s85 offen offset:0 sc1\n"
"v_mfma_f32_16x16x32_bf16 a[148:151], v[40:43], v[128:131], a[148:151]\n"
"v_mfma_f32_16x16x32_bf16 a[152:155], v[48:51], v[128:131], a[152:155]\n"
"v_mfma_f32_16x16x32_bf16 a[152:155], v[52:55], v[132:135], a[152:155]\n"
"v_mfma_f32_16x16x32_bf16 a[156:159], v[60:63], v[132:135], a[156:159]\n"
"v_mfma_f32_16x16x32_bf16 a[156:159], v[56:59], v[128:131], a[156:159]\n"
"s_waitcnt lgkmcnt(0)\n"
"v_mfma_f32_16x16x32_bf16 a[160:163], v[0:3], v[136:139], a[160:163]\n"
"ds_read_b128 v[144:147], v178 offset:4096\n"
"v_accvgpr_read_b32 v168, a8\n"
"v_mfma_f32_16x16x32_bf16 a[160:163], v[4:7], v[140:143], a[160:163]\n"
"ds_read_b128 v[148:151], v179 offset:4096\n"
"v_accvgpr_read_b32 v169, a9\n"
"v_mfma_f32_16x16x32_bf16 a[164:167], v[12:15], v[140:143], a[164:167]\n"
"v_accvgpr_read_b32 v170, a10\n"
"v_accvgpr_read_b32 v171, a11\n"
"v_mfma_f32_16x16x32_bf16 a[164:167], v[8:11], v[136:139], a[164:167]\n"
"buffer_load_dwordx4 a[8:11], v191, s[20:23], s86 offen offset:2048 sc1\n"
"v_mfma_f32_16x16x32_bf16 a[168:171], v[16:19], v[136:139], a[168:171]\n"
"v_accvgpr_read_b32 v172, a12\n"
"v_accvgpr_read_b32 v173, a13\n"
"v_mfma_f32_16x16x32_bf16 a[168:171], v[20:23], v[140:143], a[168:171]\n"
"v_accvgpr_read_b32 v174, a14\n"
"v_accvgpr_read_b32 v175, a15\n"
"v_mfma_f32_16x16x32_bf16 a[172:175], v[28:31], v[140:143], a[172:175]\n"
"buffer_load_dwordx4 a[12:15], v191, s[20:23], s86 offen offset:3072 sc1\n"
"v_mfma_f32_16x16x32_bf16 a[172:175], v[24:27], v[136:139], a[172:175]\n"
"v_cvt_pk_bf16_f32 v244, v168, v169\n"
"v_cvt_pk_bf16_f32 v245, v170, v171\n"
"v_mfma_f32_16x16x32_bf16 a[176:179], v[32:35], v[136:139], a[176:179]\n"
"v_cvt_pk_bf16_f32 v246, v172, v173\n"
"v_cvt_pk_bf16_f32 v247, v174, v175\n"
"v_mfma_f32_16x16x32_bf16 a[176:179], v[36:39], v[140:143], a[176:179]\n"
"buffer_store_dwordx4 v[244:247], v227, s[20:23], s85 offen offset:1024 sc1\n"
"v_mfma_f32_16x16x32_bf16 a[180:183], v[44:47], v[140:143], a[180:183]\n"
"v_mfma_f32_16x16x32_bf16 a[180:183], v[40:43], v[136:139], a[180:183]\n"
"v_mfma_f32_16x16x32_bf16 a[184:187], v[48:51], v[136:139], a[184:187]\n"
"v_mfma_f32_16x16x32_bf16 a[184:187], v[52:55], v[140:143], a[184:187]\n"
"v_mfma_f32_16x16x32_bf16 a[188:191], v[60:63], v[140:143], a[188:191]\n"
"v_accvgpr_read_b32 v168, a16\n"
"v_accvgpr_read_b32 v169, a17\n"
"v_mfma_f32_16x16x32_bf16 a[188:191], v[56:59], v[136:139], a[188:191]\n"
"s_add_u32 s86, s36, 4096\n"
"v_accvgpr_read_b32 v170, a18\n"
"v_accvgpr_read_b32 v171, a19\n"
"s_waitcnt lgkmcnt(0)\n"
"v_mfma_f32_16x16x32_bf16 a[192:195], v[0:3], v[144:147], a[192:195]\n"
"ds_read_b128 v[152:155], v178 offset:6144\n"
"v_accvgpr_read_b32 v172, a20\n"
"v_mfma_f32_16x16x32_bf16 a[192:195], v[4:7], v[148:151], a[192:195]\n"
"ds_read_b128 v[156:159], v179 offset:6144\n"
"v_accvgpr_read_b32 v173, a21\n"
"v_mfma_f32_16x16x32_bf16 a[196:199], v[12:15], v[148:151], a[196:199]\n"
"buffer_load_dwordx4 a[16:19], v191, s[20:23], s86 offen offset:0 sc1\n"
"v_mfma_f32_16x16x32_bf16 a[196:199], v[8:11], v[144:147], a[196:199]\n"
"v_accvgpr_read_b32 v174, a22\n"
"v_accvgpr_read_b32 v175, a23\n"
"v_mfma_f32_16x16x32_bf16 a[200:203], v[16:19], v[144:147], a[200:203]\n"
"buffer_load_dwordx4 a[20:23], v191, s[20:23], s86 offen offset:1024 sc1\n"
"v_mfma_f32_16x16x32_bf16 a[200:203], v[20:23], v[148:151], a[200:203]\n"
"v_cvt_pk_bf16_f32 v248, v168, v169\n"
"v_cvt_pk_bf16_f32 v249, v170, v171\n"
"v_mfma_f32_16x16x32_bf16 a[204:207], v[28:31], v[148:151], a[204:207]\n"
"v_cvt_pk_bf16_f32 v250, v172, v173\n"
"v_cvt_pk_bf16_f32 v251, v174, v175\n"
"v_mfma_f32_16x16x32_bf16 a[204:207], v[24:27], v[144:147], a[204:207]\n"
"buffer_store_dwordx4 v[248:251], v227, s[20:23], s85 offen offset:2048 sc1\n"
"v_mfma_f32_16x16x32_bf16 a[208:211], v[32:35], v[144:147], a[208:211]\n"
"v_mfma_f32_16x16x32_bf16 a[208:211], v[36:39], v[148:151], a[208:211]\n"
"v_mfma_f32_16x16x32_bf16 a[212:215], v[44:47], v[148:151], a[212:215]\n"
"v_mfma_f32_16x16x32_bf16 a[212:215], v[40:43], v[144:147], a[212:215]\n"
"v_add_u32 v178, 114688, v176\n"
"v_mfma_f32_16x16x32_bf16 a[216:219], v[48:51], v[144:147], a[216:219]\n"
"v_add_u32 v179, 114688, v177\n"
"v_accvgpr_read_b32 v168, a24\n"
"v_mfma_f32_16x16x32_bf16 a[216:219], v[52:55], v[148:151], a[216:219]\n"
"v_accvgpr_read_b32 v169, a25\n"
"v_accvgpr_read_b32 v170, a26\n"
"v_mfma_f32_16x16x32_bf16 a[220:223], v[60:63], v[148:151], a[220:223]\n"
"s_waitcnt lgkmcnt(0)\n"
"v_accvgpr_read_b32 v171, a27\n"
"v_accvgpr_read_b32 v172, a28\n"
"v_mfma_f32_16x16x32_bf16 a[220:223], v[56:59], v[144:147], a[220:223]\n"
"s_barrier\n"
"v_accvgpr_read_b32 v173, a29\n"
"v_accvgpr_read_b32 v174, a30\n"
"v_mfma_f32_16x16x32_bf16 a[224:227], v[0:3], v[152:155], a[224:227]\n"
"ds_read_b128 v[128:131], v178 offset:0\n"
"v_accvgpr_read_b32 v175, a31\n"
"v_mfma_f32_16x16x32_bf16 a[224:227], v[4:7], v[156:159], a[224:227]\n"
"ds_read_b128 v[132:135], v179 offset:0\n"
"buffer_load_dwordx4 a[24:27], v191, s[20:23], s86 offen offset:2048 sc1\n"
"v_mfma_f32_16x16x32_bf16 a[228:231], v[12:15], v[156:159], a[228:231]\n"
"buffer_load_dwordx4 a[28:31], v191, s[20:23], s86 offen offset:3072 sc1\n"
"v_mfma_f32_16x16x32_bf16 a[228:231], v[8:11], v[152:155], a[228:231]\n"
"v_cvt_pk_bf16_f32 v252, v168, v169\n"
"v_cvt_pk_bf16_f32 v253, v170, v171\n"
"v_mfma_f32_16x16x32_bf16 a[232:235], v[16:19], v[152:155], a[232:235]\n"
"v_cvt_pk_bf16_f32 v254, v172, v173\n"
"v_cvt_pk_bf16_f32 v255, v174, v175\n"
"v_mfma_f32_16x16x32_bf16 a[232:235], v[20:23], v[156:159], a[232:235]\n"
"buffer_store_dwordx4 v[252:255], v227, s[20:23], s85 offen offset:3072 sc1\n"
"v_mfma_f32_16x16x32_bf16 a[236:239], v[28:31], v[156:159], a[236:239]\n"
"v_mfma_f32_16x16x32_bf16 a[236:239], v[24:27], v[152:155], a[236:239]\n"
"v_mfma_f32_16x16x32_bf16 a[240:243], v[32:35], v[152:155], a[240:243]\n"
"v_mfma_f32_16x16x32_bf16 a[240:243], v[36:39], v[156:159], a[240:243]\n"
"v_mfma_f32_16x16x32_bf16 a[244:247], v[44:47], v[156:159], a[244:247]\n"
"v_mfma_f32_16x16x32_bf16 a[244:247], v[40:43], v[152:155], a[244:247]\n"
"v_mfma_f32_16x16x32_bf16 a[248:251], v[48:51], v[152:155], a[248:251]\n"
"v_mfma_f32_16x16x32_bf16 a[248:251], v[52:55], v[156:159], a[248:251]\n"
"v_mfma_f32_16x16x32_bf16 a[252:255], v[60:63], v[156:159], a[252:255]\n"
"v_mfma_f32_16x16x32_bf16 a[252:255], v[56:59], v[152:155], a[252:255]\n"
"s_waitcnt lgkmcnt(1)\n"
"v_mfma_f32_16x16x32_bf16 a[128:131], v[64:67], v[128:131], a[128:131]\n"
"ds_read_b128 v[136:139], v178 offset:2048\n"
"s_waitcnt lgkmcnt(1)\n"
"v_mfma_f32_16x16x32_bf16 a[128:131], v[68:71], v[132:135], a[128:131]\n"
"s_add_u32 m0, s47, 65536\n"
"ds_read_b128 v[140:143], v179 offset:2048\n"
"v_mfma_f32_16x16x32_bf16 a[132:135], v[76:79], v[132:135], a[132:135]\n"
"v_accvgpr_read_b32 v168, a32\n"
"v_accvgpr_read_b32 v169, a33\n"
"v_mfma_f32_16x16x32_bf16 a[132:135], v[72:75], v[128:131], a[132:135]\n"
"s_add_u32 s86, s36, 8192\n"
"v_accvgpr_read_b32 v170, a34\n"
"v_accvgpr_read_b32 v171, a35\n"
"v_mfma_f32_16x16x32_bf16 a[136:139], v[80:83], v[128:131], a[136:139]\n"
"buffer_load_dwordx4 a[32:35], v191, s[20:23], s86 offen offset:0 sc1\n"
"v_mfma_f32_16x16x32_bf16 a[136:139], v[84:87], v[132:135], a[136:139]\n"
"s_add_u32 s85, s85, s81\n"
"v_accvgpr_read_b32 v172, a36\n"
"v_accvgpr_read_b32 v173, a37\n"
"v_mfma_f32_16x16x32_bf16 a[140:143], v[92:95], v[132:135], a[140:143]\n"
"v_accvgpr_read_b32 v174, a38\n"
"v_accvgpr_read_b32 v175, a39\n"
"v_mfma_f32_16x16x32_bf16 a[140:143], v[88:91], v[128:131], a[140:143]\n"
"buffer_load_dwordx4 a[36:39], v191, s[20:23], s86 offen offset:1024 sc1\n"
"v_mfma_f32_16x16x32_bf16 a[144:147], v[96:99], v[128:131], a[144:147]\n"
"v_readlane_b32 s24, v224, s48\n"
"v_cvt_pk_bf16_f32 v240, v168, v169\n"
"v_mfma_f32_16x16x32_bf16 a[144:147], v[100:103], v[132:135], a[144:147]\n"
"v_cvt_pk_bf16_f32 v241, v170, v171\n"
"v_cvt_pk_bf16_f32 v242, v172, v173\n"
"v_mfma_f32_16x16x32_bf16 a[148:151], v[108:111], v[132:135], a[148:151]\n"
"v_cvt_pk_bf16_f32 v243, v174, v175\n"
"v_readlane_b32 s25, v225, s48\n"
"v_mfma_f32_16x16x32_bf16 a[148:151], v[104:107], v[128:131], a[148:151]\n"
"buffer_store_dwordx4 v[240:243], v227, s[20:23], s85 offen offset:0 sc1\n"
"v_mfma_f32_16x16x32_bf16 a[152:155], v[112:115], v[128:131], a[152:155]\n"
"v_readlane_b32 s26, v226, s48\n"
"v_mfma_f32_16x16x32_bf16 a[152:155], v[116:119], v[132:135], a[152:155]\n"
"s_nop 3\n"
"buffer_load_dwordx4 v208, s[24:27], s49 offen offset:0 lds\n"
"v_mfma_f32_16x16x32_bf16 a[156:159], v[124:127], v[132:135], a[156:159]\n"
"buffer_load_dwordx4 v209, s[24:27], s49 offen offset:1024 lds\n"
"v_mfma_f32_16x16x32_bf16 a[156:159], v[120:123], v[128:131], a[156:159]\n"
"s_add_u32 s48, s48, 1\n"
"s_waitcnt lgkmcnt(0)\n"
"v_mfma_f32_16x16x32_bf16 a[160:163], v[64:67], v[136:139], a[160:163]\n"
"ds_read_b128 v[144:147], v178 offset:4096\n"
"v_accvgpr_read_b32 v168, a40\n"
"v_mfma_f32_16x16x32_bf16 a[160:163], v[68:71], v[140:143], a[160:163]\n"
"ds_read_b128 v[148:151], v179 offset:4096\n"
"v_accvgpr_read_b32 v169, a41\n"
"v_mfma_f32_16x16x32_bf16 a[164:167], v[76:79], v[140:143], a[164:167]\n"
"v_accvgpr_read_b32 v170, a42\n"
"v_accvgpr_read_b32 v171, a43\n"
"v_mfma_f32_16x16x32_bf16 a[164:167], v[72:75], v[136:139], a[164:167]\n"
"buffer_load_dwordx4 a[40:43], v191, s[20:23], s86 offen offset:2048 sc1\n"
"v_mfma_f32_16x16x32_bf16 a[168:171], v[80:83], v[136:139], a[168:171]\n"
"v_accvgpr_read_b32 v172, a44\n"
"v_accvgpr_read_b32 v173, a45\n"
"v_mfma_f32_16x16x32_bf16 a[168:171], v[84:87], v[140:143], a[168:171]\n"
"v_accvgpr_read_b32 v174, a46\n"
"v_accvgpr_read_b32 v175, a47\n"
"v_mfma_f32_16x16x32_bf16 a[172:175], v[92:95], v[140:143], a[172:175]\n"
"buffer_load_dwordx4 a[44:47], v191, s[20:23], s86 offen offset:3072 sc1\n"
"v_mfma_f32_16x16x32_bf16 a[172:175], v[88:91], v[136:139], a[172:175]\n"
"v_cvt_pk_bf16_f32 v244, v168, v169\n"
"v_cvt_pk_bf16_f32 v245, v170, v171\n"
"v_mfma_f32_16x16x32_bf16 a[176:179], v[96:99], v[136:139], a[176:179]\n"
"v_cvt_pk_bf16_f32 v246, v172, v173\n"
"v_cvt_pk_bf16_f32 v247, v174, v175\n"
"v_mfma_f32_16x16x32_bf16 a[176:179], v[100:103], v[140:143], a[176:179]\n"
"buffer_store_dwordx4 v[244:247], v227, s[20:23], s85 offen offset:1024 sc1\n"
"v_mfma_f32_16x16x32_bf16 a[180:183], v[108:111], v[140:143], a[180:183]\n"
"buffer_load_dwordx4 v210, s[24:27], s49 offen offset:2048 lds\n"
"v_mfma_f32_16x16x32_bf16 a[180:183], v[104:107], v[136:139], a[180:183]\n"
"buffer_load_dwordx4 v211, s[24:27], s49 offen offset:3072 lds\n"
"v_mfma_f32_16x16x32_bf16 a[184:187], v[112:115], v[136:139], a[184:187]\n"
"v_mfma_f32_16x16x32_bf16 a[184:187], v[116:119], v[140:143], a[184:187]\n"
"s_add_u32 m0, m0, 4096\n"
"v_mfma_f32_16x16x32_bf16 a[188:191], v[124:127], v[140:143], a[188:191]\n"
"v_accvgpr_read_b32 v168, a48\n"
"v_accvgpr_read_b32 v169, a49\n"
"v_mfma_f32_16x16x32_bf16 a[188:191], v[120:123], v[136:139], a[188:191]\n"
"s_add_u32 s86, s36, 12288\n"
"v_accvgpr_read_b32 v170, a50\n"
"v_accvgpr_read_b32 v171, a51\n"
"s_waitcnt lgkmcnt(0)\n"
"v_mfma_f32_16x16x32_bf16 a[192:195], v[64:67], v[144:147], a[192:195]\n"
"ds_read_b128 v[152:155], v178 offset:6144\n"
"v_accvgpr_read_b32 v172, a52\n"
"v_mfma_f32_16x16x32_bf16 a[192:195], v[68:71], v[148:151], a[192:195]\n"
"ds_read_b128 v[156:159], v179 offset:6144\n"
"v_accvgpr_read_b32 v173, a53\n"
"v_mfma_f32_16x16x32_bf16 a[196:199], v[76:79], v[148:151], a[196:199]\n"
"buffer_load_dwordx4 a[48:51], v191, s[20:23], s86 offen offset:0 sc1\n"
"v_mfma_f32_16x16x32_bf16 a[196:199], v[72:75], v[144:147], a[196:199]\n"
"v_accvgpr_read_b32 v174, a54\n"
"v_accvgpr_read_b32 v175, a55\n"
"v_mfma_f32_16x16x32_bf16 a[200:203], v[80:83], v[144:147], a[200:203]\n"
"v_add_u32 v160, s50, v192\n"
"v_cvt_pk_bf16_f32 v248, v168, v169\n"
"v_mfma_f32_16x16x32_bf16 a[200:203], v[84:87], v[148:151], a[200:203]\n"
"v_add_u32 v161, s50, v193\n"
"v_cvt_pk_bf16_f32 v249, v170, v171\n"
"v_mfma_f32_16x16x32_bf16 a[204:207], v[92:95], v[148:151], a[204:207]\n"
"v_add_u32 v162, s50, v194\n"
"v_cvt_pk_bf16_f32 v250, v172, v173\n"
"v_mfma_f32_16x16x32_bf16 a[204:207], v[88:91], v[144:147], a[204:207]\n"
"v_add_u32 v163, s50, v195\n"
"v_cvt_pk_bf16_f32 v251, v174, v175\n"
"v_mfma_f32_16x16x32_bf16 a[208:211], v[96:99], v[144:147], a[208:211]\n"
"v_add_u32 v164, s50, v196\n"
"v_mfma_f32_16x16x32_bf16 a[208:211], v[100:103], v[148:151], a[208:211]\n"
"v_add_u32 v165, s50, v197\n"
"v_mfma_f32_16x16x32_bf16 a[212:215], v[108:111], v[148:151], a[212:215]\n"
"v_add_u32 v166, s50, v198\n"
"v_mfma_f32_16x16x32_bf16 a[212:215], v[104:107], v[144:147], a[212:215]\n"
"v_add_u32 v167, s50, v199\n"
"v_mfma_f32_16x16x32_bf16 a[216:219], v[112:115], v[144:147], a[216:219]\n"
"buffer_load_dwordx4 a[52:55], v191, s[20:23], s86 offen offset:1024 sc1\n"
"v_mfma_f32_16x16x32_bf16 a[216:219], v[116:119], v[148:151], a[216:219]\n"
"buffer_store_dwordx4 v[248:251], v227, s[20:23], s85 offen offset:2048 sc1\n"
"v_mfma_f32_16x16x32_bf16 a[220:223], v[124:127], v[148:151], a[220:223]\n"
"s_waitcnt lgkmcnt(0)\n"
"buffer_load_dwordx4 v212, s[24:27], s49 offen offset:0 lds\n"
"v_mfma_f32_16x16x32_bf16 a[220:223], v[120:123], v[144:147], a[220:223]\n"
"s_waitcnt vmcnt(42)\n"
"s_barrier\n"
"v_accvgpr_read_b32 v168, a56\n"
"v_mfma_f32_16x16x32_bf16 a[224:227], v[64:67], v[152:155], a[224:227]\n"
"ds_read_b64_tr_b16 v[0:1], v160 offset:0\n"
"buffer_load_dwordx4 v213, s[24:27], s49 offen offset:1024 lds\n"
"v_mfma_f32_16x16x32_bf16 a[224:227], v[68:71], v[156:159], a[224:227]\n"
"ds_read_b64_tr_b16 v[2:3], v160 offset:8192\n"
"buffer_load_dwordx4 v214, s[24:27], s49 offen offset:2048 lds\n"
"v_mfma_f32_16x16x32_bf16 a[228:231], v[76:79], v[156:159], a[228:231]\n"
"ds_read_b64_tr_b16 v[4:5], v160 offset:16384\n"
"v_accvgpr_read_b32 v169, a57\n"
"v_mfma_f32_16x16x32_bf16 a[228:231], v[72:75], v[152:155], a[228:231]\n"
"ds_read_b64_tr_b16 v[6:7], v160 offset:24576\n"
"v_accvgpr_read_b32 v170, a58\n"
"v_mfma_f32_16x16x32_bf16 a[232:235], v[80:83], v[152:155], a[232:235]\n"
"ds_read_b64_tr_b16 v[8:9], v161 offset:0\n"
"v_accvgpr_read_b32 v171, a59\n"
"v_mfma_f32_16x16x32_bf16 a[232:235], v[84:87], v[156:159], a[232:235]\n"
"ds_read_b64_tr_b16 v[10:11], v161 offset:8192\n"
"buffer_load_dwordx4 a[56:59], v191, s[20:23], s86 offen offset:2048 sc1\n"
"v_mfma_f32_16x16x32_bf16 a[236:239], v[92:95], v[156:159], a[236:239]\n"
"ds_read_b64_tr_b16 v[12:13], v161 offset:16384\n"
"v_accvgpr_read_b32 v172, a60\n"
"v_mfma_f32_16x16x32_bf16 a[236:239], v[88:91], v[152:155], a[236:239]\n"
"ds_read_b64_tr_b16 v[14:15], v161 offset:24576\n"
"v_accvgpr_read_b32 v173, a61\n"
"v_mfma_f32_16x16x32_bf16 a[240:243], v[96:99], v[152:155], a[240:243]\n"
"ds_read_b64_tr_b16 v[16:17], v162 offset:0\n"
"v_accvgpr_read_b32 v174, a62\n"
"v_mfma_f32_16x16x32_bf16 a[240:243], v[100:103], v[156:159], a[240:243]\n"
"ds_read_b64_tr_b16 v[18:19], v162 offset:8192\n"
"v_accvgpr_read_b32 v175, a63\n"
"v_mfma_f32_16x16x32_bf16 a[244:247], v[108:111], v[156:159], a[244:247]\n"
"ds_read_b64_tr_b16 v[20:21], v162 offset:16384\n"
"buffer_load_dwordx4 a[60:63], v191, s[20:23], s86 offen offset:3072 sc1\n"
"v_mfma_f32_16x16x32_bf16 a[244:247], v[104:107], v[152:155], a[244:247]\n"
"ds_read_b64_tr_b16 v[22:23], v162 offset:24576\n"
"v_cvt_pk_bf16_f32 v252, v168, v169\n"
"v_mfma_f32_16x16x32_bf16 a[248:251], v[112:115], v[152:155], a[248:251]\n"
"ds_read_b64_tr_b16 v[24:25], v163 offset:0\n"
"v_cvt_pk_bf16_f32 v253, v170, v171\n"
"v_mfma_f32_16x16x32_bf16 a[248:251], v[116:119], v[156:159], a[248:251]\n"
"ds_read_b64_tr_b16 v[26:27], v163 offset:8192\n"
"v_cvt_pk_bf16_f32 v254, v172, v173\n"
"v_mfma_f32_16x16x32_bf16 a[252:255], v[124:127], v[156:159], a[252:255]\n"
"ds_read_b64_tr_b16 v[28:29], v163 offset:16384\n"
"v_cvt_pk_bf16_f32 v255, v174, v175\n"
"v_mfma_f32_16x16x32_bf16 a[252:255], v[120:123], v[152:155], a[252:255]\n"
"ds_read_b64_tr_b16 v[30:31], v163 offset:24576\n"
"buffer_store_dwordx4 v[252:255], v227, s[20:23], s85 offen offset:3072 sc1\n"
"buffer_load_dwordx4 v215, s[24:27], s49 offen offset:3072 lds\n"
"s_add_u32 m0, s47, 98304\n"
"s_add_u32 s84, s75, 65536\n"
"s_branch L_k2%=\n"
"L_k1v%=:\n"
"s_waitcnt lgkmcnt(1)\n"
"v_mfma_f32_16x16x32_bf16 a[0:3], v[128:131], v[0:3], 0\n"
"s_mov_b32 s35, s36\n"
"ds_read_b128 v[136:139], v178 offset:2048\n"
"s_waitcnt lgkmcnt(1)\n"
"v_mfma_f32_16x16x32_bf16 a[0:3], v[132:135], v[4:7], a[0:3]\n"
"s_add_u32 s85, s72, 128\n"
"ds_read_b128 v[140:143], v179 offset:2048\n"
"v_mfma_f32_16x16x32_bf16 a[16:19], v[132:135], v[12:15], 0\n"
"s_mul_i32 s9, s33, 32768\n"
"ds_read_b64_tr_b16 v[64:65], v168 offset:32768\n"
"v_mfma_f32_16x16x32_bf16 a[16:19], v[128:131], v[8:11], a[16:19]\n"
"s_add_u32 s9, s9, 0x8000\n"
"v_mfma_f32_16x16x32_bf16 a[32:35], v[128:131], v[16:19], 0\n"
"s_add_u32 s82, s74, 131072\n"
"s_add_u32 s9, s9, s77\n"
"ds_read_b64_tr_b16 v[66:67], v168 offset:34816\n"
"v_mfma_f32_16x16x32_bf16 a[32:35], v[132:135], v[20:23], a[32:35]\n"
"s_mul_i32 s48, s7, 2048\n"
"v_mfma_f32_16x16x32_bf16 a[48:51], v[132:135], v[28:31], 0\n"
"s_mov_b32 m0, s82\n"
"s_mul_i32 s49, s79, 128\n"
"ds_read_b64_tr_b16 v[72:73], v169 offset:32768\n"
"v_mfma_f32_16x16x32_bf16 a[48:51], v[128:131], v[24:27], a[48:51]\n"
"s_mul_i32 s50, s79, 4\n"
"v_mfma_f32_16x16x32_bf16 a[64:67], v[128:131], v[32:35], 0\n"
"s_add_u32 s50, s50, s33\n"
"ds_read_b64_tr_b16 v[74:75], v169 offset:34816\n"
"v_mfma_f32_16x16x32_bf16 a[64:67], v[132:135], v[36:39], a[64:67]\n"
"buffer_load_dwordx4 v160, s[64:67], s85 offen offset:0 lds\n"
"v_mfma_f32_16x16x32_bf16 a[80:83], v[132:135], v[44:47], 0\n"
"s_mul_i32 s51, s50, 57344\n"
"s_mul_i32 s92, s7, 512\n"
"ds_read_b64_tr_b16 v[80:81], v170 offset:32768\n"
"v_mfma_f32_16x16x32_bf16 a[80:83], v[128:131], v[40:43], a[80:83]\n"
"buffer_load_dwordx4 v161, s[64:67], s85 offen offset:1024 lds\n"
"v_mfma_f32_16x16x32_bf16 a[96:99], v[128:131], v[48:51], 0\n"
"s_add_u32 s51, s51, s92\n"
"ds_read_b64_tr_b16 v[82:83], v170 offset:34816\n"
"v_mfma_f32_16x16x32_bf16 a[96:99], v[132:135], v[52:55], a[96:99]\n"
"buffer_load_dwordx4 v162, s[64:67], s85 offen offset:2048 lds\n"
"v_mfma_f32_16x16x32_bf16 a[112:115], v[132:135], v[60:63], 0\n"
"s_add_u32 s12, s12, s51\n"
"s_addc_u32 s13, s13, 0\n"
"ds_read_b64_tr_b16 v[88:89], v171 offset:32768\n"
"v_mfma_f32_16x16x32_bf16 a[112:115], v[128:131], v[56:59], a[112:115]\n"
"s_waitcnt lgkmcnt(7)\n"
"s_mul_i32 s51, s50, 25088\n"
"s_mul_i32 s93, s7, 224\n"
"v_mfma_f32_16x16x32_bf16 a[4:7], v[136:139], v[0:3], 0\n"
"s_add_u32 s51, s51, s93\n"
"s_add_u32 s84, s73, 1048576\n"
"ds_read_b128 v[144:147], v178 offset:4096\n"
"v_mfma_f32_16x16x32_bf16 a[4:7], v[140:143], v[4:7], a[4:7]\n"
"ds_read_b128 v[148:151], v179 offset:4096\n"
"ds_read_b64_tr_b16 v[90:91], v171 offset:34816\n"
"v_mfma_f32_16x16x32_bf16 a[20:23], v[140:143], v[12:15], 0\n"
"buffer_load_dwordx4 v163, s[64:67], s85 offen offset:3072 lds\n"
"v_mfma_f32_16x16x32_bf16 a[20:23], v[136:139], v[8:11], a[20:23]\n"
"s_add_u32 s26, s26, s51\n"
"s_addc_u32 s27, s27, 0\n"
"ds_read_b64_tr_b16 v[96:97], v172 offset:32768\n"
"v_mfma_f32_16x16x32_bf16 a[36:39], v[136:139], v[16:19], 0\n"
"s_mul_i32 s51, s50, 3584\n"
"s_add_u32 m0, s74, 0\n"
"s_mul_i32 s94, s7, 229376\n"
"v_mfma_f32_16x16x32_bf16 a[36:39], v[140:143], v[20:23], a[36:39]\n"
"s_add_u32 s51, s51, s94\n"
"ds_read_b64_tr_b16 v[98:99], v172 offset:34816\n"
"v_mfma_f32_16x16x32_bf16 a[52:55], v[140:143], v[28:31], 0\n"
"buffer_load_dwordx4 v164, s[68:71], s84 offen offset:0 lds\n"
"v_mfma_f32_16x16x32_bf16 a[52:55], v[136:139], v[24:27], a[52:55]\n"
"s_add_u32 s24, s24, s51\n"
"s_addc_u32 s25, s25, 0\n"
"ds_read_b64_tr_b16 v[104:105], v173 offset:32768\n"
"v_mfma_f32_16x16x32_bf16 a[68:71], v[136:139], v[32:35], 0\n"
"buffer_load_dwordx4 v165, s[68:71], s84 offen offset:1024 lds\n"
"v_mfma_f32_16x16x32_bf16 a[68:71], v[140:143], v[36:39], a[68:71]\n"
"s_mul_i32 s52, s50, 14\n"
"s_min_u32 s52, s52, 770\n"
"ds_read_b64_tr_b16 v[106:107], v173 offset:34816\n"
"v_mfma_f32_16x16x32_bf16 a[84:87], v[140:143], v[44:47], 0\n"
"buffer_load_dwordx4 v166, s[68:71], s84 offen offset:2048 lds\n"
"v_mfma_f32_16x16x32_bf16 a[84:87], v[136:139], v[40:43], a[84:87]\n"
"s_mul_i32 s51, s52, 128\n"
"s_mul_i32 s95, s7, 100352\n"
"ds_read_b64_tr_b16 v[112:113], v174 offset:32768\n"
"v_mfma_f32_16x16x32_bf16 a[100:103], v[136:139], v[48:51], 0\n"
"buffer_load_dwordx4 v167, s[68:71], s84 offen offset:3072 lds\n"
"v_mfma_f32_16x16x32_bf16 a[100:103], v[140:143], v[52:55], a[100:103]\n"
"s_add_u32 s51, s51, s95\n"
"s_mul_i32 s53, s7, 224\n"
"ds_read_b64_tr_b16 v[114:115], v174 offset:34816\n"
"v_mfma_f32_16x16x32_bf16 a[116:119], v[140:143], v[60:63], 0\n"
"s_add_u32 s14, s14, s51\n"
"s_addc_u32 s15, s15, 0\n"
"s_add_u32 s84, s73, 1310720\n"
"v_mfma_f32_16x16x32_bf16 a[116:119], v[136:139], v[56:59], a[116:119]\n"
"s_waitcnt lgkmcnt(7)\n"
"s_and_b32 s56, s79, 7\n"
"v_bfe_u32 v242, v227, 4, 4\n"
"v_mfma_f32_16x16x32_bf16 a[8:11], v[144:147], v[0:3], 0\n"
"s_add_u32 m0, s74, 16384\n"
"ds_read_b128 v[152:155], v178 offset:6144\n"
"ds_read_b64_tr_b16 v[120:121], v175 offset:32768\n"
"v_mfma_f32_16x16x32_bf16 a[8:11], v[148:151], v[4:7], a[8:11]\n"
"ds_read_b128 v[156:159], v179 offset:6144\n"
"v_lshl_add_u32 v182, v242, 7, s48\n"
"v_mfma_f32_16x16x32_bf16 a[24:27], v[148:151], v[12:15], 0\n"
"ds_read_b64_tr_b16 v[122:123], v175 offset:34816\n"
"v_lshl_add_u32 v240, v242, 11, s49\n"
"v_mfma_f32_16x16x32_bf16 a[24:27], v[144:147], v[8:11], a[24:27]\n"
"buffer_load_dwordx4 v164, s[68:71], s84 offen offset:0 lds\n"
"v_mfma_f32_16x16x32_bf16 a[40:43], v[144:147], v[16:19], 0\n"
"v_and_b32 v241, 256, v227\n"
"v_cmp_eq_u32 vcc, 0, v241\n"
"v_mfma_f32_16x16x32_bf16 a[40:43], v[148:151], v[20:23], a[40:43]\n"
"s_add_u32 s83, s83, 16384\n"
"v_cndmask_b32 v181, v240, v182, vcc\n"
"v_add_u32 v181, 0x2200000, v181\n"
"v_mfma_f32_16x16x32_bf16 a[56:59], v[148:151], v[28:31], 0\n"
"buffer_load_dword v180, v181, s[20:23], 0 offen offset:0 sc1\n"
"v_mfma_f32_16x16x32_bf16 a[56:59], v[144:147], v[24:27], a[56:59]\n"
"s_cmp_lt_u32 s83, 163840\n"
"s_cselect_b32 s83, s83, 65536\n"
"v_add_u32 v182, 0x2210000, v182\n"
"v_mfma_f32_16x16x32_bf16 a[72:75], v[144:147], v[32:35], 0\n"
"buffer_load_dwordx4 v165, s[68:71], s84 offen offset:1024 lds\n"
"v_mfma_f32_16x16x32_bf16 a[72:75], v[148:151], v[36:39], a[72:75]\n"
"v_add_u32 v178, s83, v176\n"
"v_mov_b32 v204, s12\n"
"v_mfma_f32_16x16x32_bf16 a[88:91], v[148:151], v[44:47], 0\n"
"v_add_u32 v179, s83, v177\n"
"v_mov_b32 v205, s13\n"
"v_mfma_f32_16x16x32_bf16 a[88:91], v[144:147], v[40:43], a[88:91]\n"
"buffer_load_dwordx4 v166, s[68:71], s84 offen offset:2048 lds\n"
"v_mfma_f32_16x16x32_bf16 a[104:107], v[144:147], v[48:51], 0\n"
"buffer_load_dwordx4 v167, s[68:71], s84 offen offset:3072 lds\n"
"v_mfma_f32_16x16x32_bf16 a[104:107], v[148:151], v[52:55], a[104:107]\n"
"v_mov_b32 v206, 8192\n"
"v_mov_b32 v207, 0\n"
"v_mfma_f32_16x16x32_bf16 a[120:123], v[148:151], v[60:63], 0\n"
"s_waitcnt lgkmcnt(0)\n"
"v_mov_b32 v241, 384\n"
"v_lshrrev_b32 v240, 6, v227\n"
"v_mfma_f32_16x16x32_bf16 a[120:123], v[144:147], v[56:59], a[120:123]\n"
"s_waitcnt vmcnt(17)\n"
"s_barrier\n"
"v_cmp_eq_u32 vcc, 1, v240\n"
"v_mfma_f32_16x16x32_bf16 a[12:15], v[152:155], v[0:3], 0\n"
"ds_read_b128 v[128:131], v178 offset:0\n"
"v_mov_b32 v242, s26\n"
"v_mfma_f32_16x16x32_bf16 a[12:15], v[156:159], v[4:7], a[12:15]\n"
"s_add_u32 s85, s72, 131200\n"
"ds_read_b128 v[132:135], v179 offset:0\n"
"v_cndmask_b32 v204, v204, v242, vcc\n"
"v_mfma_f32_16x16x32_bf16 a[28:31], v[156:159], v[12:15], 0\n"
"v_mov_b32 v242, s27\n"
"v_cndmask_b32 v205, v205, v242, vcc\n"
"v_mfma_f32_16x16x32_bf16 a[28:31], v[152:155], v[8:11], a[28:31]\n"
"s_add_u32 s82, s74, 147456\n"
"v_mov_b32 v242, 3584\n"
"v_cndmask_b32 v206, v206, v242, vcc\n"
"v_mfma_f32_16x16x32_bf16 a[44:47], v[152:155], v[16:19], 0\n"
"v_mov_b32 v242, 220\n"
"v_cndmask_b32 v241, v241, v242, vcc\n"
"v_mfma_f32_16x16x32_bf16 a[44:47], v[156:159], v[20:23], a[44:47]\n"
"s_mov_b32 m0, s82\n"
"v_cmp_eq_u32 vcc, 2, v240\n"
"v_mov_b32 v242, s24\n"
"v_mfma_f32_16x16x32_bf16 a[60:63], v[156:159], v[28:31], 0\n"
"v_cndmask_b32 v204, v204, v242, vcc\n"
"v_mov_b32 v242, s25\n"
"v_mfma_f32_16x16x32_bf16 a[60:63], v[152:155], v[24:27], a[60:63]\n"
"buffer_load_dwordx4 v160, s[64:67], s85 offen offset:0 lds\n"
"v_mfma_f32_16x16x32_bf16 a[76:79], v[152:155], v[32:35], 0\n"
"v_cndmask_b32 v205, v205, v242, vcc\n"
"v_mov_b32 v242, 512\n"
"v_mfma_f32_16x16x32_bf16 a[76:79], v[156:159], v[36:39], a[76:79]\n"
"buffer_load_dwordx4 v161, s[64:67], s85 offen offset:1024 lds\n"
"v_mfma_f32_16x16x32_bf16 a[92:95], v[156:159], v[44:47], 0\n"
"v_mfma_f32_16x16x32_bf16 a[92:95], v[152:155], v[40:43], a[92:95]\n"
"buffer_load_dwordx4 v162, s[64:67], s85 offen offset:2048 lds\n"
"v_mfma_f32_16x16x32_bf16 a[108:111], v[152:155], v[48:51], 0\n"
"v_mfma_f32_16x16x32_bf16 a[108:111], v[156:159], v[52:55], a[108:111]\n"
"buffer_load_dwordx4 v163, s[64:67], s85 offen offset:3072 lds\n"
"v_mfma_f32_16x16x32_bf16 a[124:127], v[156:159], v[60:63], 0\n"
"v_mfma_f32_16x16x32_bf16 a[124:127], v[152:155], v[56:59], a[124:127]\n"
"s_waitcnt lgkmcnt(0)\n"
"v_mfma_f32_16x16x32_bf16 a[128:131], v[128:131], v[0:3], 0\n"
"ds_read_b128 v[136:139], v178 offset:2048\n"
"v_cndmask_b32 v206, v206, v242, vcc\n"
"v_mfma_f32_16x16x32_bf16 a[128:131], v[132:135], v[4:7], a[128:131]\n"
"s_add_u32 s84, s73, 1572864\n"
"ds_read_b128 v[140:143], v179 offset:2048\n"
"v_mov_b32 v242, 384\n"
"v_mfma_f32_16x16x32_bf16 a[144:147], v[132:135], v[12:15], 0\n"
"ds_read_b64_tr_b16 v[68:69], v168 offset:49152\n"
"v_cndmask_b32 v241, v241, v242, vcc\n"
"v_mfma_f32_16x16x32_bf16 a[144:147], v[128:131], v[8:11], a[144:147]\n"
"s_add_u32 m0, s74, 32768\n"
"v_cmp_eq_u32 vcc, 3, v240\n"
"v_mov_b32 v242, s14\n"
"v_mfma_f32_16x16x32_bf16 a[160:163], v[128:131], v[16:19], 0\n"
"ds_read_b64_tr_b16 v[70:71], v168 offset:51200\n"
"v_cndmask_b32 v204, v204, v242, vcc\n"
"v_mfma_f32_16x16x32_bf16 a[160:163], v[132:135], v[20:23], a[160:163]\n"
"buffer_load_dwordx4 v164, s[68:71], s84 offen offset:0 lds\n"
"v_mfma_f32_16x16x32_bf16 a[176:179], v[132:135], v[28:31], 0\n"
"ds_read_b64_tr_b16 v[76:77], v169 offset:49152\n"
"v_mov_b32 v242, s15\n"
"v_mfma_f32_16x16x32_bf16 a[176:179], v[128:131], v[24:27], a[176:179]\n"
"buffer_load_dwordx4 v165, s[68:71], s84 offen offset:1024 lds\n"
"v_mfma_f32_16x16x32_bf16 a[192:195], v[128:131], v[32:35], 0\n"
"ds_read_b64_tr_b16 v[78:79], v169 offset:51200\n"
"v_cndmask_b32 v205, v205, v242, vcc\n"
"v_mfma_f32_16x16x32_bf16 a[192:195], v[132:135], v[36:39], a[192:195]\n"
"buffer_load_dwordx4 v166, s[68:71], s84 offen offset:2048 lds\n"
"v_mfma_f32_16x16x32_bf16 a[208:211], v[132:135], v[44:47], 0\n"
"ds_read_b64_tr_b16 v[84:85], v170 offset:49152\n"
"v_mov_b32 v242, 256\n"
"v_mfma_f32_16x16x32_bf16 a[208:211], v[128:131], v[40:43], a[208:211]\n"
"buffer_load_dwordx4 v167, s[68:71], s84 offen offset:3072 lds\n"
"v_mfma_f32_16x16x32_bf16 a[224:227], v[128:131], v[48:51], 0\n"
"ds_read_b64_tr_b16 v[86:87], v170 offset:51200\n"
"v_cndmask_b32 v206, v206, v242, vcc\n"
"v_mfma_f32_16x16x32_bf16 a[224:227], v[132:135], v[52:55], a[224:227]\n"
"s_add_u32 s85, s72, 256\n"
"v_mov_b32 v242, 128\n"
"v_cndmask_b32 v241, v241, v242, vcc\n"
"v_mfma_f32_16x16x32_bf16 a[240:243], v[132:135], v[60:63], 0\n"
"ds_read_b64_tr_b16 v[92:93], v171 offset:49152\n"
"v_cmp_eq_u32 vcc, 4, v240\n"
"v_mfma_f32_16x16x32_bf16 a[240:243], v[128:131], v[56:59], a[240:243]\n"
"s_waitcnt lgkmcnt(7)\n"
"v_mov_b32 v242, s96\n"
"v_cndmask_b32 v204, v204, v242, vcc\n"
"v_mfma_f32_16x16x32_bf16 a[132:135], v[136:139], v[0:3], 0\n"
"s_mul_i32 s36, s35, 114688\n"
"ds_read_b128 v[144:147], v178 offset:4096\n"
"v_mov_b32 v242, s97\n"
"v_mfma_f32_16x16x32_bf16 a[132:135], v[140:143], v[4:7], a[132:135]\n"
"s_add_u32 s82, s74, 65536\n"
"ds_read_b128 v[148:151], v179 offset:4096\n"
"ds_read_b64_tr_b16 v[94:95], v171 offset:51200\n"
"v_mfma_f32_16x16x32_bf16 a[148:151], v[140:143], v[12:15], 0\n"
"s_mul_i32 s92, s33, 28672\n"
"v_cndmask_b32 v205, v205, v242, vcc\n"
"v_mov_b32 v242, 128\n"
"v_mfma_f32_16x16x32_bf16 a[148:151], v[136:139], v[8:11], a[148:151]\n"
"s_add_u32 s36, s36, s92\n"
"ds_read_b64_tr_b16 v[100:101], v172 offset:49152\n"
"v_cndmask_b32 v206, v206, v242, vcc\n"
"v_mfma_f32_16x16x32_bf16 a[164:167], v[136:139], v[16:19], 0\n"
"s_add_u32 s36, s36, 0x2000000\n"
"v_mov_b32 v242, 0\n"
"v_cndmask_b32 v241, v241, v242, vcc\n"
"v_mfma_f32_16x16x32_bf16 a[164:167], v[140:143], v[20:23], a[164:167]\n"
"s_mov_b32 m0, s82\n"
"ds_read_b64_tr_b16 v[102:103], v172 offset:51200\n"
"v_cmp_eq_u32 vcc, 5, v240\n"
"v_mfma_f32_16x16x32_bf16 a[180:183], v[140:143], v[28:31], 0\n"
"s_mul_i32 s51, s7, 8\n"
"v_mov_b32 v242, s28\n"
"v_cndmask_b32 v204, v204, v242, vcc\n"
"v_mfma_f32_16x16x32_bf16 a[180:183], v[136:139], v[24:27], a[180:183]\n"
"s_add_u32 s51, s51, s56\n"
"ds_read_b64_tr_b16 v[108:109], v173 offset:49152\n"
"v_mov_b32 v242, s29\n"
"v_mfma_f32_16x16x32_bf16 a[196:199], v[136:139], v[32:35], 0\n"
"s_mul_i32 s11, s51, 65536\n"
"v_cndmask_b32 v205, v205, v242, vcc\n"
"v_mov_b32 v242, 0\n"
"v_mfma_f32_16x16x32_bf16 a[196:199], v[140:143], v[36:39], a[196:199]\n"
"s_mul_i32 s93, s33, 16384\n"
"ds_read_b64_tr_b16 v[110:111], v173 offset:51200\n"
"v_cndmask_b32 v206, v206, v242, vcc\n"
"v_mfma_f32_16x16x32_bf16 a[212:215], v[140:143], v[44:47], 0\n"
"s_add_u32 s11, s11, s93\n"
"v_mov_b32 v242, 220\n"
"v_cndmask_b32 v241, v241, v242, vcc\n"
"v_mfma_f32_16x16x32_bf16 a[212:215], v[136:139], v[40:43], a[212:215]\n"
"s_add_u32 s11, s11, 0x2400000\n"
"ds_read_b64_tr_b16 v[116:117], v174 offset:49152\n"
"v_mov_b32 v242, s53\n"
"v_mfma_f32_16x16x32_bf16 a[228:231], v[136:139], v[48:51], 0\n"
"s_or_b32 s11, s11, s43\n"
"v_cndmask_b32 v242, 0, v242, vcc\n"
"v_and_b32 v240, 48, v227\n"
"v_mfma_f32_16x16x32_bf16 a[228:231], v[140:143], v[52:55], a[228:231]\n"
"s_mul_i32 s43, s51, 65536\n"
"ds_read_b64_tr_b16 v[118:119], v174 offset:51200\n"
"v_lshlrev_b32 v240, 3, v240\n"
"v_mfma_f32_16x16x32_bf16 a[244:247], v[140:143], v[60:63], 0\n"
"s_mul_i32 s94, s33, 16384\n"
"v_min_u32 v241, v241, v240\n"
"v_add_u32 v241, v241, v242\n"
"v_mfma_f32_16x16x32_bf16 a[244:247], v[136:139], v[56:59], a[244:247]\n"
"s_waitcnt lgkmcnt(7)\n"
"v_add_co_u32 v204, vcc, v204, v241\n"
"s_nop 1\n"
"v_addc_co_u32 v205, vcc, 0, v205, vcc\n"
"v_mfma_f32_16x16x32_bf16 a[136:139], v[144:147], v[0:3], 0\n"
"s_add_u32 s43, s43, s94\n"
"ds_read_b128 v[152:155], v178 offset:6144\n"
"ds_read_b64_tr_b16 v[124:125], v175 offset:49152\n"
"v_mfma_f32_16x16x32_bf16 a[136:139], v[148:151], v[4:7], a[136:139]\n"
"s_mul_i32 s44, s51, 4096\n"
"ds_read_b128 v[156:159], v179 offset:6144\n"
"v_mov_b32 v248, 0\n"
"v_mfma_f32_16x16x32_bf16 a[152:155], v[148:151], v[12:15], 0\n"
"s_mul_i32 s95, s33, 1024\n"
"ds_read_b64_tr_b16 v[126:127], v175 offset:51200\n"
"v_mov_b32 v249, 0\n"
"v_mfma_f32_16x16x32_bf16 a[152:155], v[144:147], v[8:11], a[152:155]\n"
"s_add_u32 s44, s44, s95\n"
"v_mov_b32 v250, 0\n"
"v_mov_b32 v251, 0\n"
"v_mfma_f32_16x16x32_bf16 a[168:171], v[144:147], v[16:19], 0\n"
"buffer_store_dwordx4 v[248:251], v231, s[20:23], s77 offen offset:0 sc1\n"
"v_mfma_f32_16x16x32_bf16 a[168:171], v[148:151], v[20:23], a[168:171]\n"
"s_add_u32 s83, s83, 16384\n"
"buffer_store_dwordx4 v[248:251], v231, s[20:23], s9 offen offset:0 sc1\n"
"v_mfma_f32_16x16x32_bf16 a[184:187], v[148:151], v[28:31], 0\n"
"buffer_load_dwordx4 v160, s[64:67], s85 offen offset:0 lds\n"
"v_mfma_f32_16x16x32_bf16 a[184:187], v[144:147], v[24:27], a[184:187]\n"
"s_cmp_lt_u32 s83, 163840\n"
"s_cselect_b32 s83, s83, 65536\n"
"s_add_u32 s44, s44, 0x2300000\n"
"v_mfma_f32_16x16x32_bf16 a[200:203], v[144:147], v[32:35], 0\n"
"buffer_load_dwordx4 v161, s[64:67], s85 offen offset:1024 lds\n"
"v_mfma_f32_16x16x32_bf16 a[200:203], v[148:151], v[36:39], a[200:203]\n"
"s_mul_i32 s45, s76, -4\n"
"s_add_u32 s45, s45, 0x8\n"
"v_add_u32 v178, s83, v176\n"
"v_mfma_f32_16x16x32_bf16 a[216:219], v[148:151], v[44:47], 0\n"
"s_xor_b32 s52, s77, 1024\n"
"s_add_u32 s45, s45, s52\n"
"v_add_u32 v179, s83, v177\n"
"v_mfma_f32_16x16x32_bf16 a[216:219], v[144:147], v[40:43], a[216:219]\n"
"buffer_load_dwordx4 v162, s[64:67], s85 offen offset:2048 lds\n"
"v_mfma_f32_16x16x32_bf16 a[232:235], v[144:147], v[48:51], 0\n"
"buffer_load_dwordx4 v163, s[64:67], s85 offen offset:3072 lds\n"
"v_mfma_f32_16x16x32_bf16 a[232:235], v[148:151], v[52:55], a[232:235]\n"
"s_add_u32 s85, s72, 131328\n"
"s_mul_i32 s46, s7, 1835008\n"
"s_mul_i32 s92, s35, 114688\n"
"v_mfma_f32_16x16x32_bf16 a[248:251], v[148:151], v[60:63], 0\n"
"s_waitcnt lgkmcnt(0)\n"
"s_add_u32 s82, s74, 81920\n"
"s_add_u32 s46, s46, s92\n"
"v_mfma_f32_16x16x32_bf16 a[248:251], v[144:147], v[56:59], a[248:251]\n"
"s_waitcnt vmcnt(19)\n"
"s_barrier\n"
"s_mul_i32 s93, s33, 28672\n"
"v_mfma_f32_16x16x32_bf16 a[140:143], v[152:155], v[0:3], 0\n"
"s_add_u32 s46, s46, s93\n"
"s_mov_b32 m0, s82\n"
"ds_read_b128 v[128:131], v178 offset:0\n"
"v_mfma_f32_16x16x32_bf16 a[140:143], v[156:159], v[4:7], a[140:143]\n"
"ds_read_b128 v[132:135], v179 offset:0\n"
"buffer_load_dwordx4 v160, s[64:67], s85 offen offset:0 lds\n"
"v_mfma_f32_16x16x32_bf16 a[156:159], v[156:159], v[12:15], 0\n"
"buffer_load_dwordx4 v161, s[64:67], s85 offen offset:1024 lds\n"
"v_mfma_f32_16x16x32_bf16 a[156:159], v[152:155], v[8:11], a[156:159]\n"
"buffer_load_dwordx4 v162, s[64:67], s85 offen offset:2048 lds\n"
"v_mfma_f32_16x16x32_bf16 a[172:175], v[152:155], v[16:19], 0\n"
"buffer_load_dwordx4 v163, s[64:67], s85 offen offset:3072 lds\n"
"v_mfma_f32_16x16x32_bf16 a[172:175], v[156:159], v[20:23], a[172:175]\n"
"s_add_u32 s46, s46, 0x3400000\n"
"s_mul_i32 s47, s33, 8192\n"
"s_mul_i32 s6, s33, 224\n"
"v_mfma_f32_16x16x32_bf16 a[188:191], v[156:159], v[28:31], 0\n"
"s_mul_i32 s4, s35, 114688\n"
"s_mul_i32 s94, s7, 3584\n"
"s_add_u32 s4, s4, s94\n"
"v_mfma_f32_16x16x32_bf16 a[188:191], v[152:155], v[24:27], a[188:191]\n"
"s_add_u32 s4, s4, s6\n"
"s_add_u32 s5, s4, 57344\n"
"s_mul_i32 s8, s35, 114688\n"
"v_mfma_f32_16x16x32_bf16 a[204:207], v[152:155], v[32:35], 0\n"
"s_mul_i32 s95, s7, 7168\n"
"s_add_u32 s8, s8, s95\n"
"v_lshrrev_b32 v240, 7, v227\n"
"v_mfma_f32_16x16x32_bf16 a[204:207], v[156:159], v[36:39], a[204:207]\n"
"s_mul_i32 s92, s33, 1792\n"
"v_and_b32 v241, 112, v227\n"
"v_lshl_add_u32 v203, v240, 11, v241\n"
"v_mfma_f32_16x16x32_bf16 a[220:223], v[156:159], v[44:47], 0\n"
"s_add_u32 s8, s8, s92\n"
"v_cmp_eq_u32 vcc, 0, v252\n"
"s_nop 1\n"
"v_cndmask_b32 v183, 0, 1.0, vcc\n"
"v_mfma_f32_16x16x32_bf16 a[220:223], v[152:155], v[40:43], a[220:223]\n"
"s_add_u32 s8, s8, 0x3400000\n"
"v_lshrrev_b32 v240, 8, v227\n"
"v_lshl_add_u32 v200, v240, 3, s53\n"
"v_mfma_f32_16x16x32_bf16 a[236:239], v[152:155], v[48:51], 0\n"
"s_lshr_b32 s52, s7, 2\n"
"s_mul_i32 s10, s52, 32768\n"
"v_add_u32 v201, s47, v227\n"
"v_mfma_f32_16x16x32_bf16 a[236:239], v[156:159], v[52:55], a[236:239]\n"
"s_mul_i32 s93, s79, 128\n"
"s_add_u32 s10, s10, s93\n"
"s_add_u32 s10, s10, 0x2218000\n"
"v_mfma_f32_16x16x32_bf16 a[252:255], v[156:159], v[60:63], 0\n"
"s_mov_b64 s[18:19], s[16:17]\n"
"s_mul_i32 s52, s33, 9216\n"
"s_add_u32 s52, s52, 0x1000\n"
"v_mfma_f32_16x16x32_bf16 a[252:255], v[152:155], v[56:59], a[252:255]\n"
"s_waitcnt lgkmcnt(0)\n"
"s_add_u32 s16, s16, s52\n"
"s_addc_u32 s17, s17, 0\n"
"s_mul_i32 s52, s79, 1024\n"
"s_add_u32 s52, s52, 0x9000\n"
"s_add_u32 s18, s18, s52\n"
"s_addc_u32 s19, s19, 0\n"
"s_bfe_u32 s50, s7, 131073\n"
"s_lshr_b32 s52, s7, 3\n"
"s_and_b32 s57, s7, 1\n"
"s_add_u32 s84, s73, 1835008\n"
"s_mul_i32 s58, s35, 114688\n"
"s_mul_i32 s94, s50, 28672\n"
"s_add_u32 s58, s58, s94\n"
"s_add_u32 m0, s74, 49152\n"
"s_mul_i32 s95, s52, 1024\n"
"s_add_u32 s58, s58, s95\n"
"s_mul_i32 s92, s57, 512\n"
"buffer_load_dwordx4 v164, s[68:71], s84 offen offset:0 lds\n"
"s_add_u32 s58, s58, s92\n"
"s_mul_i32 s93, s33, 128\n"
"s_add_u32 s58, s58, s93\n"
"s_add_u32 s58, s58, 0x2000000\n"
"buffer_load_dwordx4 v165, s[68:71], s84 offen offset:1024 lds\n"
"v_add_u32 v203, s58, v203\n"
"buffer_load_dwordx4 v166, s[68:71], s84 offen offset:2048 lds\n"
"buffer_load_dwordx4 v167, s[68:71], s84 offen offset:3072 lds\n"
"global_load_dword v255, v[204:205], off\n"
"v_lshl_add_u64 v[204:205], v[206:207], 0, v[204:205]\n"
"v_mfma_f32_16x16x32_bf16 a[0:3], v[128:131], v[64:67], a[0:3]\n"
"s_mov_b32 s27, 0x20000\n"
"ds_read_b128 v[136:139], v178 offset:2048\n"
"v_and_b32 v247, 0xffff0000, v239\n"
"v_mfma_f32_16x16x32_bf16 a[0:3], v[132:135], v[68:71], a[0:3]\n"
"s_add_u32 s85, s72, 384\n"
"ds_read_b128 v[140:143], v179 offset:2048\n"
"v_lshlrev_b32 v246, 16, v239\n"
"v_mfma_f32_16x16x32_bf16 a[16:19], v[132:135], v[76:79], a[16:19]\n"
"s_mov_b32 s48, 0\n"
"ds_read_b64_tr_b16 v[0:1], v168 offset:0\n"
"v_and_b32 v245, 0xffff0000, v238\n"
"v_mfma_f32_16x16x32_bf16 a[16:19], v[128:131], v[72:75], a[16:19]\n"
"v_lshlrev_b32 v244, 16, v238\n"
"v_mfma_f32_16x16x32_bf16 a[32:35], v[128:131], v[80:83], a[32:35]\n"
"s_add_u32 s82, s74, 98304\n"
"ds_read_b64_tr_b16 v[2:3], v168 offset:2048\n"
"v_and_b32 v243, 0xffff0000, v237\n"
"v_mfma_f32_16x16x32_bf16 a[32:35], v[132:135], v[84:87], a[32:35]\n"
"s_mov_b32 s49, 0\n"
"v_lshlrev_b32 v242, 16, v237\n"
"v_and_b32 v241, 0xffff0000, v236\n"
"v_mfma_f32_16x16x32_bf16 a[48:51], v[132:135], v[92:95], a[48:51]\n"
"s_mov_b32 s50, 131072\n"
"ds_read_b64_tr_b16 v[8:9], v169 offset:0\n"
"v_lshlrev_b32 v240, 16, v236\n"
"v_mfma_f32_16x16x32_bf16 a[48:51], v[128:131], v[88:91], a[48:51]\n"
"s_mov_b32 m0, s82\n"
"v_and_b32 v239, 0xffff0000, v235\n"
"v_lshlrev_b32 v238, 16, v235\n"
"v_mfma_f32_16x16x32_bf16 a[64:67], v[128:131], v[96:99], a[64:67]\n"
"s_mov_b32 s56, 0x47800000\n"
"ds_read_b64_tr_b16 v[10:11], v169 offset:2048\n"
"v_and_b32 v237, 0xffff0000, v234\n"
"v_mfma_f32_16x16x32_bf16 a[64:67], v[132:135], v[100:103], a[64:67]\n"
"v_lshlrev_b32 v236, 16, v234\n"
"v_mfma_f32_16x16x32_bf16 a[80:83], v[132:135], v[108:111], a[80:83]\n"
"s_mov_b32 s57, 0x3db8aa3b\n"
"ds_read_b64_tr_b16 v[16:17], v170 offset:0\n"
"v_and_b32 v235, 0xffff0000, v233\n"
"v_mfma_f32_16x16x32_bf16 a[80:83], v[128:131], v[104:107], a[80:83]\n"
"buffer_load_dwordx4 v160, s[64:67], s85 offen offset:0 lds\n"
"v_mfma_f32_16x16x32_bf16 a[96:99], v[128:131], v[112:115], a[96:99]\n"
"s_mov_b32 s53, 0\n"
"ds_read_b64_tr_b16 v[18:19], v170 offset:2048\n"
"v_lshlrev_b32 v234, 16, v233\n"
"v_mfma_f32_16x16x32_bf16 a[96:99], v[132:135], v[116:119], a[96:99]\n"
"buffer_load_dwordx4 v161, s[64:67], s85 offen offset:1024 lds\n"
"v_mfma_f32_16x16x32_bf16 a[112:115], v[132:135], v[124:127], a[112:115]\n"
"ds_read_b64_tr_b16 v[24:25], v171 offset:0\n"
"v_and_b32 v233, 0xffff0000, v232\n"
"v_mfma_f32_16x16x32_bf16 a[112:115], v[128:131], v[120:123], a[112:115]\n"
"s_waitcnt lgkmcnt(7)\n"
"v_lshlrev_b32 v232, 16, v232\n"
"v_add_f32 v248, v232, v233\n"
"v_mfma_f32_16x16x32_bf16 a[4:7], v[136:139], v[64:67], a[4:7]\n"
"ds_read_b128 v[144:147], v178 offset:4096\n"
"v_add_f32 v249, v234, v235\n"
"v_mfma_f32_16x16x32_bf16 a[4:7], v[140:143], v[68:71], a[4:7]\n"
"ds_read_b128 v[148:151], v179 offset:4096\n"
"ds_read_b64_tr_b16 v[26:27], v171 offset:2048\n"
"v_mfma_f32_16x16x32_bf16 a[20:23], v[140:143], v[76:79], a[20:23]\n"
"buffer_load_dwordx4 v162, s[64:67], s85 offen offset:2048 lds\n"
"v_mfma_f32_16x16x32_bf16 a[20:23], v[136:139], v[72:75], a[20:23]\n"
"ds_read_b64_tr_b16 v[32:33], v172 offset:0\n"
"v_add_f32 v248, v248, v236\n"
"v_mfma_f32_16x16x32_bf16 a[36:39], v[136:139], v[80:83], a[36:39]\n"
"buffer_load_dwordx4 v163, s[64:67], s85 offen offset:3072 lds\n"
"v_mfma_f32_16x16x32_bf16 a[36:39], v[140:143], v[84:87], a[36:39]\n"
"s_add_u32 s85, s72, 131456\n"
"ds_read_b64_tr_b16 v[34:35], v172 offset:2048\n"
"v_add_f32 v249, v249, v237\n"
"v_mfma_f32_16x16x32_bf16 a[52:55], v[140:143], v[92:95], a[52:55]\n"
"v_add_f32 v248, v248, v238\n"
"v_add_f32 v249, v249, v239\n"
"v_mfma_f32_16x16x32_bf16 a[52:55], v[136:139], v[88:91], a[52:55]\n"
"ds_read_b64_tr_b16 v[40:41], v173 offset:0\n"
"v_add_f32 v248, v248, v240\n"
"v_mfma_f32_16x16x32_bf16 a[68:71], v[136:139], v[96:99], a[68:71]\n"
"s_add_u32 s82, s74, 114688\n"
"v_add_f32 v249, v249, v241\n"
"v_add_f32 v248, v248, v242\n"
"v_mfma_f32_16x16x32_bf16 a[68:71], v[140:143], v[100:103], a[68:71]\n"
"ds_read_b64_tr_b16 v[42:43], v173 offset:2048\n"
"v_add_f32 v249, v249, v243\n"
"v_mfma_f32_16x16x32_bf16 a[84:87], v[140:143], v[108:111], a[84:87]\n"
"v_add_f32 v248, v248, v244\n"
"v_add_f32 v249, v249, v245\n"
"v_mfma_f32_16x16x32_bf16 a[84:87], v[136:139], v[104:107], a[84:87]\n"
"s_mov_b32 m0, s82\n"
"ds_read_b64_tr_b16 v[48:49], v174 offset:0\n"
"v_add_f32 v248, v248, v246\n"
"v_mfma_f32_16x16x32_bf16 a[100:103], v[136:139], v[112:115], a[100:103]\n"
"v_add_f32 v249, v249, v247\n"
"v_add_f32 v248, v248, v249\n"
"v_mfma_f32_16x16x32_bf16 a[100:103], v[140:143], v[116:119], a[100:103]\n"
"ds_read_b64_tr_b16 v[50:51], v174 offset:2048\n"
"v_add_f32_dpp v248, v248, v248 quad_perm:[1,0,3,2] row_mask:0xf bank_mask:0xf bound_ctrl:1\n"
"v_mfma_f32_16x16x32_bf16 a[116:119], v[140:143], v[124:127], a[116:119]\n"
"buffer_load_dwordx4 v160, s[64:67], s85 offen offset:0 lds\n"
"v_mfma_f32_16x16x32_bf16 a[116:119], v[136:139], v[120:123], a[116:119]\n"
"s_waitcnt lgkmcnt(7)\n"
"v_add_f32_dpp v248, v248, v248 quad_perm:[2,3,0,1] row_mask:0xf bank_mask:0xf bound_ctrl:1\n"
"s_nop 1\n"
"v_add_f32_dpp v248, v248, v248 row_ror:8 row_mask:0xf bank_mask:0xf bound_ctrl:1\n"
"v_mfma_f32_16x16x32_bf16 a[8:11], v[144:147], v[64:67], a[8:11]\n"
"ds_read_b128 v[152:155], v178 offset:6144\n"
"ds_read_b64_tr_b16 v[56:57], v175 offset:0\n"
"v_mfma_f32_16x16x32_bf16 a[8:11], v[148:151], v[68:71], a[8:11]\n"
"ds_read_b128 v[156:159], v179 offset:6144\n"
"v_mov_b32 v250, v248\n"
"v_mfma_f32_16x16x32_bf16 a[24:27], v[148:151], v[76:79], a[24:27]\n"
"ds_read_b64_tr_b16 v[58:59], v175 offset:2048\n"
"v_permlane32_swap_b32 v250, v248\n"
"v_mfma_f32_16x16x32_bf16 a[24:27], v[144:147], v[72:75], a[24:27]\n"
"buffer_load_dwordx4 v161, s[64:67], s85 offen offset:1024 lds\n"
"v_mfma_f32_16x16x32_bf16 a[40:43], v[144:147], v[80:83], a[40:43]\n"
"buffer_load_dwordx4 v162, s[64:67], s85 offen offset:2048 lds\n"
"v_mfma_f32_16x16x32_bf16 a[40:43], v[148:151], v[84:87], a[40:43]\n"
"s_add_u32 s83, s83, 16384\n"
"v_add_f32 v248, v248, v250\n"
"v_mov_b32 v250, v248\n"
"v_mfma_f32_16x16x32_bf16 a[56:59], v[148:151], v[92:95], a[56:59]\n"
"s_nop 0\n"
"v_permlane16_swap_b32 v250, v248\n"
"v_add_f32 v248, v248, v250\n"
"v_mfma_f32_16x16x32_bf16 a[56:59], v[144:147], v[88:91], a[56:59]\n"
"s_cmp_lt_u32 s83, 163840\n"
"s_cselect_b32 s83, s83, 65536\n"
"v_mul_f32 v248, 0x3b124925, v248\n"
"v_mfma_f32_16x16x32_bf16 a[72:75], v[144:147], v[96:99], a[72:75]\n"
"buffer_load_dwordx4 v163, s[64:67], s85 offen offset:3072 lds\n"
"v_mfma_f32_16x16x32_bf16 a[72:75], v[148:151], v[100:103], a[72:75]\n"
"v_add_u32 v178, s83, v176\n"
"v_sub_f32 v250, v232, v248\n"
"v_mfma_f32_16x16x32_bf16 a[88:91], v[148:151], v[108:111], a[88:91]\n"
"v_add_u32 v179, s83, v177\n"
"v_mul_f32 v249, v250, v250\n"
"v_mfma_f32_16x16x32_bf16 a[88:91], v[144:147], v[104:107], a[88:91]\n"
"v_sub_f32 v250, v233, v248\n"
"v_fmac_f32 v249, v250, v250\n"
"v_mfma_f32_16x16x32_bf16 a[104:107], v[144:147], v[112:115], a[104:107]\n"
"v_sub_f32 v250, v234, v248\n"
"v_fmac_f32 v249, v250, v250\n"
"v_mfma_f32_16x16x32_bf16 a[104:107], v[148:151], v[116:119], a[104:107]\n"
"v_sub_f32 v250, v235, v248\n"
"v_fmac_f32 v249, v250, v250\n"
"v_mfma_f32_16x16x32_bf16 a[120:123], v[148:151], v[124:127], a[120:123]\n"
"s_waitcnt lgkmcnt(0)\n"
"v_sub_f32 v250, v236, v248\n"
"v_fmac_f32 v249, v250, v250\n"
"v_mfma_f32_16x16x32_bf16 a[120:123], v[144:147], v[120:123], a[120:123]\n"
"s_waitcnt vmcnt(27)\n"
"s_barrier\n"
"v_sub_f32 v250, v237, v248\n"
"v_mfma_f32_16x16x32_bf16 a[12:15], v[152:155], v[64:67], a[12:15]\n"
"ds_read_b128 v[128:131], v178 offset:0\n"
"v_fmac_f32 v249, v250, v250\n"
"v_mfma_f32_16x16x32_bf16 a[12:15], v[156:159], v[68:71], a[12:15]\n"
"s_add_u32 s84, s73, 2097152\n"
"ds_read_b128 v[132:135], v179 offset:0\n"
"v_sub_f32 v250, v238, v248\n"
"v_mfma_f32_16x16x32_bf16 a[28:31], v[156:159], v[76:79], a[28:31]\n"
"v_fmac_f32 v249, v250, v250\n"
"v_sub_f32 v250, v239, v248\n"
"v_mfma_f32_16x16x32_bf16 a[28:31], v[152:155], v[72:75], a[28:31]\n"
"s_add_u32 m0, s74, 0\n"
"v_fmac_f32 v249, v250, v250\n"
"v_sub_f32 v250, v240, v248\n"
"v_mfma_f32_16x16x32_bf16 a[44:47], v[152:155], v[80:83], a[44:47]\n"
"v_mul_f32 v251, v250, v250\n"
"v_sub_f32 v250, v241, v248\n"
"v_mfma_f32_16x16x32_bf16 a[44:47], v[156:159], v[84:87], a[44:47]\n"
"buffer_load_dwordx4 v164, s[68:71], s84 offen offset:0 lds\n"
"v_mfma_f32_16x16x32_bf16 a[60:63], v[156:159], v[92:95], a[60:63]\n"
"v_fmac_f32 v251, v250, v250\n"
"v_sub_f32 v250, v242, v248\n"
"v_mfma_f32_16x16x32_bf16 a[60:63], v[152:155], v[88:91], a[60:63]\n"
"buffer_load_dwordx4 v165, s[68:71], s84 offen offset:1024 lds\n"
"v_mfma_f32_16x16x32_bf16 a[76:79], v[152:155], v[96:99], a[76:79]\n"
"v_fmac_f32 v251, v250, v250\n"
"v_sub_f32 v250, v243, v248\n"
"v_mfma_f32_16x16x32_bf16 a[76:79], v[156:159], v[100:103], a[76:79]\n"
"buffer_load_dwordx4 v166, s[68:71], s84 offen offset:2048 lds\n"
"v_mfma_f32_16x16x32_bf16 a[92:95], v[156:159], v[108:111], a[92:95]\n"
"v_fmac_f32 v251, v250, v250\n"
"v_sub_f32 v250, v244, v248\n"
"v_mfma_f32_16x16x32_bf16 a[92:95], v[152:155], v[104:107], a[92:95]\n"
"buffer_load_dwordx4 v167, s[68:71], s84 offen offset:3072 lds\n"
"v_mfma_f32_16x16x32_bf16 a[108:111], v[152:155], v[112:115], a[108:111]\n"
"v_fmac_f32 v251, v250, v250\n"
"v_sub_f32 v250, v245, v248\n"
"v_mfma_f32_16x16x32_bf16 a[108:111], v[156:159], v[116:119], a[108:111]\n"
"v_fmac_f32 v251, v250, v250\n"
"v_sub_f32 v250, v246, v248\n"
"v_mfma_f32_16x16x32_bf16 a[124:127], v[156:159], v[124:127], a[124:127]\n"
"v_fmac_f32 v251, v250, v250\n"
"v_sub_f32 v250, v247, v248\n"
"v_mfma_f32_16x16x32_bf16 a[124:127], v[152:155], v[120:123], a[124:127]\n"
"s_waitcnt lgkmcnt(0)\n"
"v_fmac_f32 v251, v250, v250\n"
"v_fmac_f32 v249, v251, v183\n"
"s_nop 1\n"
"v_add_f32_dpp v249, v249, v249 quad_perm:[1,0,3,2] row_mask:0xf bank_mask:0xf bound_ctrl:1\n"
"s_nop 1\n"
"v_add_f32_dpp v249, v249, v249 quad_perm:[2,3,0,1] row_mask:0xf bank_mask:0xf bound_ctrl:1\n"
"s_nop 1\n"
"v_add_f32_dpp v249, v249, v249 row_ror:8 row_mask:0xf bank_mask:0xf bound_ctrl:1\n"
"v_mov_b32 v250, v249\n"
"s_nop 1\n"
"v_permlane32_swap_b32 v250, v249\n"
"v_add_f32 v249, v249, v250\n"
"v_mov_b32 v250, v249\n"
"v_mfma_f32_16x16x32_bf16 a[128:131], v[128:131], v[64:67], a[128:131]\n"
"ds_read_b128 v[136:139], v178 offset:2048\n"
"v_permlane16_swap_b32 v250, v249\n"
"v_mfma_f32_16x16x32_bf16 a[128:131], v[132:135], v[68:71], a[128:131]\n"
"s_add_u32 s85, s72, 512\n"
"ds_read_b128 v[140:143], v179 offset:2048\n"
"v_add_f32 v249, v249, v250\n"
"v_mfma_f32_16x16x32_bf16 a[144:147], v[132:135], v[76:79], a[144:147]\n"
"ds_read_b64_tr_b16 v[4:5], v168 offset:16384\n"
"v_mul_f32 v249, 0x3b124925, v249\n"
"v_mfma_f32_16x16x32_bf16 a[144:147], v[128:131], v[72:75], a[144:147]\n"
"v_cvt_pk_bf16_f32 v249, v249, v249\n"
"v_mfma_f32_16x16x32_bf16 a[160:163], v[128:131], v[80:83], a[160:163]\n"
"ds_read_b64_tr_b16 v[6:7], v168 offset:18432\n"
"v_and_b32 v249, 0xffff0000, v249\n"
"v_mfma_f32_16x16x32_bf16 a[160:163], v[132:135], v[84:87], a[160:163]\n"
"v_add_f32 v249, 0x3727c5ac, v249\n"
"v_cvt_pk_bf16_f32 v249, v249, v249\n"
"v_mfma_f32_16x16x32_bf16 a[176:179], v[132:135], v[92:95], a[176:179]\n"
"ds_read_b64_tr_b16 v[12:13], v169 offset:16384\n"
"v_and_b32 v249, 0xffff0000, v249\n"
"v_mfma_f32_16x16x32_bf16 a[176:179], v[128:131], v[88:91], a[176:179]\n"
"s_add_u32 s82, s74, 131072\n"
"v_sqrt_f32 v249, v249\n"
"v_mfma_f32_16x16x32_bf16 a[192:195], v[128:131], v[96:99], a[192:195]\n"
"ds_read_b64_tr_b16 v[14:15], v169 offset:18432\n"
"v_cvt_pk_bf16_f32 v249, v249, v249\n"
"v_mfma_f32_16x16x32_bf16 a[192:195], v[132:135], v[100:103], a[192:195]\n"
"v_and_b32 v249, 0xffff0000, v249\n"
"v_mfma_f32_16x16x32_bf16 a[208:211], v[132:135], v[108:111], a[208:211]\n"
"ds_read_b64_tr_b16 v[20:21], v170 offset:16384\n"
"v_cvt_pk_bf16_f32 v248, v248, v248\n"
"v_mfma_f32_16x16x32_bf16 a[208:211], v[128:131], v[104:107], a[208:211]\n"
"v_rcp_f32 v249, v249\n"
"v_mfma_f32_16x16x32_bf16 a[224:227], v[128:131], v[112:115], a[224:227]\n"
"ds_read_b64_tr_b16 v[22:23], v170 offset:18432\n"
"v_and_b32 v248, 0xffff0000, v248\n"
"v_mfma_f32_16x16x32_bf16 a[224:227], v[132:135], v[116:119], a[224:227]\n"
"s_mov_b32 m0, s82\n"
"v_sub_f32 v232, v232, v248\n"
"v_sub_f32 v233, v233, v248\n"
"v_mfma_f32_16x16x32_bf16 a[240:243], v[132:135], v[124:127], a[240:243]\n"
"ds_read_b64_tr_b16 v[28:29], v171 offset:16384\n"
"v_cvt_pk_bf16_f32 v250, v232, v233\n"
"v_mfma_f32_16x16x32_bf16 a[240:243], v[128:131], v[120:123], a[240:243]\n"
"s_waitcnt lgkmcnt(7)\n"
"v_lshlrev_b32 v232, 16, v250\n"
"v_and_b32 v233, 0xffff0000, v250\n"
"v_mfma_f32_16x16x32_bf16 a[132:135], v[136:139], v[64:67], a[132:135]\n"
"ds_read_b128 v[144:147], v178 offset:4096\n"
"v_mul_f32 v232, v232, v249\n"
"v_mfma_f32_16x16x32_bf16 a[132:135], v[140:143], v[68:71], a[132:135]\n"
"ds_read_b128 v[148:151], v179 offset:4096\n"
"ds_read_b64_tr_b16 v[30:31], v171 offset:18432\n"
"v_mfma_f32_16x16x32_bf16 a[148:151], v[140:143], v[76:79], a[148:151]\n"
"v_mul_f32 v233, v233, v249\n"
"v_cvt_pk_bf16_f32 v232, v232, v233\n"
"v_mfma_f32_16x16x32_bf16 a[148:151], v[136:139], v[72:75], a[148:151]\n"
"ds_read_b64_tr_b16 v[36:37], v172 offset:16384\n"
"v_sub_f32 v234, v234, v248\n"
"v_mfma_f32_16x16x32_bf16 a[164:167], v[136:139], v[80:83], a[164:167]\n"
"buffer_load_dwordx4 v160, s[64:67], s85 offen offset:0 lds\n"
"v_mfma_f32_16x16x32_bf16 a[164:167], v[140:143], v[84:87], a[164:167]\n"
"ds_read_b64_tr_b16 v[38:39], v172 offset:18432\n"
"v_sub_f32 v235, v235, v248\n"
"v_mfma_f32_16x16x32_bf16 a[180:183], v[140:143], v[92:95], a[180:183]\n"
"v_cvt_pk_bf16_f32 v250, v234, v235\n"
"v_lshlrev_b32 v234, 16, v250\n"
"v_mfma_f32_16x16x32_bf16 a[180:183], v[136:139], v[88:91], a[180:183]\n"
"ds_read_b64_tr_b16 v[44:45], v173 offset:16384\n"
"v_and_b32 v235, 0xffff0000, v250\n"
"v_mfma_f32_16x16x32_bf16 a[196:199], v[136:139], v[96:99], a[196:199]\n"
"buffer_load_dwordx4 v161, s[64:67], s85 offen offset:1024 lds\n"
"v_mfma_f32_16x16x32_bf16 a[196:199], v[140:143], v[100:103], a[196:199]\n"
"ds_read_b64_tr_b16 v[46:47], v173 offset:18432\n"
"v_mul_f32 v234, v234, v249\n"
"v_mfma_f32_16x16x32_bf16 a[212:215], v[140:143], v[108:111], a[212:215]\n"
"v_mul_f32 v235, v235, v249\n"
"v_cvt_pk_bf16_f32 v233, v234, v235\n"
"v_mfma_f32_16x16x32_bf16 a[212:215], v[136:139], v[104:107], a[212:215]\n"
"ds_read_b64_tr_b16 v[52:53], v174 offset:16384\n"
"v_sub_f32 v236, v236, v248\n"
"v_mfma_f32_16x16x32_bf16 a[228:231], v[136:139], v[112:115], a[228:231]\n"
"v_sub_f32 v237, v237, v248\n"
"v_cvt_pk_bf16_f32 v250, v236, v237\n"
"v_mfma_f32_16x16x32_bf16 a[228:231], v[140:143], v[116:119], a[228:231]\n"
"ds_read_b64_tr_b16 v[54:55], v174 offset:18432\n"
"v_lshlrev_b32 v236, 16, v250\n"
"v_mfma_f32_16x16x32_bf16 a[244:247], v[140:143], v[124:127], a[244:247]\n"
"buffer_load_dwordx4 v162, s[64:67], s85 offen offset:2048 lds\n"
"v_mfma_f32_16x16x32_bf16 a[244:247], v[136:139], v[120:123], a[244:247]\n"
"s_waitcnt lgkmcnt(7)\n"
"v_and_b32 v237, 0xffff0000, v250\n"
"v_mul_f32 v236, v236, v249\n"
"v_mfma_f32_16x16x32_bf16 a[136:139], v[144:147], v[64:67], a[136:139]\n"
"ds_read_b128 v[152:155], v178 offset:6144\n"
"ds_read_b64_tr_b16 v[60:61], v175 offset:16384\n"
"v_mfma_f32_16x16x32_bf16 a[136:139], v[148:151], v[68:71], a[136:139]\n"
"ds_read_b128 v[156:159], v179 offset:6144\n"
"v_mul_f32 v237, v237, v249\n"
"v_mfma_f32_16x16x32_bf16 a[152:155], v[148:151], v[76:79], a[152:155]\n"
"ds_read_b64_tr_b16 v[62:63], v175 offset:18432\n"
"v_cvt_pk_bf16_f32 v234, v236, v237\n"
"v_mfma_f32_16x16x32_bf16 a[152:155], v[144:147], v[72:75], a[152:155]\n"
"v_sub_f32 v238, v238, v248\n"
"v_sub_f32 v239, v239, v248\n"
"v_mfma_f32_16x16x32_bf16 a[168:171], v[144:147], v[80:83], a[168:171]\n"
"buffer_load_dwordx4 v163, s[64:67], s85 offen offset:3072 lds\n"
"v_mfma_f32_16x16x32_bf16 a[168:171], v[148:151], v[84:87], a[168:171]\n"
"s_add_u32 s83, s83, 16384\n"
"v_cvt_pk_bf16_f32 v250, v238, v239\n"
"v_lshlrev_b32 v238, 16, v250\n"
"v_mfma_f32_16x16x32_bf16 a[184:187], v[148:151], v[92:95], a[184:187]\n"
"v_and_b32 v239, 0xffff0000, v250\n"
"v_mul_f32 v238, v238, v249\n"
"v_mfma_f32_16x16x32_bf16 a[184:187], v[144:147], v[88:91], a[184:187]\n"
"s_cmp_lt_u32 s83, 163840\n"
"s_cselect_b32 s83, s83, 65536\n"
"v_mul_f32 v239, v239, v249\n"
"v_mfma_f32_16x16x32_bf16 a[200:203], v[144:147], v[96:99], a[200:203]\n"
"v_cvt_pk_bf16_f32 v235, v238, v239\n"
"v_sub_f32 v240, v240, v248\n"
"v_mfma_f32_16x16x32_bf16 a[200:203], v[148:151], v[100:103], a[200:203]\n"
"v_add_u32 v178, s83, v176\n"
"v_sub_f32 v241, v241, v248\n"
"v_mfma_f32_16x16x32_bf16 a[216:219], v[148:151], v[108:111], a[216:219]\n"
"v_add_u32 v179, s83, v177\n"
"v_cvt_pk_bf16_f32 v250, v240, v241\n"
"v_mfma_f32_16x16x32_bf16 a[216:219], v[144:147], v[104:107], a[216:219]\n"
"v_lshlrev_b32 v240, 16, v250\n"
"v_and_b32 v241, 0xffff0000, v250\n"
"v_mfma_f32_16x16x32_bf16 a[232:235], v[144:147], v[112:115], a[232:235]\n"
"v_mul_f32 v240, v240, v249\n"
"v_mul_f32 v241, v241, v249\n"
"v_mfma_f32_16x16x32_bf16 a[232:235], v[148:151], v[116:119], a[232:235]\n"
"v_cvt_pk_bf16_f32 v236, v240, v241\n"
"v_sub_f32 v242, v242, v248\n"
"v_mfma_f32_16x16x32_bf16 a[248:251], v[148:151], v[124:127], a[248:251]\n"
"s_waitcnt lgkmcnt(0)\n"
"v_sub_f32 v243, v243, v248\n"
"v_cvt_pk_bf16_f32 v250, v242, v243\n"
"v_mfma_f32_16x16x32_bf16 a[248:251], v[144:147], v[120:123], a[248:251]\n"
"s_waitcnt vmcnt(25)\n"
"s_barrier\n"
"v_lshlrev_b32 v242, 16, v250\n"
"v_mfma_f32_16x16x32_bf16 a[140:143], v[152:155], v[64:67], a[140:143]\n"
"ds_read_b128 v[128:131], v178 offset:0\n"
"v_and_b32 v243, 0xffff0000, v250\n"
"v_mfma_f32_16x16x32_bf16 a[140:143], v[156:159], v[68:71], a[140:143]\n"
"s_add_u32 s84, s73, 2359296\n"
"ds_read_b128 v[132:135], v179 offset:0\n"
"v_mul_f32 v242, v242, v249\n"
"v_mfma_f32_16x16x32_bf16 a[156:159], v[156:159], v[76:79], a[156:159]\n"
"v_mul_f32 v243, v243, v249\n"
"v_cvt_pk_bf16_f32 v237, v242, v243\n"
"v_mfma_f32_16x16x32_bf16 a[156:159], v[152:155], v[72:75], a[156:159]\n"
"s_add_u32 m0, s74, 16384\n"
"v_sub_f32 v244, v244, v248\n"
"v_sub_f32 v245, v245, v248\n"
"v_mfma_f32_16x16x32_bf16 a[172:175], v[152:155], v[80:83], a[172:175]\n"
"v_cvt_pk_bf16_f32 v250, v244, v245\n"
"v_lshlrev_b32 v244, 16, v250\n"
"v_mfma_f32_16x16x32_bf16 a[172:175], v[156:159], v[84:87], a[172:175]\n"
"buffer_load_dwordx4 v164, s[68:71], s84 offen offset:0 lds\n"
"v_mfma_f32_16x16x32_bf16 a[188:191], v[156:159], v[92:95], a[188:191]\n"
"v_and_b32 v245, 0xffff0000, v250\n"
"v_mul_f32 v244, v244, v249\n"
"v_mfma_f32_16x16x32_bf16 a[188:191], v[152:155], v[88:91], a[188:191]\n"
"buffer_load_dwordx4 v165, s[68:71], s84 offen offset:1024 lds\n"
"v_mfma_f32_16x16x32_bf16 a[204:207], v[152:155], v[96:99], a[204:207]\n"
"v_mul_f32 v245, v245, v249\n"
"v_cvt_pk_bf16_f32 v238, v244, v245\n"
"v_mfma_f32_16x16x32_bf16 a[204:207], v[156:159], v[100:103], a[204:207]\n"
"buffer_load_dwordx4 v166, s[68:71], s84 offen offset:2048 lds\n"
"v_mfma_f32_16x16x32_bf16 a[220:223], v[156:159], v[108:111], a[220:223]\n"
"v_sub_f32 v246, v246, v248\n"
"v_sub_f32 v247, v247, v248\n"
"v_mfma_f32_16x16x32_bf16 a[220:223], v[152:155], v[104:107], a[220:223]\n"
"buffer_load_dwordx4 v167, s[68:71], s84 offen offset:3072 lds\n"
"v_mfma_f32_16x16x32_bf16 a[236:239], v[152:155], v[112:115], a[236:239]\n"
"buffer_store_dwordx4 v[232:235], v203, s[20:23], 0 offen offset:0 sc1\n"
"v_mfma_f32_16x16x32_bf16 a[236:239], v[156:159], v[116:119], a[236:239]\n"
"s_mov_b32 s54, 16384\n"
"v_cvt_pk_bf16_f32 v250, v246, v247\n"
"v_lshlrev_b32 v246, 16, v250\n"
"v_mfma_f32_16x16x32_bf16 a[252:255], v[156:159], v[124:127], a[252:255]\n"
"v_and_b32 v247, 0xffff0000, v250\n"
"v_mul_f32 v246, v246, v249\n"
"v_mfma_f32_16x16x32_bf16 a[252:255], v[152:155], v[120:123], a[252:255]\n"
"s_waitcnt lgkmcnt(0)\n"
"v_mul_f32 v247, v247, v249\n"
"v_cvt_pk_bf16_f32 v239, v246, v247\n"
"v_add_u32 v203, v252, v203\n"
"buffer_store_dwordx4 v[236:239], v203, s[20:23], s54 offen offset:0 sc1\n"
"s_mov_b32 s81, 512\n"
"s_add_u32 s82, s74, 147456\n"
".p2align 6\n"
"L_k1loopv%=:\n"
"v_mfma_f32_16x16x32_bf16 a[0:3], v[128:131], v[0:3], a[0:3]\n"
"ds_read_b128 v[136:139], v178 offset:2048\n"
"v_mfma_f32_16x16x32_bf16 a[0:3], v[132:135], v[4:7], a[0:3]\n"
"ds_read_b128 v[140:143], v179 offset:2048\n"
"v_mfma_f32_16x16x32_bf16 a[16:19], v[132:135], v[12:15], a[16:19]\n"
"s_add_u32 s85, s72, s81\n"
"ds_read_b64_tr_b16 v[64:65], v168 offset:32768\n"
"v_mfma_f32_16x16x32_bf16 a[16:19], v[128:131], v[8:11], a[16:19]\n"
"s_cmp_lt_u32 s81, 2048\n"
"s_cselect_b32 s85, s85, 0x40000000\n"
"v_mfma_f32_16x16x32_bf16 a[32:35], v[128:131], v[16:19], a[32:35]\n"
"ds_read_b64_tr_b16 v[66:67], v168 offset:34816\n"
"v_mfma_f32_16x16x32_bf16 a[32:35], v[132:135], v[20:23], a[32:35]\n"
"s_add_u32 s85, s85, 131072\n"
"s_mov_b32 m0, s82\n"
"v_mfma_f32_16x16x32_bf16 a[48:51], v[132:135], v[28:31], a[48:51]\n"
"ds_read_b64_tr_b16 v[72:73], v169 offset:32768\n"
"v_mfma_f32_16x16x32_bf16 a[48:51], v[128:131], v[24:27], a[48:51]\n"
"buffer_load_dwordx4 v160, s[64:67], s85 offen offset:0 lds\n"
"v_mfma_f32_16x16x32_bf16 a[64:67], v[128:131], v[32:35], a[64:67]\n"
"ds_read_b64_tr_b16 v[74:75], v169 offset:34816\n"
"v_mfma_f32_16x16x32_bf16 a[64:67], v[132:135], v[36:39], a[64:67]\n"
"v_mfma_f32_16x16x32_bf16 a[80:83], v[132:135], v[44:47], a[80:83]\n"
"ds_read_b64_tr_b16 v[80:81], v170 offset:32768\n"
"v_mfma_f32_16x16x32_bf16 a[80:83], v[128:131], v[40:43], a[80:83]\n"
"v_mfma_f32_16x16x32_bf16 a[96:99], v[128:131], v[48:51], a[96:99]\n"
"ds_read_b64_tr_b16 v[82:83], v170 offset:34816\n"
"v_mfma_f32_16x16x32_bf16 a[96:99], v[132:135], v[52:55], a[96:99]\n"
"buffer_load_dwordx4 v161, s[64:67], s85 offen offset:1024 lds\n"
"v_mfma_f32_16x16x32_bf16 a[112:115], v[132:135], v[60:63], a[112:115]\n"
"ds_read_b64_tr_b16 v[88:89], v171 offset:32768\n"
"v_mfma_f32_16x16x32_bf16 a[112:115], v[128:131], v[56:59], a[112:115]\n"
"s_waitcnt lgkmcnt(7)\n"
"v_mfma_f32_16x16x32_bf16 a[4:7], v[136:139], v[0:3], a[4:7]\n"
"ds_read_b128 v[144:147], v178 offset:4096\n"
"v_mfma_f32_16x16x32_bf16 a[4:7], v[140:143], v[4:7], a[4:7]\n"
"ds_read_b128 v[148:151], v179 offset:4096\n"
"ds_read_b64_tr_b16 v[90:91], v171 offset:34816\n"
"v_mfma_f32_16x16x32_bf16 a[20:23], v[140:143], v[12:15], a[20:23]\n"
"v_mfma_f32_16x16x32_bf16 a[20:23], v[136:139], v[8:11], a[20:23]\n"
"ds_read_b64_tr_b16 v[96:97], v172 offset:32768\n"
"v_mfma_f32_16x16x32_bf16 a[36:39], v[136:139], v[16:19], a[36:39]\n"
"buffer_load_dwordx4 v162, s[64:67], s85 offen offset:2048 lds\n"
"v_mfma_f32_16x16x32_bf16 a[36:39], v[140:143], v[20:23], a[36:39]\n"
"ds_read_b64_tr_b16 v[98:99], v172 offset:34816\n"
"v_mfma_f32_16x16x32_bf16 a[52:55], v[140:143], v[28:31], a[52:55]\n"
"v_mfma_f32_16x16x32_bf16 a[52:55], v[136:139], v[24:27], a[52:55]\n"
"ds_read_b64_tr_b16 v[104:105], v173 offset:32768\n"
"v_mfma_f32_16x16x32_bf16 a[68:71], v[136:139], v[32:35], a[68:71]\n"
"v_mfma_f32_16x16x32_bf16 a[68:71], v[140:143], v[36:39], a[68:71]\n"
"ds_read_b64_tr_b16 v[106:107], v173 offset:34816\n"
"v_mfma_f32_16x16x32_bf16 a[84:87], v[140:143], v[44:47], a[84:87]\n"
"buffer_load_dwordx4 v163, s[64:67], s85 offen offset:3072 lds\n"
"v_mfma_f32_16x16x32_bf16 a[84:87], v[136:139], v[40:43], a[84:87]\n"
"ds_read_b64_tr_b16 v[112:113], v174 offset:32768\n"
"v_mfma_f32_16x16x32_bf16 a[100:103], v[136:139], v[48:51], a[100:103]\n"
"s_add_u32 s82, s82, 16384\n"
"v_mfma_f32_16x16x32_bf16 a[100:103], v[140:143], v[52:55], a[100:103]\n"
"s_cmp_lt_u32 s82, 163840\n"
"s_cselect_b32 s86, 0, 98304\n"
"ds_read_b64_tr_b16 v[114:115], v174 offset:34816\n"
"v_mfma_f32_16x16x32_bf16 a[116:119], v[140:143], v[60:63], a[116:119]\n"
"v_mfma_f32_16x16x32_bf16 a[116:119], v[136:139], v[56:59], a[116:119]\n"
"s_waitcnt lgkmcnt(7)\n"
"s_sub_u32 s82, s82, s86\n"
"v_mfma_f32_16x16x32_bf16 a[8:11], v[144:147], v[0:3], a[8:11]\n"
"ds_read_b128 v[152:155], v178 offset:6144\n"
"ds_read_b64_tr_b16 v[120:121], v175 offset:32768\n"
"v_mfma_f32_16x16x32_bf16 a[8:11], v[148:151], v[4:7], a[8:11]\n"
"ds_read_b128 v[156:159], v179 offset:6144\n"
"v_mfma_f32_16x16x32_bf16 a[24:27], v[148:151], v[12:15], a[24:27]\n"
"ds_read_b64_tr_b16 v[122:123], v175 offset:34816\n"
"v_mfma_f32_16x16x32_bf16 a[24:27], v[144:147], v[8:11], a[24:27]\n"
"s_add_u32 s81, s81, 128\n"
"v_mfma_f32_16x16x32_bf16 a[40:43], v[144:147], v[16:19], a[40:43]\n"
"v_mfma_f32_16x16x32_bf16 a[40:43], v[148:151], v[20:23], a[40:43]\n"
"s_add_u32 s83, s83, 16384\n"
"v_mfma_f32_16x16x32_bf16 a[56:59], v[148:151], v[28:31], a[56:59]\n"
"v_mfma_f32_16x16x32_bf16 a[56:59], v[144:147], v[24:27], a[56:59]\n"
"s_cmp_lt_u32 s83, 163840\n"
"s_cselect_b32 s83, s83, 65536\n"
"v_mfma_f32_16x16x32_bf16 a[72:75], v[144:147], v[32:35], a[72:75]\n"
"v_mfma_f32_16x16x32_bf16 a[72:75], v[148:151], v[36:39], a[72:75]\n"
"v_add_u32 v178, s83, v176\n"
"v_mfma_f32_16x16x32_bf16 a[88:91], v[148:151], v[44:47], a[88:91]\n"
"v_add_u32 v179, s83, v177\n"
"v_mfma_f32_16x16x32_bf16 a[88:91], v[144:147], v[40:43], a[88:91]\n"
"v_mfma_f32_16x16x32_bf16 a[104:107], v[144:147], v[48:51], a[104:107]\n"
"s_add_u32 s84, s84, 262144\n"
"v_mfma_f32_16x16x32_bf16 a[104:107], v[148:151], v[52:55], a[104:107]\n"
"v_mfma_f32_16x16x32_bf16 a[120:123], v[148:151], v[60:63], a[120:123]\n"
"s_waitcnt lgkmcnt(0)\n"
"v_mfma_f32_16x16x32_bf16 a[120:123], v[144:147], v[56:59], a[120:123]\n"
"s_waitcnt vmcnt(20)\n"
"s_barrier\n"
"v_mfma_f32_16x16x32_bf16 a[12:15], v[152:155], v[0:3], a[12:15]\n"
"ds_read_b128 v[128:131], v178 offset:0\n"
"v_mfma_f32_16x16x32_bf16 a[12:15], v[156:159], v[4:7], a[12:15]\n"
"s_add_u32 m0, s74, 32768\n"
"ds_read_b128 v[132:135], v179 offset:0\n"
"v_mfma_f32_16x16x32_bf16 a[28:31], v[156:159], v[12:15], a[28:31]\n"
"v_mfma_f32_16x16x32_bf16 a[28:31], v[152:155], v[8:11], a[28:31]\n"
"buffer_load_dwordx4 v164, s[68:71], s84 offen offset:0 lds\n"
"v_mfma_f32_16x16x32_bf16 a[44:47], v[152:155], v[16:19], a[44:47]\n"
"v_mfma_f32_16x16x32_bf16 a[44:47], v[156:159], v[20:23], a[44:47]\n"
"v_mfma_f32_16x16x32_bf16 a[60:63], v[156:159], v[28:31], a[60:63]\n"
"buffer_load_dwordx4 v165, s[68:71], s84 offen offset:1024 lds\n"
"v_mfma_f32_16x16x32_bf16 a[60:63], v[152:155], v[24:27], a[60:63]\n"
"v_mfma_f32_16x16x32_bf16 a[76:79], v[152:155], v[32:35], a[76:79]\n"
"v_mfma_f32_16x16x32_bf16 a[76:79], v[156:159], v[36:39], a[76:79]\n"
"buffer_load_dwordx4 v166, s[68:71], s84 offen offset:2048 lds\n"
"v_mfma_f32_16x16x32_bf16 a[92:95], v[156:159], v[44:47], a[92:95]\n"
"v_mfma_f32_16x16x32_bf16 a[92:95], v[152:155], v[40:43], a[92:95]\n"
"v_mfma_f32_16x16x32_bf16 a[108:111], v[152:155], v[48:51], a[108:111]\n"
"buffer_load_dwordx4 v167, s[68:71], s84 offen offset:3072 lds\n"
"v_mfma_f32_16x16x32_bf16 a[108:111], v[156:159], v[52:55], a[108:111]\n"
"global_load_dword v255, v[204:205], off\n"
"v_mfma_f32_16x16x32_bf16 a[124:127], v[156:159], v[60:63], a[124:127]\n"
"v_lshl_add_u64 v[204:205], v[206:207], 0, v[204:205]\n"
"v_mfma_f32_16x16x32_bf16 a[124:127], v[152:155], v[56:59], a[124:127]\n"
"s_waitcnt lgkmcnt(0)\n"
"v_mfma_f32_16x16x32_bf16 a[128:131], v[128:131], v[0:3], a[128:131]\n"
"ds_read_b128 v[136:139], v178 offset:2048\n"
"v_mfma_f32_16x16x32_bf16 a[128:131], v[132:135], v[4:7], a[128:131]\n"
"ds_read_b128 v[140:143], v179 offset:2048\n"
"v_mfma_f32_16x16x32_bf16 a[144:147], v[132:135], v[12:15], a[144:147]\n"
"s_add_u32 s85, s72, s81\n"
"ds_read_b64_tr_b16 v[68:69], v168 offset:49152\n"
"v_mfma_f32_16x16x32_bf16 a[144:147], v[128:131], v[8:11], a[144:147]\n"
"s_cmp_lt_u32 s81, 2048\n"
"s_cselect_b32 s85, s85, 0x40000000\n"
"v_mfma_f32_16x16x32_bf16 a[160:163], v[128:131], v[16:19], a[160:163]\n"
"ds_read_b64_tr_b16 v[70:71], v168 offset:51200\n"
"v_mfma_f32_16x16x32_bf16 a[160:163], v[132:135], v[20:23], a[160:163]\n"
"s_mov_b32 m0, s82\n"
"v_mfma_f32_16x16x32_bf16 a[176:179], v[132:135], v[28:31], a[176:179]\n"
"ds_read_b64_tr_b16 v[76:77], v169 offset:49152\n"
"v_mfma_f32_16x16x32_bf16 a[176:179], v[128:131], v[24:27], a[176:179]\n"
"buffer_load_dwordx4 v160, s[64:67], s85 offen offset:0 lds\n"
"v_mfma_f32_16x16x32_bf16 a[192:195], v[128:131], v[32:35], a[192:195]\n"
"ds_read_b64_tr_b16 v[78:79], v169 offset:51200\n"
"v_mfma_f32_16x16x32_bf16 a[192:195], v[132:135], v[36:39], a[192:195]\n"
"v_mfma_f32_16x16x32_bf16 a[208:211], v[132:135], v[44:47], a[208:211]\n"
"ds_read_b64_tr_b16 v[84:85], v170 offset:49152\n"
"v_mfma_f32_16x16x32_bf16 a[208:211], v[128:131], v[40:43], a[208:211]\n"
"v_mfma_f32_16x16x32_bf16 a[224:227], v[128:131], v[48:51], a[224:227]\n"
"ds_read_b64_tr_b16 v[86:87], v170 offset:51200\n"
"v_mfma_f32_16x16x32_bf16 a[224:227], v[132:135], v[52:55], a[224:227]\n"
"buffer_load_dwordx4 v161, s[64:67], s85 offen offset:1024 lds\n"
"v_mfma_f32_16x16x32_bf16 a[240:243], v[132:135], v[60:63], a[240:243]\n"
"ds_read_b64_tr_b16 v[92:93], v171 offset:49152\n"
"v_mfma_f32_16x16x32_bf16 a[240:243], v[128:131], v[56:59], a[240:243]\n"
"s_waitcnt lgkmcnt(7)\n"
"v_mfma_f32_16x16x32_bf16 a[132:135], v[136:139], v[0:3], a[132:135]\n"
"ds_read_b128 v[144:147], v178 offset:4096\n"
"v_mfma_f32_16x16x32_bf16 a[132:135], v[140:143], v[4:7], a[132:135]\n"
"ds_read_b128 v[148:151], v179 offset:4096\n"
"ds_read_b64_tr_b16 v[94:95], v171 offset:51200\n"
"v_mfma_f32_16x16x32_bf16 a[148:151], v[140:143], v[12:15], a[148:151]\n"
"v_mfma_f32_16x16x32_bf16 a[148:151], v[136:139], v[8:11], a[148:151]\n"
"ds_read_b64_tr_b16 v[100:101], v172 offset:49152\n"
"v_mfma_f32_16x16x32_bf16 a[164:167], v[136:139], v[16:19], a[164:167]\n"
"buffer_load_dwordx4 v162, s[64:67], s85 offen offset:2048 lds\n"
"v_mfma_f32_16x16x32_bf16 a[164:167], v[140:143], v[20:23], a[164:167]\n"
"ds_read_b64_tr_b16 v[102:103], v172 offset:51200\n"
"v_mfma_f32_16x16x32_bf16 a[180:183], v[140:143], v[28:31], a[180:183]\n"
"v_mfma_f32_16x16x32_bf16 a[180:183], v[136:139], v[24:27], a[180:183]\n"
"ds_read_b64_tr_b16 v[108:109], v173 offset:49152\n"
"v_mfma_f32_16x16x32_bf16 a[196:199], v[136:139], v[32:35], a[196:199]\n"
"v_mfma_f32_16x16x32_bf16 a[196:199], v[140:143], v[36:39], a[196:199]\n"
"ds_read_b64_tr_b16 v[110:111], v173 offset:51200\n"
"v_mfma_f32_16x16x32_bf16 a[212:215], v[140:143], v[44:47], a[212:215]\n"
"buffer_load_dwordx4 v163, s[64:67], s85 offen offset:3072 lds\n"
"v_mfma_f32_16x16x32_bf16 a[212:215], v[136:139], v[40:43], a[212:215]\n"
"ds_read_b64_tr_b16 v[116:117], v174 offset:49152\n"
"v_mfma_f32_16x16x32_bf16 a[228:231], v[136:139], v[48:51], a[228:231]\n"
"s_add_u32 s82, s82, 16384\n"
"v_mfma_f32_16x16x32_bf16 a[228:231], v[140:143], v[52:55], a[228:231]\n"
"s_cmp_lt_u32 s82, 163840\n"
"s_cselect_b32 s86, 0, 98304\n"
"ds_read_b64_tr_b16 v[118:119], v174 offset:51200\n"
"v_mfma_f32_16x16x32_bf16 a[244:247], v[140:143], v[60:63], a[244:247]\n"
"v_mfma_f32_16x16x32_bf16 a[244:247], v[136:139], v[56:59], a[244:247]\n"
"s_waitcnt lgkmcnt(7)\n"
"s_sub_u32 s82, s82, s86\n"
"v_mfma_f32_16x16x32_bf16 a[136:139], v[144:147], v[0:3], a[136:139]\n"
"ds_read_b128 v[152:155], v178 offset:6144\n"
"ds_read_b64_tr_b16 v[124:125], v175 offset:49152\n"
"v_mfma_f32_16x16x32_bf16 a[136:139], v[148:151], v[4:7], a[136:139]\n"
"ds_read_b128 v[156:159], v179 offset:6144\n"
"v_mfma_f32_16x16x32_bf16 a[152:155], v[148:151], v[12:15], a[152:155]\n"
"ds_read_b64_tr_b16 v[126:127], v175 offset:51200\n"
"v_mfma_f32_16x16x32_bf16 a[152:155], v[144:147], v[8:11], a[152:155]\n"
"v_mfma_f32_16x16x32_bf16 a[168:171], v[144:147], v[16:19], a[168:171]\n"
"v_mfma_f32_16x16x32_bf16 a[168:171], v[148:151], v[20:23], a[168:171]\n"
"s_add_u32 s83, s83, 16384\n"
"v_mfma_f32_16x16x32_bf16 a[184:187], v[148:151], v[28:31], a[184:187]\n"
"v_mfma_f32_16x16x32_bf16 a[184:187], v[144:147], v[24:27], a[184:187]\n"
"s_cmp_lt_u32 s83, 163840\n"
"s_cselect_b32 s83, s83, 65536\n"
"v_mfma_f32_16x16x32_bf16 a[200:203], v[144:147], v[32:35], a[200:203]\n"
"v_mfma_f32_16x16x32_bf16 a[200:203], v[148:151], v[36:39], a[200:203]\n"
"v_add_u32 v178, s83, v176\n"
"v_mfma_f32_16x16x32_bf16 a[216:219], v[148:151], v[44:47], a[216:219]\n"
"v_add_u32 v179, s83, v177\n"
"v_mfma_f32_16x16x32_bf16 a[216:219], v[144:147], v[40:43], a[216:219]\n"
"v_mfma_f32_16x16x32_bf16 a[232:235], v[144:147], v[48:51], a[232:235]\n"
"s_add_u32 s84, s84, 262144\n"
"v_mfma_f32_16x16x32_bf16 a[232:235], v[148:151], v[52:55], a[232:235]\n"
"v_mfma_f32_16x16x32_bf16 a[248:251], v[148:151], v[60:63], a[248:251]\n"
"s_waitcnt lgkmcnt(0)\n"
"v_mfma_f32_16x16x32_bf16 a[248:251], v[144:147], v[56:59], a[248:251]\n"
"s_waitcnt vmcnt(21)\n"
"s_barrier\n"
"v_mfma_f32_16x16x32_bf16 a[140:143], v[152:155], v[0:3], a[140:143]\n"
"ds_read_b128 v[128:131], v178 offset:0\n"
"v_mfma_f32_16x16x32_bf16 a[140:143], v[156:159], v[4:7], a[140:143]\n"
"s_add_u32 m0, s74, 49152\n"
"ds_read_b128 v[132:135], v179 offset:0\n"
"v_mfma_f32_16x16x32_bf16 a[156:159], v[156:159], v[12:15], a[156:159]\n"
"v_mfma_f32_16x16x32_bf16 a[156:159], v[152:155], v[8:11], a[156:159]\n"
"buffer_load_dwordx4 v164, s[68:71], s84 offen offset:0 lds\n"
"v_mfma_f32_16x16x32_bf16 a[172:175], v[152:155], v[16:19], a[172:175]\n"
"v_mfma_f32_16x16x32_bf16 a[172:175], v[156:159], v[20:23], a[172:175]\n"
"v_mfma_f32_16x16x32_bf16 a[188:191], v[156:159], v[28:31], a[188:191]\n"
"buffer_load_dwordx4 v165, s[68:71], s84 offen offset:1024 lds\n"
"v_mfma_f32_16x16x32_bf16 a[188:191], v[152:155], v[24:27], a[188:191]\n"
"v_mfma_f32_16x16x32_bf16 a[204:207], v[152:155], v[32:35], a[204:207]\n"
"v_mfma_f32_16x16x32_bf16 a[204:207], v[156:159], v[36:39], a[204:207]\n"
"buffer_load_dwordx4 v166, s[68:71], s84 offen offset:2048 lds\n"
"v_mfma_f32_16x16x32_bf16 a[220:223], v[156:159], v[44:47], a[220:223]\n"
"v_mfma_f32_16x16x32_bf16 a[220:223], v[152:155], v[40:43], a[220:223]\n"
"v_mfma_f32_16x16x32_bf16 a[236:239], v[152:155], v[48:51], a[236:239]\n"
"buffer_load_dwordx4 v167, s[68:71], s84 offen offset:3072 lds\n"
"v_mfma_f32_16x16x32_bf16 a[236:239], v[156:159], v[52:55], a[236:239]\n"
"v_mfma_f32_16x16x32_bf16 a[252:255], v[156:159], v[60:63], a[252:255]\n"
"v_mfma_f32_16x16x32_bf16 a[252:255], v[152:155], v[56:59], a[252:255]\n"
"s_waitcnt lgkmcnt(0)\n"
"v_mfma_f32_16x16x32_bf16 a[0:3], v[128:131], v[64:67], a[0:3]\n"
"ds_read_b128 v[136:139], v178 offset:2048\n"
"v_mfma_f32_16x16x32_bf16 a[0:3], v[132:135], v[68:71], a[0:3]\n"
"ds_read_b128 v[140:143], v179 offset:2048\n"
"v_mfma_f32_16x16x32_bf16 a[16:19], v[132:135], v[76:79], a[16:19]\n"
"s_add_u32 s85, s72, s81\n"
"ds_read_b64_tr_b16 v[0:1], v168 offset:0\n"
"v_mfma_f32_16x16x32_bf16 a[16:19], v[128:131], v[72:75], a[16:19]\n"
"s_cmp_lt_u32 s81, 2048\n"
"s_cselect_b32 s85, s85, 0x40000000\n"
"v_mfma_f32_16x16x32_bf16 a[32:35], v[128:131], v[80:83], a[32:35]\n"
"ds_read_b64_tr_b16 v[2:3], v168 offset:2048\n"
"v_mfma_f32_16x16x32_bf16 a[32:35], v[132:135], v[84:87], a[32:35]\n"
"s_add_u32 s85, s85, 131072\n"
"s_mov_b32 m0, s82\n"
"v_mfma_f32_16x16x32_bf16 a[48:51], v[132:135], v[92:95], a[48:51]\n"
"ds_read_b64_tr_b16 v[8:9], v169 offset:0\n"
"v_mfma_f32_16x16x32_bf16 a[48:51], v[128:131], v[88:91], a[48:51]\n"
"buffer_load_dwordx4 v160, s[64:67], s85 offen offset:0 lds\n"
"v_mfma_f32_16x16x32_bf16 a[64:67], v[128:131], v[96:99], a[64:67]\n"
"ds_read_b64_tr_b16 v[10:11], v169 offset:2048\n"
"v_mfma_f32_16x16x32_bf16 a[64:67], v[132:135], v[100:103], a[64:67]\n"
"v_mfma_f32_16x16x32_bf16 a[80:83], v[132:135], v[108:111], a[80:83]\n"
"ds_read_b64_tr_b16 v[16:17], v170 offset:0\n"
"v_mfma_f32_16x16x32_bf16 a[80:83], v[128:131], v[104:107], a[80:83]\n"
"v_mfma_f32_16x16x32_bf16 a[96:99], v[128:131], v[112:115], a[96:99]\n"
"ds_read_b64_tr_b16 v[18:19], v170 offset:2048\n"
"v_mfma_f32_16x16x32_bf16 a[96:99], v[132:135], v[116:119], a[96:99]\n"
"buffer_load_dwordx4 v161, s[64:67], s85 offen offset:1024 lds\n"
"v_mfma_f32_16x16x32_bf16 a[112:115], v[132:135], v[124:127], a[112:115]\n"
"ds_read_b64_tr_b16 v[24:25], v171 offset:0\n"
"v_mfma_f32_16x16x32_bf16 a[112:115], v[128:131], v[120:123], a[112:115]\n"
"s_waitcnt lgkmcnt(7)\n"
"v_mfma_f32_16x16x32_bf16 a[4:7], v[136:139], v[64:67], a[4:7]\n"
"ds_read_b128 v[144:147], v178 offset:4096\n"
"v_mfma_f32_16x16x32_bf16 a[4:7], v[140:143], v[68:71], a[4:7]\n"
"ds_read_b128 v[148:151], v179 offset:4096\n"
"ds_read_b64_tr_b16 v[26:27], v171 offset:2048\n"
"v_mfma_f32_16x16x32_bf16 a[20:23], v[140:143], v[76:79], a[20:23]\n"
"v_mfma_f32_16x16x32_bf16 a[20:23], v[136:139], v[72:75], a[20:23]\n"
"ds_read_b64_tr_b16 v[32:33], v172 offset:0\n"
"v_mfma_f32_16x16x32_bf16 a[36:39], v[136:139], v[80:83], a[36:39]\n"
"buffer_load_dwordx4 v162, s[64:67], s85 offen offset:2048 lds\n"
"v_mfma_f32_16x16x32_bf16 a[36:39], v[140:143], v[84:87], a[36:39]\n"
"ds_read_b64_tr_b16 v[34:35], v172 offset:2048\n"
"v_mfma_f32_16x16x32_bf16 a[52:55], v[140:143], v[92:95], a[52:55]\n"
"v_mfma_f32_16x16x32_bf16 a[52:55], v[136:139], v[88:91], a[52:55]\n"
"ds_read_b64_tr_b16 v[40:41], v173 offset:0\n"
"v_mfma_f32_16x16x32_bf16 a[68:71], v[136:139], v[96:99], a[68:71]\n"
"v_mfma_f32_16x16x32_bf16 a[68:71], v[140:143], v[100:103], a[68:71]\n"
"ds_read_b64_tr_b16 v[42:43], v173 offset:2048\n"
"v_mfma_f32_16x16x32_bf16 a[84:87], v[140:143], v[108:111], a[84:87]\n"
"buffer_load_dwordx4 v163, s[64:67], s85 offen offset:3072 lds\n"
"v_mfma_f32_16x16x32_bf16 a[84:87], v[136:139], v[104:107], a[84:87]\n"
"ds_read_b64_tr_b16 v[48:49], v174 offset:0\n"
"v_mfma_f32_16x16x32_bf16 a[100:103], v[136:139], v[112:115], a[100:103]\n"
"s_add_u32 s82, s82, 16384\n"
"v_mfma_f32_16x16x32_bf16 a[100:103], v[140:143], v[116:119], a[100:103]\n"
"s_cmp_lt_u32 s82, 163840\n"
"s_cselect_b32 s86, 0, 98304\n"
"ds_read_b64_tr_b16 v[50:51], v174 offset:2048\n"
"v_mfma_f32_16x16x32_bf16 a[116:119], v[140:143], v[124:127], a[116:119]\n"
"v_mfma_f32_16x16x32_bf16 a[116:119], v[136:139], v[120:123], a[116:119]\n"
"s_waitcnt lgkmcnt(7)\n"
"s_sub_u32 s82, s82, s86\n"
"v_mfma_f32_16x16x32_bf16 a[8:11], v[144:147], v[64:67], a[8:11]\n"
"ds_read_b128 v[152:155], v178 offset:6144\n"
"ds_read_b64_tr_b16 v[56:57], v175 offset:0\n"
"v_mfma_f32_16x16x32_bf16 a[8:11], v[148:151], v[68:71], a[8:11]\n"
"ds_read_b128 v[156:159], v179 offset:6144\n"
"v_mfma_f32_16x16x32_bf16 a[24:27], v[148:151], v[76:79], a[24:27]\n"
"ds_read_b64_tr_b16 v[58:59], v175 offset:2048\n"
"v_mfma_f32_16x16x32_bf16 a[24:27], v[144:147], v[72:75], a[24:27]\n"
"s_add_u32 s81, s81, 128\n"
"v_mfma_f32_16x16x32_bf16 a[40:43], v[144:147], v[80:83], a[40:43]\n"
"v_mfma_f32_16x16x32_bf16 a[40:43], v[148:151], v[84:87], a[40:43]\n"
"s_add_u32 s83, s83, 16384\n"
"v_mfma_f32_16x16x32_bf16 a[56:59], v[148:151], v[92:95], a[56:59]\n"
"v_mfma_f32_16x16x32_bf16 a[56:59], v[144:147], v[88:91], a[56:59]\n"
"s_cmp_lt_u32 s83, 163840\n"
"s_cselect_b32 s83, s83, 65536\n"
"v_mfma_f32_16x16x32_bf16 a[72:75], v[144:147], v[96:99], a[72:75]\n"
"v_mfma_f32_16x16x32_bf16 a[72:75], v[148:151], v[100:103], a[72:75]\n"
"v_add_u32 v178, s83, v176\n"
"v_mfma_f32_16x16x32_bf16 a[88:91], v[148:151], v[108:111], a[88:91]\n"
"v_add_u32 v179, s83, v177\n"
"v_mfma_f32_16x16x32_bf16 a[88:91], v[144:147], v[104:107], a[88:91]\n"
"v_mfma_f32_16x16x32_bf16 a[104:107], v[144:147], v[112:115], a[104:107]\n"
"s_add_u32 s84, s84, 262144\n"
"v_mfma_f32_16x16x32_bf16 a[104:107], v[148:151], v[116:119], a[104:107]\n"
"v_mfma_f32_16x16x32_bf16 a[120:123], v[148:151], v[124:127], a[120:123]\n"
"s_waitcnt lgkmcnt(0)\n"
"v_mfma_f32_16x16x32_bf16 a[120:123], v[144:147], v[120:123], a[120:123]\n"
"s_waitcnt vmcnt(21)\n"
"s_barrier\n"
"v_mfma_f32_16x16x32_bf16 a[12:15], v[152:155], v[64:67], a[12:15]\n"
"ds_read_b128 v[128:131], v178 offset:0\n"
"v_mfma_f32_16x16x32_bf16 a[12:15], v[156:159], v[68:71], a[12:15]\n"
"s_add_u32 m0, s74, 0\n"
"ds_read_b128 v[132:135], v179 offset:0\n"
"v_mfma_f32_16x16x32_bf16 a[28:31], v[156:159], v[76:79], a[28:31]\n"
"v_mfma_f32_16x16x32_bf16 a[28:31], v[152:155], v[72:75], a[28:31]\n"
"buffer_load_dwordx4 v164, s[68:71], s84 offen offset:0 lds\n"
"v_mfma_f32_16x16x32_bf16 a[44:47], v[152:155], v[80:83], a[44:47]\n"
"v_mfma_f32_16x16x32_bf16 a[44:47], v[156:159], v[84:87], a[44:47]\n"
"v_mfma_f32_16x16x32_bf16 a[60:63], v[156:159], v[92:95], a[60:63]\n"
"buffer_load_dwordx4 v165, s[68:71], s84 offen offset:1024 lds\n"
"v_mfma_f32_16x16x32_bf16 a[60:63], v[152:155], v[88:91], a[60:63]\n"
"v_mfma_f32_16x16x32_bf16 a[76:79], v[152:155], v[96:99], a[76:79]\n"
"v_mfma_f32_16x16x32_bf16 a[76:79], v[156:159], v[100:103], a[76:79]\n"
"buffer_load_dwordx4 v166, s[68:71], s84 offen offset:2048 lds\n"
"v_mfma_f32_16x16x32_bf16 a[92:95], v[156:159], v[108:111], a[92:95]\n"
"v_mfma_f32_16x16x32_bf16 a[92:95], v[152:155], v[104:107], a[92:95]\n"
"v_mfma_f32_16x16x32_bf16 a[108:111], v[152:155], v[112:115], a[108:111]\n"
"buffer_load_dwordx4 v167, s[68:71], s84 offen offset:3072 lds\n"
"v_mfma_f32_16x16x32_bf16 a[108:111], v[156:159], v[116:119], a[108:111]\n"
"v_mfma_f32_16x16x32_bf16 a[124:127], v[156:159], v[124:127], a[124:127]\n"
"v_mfma_f32_16x16x32_bf16 a[124:127], v[152:155], v[120:123], a[124:127]\n"
"s_waitcnt lgkmcnt(0)\n"
"v_mfma_f32_16x16x32_bf16 a[128:131], v[128:131], v[64:67], a[128:131]\n"
"ds_read_b128 v[136:139], v178 offset:2048\n"
"v_mfma_f32_16x16x32_bf16 a[128:131], v[132:135], v[68:71], a[128:131]\n"
"ds_read_b128 v[140:143], v179 offset:2048\n"
"v_mfma_f32_16x16x32_bf16 a[144:147], v[132:135], v[76:79], a[144:147]\n"
"s_add_u32 s85, s72, s81\n"
"ds_read_b64_tr_b16 v[4:5], v168 offset:16384\n"
"v_mfma_f32_16x16x32_bf16 a[144:147], v[128:131], v[72:75], a[144:147]\n"
"s_cmp_lt_u32 s81, 2048\n"
"s_cselect_b32 s85, s85, 0x40000000\n"
"v_mfma_f32_16x16x32_bf16 a[160:163], v[128:131], v[80:83], a[160:163]\n"
"ds_read_b64_tr_b16 v[6:7], v168 offset:18432\n"
"v_mfma_f32_16x16x32_bf16 a[160:163], v[132:135], v[84:87], a[160:163]\n"
"s_mov_b32 m0, s82\n"
"v_mfma_f32_16x16x32_bf16 a[176:179], v[132:135], v[92:95], a[176:179]\n"
"ds_read_b64_tr_b16 v[12:13], v169 offset:16384\n"
"v_mfma_f32_16x16x32_bf16 a[176:179], v[128:131], v[88:91], a[176:179]\n"
"buffer_load_dwordx4 v160, s[64:67], s85 offen offset:0 lds\n"
"v_mfma_f32_16x16x32_bf16 a[192:195], v[128:131], v[96:99], a[192:195]\n"
"ds_read_b64_tr_b16 v[14:15], v169 offset:18432\n"
"v_mfma_f32_16x16x32_bf16 a[192:195], v[132:135], v[100:103], a[192:195]\n"
"v_mfma_f32_16x16x32_bf16 a[208:211], v[132:135], v[108:111], a[208:211]\n"
"ds_read_b64_tr_b16 v[20:21], v170 offset:16384\n"
"v_mfma_f32_16x16x32_bf16 a[208:211], v[128:131], v[104:107], a[208:211]\n"
"v_mfma_f32_16x16x32_bf16 a[224:227], v[128:131], v[112:115], a[224:227]\n"
"ds_read_b64_tr_b16 v[22:23], v170 offset:18432\n"
"v_mfma_f32_16x16x32_bf16 a[224:227], v[132:135], v[116:119], a[224:227]\n"
"buffer_load_dwordx4 v161, s[64:67], s85 offen offset:1024 lds\n"
"v_mfma_f32_16x16x32_bf16 a[240:243], v[132:135], v[124:127], a[240:243]\n"
"ds_read_b64_tr_b16 v[28:29], v171 offset:16384\n"
"v_mfma_f32_16x16x32_bf16 a[240:243], v[128:131], v[120:123], a[240:243]\n"
"s_waitcnt lgkmcnt(7)\n"
"v_mfma_f32_16x16x32_bf16 a[132:135], v[136:139], v[64:67], a[132:135]\n"
"ds_read_b128 v[144:147], v178 offset:4096\n"
"v_mfma_f32_16x16x32_bf16 a[132:135], v[140:143], v[68:71], a[132:135]\n"
"ds_read_b128 v[148:151], v179 offset:4096\n"
"ds_read_b64_tr_b16 v[30:31], v171 offset:18432\n"
"v_mfma_f32_16x16x32_bf16 a[148:151], v[140:143], v[76:79], a[148:151]\n"
"v_mfma_f32_16x16x32_bf16 a[148:151], v[136:139], v[72:75], a[148:151]\n"
"ds_read_b64_tr_b16 v[36:37], v172 offset:16384\n"
"v_mfma_f32_16x16x32_bf16 a[164:167], v[136:139], v[80:83], a[164:167]\n"
"buffer_load_dwordx4 v162, s[64:67], s85 offen offset:2048 lds\n"
"v_mfma_f32_16x16x32_bf16 a[164:167], v[140:143], v[84:87], a[164:167]\n"
"ds_read_b64_tr_b16 v[38:39], v172 offset:18432\n"
"v_mfma_f32_16x16x32_bf16 a[180:183], v[140:143], v[92:95], a[180:183]\n"
"v_mfma_f32_16x16x32_bf16 a[180:183], v[136:139], v[88:91], a[180:183]\n"
"ds_read_b64_tr_b16 v[44:45], v173 offset:16384\n"
"v_mfma_f32_16x16x32_bf16 a[196:199], v[136:139], v[96:99], a[196:199]\n"
"v_mfma_f32_16x16x32_bf16 a[196:199], v[140:143], v[100:103], a[196:199]\n"
"ds_read_b64_tr_b16 v[46:47], v173 offset:18432\n"
"v_mfma_f32_16x16x32_bf16 a[212:215], v[140:143], v[108:111], a[212:215]\n"
"buffer_load_dwordx4 v163, s[64:67], s85 offen offset:3072 lds\n"
"v_mfma_f32_16x16x32_bf16 a[212:215], v[136:139], v[104:107], a[212:215]\n"
"ds_read_b64_tr_b16 v[52:53], v174 offset:16384\n"
"v_mfma_f32_16x16x32_bf16 a[228:231], v[136:139], v[112:115], a[228:231]\n"
"s_add_u32 s82, s82, 16384\n"
"v_mfma_f32_16x16x32_bf16 a[228:231], v[140:143], v[116:119], a[228:231]\n"
"s_cmp_lt_u32 s82, 163840\n"
"s_cselect_b32 s86, 0, 98304\n"
"ds_read_b64_tr_b16 v[54:55], v174 offset:18432\n"
"v_mfma_f32_16x16x32_bf16 a[244:247], v[140:143], v[124:127], a[244:247]\n"
"v_mfma_f32_16x16x32_bf16 a[244:247], v[136:139], v[120:123], a[244:247]\n"
"s_waitcnt lgkmcnt(7)\n"
"s_sub_u32 s82, s82, s86\n"
"v_mfma_f32_16x16x32_bf16 a[136:139], v[144:147], v[64:67], a[136:139]\n"
"ds_read_b128 v[152:155], v178 offset:6144\n"
"ds_read_b64_tr_b16 v[60:61], v175 offset:16384\n"
"v_mfma_f32_16x16x32_bf16 a[136:139], v[148:151], v[68:71], a[136:139]\n"
"ds_read_b128 v[156:159], v179 offset:6144\n"
"v_mfma_f32_16x16x32_bf16 a[152:155], v[148:151], v[76:79], a[152:155]\n"
"ds_read_b64_tr_b16 v[62:63], v175 offset:18432\n"
"v_mfma_f32_16x16x32_bf16 a[152:155], v[144:147], v[72:75], a[152:155]\n"
"v_mfma_f32_16x16x32_bf16 a[168:171], v[144:147], v[80:83], a[168:171]\n"
"v_mfma_f32_16x16x32_bf16 a[168:171], v[148:151], v[84:87], a[168:171]\n"
"s_add_u32 s83, s83, 16384\n"
"v_mfma_f32_16x16x32_bf16 a[184:187], v[148:151], v[92:95], a[184:187]\n"
"v_mfma_f32_16x16x32_bf16 a[184:187], v[144:147], v[88:91], a[184:187]\n"
"s_cmp_lt_u32 s83, 163840\n"
"s_cselect_b32 s83, s83, 65536\n"
"v_mfma_f32_16x16x32_bf16 a[200:203], v[144:147], v[96:99], a[200:203]\n"
"v_mfma_f32_16x16x32_bf16 a[200:203], v[148:151], v[100:103], a[200:203]\n"
"v_add_u32 v178, s83, v176\n"
"v_mfma_f32_16x16x32_bf16 a[216:219], v[148:151], v[108:111], a[216:219]\n"
"v_add_u32 v179, s83, v177\n"
"v_mfma_f32_16x16x32_bf16 a[216:219], v[144:147], v[104:107], a[216:219]\n"
"v_mfma_f32_16x16x32_bf16 a[232:235], v[144:147], v[112:115], a[232:235]\n"
"s_add_u32 s84, s84, 262144\n"
"v_mfma_f32_16x16x32_bf16 a[232:235], v[148:151], v[116:119], a[232:235]\n"
"v_mfma_f32_16x16x32_bf16 a[248:251], v[148:151], v[124:127], a[248:251]\n"
"s_waitcnt lgkmcnt(0)\n"
"v_mfma_f32_16x16x32_bf16 a[248:251], v[144:147], v[120:123], a[248:251]\n"
"s_waitcnt vmcnt(21)\n"
"s_barrier\n"
"v_mfma_f32_16x16x32_bf16 a[140:143], v[152:155], v[64:67], a[140:143]\n"
"ds_read_b128 v[128:131], v178 offset:0\n"
"v_mfma_f32_16x16x32_bf16 a[140:143], v[156:159], v[68:71], a[140:143]\n"
"s_add_u32 m0, s74, 16384\n"
"ds_read_b128 v[132:135], v179 offset:0\n"
"v_mfma_f32_16x16x32_bf16 a[156:159], v[156:159], v[76:79], a[156:159]\n"
"v_mfma_f32_16x16x32_bf16 a[156:159], v[152:155], v[72:75], a[156:159]\n"
"buffer_load_dwordx4 v164, s[68:71], s84 offen offset:0 lds\n"
"v_mfma_f32_16x16x32_bf16 a[172:175], v[152:155], v[80:83], a[172:175]\n"
"v_mfma_f32_16x16x32_bf16 a[172:175], v[156:159], v[84:87], a[172:175]\n"
"v_mfma_f32_16x16x32_bf16 a[188:191], v[156:159], v[92:95], a[188:191]\n"
"buffer_load_dwordx4 v165, s[68:71], s84 offen offset:1024 lds\n"
"v_mfma_f32_16x16x32_bf16 a[188:191], v[152:155], v[88:91], a[188:191]\n"
"v_mfma_f32_16x16x32_bf16 a[204:207], v[152:155], v[96:99], a[204:207]\n"
"v_mfma_f32_16x16x32_bf16 a[204:207], v[156:159], v[100:103], a[204:207]\n"
"buffer_load_dwordx4 v166, s[68:71], s84 offen offset:2048 lds\n"
"v_mfma_f32_16x16x32_bf16 a[220:223], v[156:159], v[108:111], a[220:223]\n"
"v_mfma_f32_16x16x32_bf16 a[220:223], v[152:155], v[104:107], a[220:223]\n"
"v_mfma_f32_16x16x32_bf16 a[236:239], v[152:155], v[112:115], a[236:239]\n"
"buffer_load_dwordx4 v167, s[68:71], s84 offen offset:3072 lds\n"
"v_mfma_f32_16x16x32_bf16 a[236:239], v[156:159], v[116:119], a[236:239]\n"
"v_mfma_f32_16x16x32_bf16 a[252:255], v[156:159], v[124:127], a[252:255]\n"
"v_mfma_f32_16x16x32_bf16 a[252:255], v[152:155], v[120:123], a[252:255]\n"
"s_waitcnt lgkmcnt(0)\n"
"s_cmp_ge_u32 s80, 4\n"
"s_cbranch_scc1 L_b1v%=\n"
"L_b1rv%=:\n"
"s_sub_u32 s80, s80, 1\n"
"s_cmp_lg_u32 s80, 0\n"
"s_cbranch_scc1 L_k1loopv%=\n"
"v_mfma_f32_16x16x32_bf16 a[0:3], v[128:131], v[0:3], a[0:3]\n"
"ds_read_b128 v[136:139], v178 offset:2048\n"
"v_mfma_f32_16x16x32_bf16 a[0:3], v[132:135], v[4:7], a[0:3]\n"
"ds_read_b128 v[140:143], v179 offset:2048\n"
"v_mfma_f32_16x16x32_bf16 a[16:19], v[132:135], v[12:15], a[16:19]\n"
"ds_read_b64_tr_b16 v[64:65], v168 offset:32768\n"
"v_mfma_f32_16x16x32_bf16 a[16:19], v[128:131], v[8:11], a[16:19]\n"
"ds_read_b64_tr_b16 v[66:67], v168 offset:34816\n"
"v_mfma_f32_16x16x32_bf16 a[32:35], v[128:131], v[16:19], a[32:35]\n"
"ds_read_b64_tr_b16 v[72:73], v169 offset:32768\n"
"v_mfma_f32_16x16x32_bf16 a[32:35], v[132:135], v[20:23], a[32:35]\n"
"ds_read_b64_tr_b16 v[74:75], v169 offset:34816\n"
"v_mfma_f32_16x16x32_bf16 a[48:51], v[132:135], v[28:31], a[48:51]\n"
"ds_read_b64_tr_b16 v[80:81], v170 offset:32768\n"
"v_mfma_f32_16x16x32_bf16 a[48:51], v[128:131], v[24:27], a[48:51]\n"
"ds_read_b64_tr_b16 v[82:83], v170 offset:34816\n"
"v_mfma_f32_16x16x32_bf16 a[64:67], v[128:131], v[32:35], a[64:67]\n"
"ds_read_b64_tr_b16 v[88:89], v171 offset:32768\n"
"v_mfma_f32_16x16x32_bf16 a[64:67], v[132:135], v[36:39], a[64:67]\n"
"ds_read_b64_tr_b16 v[90:91], v171 offset:34816\n"
"v_mfma_f32_16x16x32_bf16 a[80:83], v[132:135], v[44:47], a[80:83]\n"
"ds_read_b64_tr_b16 v[96:97], v172 offset:32768\n"
"v_mfma_f32_16x16x32_bf16 a[80:83], v[128:131], v[40:43], a[80:83]\n"
"ds_read_b64_tr_b16 v[98:99], v172 offset:34816\n"
"v_mfma_f32_16x16x32_bf16 a[96:99], v[128:131], v[48:51], a[96:99]\n"
"ds_read_b64_tr_b16 v[104:105], v173 offset:32768\n"
"v_mfma_f32_16x16x32_bf16 a[96:99], v[132:135], v[52:55], a[96:99]\n"
"ds_read_b64_tr_b16 v[106:107], v173 offset:34816\n"
"v_mfma_f32_16x16x32_bf16 a[112:115], v[132:135], v[60:63], a[112:115]\n"
"ds_read_b64_tr_b16 v[112:113], v174 offset:32768\n"
"v_mfma_f32_16x16x32_bf16 a[112:115], v[128:131], v[56:59], a[112:115]\n"
"ds_read_b64_tr_b16 v[114:115], v174 offset:34816\n"
"s_waitcnt lgkmcnt(12)\n"
"v_mfma_f32_16x16x32_bf16 a[4:7], v[136:139], v[0:3], a[4:7]\n"
"ds_read_b128 v[144:147], v178 offset:4096\n"
"v_mfma_f32_16x16x32_bf16 a[4:7], v[140:143], v[4:7], a[4:7]\n"
"ds_read_b128 v[148:151], v179 offset:4096\n"
"v_mfma_f32_16x16x32_bf16 a[20:23], v[140:143], v[12:15], a[20:23]\n"
"ds_read_b64_tr_b16 v[120:121], v175 offset:32768\n"
"v_mfma_f32_16x16x32_bf16 a[20:23], v[136:139], v[8:11], a[20:23]\n"
"ds_read_b64_tr_b16 v[122:123], v175 offset:34816\n"
"v_mfma_f32_16x16x32_bf16 a[36:39], v[136:139], v[16:19], a[36:39]\n"
"v_mfma_f32_16x16x32_bf16 a[36:39], v[140:143], v[20:23], a[36:39]\n"
"s_waitcnt vmcnt(0)\n"
"s_barrier\n"
"v_mfma_f32_16x16x32_bf16 a[52:55], v[140:143], v[28:31], a[52:55]\n"
"ds_read_b64_tr_b16 v[68:69], v168 offset:49152\n"
"v_mfma_f32_16x16x32_bf16 a[52:55], v[136:139], v[24:27], a[52:55]\n"
"s_add_u32 m0, s47, 131072\n"
"ds_read_b64_tr_b16 v[70:71], v168 offset:51200\n"
"v_mfma_f32_16x16x32_bf16 a[68:71], v[136:139], v[32:35], a[68:71]\n"
"ds_read_b64_tr_b16 v[76:77], v169 offset:49152\n"
"v_readlane_b32 s24, v224, s48\n"
"v_mfma_f32_16x16x32_bf16 a[68:71], v[140:143], v[36:39], a[68:71]\n"
"ds_read_b64_tr_b16 v[78:79], v169 offset:51200\n"
"v_readlane_b32 s25, v225, s48\n"
"v_mfma_f32_16x16x32_bf16 a[84:87], v[140:143], v[44:47], a[84:87]\n"
"ds_read_b64_tr_b16 v[84:85], v170 offset:49152\n"
"buffer_load_dword v239, v181, s[20:23], 0 offen offset:0 sc1\n"
"v_mfma_f32_16x16x32_bf16 a[84:87], v[136:139], v[40:43], a[84:87]\n"
"ds_read_b64_tr_b16 v[86:87], v170 offset:51200\n"
"v_readlane_b32 s26, v226, s48\n"
"v_mfma_f32_16x16x32_bf16 a[100:103], v[136:139], v[48:51], a[100:103]\n"
"s_add_u32 s48, s48, 1\n"
"ds_read_b64_tr_b16 v[92:93], v171 offset:49152\n"
"v_mfma_f32_16x16x32_bf16 a[100:103], v[140:143], v[52:55], a[100:103]\n"
"ds_read_b64_tr_b16 v[94:95], v171 offset:51200\n"
"v_mfma_f32_16x16x32_bf16 a[116:119], v[140:143], v[60:63], a[116:119]\n"
"ds_read_b64_tr_b16 v[100:101], v172 offset:49152\n"
"v_mfma_f32_16x16x32_bf16 a[116:119], v[136:139], v[56:59], a[116:119]\n"
"ds_read_b64_tr_b16 v[102:103], v172 offset:51200\n"
"s_waitcnt lgkmcnt(9)\n"
"v_mfma_f32_16x16x32_bf16 a[8:11], v[144:147], v[0:3], a[8:11]\n"
"ds_read_b128 v[152:155], v178 offset:6144\n"
"v_mfma_f32_16x16x32_bf16 a[8:11], v[148:151], v[4:7], a[8:11]\n"
"ds_read_b128 v[156:159], v179 offset:6144\n"
"buffer_load_dwordx4 v208, s[24:27], s49 offen offset:0 lds\n"
"v_mfma_f32_16x16x32_bf16 a[24:27], v[148:151], v[12:15], a[24:27]\n"
"ds_read_b64_tr_b16 v[108:109], v173 offset:49152\n"
"v_mfma_f32_16x16x32_bf16 a[24:27], v[144:147], v[8:11], a[24:27]\n"
"ds_read_b64_tr_b16 v[110:111], v173 offset:51200\n"
"v_mfma_f32_16x16x32_bf16 a[40:43], v[144:147], v[16:19], a[40:43]\n"
"ds_read_b64_tr_b16 v[116:117], v174 offset:49152\n"
"v_mfma_f32_16x16x32_bf16 a[40:43], v[148:151], v[20:23], a[40:43]\n"
"ds_read_b64_tr_b16 v[118:119], v174 offset:51200\n"
"buffer_load_dwordx4 v209, s[24:27], s49 offen offset:1024 lds\n"
"v_mfma_f32_16x16x32_bf16 a[56:59], v[148:151], v[28:31], a[56:59]\n"
"ds_read_b64_tr_b16 v[124:125], v175 offset:49152\n"
"v_mfma_f32_16x16x32_bf16 a[56:59], v[144:147], v[24:27], a[56:59]\n"
"ds_read_b64_tr_b16 v[126:127], v175 offset:51200\n"
"v_mfma_f32_16x16x32_bf16 a[72:75], v[144:147], v[32:35], a[72:75]\n"
"buffer_load_dwordx4 v210, s[24:27], s49 offen offset:2048 lds\n"
"v_mfma_f32_16x16x32_bf16 a[72:75], v[148:151], v[36:39], a[72:75]\n"
"buffer_load_dwordx4 v211, s[24:27], s49 offen offset:3072 lds\n"
"v_mfma_f32_16x16x32_bf16 a[88:91], v[148:151], v[44:47], a[88:91]\n"
"v_mfma_f32_16x16x32_bf16 a[88:91], v[144:147], v[40:43], a[88:91]\n"
"s_add_u32 m0, m0, 4096\n"
"v_add_u32 v178, 98304, v176\n"
"v_mfma_f32_16x16x32_bf16 a[104:107], v[144:147], v[48:51], a[104:107]\n"
"v_add_u32 v179, 98304, v177\n"
"v_mfma_f32_16x16x32_bf16 a[104:107], v[148:151], v[52:55], a[104:107]\n"
"buffer_load_dwordx4 v212, s[24:27], s49 offen offset:0 lds\n"
"v_mfma_f32_16x16x32_bf16 a[120:123], v[148:151], v[60:63], a[120:123]\n"
"s_waitcnt lgkmcnt(0)\n"
"v_mfma_f32_16x16x32_bf16 a[120:123], v[144:147], v[56:59], a[120:123]\n"
"s_barrier\n"
"v_mfma_f32_16x16x32_bf16 a[12:15], v[152:155], v[0:3], a[12:15]\n"
"ds_read_b128 v[128:131], v178 offset:0\n"
"v_mfma_f32_16x16x32_bf16 a[12:15], v[156:159], v[4:7], a[12:15]\n"
"ds_read_b128 v[132:135], v179 offset:0\n"
"v_mfma_f32_16x16x32_bf16 a[28:31], v[156:159], v[12:15], a[28:31]\n"
"buffer_load_dwordx4 v213, s[24:27], s49 offen offset:1024 lds\n"
"v_mfma_f32_16x16x32_bf16 a[28:31], v[152:155], v[8:11], a[28:31]\n"
"v_mfma_f32_16x16x32_bf16 a[44:47], v[152:155], v[16:19], a[44:47]\n"
"v_mfma_f32_16x16x32_bf16 a[44:47], v[156:159], v[20:23], a[44:47]\n"
"buffer_load_dwordx4 v214, s[24:27], s49 offen offset:2048 lds\n"
"v_mfma_f32_16x16x32_bf16 a[60:63], v[156:159], v[28:31], a[60:63]\n"
"v_mfma_f32_16x16x32_bf16 a[60:63], v[152:155], v[24:27], a[60:63]\n"
"v_mfma_f32_16x16x32_bf16 a[76:79], v[152:155], v[32:35], a[76:79]\n"
"v_mfma_f32_16x16x32_bf16 a[76:79], v[156:159], v[36:39], a[76:79]\n"
"buffer_load_dwordx4 v215, s[24:27], s49 offen offset:3072 lds\n"
"v_mfma_f32_16x16x32_bf16 a[92:95], v[156:159], v[44:47], a[92:95]\n"
"v_mfma_f32_16x16x32_bf16 a[92:95], v[152:155], v[40:43], a[92:95]\n"
"v_mfma_f32_16x16x32_bf16 a[108:111], v[152:155], v[48:51], a[108:111]\n"
"v_mfma_f32_16x16x32_bf16 a[108:111], v[156:159], v[52:55], a[108:111]\n"
"v_mfma_f32_16x16x32_bf16 a[124:127], v[156:159], v[60:63], a[124:127]\n"
"v_mfma_f32_16x16x32_bf16 a[124:127], v[152:155], v[56:59], a[124:127]\n"
"s_waitcnt lgkmcnt(1)\n"
"v_mfma_f32_16x16x32_bf16 a[0:3], v[128:131], v[64:67], a[0:3]\n"
"s_add_u32 m0, s47, 0\n"
"ds_read_b128 v[136:139], v178 offset:2048\n"
"s_waitcnt lgkmcnt(1)\n"
"v_mfma_f32_16x16x32_bf16 a[0:3], v[132:135], v[68:71], a[0:3]\n"
"ds_read_b128 v[140:143], v179 offset:2048\n"
"v_readlane_b32 s24, v224, s48\n"
"v_mfma_f32_16x16x32_bf16 a[16:19], v[132:135], v[76:79], a[16:19]\n"
"v_readlane_b32 s25, v225, s48\n"
"v_mfma_f32_16x16x32_bf16 a[16:19], v[128:131], v[72:75], a[16:19]\n"
"v_readlane_b32 s26, v226, s48\n"
"v_mfma_f32_16x16x32_bf16 a[32:35], v[128:131], v[80:83], a[32:35]\n"
"s_add_u32 s48, s48, 1\n"
"v_mfma_f32_16x16x32_bf16 a[32:35], v[132:135], v[84:87], a[32:35]\n"
"v_mfma_f32_16x16x32_bf16 a[48:51], v[132:135], v[92:95], a[48:51]\n"
"s_nop 0\n"
"buffer_load_dwordx4 v208, s[24:27], s49 offen offset:0 lds\n"
"v_mfma_f32_16x16x32_bf16 a[48:51], v[128:131], v[88:91], a[48:51]\n"
"v_mfma_f32_16x16x32_bf16 a[64:67], v[128:131], v[96:99], a[64:67]\n"
"v_mfma_f32_16x16x32_bf16 a[64:67], v[132:135], v[100:103], a[64:67]\n"
"buffer_load_dwordx4 v209, s[24:27], s49 offen offset:1024 lds\n"
"v_mfma_f32_16x16x32_bf16 a[80:83], v[132:135], v[108:111], a[80:83]\n"
"v_mfma_f32_16x16x32_bf16 a[80:83], v[128:131], v[104:107], a[80:83]\n"
"v_mfma_f32_16x16x32_bf16 a[96:99], v[128:131], v[112:115], a[96:99]\n"
"buffer_load_dwordx4 v210, s[24:27], s49 offen offset:2048 lds\n"
"v_mfma_f32_16x16x32_bf16 a[96:99], v[132:135], v[116:119], a[96:99]\n"
"v_mfma_f32_16x16x32_bf16 a[112:115], v[132:135], v[124:127], a[112:115]\n"
"v_mfma_f32_16x16x32_bf16 a[112:115], v[128:131], v[120:123], a[112:115]\n"
"buffer_load_dwordx4 v211, s[24:27], s49 offen offset:3072 lds\n"
"s_waitcnt lgkmcnt(0)\n"
"v_mfma_f32_16x16x32_bf16 a[4:7], v[136:139], v[64:67], a[4:7]\n"
"s_add_u32 m0, m0, 4096\n"
"ds_read_b128 v[144:147], v178 offset:4096\n"
"v_mfma_f32_16x16x32_bf16 a[4:7], v[140:143], v[68:71], a[4:7]\n"
"ds_read_b128 v[148:151], v179 offset:4096\n"
"v_mfma_f32_16x16x32_bf16 a[20:23], v[140:143], v[76:79], a[20:23]\n"
"buffer_load_dwordx4 v212, s[24:27], s49 offen offset:0 lds\n"
"v_mfma_f32_16x16x32_bf16 a[20:23], v[136:139], v[72:75], a[20:23]\n"
"v_mfma_f32_16x16x32_bf16 a[36:39], v[136:139], v[80:83], a[36:39]\n"
"v_mfma_f32_16x16x32_bf16 a[36:39], v[140:143], v[84:87], a[36:39]\n"
"buffer_load_dwordx4 v213, s[24:27], s49 offen offset:1024 lds\n"
"v_mfma_f32_16x16x32_bf16 a[52:55], v[140:143], v[92:95], a[52:55]\n"
"v_mfma_f32_16x16x32_bf16 a[52:55], v[136:139], v[88:91], a[52:55]\n"
"v_mfma_f32_16x16x32_bf16 a[68:71], v[136:139], v[96:99], a[68:71]\n"
"buffer_load_dwordx4 v214, s[24:27], s49 offen offset:2048 lds\n"
"v_mfma_f32_16x16x32_bf16 a[68:71], v[140:143], v[100:103], a[68:71]\n"
"v_mfma_f32_16x16x32_bf16 a[84:87], v[140:143], v[108:111], a[84:87]\n"
"v_mfma_f32_16x16x32_bf16 a[84:87], v[136:139], v[104:107], a[84:87]\n"
"buffer_load_dwordx4 v215, s[24:27], s49 offen offset:3072 lds\n"
"v_mfma_f32_16x16x32_bf16 a[100:103], v[136:139], v[112:115], a[100:103]\n"
"v_mfma_f32_16x16x32_bf16 a[100:103], v[140:143], v[116:119], a[100:103]\n"
"v_mfma_f32_16x16x32_bf16 a[116:119], v[140:143], v[124:127], a[116:119]\n"
"v_mfma_f32_16x16x32_bf16 a[116:119], v[136:139], v[120:123], a[116:119]\n"
"s_add_u32 m0, s47, 32768\n"
"s_waitcnt lgkmcnt(1)\n"
"v_mfma_f32_16x16x32_bf16 a[8:11], v[144:147], v[64:67], a[8:11]\n"
"ds_read_b128 v[152:155], v178 offset:6144\n"
"v_readlane_b32 s24, v224, s48\n"
"s_waitcnt lgkmcnt(1)\n"
"v_mfma_f32_16x16x32_bf16 a[8:11], v[148:151], v[68:71], a[8:11]\n"
"ds_read_b128 v[156:159], v179 offset:6144\n"
"v_readlane_b32 s25, v225, s48\n"
"v_mfma_f32_16x16x32_bf16 a[24:27], v[148:151], v[76:79], a[24:27]\n"
"v_readlane_b32 s26, v226, s48\n"
"v_mfma_f32_16x16x32_bf16 a[24:27], v[144:147], v[72:75], a[24:27]\n"
"s_add_u32 s48, s48, 1\n"
"v_mfma_f32_16x16x32_bf16 a[40:43], v[144:147], v[80:83], a[40:43]\n"
"v_mfma_f32_16x16x32_bf16 a[40:43], v[148:151], v[84:87], a[40:43]\n"
"s_nop 0\n"
"buffer_load_dwordx4 v208, s[24:27], s49 offen offset:0 lds\n"
"v_mfma_f32_16x16x32_bf16 a[56:59], v[148:151], v[92:95], a[56:59]\n"
"v_mfma_f32_16x16x32_bf16 a[56:59], v[144:147], v[88:91], a[56:59]\n"
"v_mfma_f32_16x16x32_bf16 a[72:75], v[144:147], v[96:99], a[72:75]\n"
"buffer_load_dwordx4 v209, s[24:27], s49 offen offset:1024 lds\n"
"v_mfma_f32_16x16x32_bf16 a[72:75], v[148:151], v[100:103], a[72:75]\n"
"v_mfma_f32_16x16x32_bf16 a[88:91], v[148:151], v[108:111], a[88:91]\n"
"v_mfma_f32_16x16x32_bf16 a[88:91], v[144:147], v[104:107], a[88:91]\n"
"v_add_u32 v178, 81920, v176\n"
"v_mfma_f32_16x16x32_bf16 a[104:107], v[144:147], v[112:115], a[104:107]\n"
"v_add_u32 v179, 81920, v177\n"
"v_mfma_f32_16x16x32_bf16 a[104:107], v[148:151], v[116:119], a[104:107]\n"
"buffer_load_dwordx4 v210, s[24:27], s49 offen offset:2048 lds\n"
"v_mfma_f32_16x16x32_bf16 a[120:123], v[148:151], v[124:127], a[120:123]\n"
"s_waitcnt lgkmcnt(0)\n"
"v_mfma_f32_16x16x32_bf16 a[120:123], v[144:147], v[120:123], a[120:123]\n"
"s_barrier\n"
"v_mfma_f32_16x16x32_bf16 a[12:15], v[152:155], v[64:67], a[12:15]\n"
"ds_read_b128 v[128:131], v178 offset:0\n"
"v_mfma_f32_16x16x32_bf16 a[12:15], v[156:159], v[68:71], a[12:15]\n"
"ds_read_b128 v[132:135], v179 offset:0\n"
"v_mfma_f32_16x16x32_bf16 a[28:31], v[156:159], v[76:79], a[28:31]\n"
"buffer_load_dwordx4 v211, s[24:27], s49 offen offset:3072 lds\n"
"v_mfma_f32_16x16x32_bf16 a[28:31], v[152:155], v[72:75], a[28:31]\n"
"s_add_u32 m0, m0, 4096\n"
"v_mfma_f32_16x16x32_bf16 a[44:47], v[152:155], v[80:83], a[44:47]\n"
"buffer_load_dwordx4 v212, s[24:27], s49 offen offset:0 lds\n"
"v_mfma_f32_16x16x32_bf16 a[44:47], v[156:159], v[84:87], a[44:47]\n"
"buffer_load_dwordx4 v213, s[24:27], s49 offen offset:1024 lds\n"
"v_mfma_f32_16x16x32_bf16 a[60:63], v[156:159], v[92:95], a[60:63]\n"
"v_mfma_f32_16x16x32_bf16 a[60:63], v[152:155], v[88:91], a[60:63]\n"
"buffer_load_dwordx4 v214, s[24:27], s49 offen offset:2048 lds\n"
"v_mfma_f32_16x16x32_bf16 a[76:79], v[152:155], v[96:99], a[76:79]\n"
"v_mfma_f32_16x16x32_bf16 a[76:79], v[156:159], v[100:103], a[76:79]\n"
"v_mfma_f32_16x16x32_bf16 a[92:95], v[156:159], v[108:111], a[92:95]\n"
"buffer_load_dwordx4 v215, s[24:27], s49 offen offset:3072 lds\n"
"v_mfma_f32_16x16x32_bf16 a[92:95], v[152:155], v[104:107], a[92:95]\n"
"v_mfma_f32_16x16x32_bf16 a[108:111], v[152:155], v[112:115], a[108:111]\n"
"v_mfma_f32_16x16x32_bf16 a[108:111], v[156:159], v[116:119], a[108:111]\n"
"v_mfma_f32_16x16x32_bf16 a[124:127], v[156:159], v[124:127], a[124:127]\n"
"v_mfma_f32_16x16x32_bf16 a[124:127], v[152:155], v[120:123], a[124:127]\n"
"s_waitcnt vmcnt(24)\n"
"s_mov_b32 s55, 0\n"
"L_syv%=_chk:\n"
"v_cmp_ne_u32 vcc, v180, v239\n"
"s_nop 0\n"
"s_cmp_eq_u64 vcc, -1\n"
"s_cbranch_scc1 L_syv%=_ok\n"
"s_sleep 2\n"
"buffer_load_dword v239, v181, s[20:23], 0 offen offset:0 sc1\n"
"s_add_u32 s55, s55, 1\n"
"s_waitcnt vmcnt(0)\n"
"s_cmp_lt_u32 s55, 500000\n"
"s_cbranch_scc1 L_syv%=_chk\n"
"L_syv%=_ok:\n"
"s_cmp_lg_u32 s47, 0\n"
"s_cbranch_scc1 L_b2v%=\n"
"v_readlane_b32 s54, v180, s79\n"
"s_nop 0\n"
"s_add_u32 s54, s54, 2\n"
"v_mov_b32 v240, s54\n"
"s_mov_b64 exec, 1\n"
"buffer_store_dword v240, v231, s[20:23], s78 offen offset:0 sc1\n"
"s_mov_b64 exec, -1\n"
"s_nop 3\n"
"L_b2v%=:\n"
"s_waitcnt lgkmcnt(0)\n"
"v_mfma_f32_16x16x32_bf16 a[128:131], v[128:131], v[0:3], a[128:131]\n"
"s_mov_b32 s81, 4096\n"
"s_mov_b32 s85, s75\n"
"ds_read_b128 v[136:139], v178 offset:2048\n"
"v_mfma_f32_16x16x32_bf16 a[128:131], v[132:135], v[4:7], a[128:131]\n"
"ds_read_b128 v[140:143], v179 offset:2048\n"
"v_mfma_f32_16x16x32_bf16 a[144:147], v[132:135], v[12:15], a[144:147]\n"
"v_accvgpr_read_b32 v168, a0\n"
"v_accvgpr_read_b32 v169, a1\n"
"v_mfma_f32_16x16x32_bf16 a[144:147], v[128:131], v[8:11], a[144:147]\n"
"s_add_u32 s86, s36, 0\n"
"v_accvgpr_read_b32 v170, a2\n"
"v_accvgpr_read_b32 v171, a3\n"
"v_mfma_f32_16x16x32_bf16 a[160:163], v[128:131], v[16:19], a[160:163]\n"
"buffer_load_dwordx4 a[0:3], v191, s[20:23], s86 offen offset:0 sc1\n"
"v_mfma_f32_16x16x32_bf16 a[160:163], v[132:135], v[20:23], a[160:163]\n"
"v_accvgpr_read_b32 v172, a4\n"
"v_accvgpr_read_b32 v173, a5\n"
"v_mfma_f32_16x16x32_bf16 a[176:179], v[132:135], v[28:31], a[176:179]\n"
"v_accvgpr_read_b32 v174, a6\n"
"v_accvgpr_read_b32 v175, a7\n"
"v_mfma_f32_16x16x32_bf16 a[176:179], v[128:131], v[24:27], a[176:179]\n"
"buffer_load_dwordx4 a[4:7], v191, s[20:23], s86 offen offset:1024 sc1\n"
"v_mfma_f32_16x16x32_bf16 a[192:195], v[128:131], v[32:35], a[192:195]\n"
"v_cvt_pk_bf16_f32 v240, v168, v169\n"
"v_cvt_pk_bf16_f32 v241, v170, v171\n"
"v_mfma_f32_16x16x32_bf16 a[192:195], v[132:135], v[36:39], a[192:195]\n"
"v_cvt_pk_bf16_f32 v242, v172, v173\n"
"v_cvt_pk_bf16_f32 v243, v174, v175\n"
"v_mfma_f32_16x16x32_bf16 a[208:211], v[132:135], v[44:47], a[208:211]\n"
"buffer_store_dwordx4 v[240:243], v227, s[20:23], s85 offen offset:0 sc1\n"
"v_mfma_f32_16x16x32_bf16 a[208:211], v[128:131], v[40:43], a[208:211]\n"
"v_mfma_f32_16x16x32_bf16 a[224:227], v[128:131], v[48:51], a[224:227]\n"
"v_mfma_f32_16x16x32_bf16 a[224:227], v[132:135], v[52:55], a[224:227]\n"
"v_mfma_f32_16x16x32_bf16 a[240:243], v[132:135], v[60:63], a[240:243]\n"
"v_mfma_f32_16x16x32_bf16 a[240:243], v[128:131], v[56:59], a[240:243]\n"
"s_waitcnt lgkmcnt(0)\n"
"v_mfma_f32_16x16x32_bf16 a[132:135], v[136:139], v[0:3], a[132:135]\n"
"ds_read_b128 v[144:147], v178 offset:4096\n"
"v_accvgpr_read_b32 v168, a8\n"
"v_mfma_f32_16x16x32_bf16 a[132:135], v[140:143], v[4:7], a[132:135]\n"
"ds_read_b128 v[148:151], v179 offset:4096\n"
"v_accvgpr_read_b32 v169, a9\n"
"v_mfma_f32_16x16x32_bf16 a[148:151], v[140:143], v[12:15], a[148:151]\n"
"v_accvgpr_read_b32 v170, a10\n"
"v_accvgpr_read_b32 v171, a11\n"
"v_mfma_f32_16x16x32_bf16 a[148:151], v[136:139], v[8:11], a[148:151]\n"
"buffer_load_dwordx4 a[8:11], v191, s[20:23], s86 offen offset:2048 sc1\n"
"v_mfma_f32_16x16x32_bf16 a[164:167], v[136:139], v[16:19], a[164:167]\n"
"v_accvgpr_read_b32 v172, a12\n"
"v_accvgpr_read_b32 v173, a13\n"
"v_mfma_f32_16x16x32_bf16 a[164:167], v[140:143], v[20:23], a[164:167]\n"
"v_accvgpr_read_b32 v174, a14\n"
"v_accvgpr_read_b32 v175, a15\n"
"v_mfma_f32_16x16x32_bf16 a[180:183], v[140:143], v[28:31], a[180:183]\n"
"buffer_load_dwordx4 a[12:15], v191, s[20:23], s86 offen offset:3072 sc1\n"
"v_mfma_f32_16x16x32_bf16 a[180:183], v[136:139], v[24:27], a[180:183]\n"
"v_cvt_pk_bf16_f32 v244, v168, v169\n"
"v_cvt_pk_bf16_f32 v245, v170, v171\n"
"v_mfma_f32_16x16x32_bf16 a[196:199], v[136:139], v[32:35], a[196:199]\n"
"v_cvt_pk_bf16_f32 v246, v172, v173\n"
"v_cvt_pk_bf16_f32 v247, v174, v175\n"
"v_mfma_f32_16x16x32_bf16 a[196:199], v[140:143], v[36:39], a[196:199]\n"
"buffer_store_dwordx4 v[244:247], v227, s[20:23], s85 offen offset:1024 sc1\n"
"v_mfma_f32_16x16x32_bf16 a[212:215], v[140:143], v[44:47], a[212:215]\n"
"v_mfma_f32_16x16x32_bf16 a[212:215], v[136:139], v[40:43], a[212:215]\n"
"v_mfma_f32_16x16x32_bf16 a[228:231], v[136:139], v[48:51], a[228:231]\n"
"v_mfma_f32_16x16x32_bf16 a[228:231], v[140:143], v[52:55], a[228:231]\n"
"v_mfma_f32_16x16x32_bf16 a[244:247], v[140:143], v[60:63], a[244:247]\n"
"v_accvgpr_read_b32 v168, a16\n"
"v_accvgpr_read_b32 v169, a17\n"
"v_mfma_f32_16x16x32_bf16 a[244:247], v[136:139], v[56:59], a[244:247]\n"
"s_add_u32 s86, s36, 4096\n"
"v_accvgpr_read_b32 v170, a18\n"
"v_accvgpr_read_b32 v171, a19\n"
"s_waitcnt lgkmcnt(0)\n"
"v_mfma_f32_16x16x32_bf16 a[136:139], v[144:147], v[0:3], a[136:139]\n"
"ds_read_b128 v[152:155], v178 offset:6144\n"
"v_accvgpr_read_b32 v172, a20\n"
"v_mfma_f32_16x16x32_bf16 a[136:139], v[148:151], v[4:7], a[136:139]\n"
"ds_read_b128 v[156:159], v179 offset:6144\n"
"v_accvgpr_read_b32 v173, a21\n"
"v_mfma_f32_16x16x32_bf16 a[152:155], v[148:151], v[12:15], a[152:155]\n"
"buffer_load_dwordx4 a[16:19], v191, s[20:23], s86 offen offset:0 sc1\n"
"v_mfma_f32_16x16x32_bf16 a[152:155], v[144:147], v[8:11], a[152:155]\n"
"v_accvgpr_read_b32 v174, a22\n"
"v_accvgpr_read_b32 v175, a23\n"
"v_mfma_f32_16x16x32_bf16 a[168:171], v[144:147], v[16:19], a[168:171]\n"
"buffer_load_dwordx4 a[20:23], v191, s[20:23], s86 offen offset:1024 sc1\n"
"v_mfma_f32_16x16x32_bf16 a[168:171], v[148:151], v[20:23], a[168:171]\n"
"v_cvt_pk_bf16_f32 v248, v168, v169\n"
"v_cvt_pk_bf16_f32 v249, v170, v171\n"
"v_mfma_f32_16x16x32_bf16 a[184:187], v[148:151], v[28:31], a[184:187]\n"
"v_cvt_pk_bf16_f32 v250, v172, v173\n"
"v_cvt_pk_bf16_f32 v251, v174, v175\n"
"v_mfma_f32_16x16x32_bf16 a[184:187], v[144:147], v[24:27], a[184:187]\n"
"buffer_store_dwordx4 v[248:251], v227, s[20:23], s85 offen offset:2048 sc1\n"
"v_mfma_f32_16x16x32_bf16 a[200:203], v[144:147], v[32:35], a[200:203]\n"
"v_mfma_f32_16x16x32_bf16 a[200:203], v[148:151], v[36:39], a[200:203]\n"
"v_mfma_f32_16x16x32_bf16 a[216:219], v[148:151], v[44:47], a[216:219]\n"
"v_mfma_f32_16x16x32_bf16 a[216:219], v[144:147], v[40:43], a[216:219]\n"
"v_add_u32 v178, 114688, v176\n"
"v_mfma_f32_16x16x32_bf16 a[232:235], v[144:147], v[48:51], a[232:235]\n"
"v_add_u32 v179, 114688, v177\n"
"v_accvgpr_read_b32 v168, a24\n"
"v_mfma_f32_16x16x32_bf16 a[232:235], v[148:151], v[52:55], a[232:235]\n"
"v_accvgpr_read_b32 v169, a25\n"
"v_accvgpr_read_b32 v170, a26\n"
"v_mfma_f32_16x16x32_bf16 a[248:251], v[148:151], v[60:63], a[248:251]\n"
"s_waitcnt lgkmcnt(0)\n"
"v_accvgpr_read_b32 v171, a27\n"
"v_accvgpr_read_b32 v172, a28\n"
"v_mfma_f32_16x16x32_bf16 a[248:251], v[144:147], v[56:59], a[248:251]\n"
"s_barrier\n"
"v_accvgpr_read_b32 v173, a29\n"
"v_accvgpr_read_b32 v174, a30\n"
"v_mfma_f32_16x16x32_bf16 a[140:143], v[152:155], v[0:3], a[140:143]\n"
"ds_read_b128 v[128:131], v178 offset:0\n"
"v_accvgpr_read_b32 v175, a31\n"
"v_mfma_f32_16x16x32_bf16 a[140:143], v[156:159], v[4:7], a[140:143]\n"
"ds_read_b128 v[132:135], v179 offset:0\n"
"buffer_load_dwordx4 a[24:27], v191, s[20:23], s86 offen offset:2048 sc1\n"
"v_mfma_f32_16x16x32_bf16 a[156:159], v[156:159], v[12:15], a[156:159]\n"
"buffer_load_dwordx4 a[28:31], v191, s[20:23], s86 offen offset:3072 sc1\n"
"v_mfma_f32_16x16x32_bf16 a[156:159], v[152:155], v[8:11], a[156:159]\n"
"v_cvt_pk_bf16_f32 v252, v168, v169\n"
"v_cvt_pk_bf16_f32 v253, v170, v171\n"
"v_mfma_f32_16x16x32_bf16 a[172:175], v[152:155], v[16:19], a[172:175]\n"
"v_cvt_pk_bf16_f32 v254, v172, v173\n"
"v_cvt_pk_bf16_f32 v255, v174, v175\n"
"v_mfma_f32_16x16x32_bf16 a[172:175], v[156:159], v[20:23], a[172:175]\n"
"buffer_store_dwordx4 v[252:255], v227, s[20:23], s85 offen offset:3072 sc1\n"
"v_mfma_f32_16x16x32_bf16 a[188:191], v[156:159], v[28:31], a[188:191]\n"
"v_mfma_f32_16x16x32_bf16 a[188:191], v[152:155], v[24:27], a[188:191]\n"
"v_mfma_f32_16x16x32_bf16 a[204:207], v[152:155], v[32:35], a[204:207]\n"
"v_mfma_f32_16x16x32_bf16 a[204:207], v[156:159], v[36:39], a[204:207]\n"
"v_mfma_f32_16x16x32_bf16 a[220:223], v[156:159], v[44:47], a[220:223]\n"
"v_mfma_f32_16x16x32_bf16 a[220:223], v[152:155], v[40:43], a[220:223]\n"
"v_mfma_f32_16x16x32_bf16 a[236:239], v[152:155], v[48:51], a[236:239]\n"
"v_mfma_f32_16x16x32_bf16 a[236:239], v[156:159], v[52:55], a[236:239]\n"
"v_mfma_f32_16x16x32_bf16 a[252:255], v[156:159], v[60:63], a[252:255]\n"
"v_mfma_f32_16x16x32_bf16 a[252:255], v[152:155], v[56:59], a[252:255]\n"
"s_waitcnt lgkmcnt(1)\n"
"v_mfma_f32_16x16x32_bf16 a[128:131], v[128:131], v[64:67], a[128:131]\n"
"ds_read_b128 v[136:139], v178 offset:2048\n"
"s_waitcnt lgkmcnt(1)\n"
"v_mfma_f32_16x16x32_bf16 a[128:131], v[132:135], v[68:71], a[128:131]\n"
"s_add_u32 m0, s47, 65536\n"
"ds_read_b128 v[140:143], v179 offset:2048\n"
"v_mfma_f32_16x16x32_bf16 a[144:147], v[132:135], v[76:79], a[144:147]\n"
"v_accvgpr_read_b32 v168, a32\n"
"v_accvgpr_read_b32 v169, a33\n"
"v_mfma_f32_16x16x32_bf16 a[144:147], v[128:131], v[72:75], a[144:147]\n"
"s_add_u32 s86, s36, 8192\n"
"v_accvgpr_read_b32 v170, a34\n"
"v_accvgpr_read_b32 v171, a35\n"
"v_mfma_f32_16x16x32_bf16 a[160:163], v[128:131], v[80:83], a[160:163]\n"
"buffer_load_dwordx4 a[32:35], v191, s[20:23], s86 offen offset:0 sc1\n"
"v_mfma_f32_16x16x32_bf16 a[160:163], v[132:135], v[84:87], a[160:163]\n"
"s_add_u32 s85, s85, s81\n"
"v_accvgpr_read_b32 v172, a36\n"
"v_accvgpr_read_b32 v173, a37\n"
"v_mfma_f32_16x16x32_bf16 a[176:179], v[132:135], v[92:95], a[176:179]\n"
"v_accvgpr_read_b32 v174, a38\n"
"v_accvgpr_read_b32 v175, a39\n"
"v_mfma_f32_16x16x32_bf16 a[176:179], v[128:131], v[88:91], a[176:179]\n"
"buffer_load_dwordx4 a[36:39], v191, s[20:23], s86 offen offset:1024 sc1\n"
"v_mfma_f32_16x16x32_bf16 a[192:195], v[128:131], v[96:99], a[192:195]\n"
"v_readlane_b32 s24, v224, s48\n"
"v_cvt_pk_bf16_f32 v240, v168, v169\n"
"v_mfma_f32_16x16x32_bf16 a[192:195], v[132:135], v[100:103], a[192:195]\n"
"v_cvt_pk_bf16_f32 v241, v170, v171\n"
"v_cvt_pk_bf16_f32 v242, v172, v173\n"
"v_mfma_f32_16x16x32_bf16 a[208:211], v[132:135], v[108:111], a[208:211]\n"
"v_cvt_pk_bf16_f32 v243, v174, v175\n"
"v_readlane_b32 s25, v225, s48\n"
"v_mfma_f32_16x16x32_bf16 a[208:211], v[128:131], v[104:107], a[208:211]\n"
"buffer_store_dwordx4 v[240:243], v227, s[20:23], s85 offen offset:0 sc1\n"
"v_mfma_f32_16x16x32_bf16 a[224:227], v[128:131], v[112:115], a[224:227]\n"
"v_readlane_b32 s26, v226, s48\n"
"v_mfma_f32_16x16x32_bf16 a[224:227], v[132:135], v[116:119], a[224:227]\n"
"s_nop 3\n"
"buffer_load_dwordx4 v208, s[24:27], s49 offen offset:0 lds\n"
"v_mfma_f32_16x16x32_bf16 a[240:243], v[132:135], v[124:127], a[240:243]\n"
"buffer_load_dwordx4 v209, s[24:27], s49 offen offset:1024 lds\n"
"v_mfma_f32_16x16x32_bf16 a[240:243], v[128:131], v[120:123], a[240:243]\n"
"s_add_u32 s48, s48, 1\n"
"s_waitcnt lgkmcnt(0)\n"
"v_mfma_f32_16x16x32_bf16 a[132:135], v[136:139], v[64:67], a[132:135]\n"
"ds_read_b128 v[144:147], v178 offset:4096\n"
"v_accvgpr_read_b32 v168, a40\n"
"v_mfma_f32_16x16x32_bf16 a[132:135], v[140:143], v[68:71], a[132:135]\n"
"ds_read_b128 v[148:151], v179 offset:4096\n"
"v_accvgpr_read_b32 v169, a41\n"
"v_mfma_f32_16x16x32_bf16 a[148:151], v[140:143], v[76:79], a[148:151]\n"
"v_accvgpr_read_b32 v170, a42\n"
"v_accvgpr_read_b32 v171, a43\n"
"v_mfma_f32_16x16x32_bf16 a[148:151], v[136:139], v[72:75], a[148:151]\n"
"buffer_load_dwordx4 a[40:43], v191, s[20:23], s86 offen offset:2048 sc1\n"
"v_mfma_f32_16x16x32_bf16 a[164:167], v[136:139], v[80:83], a[164:167]\n"
"v_accvgpr_read_b32 v172, a44\n"
"v_accvgpr_read_b32 v173, a45\n"
"v_mfma_f32_16x16x32_bf16 a[164:167], v[140:143], v[84:87], a[164:167]\n"
"v_accvgpr_read_b32 v174, a46\n"
"v_accvgpr_read_b32 v175, a47\n"
"v_mfma_f32_16x16x32_bf16 a[180:183], v[140:143], v[92:95], a[180:183]\n"
"buffer_load_dwordx4 a[44:47], v191, s[20:23], s86 offen offset:3072 sc1\n"
"v_mfma_f32_16x16x32_bf16 a[180:183], v[136:139], v[88:91], a[180:183]\n"
"v_cvt_pk_bf16_f32 v244, v168, v169\n"
"v_cvt_pk_bf16_f32 v245, v170, v171\n"
"v_mfma_f32_16x16x32_bf16 a[196:199], v[136:139], v[96:99], a[196:199]\n"
"v_cvt_pk_bf16_f32 v246, v172, v173\n"
"v_cvt_pk_bf16_f32 v247, v174, v175\n"
"v_mfma_f32_16x16x32_bf16 a[196:199], v[140:143], v[100:103], a[196:199]\n"
"buffer_store_dwordx4 v[244:247], v227, s[20:23], s85 offen offset:1024 sc1\n"
"v_mfma_f32_16x16x32_bf16 a[212:215], v[140:143], v[108:111], a[212:215]\n"
"buffer_load_dwordx4 v210, s[24:27], s49 offen offset:2048 lds\n"
"v_mfma_f32_16x16x32_bf16 a[212:215], v[136:139], v[104:107], a[212:215]\n"
"buffer_load_dwordx4 v211, s[24:27], s49 offen offset:3072 lds\n"
"v_mfma_f32_16x16x32_bf16 a[228:231], v[136:139], v[112:115], a[228:231]\n"
"v_mfma_f32_16x16x32_bf16 a[228:231], v[140:143], v[116:119], a[228:231]\n"
"s_add_u32 m0, m0, 4096\n"
"v_mfma_f32_16x16x32_bf16 a[244:247], v[140:143], v[124:127], a[244:247]\n"
"v_accvgpr_read_b32 v168, a48\n"
"v_accvgpr_read_b32 v169, a49\n"
"v_mfma_f32_16x16x32_bf16 a[244:247], v[136:139], v[120:123], a[244:247]\n"
"s_add_u32 s86, s36, 12288\n"
"v_accvgpr_read_b32 v170, a50\n"
"v_accvgpr_read_b32 v171, a51\n"
"s_waitcnt lgkmcnt(0)\n"
"v_mfma_f32_16x16x32_bf16 a[136:139], v[144:147], v[64:67], a[136:139]\n"
"ds_read_b128 v[152:155], v178 offset:6144\n"
"v_accvgpr_read_b32 v172, a52\n"
"v_mfma_f32_16x16x32_bf16 a[136:139], v[148:151], v[68:71], a[136:139]\n"
"ds_read_b128 v[156:159], v179 offset:6144\n"
"v_accvgpr_read_b32 v173, a53\n"
"v_mfma_f32_16x16x32_bf16 a[152:155], v[148:151], v[76:79], a[152:155]\n"
"buffer_load_dwordx4 a[48:51], v191, s[20:23], s86 offen offset:0 sc1\n"
"v_mfma_f32_16x16x32_bf16 a[152:155], v[144:147], v[72:75], a[152:155]\n"
"v_accvgpr_read_b32 v174, a54\n"
"v_accvgpr_read_b32 v175, a55\n"
"v_mfma_f32_16x16x32_bf16 a[168:171], v[144:147], v[80:83], a[168:171]\n"
"v_add_u32 v160, s50, v192\n"
"v_cvt_pk_bf16_f32 v248, v168, v169\n"
"v_mfma_f32_16x16x32_bf16 a[168:171], v[148:151], v[84:87], a[168:171]\n"
"v_add_u32 v161, s50, v193\n"
"v_cvt_pk_bf16_f32 v249, v170, v171\n"
"v_mfma_f32_16x16x32_bf16 a[184:187], v[148:151], v[92:95], a[184:187]\n"
"v_add_u32 v162, s50, v194\n"
"v_cvt_pk_bf16_f32 v250, v172, v173\n"
"v_mfma_f32_16x16x32_bf16 a[184:187], v[144:147], v[88:91], a[184:187]\n"
"v_add_u32 v163, s50, v195\n"
"v_cvt_pk_bf16_f32 v251, v174, v175\n"
"v_mfma_f32_16x16x32_bf16 a[200:203], v[144:147], v[96:99], a[200:203]\n"
"v_add_u32 v164, s50, v196\n"
"v_mfma_f32_16x16x32_bf16 a[200:203], v[148:151], v[100:103], a[200:203]\n"
"v_add_u32 v165, s50, v197\n"
"v_mfma_f32_16x16x32_bf16 a[216:219], v[148:151], v[108:111], a[216:219]\n"
"v_add_u32 v166, s50, v198\n"
"v_mfma_f32_16x16x32_bf16 a[216:219], v[144:147], v[104:107], a[216:219]\n"
"v_add_u32 v167, s50, v199\n"
"v_mfma_f32_16x16x32_bf16 a[232:235], v[144:147], v[112:115], a[232:235]\n"
"buffer_load_dwordx4 a[52:55], v191, s[20:23], s86 offen offset:1024 sc1\n"
"v_mfma_f32_16x16x32_bf16 a[232:235], v[148:151], v[116:119], a[232:235]\n"
"buffer_store_dwordx4 v[248:251], v227, s[20:23], s85 offen offset:2048 sc1\n"
"v_mfma_f32_16x16x32_bf16 a[248:251], v[148:151], v[124:127], a[248:251]\n"
"s_waitcnt lgkmcnt(0)\n"
"buffer_load_dwordx4 v212, s[24:27], s49 offen offset:0 lds\n"
"v_mfma_f32_16x16x32_bf16 a[248:251], v[144:147], v[120:123], a[248:251]\n"
"s_waitcnt vmcnt(42)\n"
"s_barrier\n"
"v_accvgpr_read_b32 v168, a56\n"
"v_mfma_f32_16x16x32_bf16 a[140:143], v[152:155], v[64:67], a[140:143]\n"
"ds_read_b64_tr_b16 v[0:1], v160 offset:0\n"
"buffer_load_dwordx4 v213, s[24:27], s49 offen offset:1024 lds\n"
"v_mfma_f32_16x16x32_bf16 a[140:143], v[156:159], v[68:71], a[140:143]\n"
"ds_read_b64_tr_b16 v[2:3], v160 offset:8192\n"
"buffer_load_dwordx4 v214, s[24:27], s49 offen offset:2048 lds\n"
"v_mfma_f32_16x16x32_bf16 a[156:159], v[156:159], v[76:79], a[156:159]\n"
"ds_read_b64_tr_b16 v[4:5], v160 offset:16384\n"
"v_accvgpr_read_b32 v169, a57\n"
"v_mfma_f32_16x16x32_bf16 a[156:159], v[152:155], v[72:75], a[156:159]\n"
"ds_read_b64_tr_b16 v[6:7], v160 offset:24576\n"
"v_accvgpr_read_b32 v170, a58\n"
"v_mfma_f32_16x16x32_bf16 a[172:175], v[152:155], v[80:83], a[172:175]\n"
"ds_read_b64_tr_b16 v[8:9], v161 offset:0\n"
"v_accvgpr_read_b32 v171, a59\n"
"v_mfma_f32_16x16x32_bf16 a[172:175], v[156:159], v[84:87], a[172:175]\n"
"ds_read_b64_tr_b16 v[10:11], v161 offset:8192\n"
"buffer_load_dwordx4 a[56:59], v191, s[20:23], s86 offen offset:2048 sc1\n"
"v_mfma_f32_16x16x32_bf16 a[188:191], v[156:159], v[92:95], a[188:191]\n"
"ds_read_b64_tr_b16 v[12:13], v161 offset:16384\n"
"v_accvgpr_read_b32 v172, a60\n"
"v_mfma_f32_16x16x32_bf16 a[188:191], v[152:155], v[88:91], a[188:191]\n"
"ds_read_b64_tr_b16 v[14:15], v161 offset:24576\n"
"v_accvgpr_read_b32 v173, a61\n"
"v_mfma_f32_16x16x32_bf16 a[204:207], v[152:155], v[96:99], a[204:207]\n"
"ds_read_b64_tr_b16 v[16:17], v162 offset:0\n"
"v_accvgpr_read_b32 v174, a62\n"
"v_mfma_f32_16x16x32_bf16 a[204:207], v[156:159], v[100:103], a[204:207]\n"
"ds_read_b64_tr_b16 v[18:19], v162 offset:8192\n"
"v_accvgpr_read_b32 v175, a63\n"
"v_mfma_f32_16x16x32_bf16 a[220:223], v[156:159], v[108:111], a[220:223]\n"
"ds_read_b64_tr_b16 v[20:21], v162 offset:16384\n"
"buffer_load_dwordx4 a[60:63], v191, s[20:23], s86 offen offset:3072 sc1\n"
"v_mfma_f32_16x16x32_bf16 a[220:223], v[152:155], v[104:107], a[220:223]\n"
"ds_read_b64_tr_b16 v[22:23], v162 offset:24576\n"
"v_cvt_pk_bf16_f32 v252, v168, v169\n"
"v_mfma_f32_16x16x32_bf16 a[236:239], v[152:155], v[112:115], a[236:239]\n"
"ds_read_b64_tr_b16 v[24:25], v163 offset:0\n"
"v_cvt_pk_bf16_f32 v253, v170, v171\n"
"v_mfma_f32_16x16x32_bf16 a[236:239], v[156:159], v[116:119], a[236:239]\n"
"ds_read_b64_tr_b16 v[26:27], v163 offset:8192\n"
"v_cvt_pk_bf16_f32 v254, v172, v173\n"
"v_mfma_f32_16x16x32_bf16 a[252:255], v[156:159], v[124:127], a[252:255]\n"
"ds_read_b64_tr_b16 v[28:29], v163 offset:16384\n"
"v_cvt_pk_bf16_f32 v255, v174, v175\n"
"v_mfma_f32_16x16x32_bf16 a[252:255], v[152:155], v[120:123], a[252:255]\n"
"ds_read_b64_tr_b16 v[30:31], v163 offset:24576\n"
"buffer_store_dwordx4 v[252:255], v227, s[20:23], s85 offen offset:3072 sc1\n"
"buffer_load_dwordx4 v215, s[24:27], s49 offen offset:3072 lds\n"
"s_add_u32 m0, s47, 98304\n"
"s_add_u32 s84, s75, 65536\n"
"L_k2%=:\n"
"s_nop 7\n"
"v_accvgpr_read_b32 v128, a128\n"
"v_accvgpr_read_b32 v129, a129\n"
"v_accvgpr_read_b32 v130, a130\n"
"v_accvgpr_read_b32 v131, a131\n"
"v_accvgpr_read_b32 v132, a132\n"
"v_accvgpr_read_b32 v133, a133\n"
"v_accvgpr_read_b32 v134, a134\n"
"v_accvgpr_read_b32 v135, a135\n"
"v_cvt_pk_bf16_f32 v64, v128, v129\n"
"v_cvt_pk_bf16_f32 v65, v130, v131\n"
"v_cvt_pk_bf16_f32 v66, v132, v133\n"
"v_cvt_pk_bf16_f32 v67, v134, v135\n"
"v_accvgpr_read_b32 v136, a136\n"
"v_accvgpr_read_b32 v137, a137\n"
"v_accvgpr_read_b32 v138, a138\n"
"v_accvgpr_read_b32 v139, a139\n"
"v_accvgpr_read_b32 v140, a140\n"
"v_accvgpr_read_b32 v141, a141\n"
"v_accvgpr_read_b32 v142, a142\n"
"v_accvgpr_read_b32 v143, a143\n"
"v_cvt_pk_bf16_f32 v68, v136, v137\n"
"v_cvt_pk_bf16_f32 v69, v138, v139\n"
"v_cvt_pk_bf16_f32 v70, v140, v141\n"
"v_cvt_pk_bf16_f32 v71, v142, v143\n"
"v_accvgpr_read_b32 v144, a144\n"
"v_accvgpr_read_b32 v145, a145\n"
"v_accvgpr_read_b32 v146, a146\n"
"v_accvgpr_read_b32 v147, a147\n"
"v_accvgpr_read_b32 v148, a148\n"
"v_accvgpr_read_b32 v149, a149\n"
"v_accvgpr_read_b32 v150, a150\n"
"v_accvgpr_read_b32 v151, a151\n"
"v_cvt_pk_bf16_f32 v72, v144, v145\n"
"v_cvt_pk_bf16_f32 v73, v146, v147\n"
"v_cvt_pk_bf16_f32 v74, v148, v149\n"
"v_cvt_pk_bf16_f32 v75, v150, v151\n"
"v_accvgpr_read_b32 v152, a152\n"
"v_accvgpr_read_b32 v153, a153\n"
"v_accvgpr_read_b32 v154, a154\n"
"v_accvgpr_read_b32 v155, a155\n"
"v_accvgpr_read_b32 v156, a156\n"
"v_accvgpr_read_b32 v157, a157\n"
"v_accvgpr_read_b32 v158, a158\n"
"v_accvgpr_read_b32 v159, a159\n"
"v_cvt_pk_bf16_f32 v76, v152, v153\n"
"v_cvt_pk_bf16_f32 v77, v154, v155\n"
"v_cvt_pk_bf16_f32 v78, v156, v157\n"
"v_cvt_pk_bf16_f32 v79, v158, v159\n"
"v_accvgpr_read_b32 v128, a160\n"
"v_accvgpr_read_b32 v129, a161\n"
"v_accvgpr_read_b32 v130, a162\n"
"v_accvgpr_read_b32 v131, a163\n"
"v_accvgpr_read_b32 v132, a164\n"
"v_accvgpr_read_b32 v133, a165\n"
"v_accvgpr_read_b32 v134, a166\n"
"v_accvgpr_read_b32 v135, a167\n"
"v_cvt_pk_bf16_f32 v80, v128, v129\n"
"v_cvt_pk_bf16_f32 v81, v130, v131\n"
"v_cvt_pk_bf16_f32 v82, v132, v133\n"
"v_cvt_pk_bf16_f32 v83, v134, v135\n"
"v_accvgpr_read_b32 v136, a168\n"
"v_accvgpr_read_b32 v137, a169\n"
"v_accvgpr_read_b32 v138, a170\n"
"v_accvgpr_read_b32 v139, a171\n"
"v_accvgpr_read_b32 v140, a172\n"
"v_accvgpr_read_b32 v141, a173\n"
"v_accvgpr_read_b32 v142, a174\n"
"v_accvgpr_read_b32 v143, a175\n"
"v_cvt_pk_bf16_f32 v84, v136, v137\n"
"v_cvt_pk_bf16_f32 v85, v138, v139\n"
"v_cvt_pk_bf16_f32 v86, v140, v141\n"
"v_cvt_pk_bf16_f32 v87, v142, v143\n"
"v_accvgpr_read_b32 v144, a176\n"
"v_accvgpr_read_b32 v145, a177\n"
"v_accvgpr_read_b32 v146, a178\n"
"v_accvgpr_read_b32 v147, a179\n"
"v_accvgpr_read_b32 v148, a180\n"
"v_accvgpr_read_b32 v149, a181\n"
"v_accvgpr_read_b32 v150, a182\n"
"v_accvgpr_read_b32 v151, a183\n"
"v_cvt_pk_bf16_f32 v88, v144, v145\n"
"v_cvt_pk_bf16_f32 v89, v146, v147\n"
"v_cvt_pk_bf16_f32 v90, v148, v149\n"
"v_cvt_pk_bf16_f32 v91, v150, v151\n"
"v_accvgpr_read_b32 v152, a184\n"
"v_accvgpr_read_b32 v153, a185\n"
"v_accvgpr_read_b32 v154, a186\n"
"v_accvgpr_read_b32 v155, a187\n"
"v_accvgpr_read_b32 v156, a188\n"
"v_accvgpr_read_b32 v157, a189\n"
"v_accvgpr_read_b32 v158, a190\n"
"v_accvgpr_read_b32 v159, a191\n"
"v_cvt_pk_bf16_f32 v92, v152, v153\n"
"v_cvt_pk_bf16_f32 v93, v154, v155\n"
"v_cvt_pk_bf16_f32 v94, v156, v157\n"
"v_cvt_pk_bf16_f32 v95, v158, v159\n"
"v_accvgpr_read_b32 v128, a192\n"
"v_accvgpr_read_b32 v129, a193\n"
"v_accvgpr_read_b32 v130, a194\n"
"v_accvgpr_read_b32 v131, a195\n"
"v_accvgpr_read_b32 v132, a196\n"
"v_accvgpr_read_b32 v133, a197\n"
"v_accvgpr_read_b32 v134, a198\n"
"v_accvgpr_read_b32 v135, a199\n"
"v_cvt_pk_bf16_f32 v96, v128, v129\n"
"v_cvt_pk_bf16_f32 v97, v130, v131\n"
"v_cvt_pk_bf16_f32 v98, v132, v133\n"
"v_cvt_pk_bf16_f32 v99, v134, v135\n"
"v_accvgpr_read_b32 v136, a200\n"
"v_accvgpr_read_b32 v137, a201\n"
"v_accvgpr_read_b32 v138, a202\n"
"v_accvgpr_read_b32 v139, a203\n"
"v_accvgpr_read_b32 v140, a204\n"
"v_accvgpr_read_b32 v141, a205\n"
"v_accvgpr_read_b32 v142, a206\n"
"v_accvgpr_read_b32 v143, a207\n"
"v_cvt_pk_bf16_f32 v100, v136, v137\n"
"v_cvt_pk_bf16_f32 v101, v138, v139\n"
"v_cvt_pk_bf16_f32 v102, v140, v141\n"
"v_cvt_pk_bf16_f32 v103, v142, v143\n"
"v_accvgpr_read_b32 v144, a208\n"
"v_accvgpr_read_b32 v145, a209\n"
"v_accvgpr_read_b32 v146, a210\n"
"v_accvgpr_read_b32 v147, a211\n"
"v_accvgpr_read_b32 v148, a212\n"
"v_accvgpr_read_b32 v149, a213\n"
"v_accvgpr_read_b32 v150, a214\n"
"v_accvgpr_read_b32 v151, a215\n"
"v_cvt_pk_bf16_f32 v104, v144, v145\n"
"v_cvt_pk_bf16_f32 v105, v146, v147\n"
"v_cvt_pk_bf16_f32 v106, v148, v149\n"
"v_cvt_pk_bf16_f32 v107, v150, v151\n"
"v_accvgpr_read_b32 v152, a216\n"
"v_accvgpr_read_b32 v153, a217\n"
"v_accvgpr_read_b32 v154, a218\n"
"v_accvgpr_read_b32 v155, a219\n"
"v_accvgpr_read_b32 v156, a220\n"
"v_accvgpr_read_b32 v157, a221\n"
"v_accvgpr_read_b32 v158, a222\n"
"v_accvgpr_read_b32 v159, a223\n"
"v_cvt_pk_bf16_f32 v108, v152, v153\n"
"v_cvt_pk_bf16_f32 v109, v154, v155\n"
"v_cvt_pk_bf16_f32 v110, v156, v157\n"
"v_cvt_pk_bf16_f32 v111, v158, v159\n"
"v_accvgpr_read_b32 v128, a224\n"
"v_accvgpr_read_b32 v129, a225\n"
"v_accvgpr_read_b32 v130, a226\n"
"v_accvgpr_read_b32 v131, a227\n"
"v_accvgpr_read_b32 v132, a228\n"
"v_accvgpr_read_b32 v133, a229\n"
"v_accvgpr_read_b32 v134, a230\n"
"v_accvgpr_read_b32 v135, a231\n"
"v_cvt_pk_bf16_f32 v112, v128, v129\n"
"v_cvt_pk_bf16_f32 v113, v130, v131\n"
"v_cvt_pk_bf16_f32 v114, v132, v133\n"
"v_cvt_pk_bf16_f32 v115, v134, v135\n"
"v_accvgpr_read_b32 v136, a232\n"
"v_accvgpr_read_b32 v137, a233\n"
"v_accvgpr_read_b32 v138, a234\n"
"v_accvgpr_read_b32 v139, a235\n"
"v_accvgpr_read_b32 v140, a236\n"
"v_accvgpr_read_b32 v141, a237\n"
"v_accvgpr_read_b32 v142, a238\n"
"v_accvgpr_read_b32 v143, a239\n"
"v_cvt_pk_bf16_f32 v116, v136, v137\n"
"v_cvt_pk_bf16_f32 v117, v138, v139\n"
"v_cvt_pk_bf16_f32 v118, v140, v141\n"
"v_cvt_pk_bf16_f32 v119, v142, v143\n"
"v_accvgpr_read_b32 v144, a240\n"
"v_accvgpr_read_b32 v145, a241\n"
"v_accvgpr_read_b32 v146, a242\n"
"v_accvgpr_read_b32 v147, a243\n"
"v_accvgpr_read_b32 v148, a244\n"
"v_accvgpr_read_b32 v149, a245\n"
"v_accvgpr_read_b32 v150, a246\n"
"v_accvgpr_read_b32 v151, a247\n"
"v_cvt_pk_bf16_f32 v120, v144, v145\n"
"v_cvt_pk_bf16_f32 v121, v146, v147\n"
"v_cvt_pk_bf16_f32 v122, v148, v149\n"
"v_cvt_pk_bf16_f32 v123, v150, v151\n"
"v_accvgpr_read_b32 v152, a248\n"
"v_accvgpr_read_b32 v153, a249\n"
"v_accvgpr_read_b32 v154, a250\n"
"v_accvgpr_read_b32 v155, a251\n"
"v_accvgpr_read_b32 v156, a252\n"
"v_accvgpr_read_b32 v157, a253\n"
"v_accvgpr_read_b32 v158, a254\n"
"v_accvgpr_read_b32 v159, a255\n"
"v_cvt_pk_bf16_f32 v124, v152, v153\n"
"v_cvt_pk_bf16_f32 v125, v154, v155\n"
"v_cvt_pk_bf16_f32 v126, v156, v157\n"
"v_cvt_pk_bf16_f32 v127, v158, v159\n"
"buffer_load_dwordx2 v[128:129], v200, s[28:31], 0 offen offset:0\n"
"buffer_load_dwordx2 v[130:131], v200, s[28:31], 0 offen offset:32\n"
"buffer_load_dwordx2 v[132:133], v200, s[28:31], 0 offen offset:64\n"
"buffer_load_dwordx2 v[134:135], v200, s[28:31], 0 offen offset:96\n"
"buffer_load_dwordx2 v[136:137], v200, s[28:31], 0 offen offset:128\n"
"buffer_load_dwordx2 v[138:139], v200, s[28:31], 0 offen offset:160\n"
"buffer_load_dwordx2 v[140:141], v200, s[28:31], 0 offen offset:192\n"
"s_waitcnt vmcnt(34)\n"
"s_waitcnt lgkmcnt(0)\n"
"v_mfma_f32_16x16x32_bf16 a[128:131], v[0:3], a[0:3], 0\n"
"ds_read_b64_tr_b16 v[32:33], v164 offset:0\n"
"v_readlane_b32 s24, v224, s48\n"
"v_mfma_f32_16x16x32_bf16 a[128:131], v[4:7], a[8:11], a[128:131]\n"
"ds_read_b64_tr_b16 v[34:35], v164 offset:8192\n"
"v_readlane_b32 s25, v225, s48\n"
"v_mfma_f32_16x16x32_bf16 a[132:135], v[4:7], a[12:15], 0\n"
"ds_read_b64_tr_b16 v[36:37], v164 offset:16384\n"
"v_readlane_b32 s26, v226, s48\n"
"v_mfma_f32_16x16x32_bf16 a[132:135], v[0:3], a[4:7], a[132:135]\n"
"s_add_u32 s86, s36, 16384\n"
"ds_read_b64_tr_b16 v[38:39], v164 offset:24576\n"
"ds_read_b64_tr_b16 v[40:41], v165 offset:0\n"
"v_mfma_f32_16x16x32_bf16 a[136:139], v[8:11], a[0:3], 0\n"
"s_add_u32 s85, s85, s81\n"
"v_accvgpr_read_b32 v168, a64\n"
"v_accvgpr_read_b32 v169, a65\n"
"v_mfma_f32_16x16x32_bf16 a[136:139], v[12:15], a[8:11], a[136:139]\n"
"s_add_u32 s48, s48, 1\n"
"ds_read_b64_tr_b16 v[42:43], v165 offset:8192\n"
"v_accvgpr_read_b32 v170, a66\n"
"v_mfma_f32_16x16x32_bf16 a[140:143], v[12:15], a[12:15], 0\n"
"ds_read_b64_tr_b16 v[44:45], v165 offset:16384\n"
"v_accvgpr_read_b32 v171, a67\n"
"v_mfma_f32_16x16x32_bf16 a[140:143], v[8:11], a[4:7], a[140:143]\n"
"ds_read_b64_tr_b16 v[46:47], v165 offset:24576\n"
"buffer_load_dwordx4 a[64:67], v191, s[20:23], s86 offen offset:0 sc1\n"
"v_mfma_f32_16x16x32_bf16 a[144:147], v[16:19], a[0:3], 0\n"
"ds_read_b64_tr_b16 v[48:49], v166 offset:0\n"
"v_accvgpr_read_b32 v172, a68\n"
"v_mfma_f32_16x16x32_bf16 a[144:147], v[20:23], a[8:11], a[144:147]\n"
"ds_read_b64_tr_b16 v[50:51], v166 offset:8192\n"
"v_accvgpr_read_b32 v173, a69\n"
"v_mfma_f32_16x16x32_bf16 a[148:151], v[20:23], a[12:15], 0\n"
"ds_read_b64_tr_b16 v[52:53], v166 offset:16384\n"
"v_accvgpr_read_b32 v174, a70\n"
"v_mfma_f32_16x16x32_bf16 a[148:151], v[16:19], a[4:7], a[148:151]\n"
"ds_read_b64_tr_b16 v[54:55], v166 offset:24576\n"
"ds_read_b64_tr_b16 v[56:57], v167 offset:0\n"
"v_mfma_f32_16x16x32_bf16 a[152:155], v[24:27], a[0:3], 0\n"
"v_accvgpr_read_b32 v175, a71\n"
"v_cvt_pk_bf16_f32 v240, v168, v169\n"
"v_mfma_f32_16x16x32_bf16 a[152:155], v[28:31], a[8:11], a[152:155]\n"
"ds_read_b64_tr_b16 v[58:59], v167 offset:8192\n"
"buffer_load_dwordx4 a[68:71], v191, s[20:23], s86 offen offset:1024 sc1\n"
"v_mfma_f32_16x16x32_bf16 a[156:159], v[28:31], a[12:15], 0\n"
"ds_read_b64_tr_b16 v[60:61], v167 offset:16384\n"
"v_cvt_pk_bf16_f32 v241, v170, v171\n"
"v_mfma_f32_16x16x32_bf16 a[156:159], v[24:27], a[4:7], a[156:159]\n"
"s_waitcnt lgkmcnt(7)\n"
"ds_read_b64_tr_b16 v[62:63], v167 offset:24576\n"
"v_cvt_pk_bf16_f32 v242, v172, v173\n"
"v_mfma_f32_16x16x32_bf16 a[160:163], v[32:35], a[0:3], 0\n"
"ds_read_b64_tr_b16 v[0:1], v160 offset:256\n"
"v_cvt_pk_bf16_f32 v243, v174, v175\n"
"v_mfma_f32_16x16x32_bf16 a[160:163], v[36:39], a[8:11], a[160:163]\n"
"ds_read_b64_tr_b16 v[2:3], v160 offset:8448\n"
"buffer_store_dwordx4 v[240:243], v227, s[20:23], s85 offen offset:0 sc1\n"
"v_mfma_f32_16x16x32_bf16 a[164:167], v[36:39], a[12:15], 0\n"
"ds_read_b64_tr_b16 v[4:5], v160 offset:16640\n"
"buffer_load_dwordx4 v208, s[24:27], s49 offen offset:0 lds\n"
"v_mfma_f32_16x16x32_bf16 a[164:167], v[32:35], a[4:7], a[164:167]\n"
"ds_read_b64_tr_b16 v[6:7], v160 offset:24832\n"
"ds_read_b64_tr_b16 v[8:9], v161 offset:256\n"
"v_mfma_f32_16x16x32_bf16 a[168:171], v[40:43], a[0:3], 0\n"
"buffer_load_dwordx4 v209, s[24:27], s49 offen offset:1024 lds\n"
"v_mfma_f32_16x16x32_bf16 a[168:171], v[44:47], a[8:11], a[168:171]\n"
"ds_read_b64_tr_b16 v[10:11], v161 offset:8448\n"
"buffer_load_dwordx4 v210, s[24:27], s49 offen offset:2048 lds\n"
"v_mfma_f32_16x16x32_bf16 a[172:175], v[44:47], a[12:15], 0\n"
"ds_read_b64_tr_b16 v[12:13], v161 offset:16640\n"
"buffer_store_dwordx4 v[64:67], v227, s[20:23], s84 offen offset:0 sc1\n"
"v_mfma_f32_16x16x32_bf16 a[172:175], v[40:43], a[4:7], a[172:175]\n"
"s_waitcnt lgkmcnt(7)\n"
"ds_read_b64_tr_b16 v[14:15], v161 offset:24832\n"
"v_mfma_f32_16x16x32_bf16 a[176:179], v[48:51], a[0:3], 0\n"
"ds_read_b64_tr_b16 v[16:17], v162 offset:256\n"
"buffer_load_dwordx4 v211, s[24:27], s49 offen offset:3072 lds\n"
"v_mfma_f32_16x16x32_bf16 a[176:179], v[52:55], a[8:11], a[176:179]\n"
"s_add_u32 m0, m0, 4096\n"
"ds_read_b64_tr_b16 v[18:19], v162 offset:8448\n"
"v_mfma_f32_16x16x32_bf16 a[180:183], v[52:55], a[12:15], 0\n"
"ds_read_b64_tr_b16 v[20:21], v162 offset:16640\n"
"v_mfma_f32_16x16x32_bf16 a[180:183], v[48:51], a[4:7], a[180:183]\n"
"ds_read_b64_tr_b16 v[22:23], v162 offset:24832\n"
"ds_read_b64_tr_b16 v[24:25], v163 offset:256\n"
"v_mfma_f32_16x16x32_bf16 a[184:187], v[56:59], a[0:3], 0\n"
"buffer_load_dwordx4 v212, s[24:27], s49 offen offset:0 lds\n"
"v_mfma_f32_16x16x32_bf16 a[184:187], v[60:63], a[8:11], a[184:187]\n"
"ds_read_b64_tr_b16 v[26:27], v163 offset:8448\n"
"v_mfma_f32_16x16x32_bf16 a[188:191], v[60:63], a[12:15], 0\n"
"ds_read_b64_tr_b16 v[28:29], v163 offset:16640\n"
"v_accvgpr_read_b32 v168, a72\n"
"v_mfma_f32_16x16x32_bf16 a[188:191], v[56:59], a[4:7], a[188:191]\n"
"s_waitcnt lgkmcnt(7)\n"
"ds_read_b64_tr_b16 v[30:31], v163 offset:24832\n"
"v_accvgpr_read_b32 v169, a73\n"
"v_mfma_f32_16x16x32_bf16 a[192:195], v[0:3], a[0:3], 0\n"
"ds_read_b64_tr_b16 v[32:33], v164 offset:256\n"
"ds_read_b64_tr_b16 v[34:35], v164 offset:8448\n"
"v_mfma_f32_16x16x32_bf16 a[192:195], v[4:7], a[8:11], a[192:195]\n"
"ds_read_b64_tr_b16 v[36:37], v164 offset:16640\n"
"ds_read_b64_tr_b16 v[38:39], v164 offset:24832\n"
"v_mfma_f32_16x16x32_bf16 a[196:199], v[4:7], a[12:15], 0\n"
"ds_read_b64_tr_b16 v[40:41], v165 offset:256\n"
"ds_read_b64_tr_b16 v[42:43], v165 offset:8448\n"
"v_mfma_f32_16x16x32_bf16 a[196:199], v[0:3], a[4:7], a[196:199]\n"
"ds_read_b64_tr_b16 v[44:45], v165 offset:16640\n"
"ds_read_b64_tr_b16 v[46:47], v165 offset:24832\n"
"v_mfma_f32_16x16x32_bf16 a[200:203], v[8:11], a[0:3], 0\n"
"ds_read_b64_tr_b16 v[48:49], v166 offset:256\n"
"ds_read_b64_tr_b16 v[50:51], v166 offset:8448\n"
"v_mfma_f32_16x16x32_bf16 a[200:203], v[12:15], a[8:11], a[200:203]\n"
"ds_read_b64_tr_b16 v[52:53], v166 offset:16640\n"
"ds_read_b64_tr_b16 v[54:55], v166 offset:24832\n"
"v_mfma_f32_16x16x32_bf16 a[204:207], v[12:15], a[12:15], 0\n"
"ds_read_b64_tr_b16 v[56:57], v167 offset:256\n"
"ds_read_b64_tr_b16 v[58:59], v167 offset:8448\n"
"v_mfma_f32_16x16x32_bf16 a[204:207], v[8:11], a[4:7], a[204:207]\n"
"s_waitcnt lgkmcnt(14)\n"
"ds_read_b64_tr_b16 v[60:61], v167 offset:16640\n"
"ds_read_b64_tr_b16 v[62:63], v167 offset:24832\n"
"v_mfma_f32_16x16x32_bf16 a[208:211], v[16:19], a[0:3], 0\n"
"buffer_load_dwordx4 v213, s[24:27], s49 offen offset:1024 lds\n"
"v_mfma_f32_16x16x32_bf16 a[208:211], v[20:23], a[8:11], a[208:211]\n"
"v_accvgpr_read_b32 v170, a74\n"
"v_accvgpr_read_b32 v171, a75\n"
"v_mfma_f32_16x16x32_bf16 a[212:215], v[20:23], a[12:15], 0\n"
"s_add_u32 s54, s50, s47\n"
"buffer_load_dwordx4 a[72:75], v191, s[20:23], s86 offen offset:2048 sc1\n"
"v_mfma_f32_16x16x32_bf16 a[212:215], v[16:19], a[4:7], a[212:215]\n"
"s_add_u32 s50, s50, 32768\n"
"v_accvgpr_read_b32 v172, a76\n"
"v_accvgpr_read_b32 v173, a77\n"
"v_mfma_f32_16x16x32_bf16 a[216:219], v[24:27], a[0:3], 0\n"
"s_cmp_lt_u32 s50, 163840\n"
"s_cselect_b32 s50, s50, 0\n"
"v_accvgpr_read_b32 v174, a78\n"
"v_mfma_f32_16x16x32_bf16 a[216:219], v[28:31], a[8:11], a[216:219]\n"
"v_add_u32 v160, s50, v192\n"
"v_add_u32 v161, s50, v193\n"
"v_mfma_f32_16x16x32_bf16 a[220:223], v[28:31], a[12:15], 0\n"
"s_waitcnt lgkmcnt(0)\n"
"v_add_u32 v162, s50, v194\n"
"v_add_u32 v163, s50, v195\n"
"v_mfma_f32_16x16x32_bf16 a[220:223], v[24:27], a[4:7], a[220:223]\n"
"s_waitcnt vmcnt(39)\n"
"s_barrier\n"
"v_mfma_f32_16x16x32_bf16 a[224:227], v[32:35], a[0:3], 0\n"
"ds_read_b64_tr_b16 v[0:1], v160 offset:0\n"
"v_add_u32 v164, s50, v196\n"
"v_mfma_f32_16x16x32_bf16 a[224:227], v[36:39], a[8:11], a[224:227]\n"
"ds_read_b64_tr_b16 v[2:3], v160 offset:8192\n"
"v_add_u32 v165, s50, v197\n"
"v_mfma_f32_16x16x32_bf16 a[228:231], v[36:39], a[12:15], 0\n"
"ds_read_b64_tr_b16 v[4:5], v160 offset:16384\n"
"v_add_u32 v166, s50, v198\n"
"v_mfma_f32_16x16x32_bf16 a[228:231], v[32:35], a[4:7], a[228:231]\n"
"ds_read_b64_tr_b16 v[6:7], v160 offset:24576\n"
"ds_read_b64_tr_b16 v[8:9], v161 offset:0\n"
"v_add_u32 v167, s50, v199\n"
"v_mfma_f32_16x16x32_bf16 a[232:235], v[40:43], a[0:3], 0\n"
"v_accvgpr_read_b32 v175, a79\n"
"v_cvt_pk_bf16_f32 v244, v168, v169\n"
"v_mfma_f32_16x16x32_bf16 a[232:235], v[44:47], a[8:11], a[232:235]\n"
"ds_read_b64_tr_b16 v[10:11], v161 offset:8192\n"
"buffer_load_dwordx4 a[76:79], v191, s[20:23], s86 offen offset:3072 sc1\n"
"v_mfma_f32_16x16x32_bf16 a[236:239], v[44:47], a[12:15], 0\n"
"ds_read_b64_tr_b16 v[12:13], v161 offset:16384\n"
"v_cvt_pk_bf16_f32 v245, v170, v171\n"
"v_mfma_f32_16x16x32_bf16 a[236:239], v[40:43], a[4:7], a[236:239]\n"
"ds_read_b64_tr_b16 v[14:15], v161 offset:24576\n"
"v_cvt_pk_bf16_f32 v246, v172, v173\n"
"v_mfma_f32_16x16x32_bf16 a[240:243], v[48:51], a[0:3], 0\n"
"ds_read_b64_tr_b16 v[16:17], v162 offset:0\n"
"v_cvt_pk_bf16_f32 v247, v174, v175\n"
"v_mfma_f32_16x16x32_bf16 a[240:243], v[52:55], a[8:11], a[240:243]\n"
"ds_read_b64_tr_b16 v[18:19], v162 offset:8192\n"
"buffer_store_dwordx4 v[244:247], v227, s[20:23], s85 offen offset:1024 sc1\n"
"v_mfma_f32_16x16x32_bf16 a[244:247], v[52:55], a[12:15], 0\n"
"ds_read_b64_tr_b16 v[20:21], v162 offset:16384\n"
"buffer_load_dwordx4 v214, s[24:27], s49 offen offset:2048 lds\n"
"v_mfma_f32_16x16x32_bf16 a[244:247], v[48:51], a[4:7], a[244:247]\n"
"ds_read_b64_tr_b16 v[22:23], v162 offset:24576\n"
"ds_read_b64_tr_b16 v[24:25], v163 offset:0\n"
"v_mfma_f32_16x16x32_bf16 a[248:251], v[56:59], a[0:3], 0\n"
"buffer_load_dwordx4 v215, s[24:27], s49 offen offset:3072 lds\n"
"v_mfma_f32_16x16x32_bf16 a[248:251], v[60:63], a[8:11], a[248:251]\n"
"ds_read_b64_tr_b16 v[26:27], v163 offset:8192\n"
"buffer_store_dwordx4 v[68:71], v227, s[20:23], s84 offen offset:1024 sc1\n"
"v_mfma_f32_16x16x32_bf16 a[252:255], v[60:63], a[12:15], 0\n"
"s_mov_b32 m0, s54\n"
"ds_read_b64_tr_b16 v[28:29], v163 offset:16384\n"
"v_mfma_f32_16x16x32_bf16 a[252:255], v[56:59], a[4:7], a[252:255]\n"
"s_waitcnt lgkmcnt(7)\n"
"ds_read_b64_tr_b16 v[30:31], v163 offset:24576\n"
"v_mfma_f32_16x16x32_bf16 a[128:131], v[0:3], a[16:19], a[128:131]\n"
"ds_read_b64_tr_b16 v[32:33], v164 offset:0\n"
"v_readlane_b32 s24, v224, s48\n"
"v_mfma_f32_16x16x32_bf16 a[128:131], v[4:7], a[24:27], a[128:131]\n"
"ds_read_b64_tr_b16 v[34:35], v164 offset:8192\n"
"v_readlane_b32 s25, v225, s48\n"
"v_mfma_f32_16x16x32_bf16 a[132:135], v[4:7], a[28:31], a[132:135]\n"
"ds_read_b64_tr_b16 v[36:37], v164 offset:16384\n"
"v_readlane_b32 s26, v226, s48\n"
"v_mfma_f32_16x16x32_bf16 a[132:135], v[0:3], a[20:23], a[132:135]\n"
"s_add_u32 s86, s36, 20480\n"
"ds_read_b64_tr_b16 v[38:39], v164 offset:24576\n"
"ds_read_b64_tr_b16 v[40:41], v165 offset:0\n"
"v_mfma_f32_16x16x32_bf16 a[136:139], v[8:11], a[16:19], a[136:139]\n"
"s_add_u32 s48, s48, 1\n"
"v_accvgpr_read_b32 v168, a80\n"
"v_accvgpr_read_b32 v169, a81\n"
"v_mfma_f32_16x16x32_bf16 a[136:139], v[12:15], a[24:27], a[136:139]\n"
"ds_read_b64_tr_b16 v[42:43], v165 offset:8192\n"
"v_accvgpr_read_b32 v170, a82\n"
"v_mfma_f32_16x16x32_bf16 a[140:143], v[12:15], a[28:31], a[140:143]\n"
"ds_read_b64_tr_b16 v[44:45], v165 offset:16384\n"
"v_accvgpr_read_b32 v171, a83\n"
"v_mfma_f32_16x16x32_bf16 a[140:143], v[8:11], a[20:23], a[140:143]\n"
"s_waitcnt lgkmcnt(7)\n"
"ds_read_b64_tr_b16 v[46:47], v165 offset:24576\n"
"buffer_load_dwordx4 a[80:83], v191, s[20:23], s86 offen offset:0 sc1\n"
"v_mfma_f32_16x16x32_bf16 a[144:147], v[16:19], a[16:19], a[144:147]\n"
"ds_read_b64_tr_b16 v[48:49], v166 offset:0\n"
"v_accvgpr_read_b32 v172, a84\n"
"v_mfma_f32_16x16x32_bf16 a[144:147], v[20:23], a[24:27], a[144:147]\n"
"ds_read_b64_tr_b16 v[50:51], v166 offset:8192\n"
"v_accvgpr_read_b32 v173, a85\n"
"v_mfma_f32_16x16x32_bf16 a[148:151], v[20:23], a[28:31], a[148:151]\n"
"ds_read_b64_tr_b16 v[52:53], v166 offset:16384\n"
"v_accvgpr_read_b32 v174, a86\n"
"v_mfma_f32_16x16x32_bf16 a[148:151], v[16:19], a[20:23], a[148:151]\n"
"ds_read_b64_tr_b16 v[54:55], v166 offset:24576\n"
"ds_read_b64_tr_b16 v[56:57], v167 offset:0\n"
"v_mfma_f32_16x16x32_bf16 a[152:155], v[24:27], a[16:19], a[152:155]\n"
"v_accvgpr_read_b32 v175, a87\n"
"v_cvt_pk_bf16_f32 v248, v168, v169\n"
"v_mfma_f32_16x16x32_bf16 a[152:155], v[28:31], a[24:27], a[152:155]\n"
"ds_read_b64_tr_b16 v[58:59], v167 offset:8192\n"
"buffer_load_dwordx4 a[84:87], v191, s[20:23], s86 offen offset:1024 sc1\n"
"v_mfma_f32_16x16x32_bf16 a[156:159], v[28:31], a[28:31], a[156:159]\n"
"ds_read_b64_tr_b16 v[60:61], v167 offset:16384\n"
"v_cvt_pk_bf16_f32 v249, v170, v171\n"
"v_mfma_f32_16x16x32_bf16 a[156:159], v[24:27], a[20:23], a[156:159]\n"
"s_waitcnt lgkmcnt(7)\n"
"ds_read_b64_tr_b16 v[62:63], v167 offset:24576\n"
"v_cvt_pk_bf16_f32 v250, v172, v173\n"
"v_mfma_f32_16x16x32_bf16 a[160:163], v[32:35], a[16:19], a[160:163]\n"
"ds_read_b64_tr_b16 v[0:1], v160 offset:256\n"
"v_cvt_pk_bf16_f32 v251, v174, v175\n"
"v_mfma_f32_16x16x32_bf16 a[160:163], v[36:39], a[24:27], a[160:163]\n"
"ds_read_b64_tr_b16 v[2:3], v160 offset:8448\n"
"buffer_store_dwordx4 v[248:251], v227, s[20:23], s85 offen offset:2048 sc1\n"
"v_mfma_f32_16x16x32_bf16 a[164:167], v[36:39], a[28:31], a[164:167]\n"
"ds_read_b64_tr_b16 v[4:5], v160 offset:16640\n"
"buffer_load_dwordx4 v208, s[24:27], s49 offen offset:0 lds\n"
"v_mfma_f32_16x16x32_bf16 a[164:167], v[32:35], a[20:23], a[164:167]\n"
"ds_read_b64_tr_b16 v[6:7], v160 offset:24832\n"
"ds_read_b64_tr_b16 v[8:9], v161 offset:256\n"
"v_mfma_f32_16x16x32_bf16 a[168:171], v[40:43], a[16:19], a[168:171]\n"
"buffer_load_dwordx4 v209, s[24:27], s49 offen offset:1024 lds\n"
"v_mfma_f32_16x16x32_bf16 a[168:171], v[44:47], a[24:27], a[168:171]\n"
"ds_read_b64_tr_b16 v[10:11], v161 offset:8448\n"
"buffer_load_dwordx4 v210, s[24:27], s49 offen offset:2048 lds\n"
"v_mfma_f32_16x16x32_bf16 a[172:175], v[44:47], a[28:31], a[172:175]\n"
"ds_read_b64_tr_b16 v[12:13], v161 offset:16640\n"
"buffer_store_dwordx4 v[72:75], v227, s[20:23], s84 offen offset:2048 sc1\n"
"v_mfma_f32_16x16x32_bf16 a[172:175], v[40:43], a[20:23], a[172:175]\n"
"s_waitcnt lgkmcnt(7)\n"
"ds_read_b64_tr_b16 v[14:15], v161 offset:24832\n"
"v_mfma_f32_16x16x32_bf16 a[176:179], v[48:51], a[16:19], a[176:179]\n"
"ds_read_b64_tr_b16 v[16:17], v162 offset:256\n"
"buffer_load_dwordx4 v211, s[24:27], s49 offen offset:3072 lds\n"
"v_mfma_f32_16x16x32_bf16 a[176:179], v[52:55], a[24:27], a[176:179]\n"
"s_add_u32 m0, m0, 4096\n"
"ds_read_b64_tr_b16 v[18:19], v162 offset:8448\n"
"v_mfma_f32_16x16x32_bf16 a[180:183], v[52:55], a[28:31], a[180:183]\n"
"ds_read_b64_tr_b16 v[20:21], v162 offset:16640\n"
"v_mfma_f32_16x16x32_bf16 a[180:183], v[48:51], a[20:23], a[180:183]\n"
"ds_read_b64_tr_b16 v[22:23], v162 offset:24832\n"
"ds_read_b64_tr_b16 v[24:25], v163 offset:256\n"
"v_mfma_f32_16x16x32_bf16 a[184:187], v[56:59], a[16:19], a[184:187]\n"
"buffer_load_dwordx4 v212, s[24:27], s49 offen offset:0 lds\n"
"v_mfma_f32_16x16x32_bf16 a[184:187], v[60:63], a[24:27], a[184:187]\n"
"ds_read_b64_tr_b16 v[26:27], v163 offset:8448\n"
"v_mfma_f32_16x16x32_bf16 a[188:191], v[60:63], a[28:31], a[188:191]\n"
"ds_read_b64_tr_b16 v[28:29], v163 offset:16640\n"
"v_accvgpr_read_b32 v168, a88\n"
"v_mfma_f32_16x16x32_bf16 a[188:191], v[56:59], a[20:23], a[188:191]\n"
"s_waitcnt lgkmcnt(7)\n"
"ds_read_b64_tr_b16 v[30:31], v163 offset:24832\n"
"v_accvgpr_read_b32 v169, a89\n"
"v_mfma_f32_16x16x32_bf16 a[192:195], v[0:3], a[16:19], a[192:195]\n"
"ds_read_b64_tr_b16 v[32:33], v164 offset:256\n"
"ds_read_b64_tr_b16 v[34:35], v164 offset:8448\n"
"v_mfma_f32_16x16x32_bf16 a[192:195], v[4:7], a[24:27], a[192:195]\n"
"ds_read_b64_tr_b16 v[36:37], v164 offset:16640\n"
"ds_read_b64_tr_b16 v[38:39], v164 offset:24832\n"
"v_mfma_f32_16x16x32_bf16 a[196:199], v[4:7], a[28:31], a[196:199]\n"
"ds_read_b64_tr_b16 v[40:41], v165 offset:256\n"
"ds_read_b64_tr_b16 v[42:43], v165 offset:8448\n"
"v_mfma_f32_16x16x32_bf16 a[196:199], v[0:3], a[20:23], a[196:199]\n"
"ds_read_b64_tr_b16 v[44:45], v165 offset:16640\n"
"ds_read_b64_tr_b16 v[46:47], v165 offset:24832\n"
"v_mfma_f32_16x16x32_bf16 a[200:203], v[8:11], a[16:19], a[200:203]\n"
"ds_read_b64_tr_b16 v[48:49], v166 offset:256\n"
"ds_read_b64_tr_b16 v[50:51], v166 offset:8448\n"
"v_mfma_f32_16x16x32_bf16 a[200:203], v[12:15], a[24:27], a[200:203]\n"
"ds_read_b64_tr_b16 v[52:53], v166 offset:16640\n"
"ds_read_b64_tr_b16 v[54:55], v166 offset:24832\n"
"v_mfma_f32_16x16x32_bf16 a[204:207], v[12:15], a[28:31], a[204:207]\n"
"ds_read_b64_tr_b16 v[56:57], v167 offset:256\n"
"ds_read_b64_tr_b16 v[58:59], v167 offset:8448\n"
"v_mfma_f32_16x16x32_bf16 a[204:207], v[8:11], a[20:23], a[204:207]\n"
"s_waitcnt lgkmcnt(14)\n"
"ds_read_b64_tr_b16 v[60:61], v167 offset:16640\n"
"ds_read_b64_tr_b16 v[62:63], v167 offset:24832\n"
"v_mfma_f32_16x16x32_bf16 a[208:211], v[16:19], a[16:19], a[208:211]\n"
"buffer_load_dwordx4 v213, s[24:27], s49 offen offset:1024 lds\n"
"v_mfma_f32_16x16x32_bf16 a[208:211], v[20:23], a[24:27], a[208:211]\n"
"v_accvgpr_read_b32 v170, a90\n"
"v_accvgpr_read_b32 v171, a91\n"
"v_mfma_f32_16x16x32_bf16 a[212:215], v[20:23], a[28:31], a[212:215]\n"
"s_add_u32 s54, s50, s47\n"
"buffer_load_dwordx4 a[88:91], v191, s[20:23], s86 offen offset:2048 sc1\n"
"v_mfma_f32_16x16x32_bf16 a[212:215], v[16:19], a[20:23], a[212:215]\n"
"s_add_u32 s50, s50, 32768\n"
"v_accvgpr_read_b32 v172, a92\n"
"v_accvgpr_read_b32 v173, a93\n"
"v_mfma_f32_16x16x32_bf16 a[216:219], v[24:27], a[16:19], a[216:219]\n"
"s_cmp_lt_u32 s50, 163840\n"
"s_cselect_b32 s50, s50, 0\n"
"v_accvgpr_read_b32 v174, a94\n"
"v_mfma_f32_16x16x32_bf16 a[216:219], v[28:31], a[24:27], a[216:219]\n"
"v_add_u32 v160, s50, v192\n"
"v_add_u32 v161, s50, v193\n"
"v_mfma_f32_16x16x32_bf16 a[220:223], v[28:31], a[28:31], a[220:223]\n"
"s_waitcnt lgkmcnt(0)\n"
"v_add_u32 v162, s50, v194\n"
"v_add_u32 v163, s50, v195\n"
"v_mfma_f32_16x16x32_bf16 a[220:223], v[24:27], a[20:23], a[220:223]\n"
"s_waitcnt vmcnt(47)\n"
"s_barrier\n"
"v_mfma_f32_16x16x32_bf16 a[224:227], v[32:35], a[16:19], a[224:227]\n"
"ds_read_b64_tr_b16 v[0:1], v160 offset:0\n"
"v_add_u32 v164, s50, v196\n"
"v_mfma_f32_16x16x32_bf16 a[224:227], v[36:39], a[24:27], a[224:227]\n"
"ds_read_b64_tr_b16 v[2:3], v160 offset:8192\n"
"v_add_u32 v165, s50, v197\n"
"v_mfma_f32_16x16x32_bf16 a[228:231], v[36:39], a[28:31], a[228:231]\n"
"ds_read_b64_tr_b16 v[4:5], v160 offset:16384\n"
"v_add_u32 v166, s50, v198\n"
"v_mfma_f32_16x16x32_bf16 a[228:231], v[32:35], a[20:23], a[228:231]\n"
"ds_read_b64_tr_b16 v[6:7], v160 offset:24576\n"
"ds_read_b64_tr_b16 v[8:9], v161 offset:0\n"
"v_add_u32 v167, s50, v199\n"
"v_mfma_f32_16x16x32_bf16 a[232:235], v[40:43], a[16:19], a[232:235]\n"
"v_accvgpr_read_b32 v175, a95\n"
"v_cvt_pk_bf16_f32 v252, v168, v169\n"
"v_mfma_f32_16x16x32_bf16 a[232:235], v[44:47], a[24:27], a[232:235]\n"
"ds_read_b64_tr_b16 v[10:11], v161 offset:8192\n"
"buffer_load_dwordx4 a[92:95], v191, s[20:23], s86 offen offset:3072 sc1\n"
"v_mfma_f32_16x16x32_bf16 a[236:239], v[44:47], a[28:31], a[236:239]\n"
"ds_read_b64_tr_b16 v[12:13], v161 offset:16384\n"
"v_cvt_pk_bf16_f32 v253, v170, v171\n"
"v_mfma_f32_16x16x32_bf16 a[236:239], v[40:43], a[20:23], a[236:239]\n"
"ds_read_b64_tr_b16 v[14:15], v161 offset:24576\n"
"v_cvt_pk_bf16_f32 v254, v172, v173\n"
"v_mfma_f32_16x16x32_bf16 a[240:243], v[48:51], a[16:19], a[240:243]\n"
"ds_read_b64_tr_b16 v[16:17], v162 offset:0\n"
"v_cvt_pk_bf16_f32 v255, v174, v175\n"
"v_mfma_f32_16x16x32_bf16 a[240:243], v[52:55], a[24:27], a[240:243]\n"
"ds_read_b64_tr_b16 v[18:19], v162 offset:8192\n"
"buffer_store_dwordx4 v[252:255], v227, s[20:23], s85 offen offset:3072 sc1\n"
"v_mfma_f32_16x16x32_bf16 a[244:247], v[52:55], a[28:31], a[244:247]\n"
"ds_read_b64_tr_b16 v[20:21], v162 offset:16384\n"
"buffer_load_dwordx4 v214, s[24:27], s49 offen offset:2048 lds\n"
"v_mfma_f32_16x16x32_bf16 a[244:247], v[48:51], a[20:23], a[244:247]\n"
"ds_read_b64_tr_b16 v[22:23], v162 offset:24576\n"
"ds_read_b64_tr_b16 v[24:25], v163 offset:0\n"
"v_mfma_f32_16x16x32_bf16 a[248:251], v[56:59], a[16:19], a[248:251]\n"
"buffer_load_dwordx4 v215, s[24:27], s49 offen offset:3072 lds\n"
"v_mfma_f32_16x16x32_bf16 a[248:251], v[60:63], a[24:27], a[248:251]\n"
"ds_read_b64_tr_b16 v[26:27], v163 offset:8192\n"
"buffer_store_dwordx4 v[76:79], v227, s[20:23], s84 offen offset:3072 sc1\n"
"v_mfma_f32_16x16x32_bf16 a[252:255], v[60:63], a[28:31], a[252:255]\n"
"s_mov_b32 m0, s54\n"
"ds_read_b64_tr_b16 v[28:29], v163 offset:16384\n"
"v_mfma_f32_16x16x32_bf16 a[252:255], v[56:59], a[20:23], a[252:255]\n"
"s_waitcnt lgkmcnt(7)\n"
"ds_read_b64_tr_b16 v[30:31], v163 offset:24576\n"
"v_mfma_f32_16x16x32_bf16 a[128:131], v[0:3], a[32:35], a[128:131]\n"
"ds_read_b64_tr_b16 v[32:33], v164 offset:0\n"
"v_readlane_b32 s24, v224, s48\n"
"v_mfma_f32_16x16x32_bf16 a[128:131], v[4:7], a[40:43], a[128:131]\n"
"ds_read_b64_tr_b16 v[34:35], v164 offset:8192\n"
"v_readlane_b32 s25, v225, s48\n"
"v_mfma_f32_16x16x32_bf16 a[132:135], v[4:7], a[44:47], a[132:135]\n"
"ds_read_b64_tr_b16 v[36:37], v164 offset:16384\n"
"v_readlane_b32 s26, v226, s48\n"
"v_mfma_f32_16x16x32_bf16 a[132:135], v[0:3], a[36:39], a[132:135]\n"
"s_add_u32 s86, s36, 24576\n"
"ds_read_b64_tr_b16 v[38:39], v164 offset:24576\n"
"ds_read_b64_tr_b16 v[40:41], v165 offset:0\n"
"v_mfma_f32_16x16x32_bf16 a[136:139], v[8:11], a[32:35], a[136:139]\n"
"s_add_u32 s85, s85, s81\n"
"v_accvgpr_read_b32 v168, a96\n"
"v_accvgpr_read_b32 v169, a97\n"
"v_mfma_f32_16x16x32_bf16 a[136:139], v[12:15], a[40:43], a[136:139]\n"
"s_add_u32 s48, s48, 1\n"
"ds_read_b64_tr_b16 v[42:43], v165 offset:8192\n"
"v_accvgpr_read_b32 v170, a98\n"
"v_mfma_f32_16x16x32_bf16 a[140:143], v[12:15], a[44:47], a[140:143]\n"
"ds_read_b64_tr_b16 v[44:45], v165 offset:16384\n"
"v_accvgpr_read_b32 v171, a99\n"
"v_mfma_f32_16x16x32_bf16 a[140:143], v[8:11], a[36:39], a[140:143]\n"
"s_waitcnt lgkmcnt(7)\n"
"ds_read_b64_tr_b16 v[46:47], v165 offset:24576\n"
"buffer_load_dwordx4 a[96:99], v191, s[20:23], s86 offen offset:0 sc1\n"
"v_mfma_f32_16x16x32_bf16 a[144:147], v[16:19], a[32:35], a[144:147]\n"
"ds_read_b64_tr_b16 v[48:49], v166 offset:0\n"
"v_accvgpr_read_b32 v172, a100\n"
"v_mfma_f32_16x16x32_bf16 a[144:147], v[20:23], a[40:43], a[144:147]\n"
"ds_read_b64_tr_b16 v[50:51], v166 offset:8192\n"
"v_accvgpr_read_b32 v173, a101\n"
"v_mfma_f32_16x16x32_bf16 a[148:151], v[20:23], a[44:47], a[148:151]\n"
"ds_read_b64_tr_b16 v[52:53], v166 offset:16384\n"
"v_accvgpr_read_b32 v174, a102\n"
"v_mfma_f32_16x16x32_bf16 a[148:151], v[16:19], a[36:39], a[148:151]\n"
"ds_read_b64_tr_b16 v[54:55], v166 offset:24576\n"
"ds_read_b64_tr_b16 v[56:57], v167 offset:0\n"
"v_mfma_f32_16x16x32_bf16 a[152:155], v[24:27], a[32:35], a[152:155]\n"
"v_accvgpr_read_b32 v175, a103\n"
"v_cvt_pk_bf16_f32 v240, v168, v169\n"
"v_mfma_f32_16x16x32_bf16 a[152:155], v[28:31], a[40:43], a[152:155]\n"
"ds_read_b64_tr_b16 v[58:59], v167 offset:8192\n"
"buffer_load_dwordx4 a[100:103], v191, s[20:23], s86 offen offset:1024 sc1\n"
"v_mfma_f32_16x16x32_bf16 a[156:159], v[28:31], a[44:47], a[156:159]\n"
"ds_read_b64_tr_b16 v[60:61], v167 offset:16384\n"
"v_cvt_pk_bf16_f32 v241, v170, v171\n"
"v_mfma_f32_16x16x32_bf16 a[156:159], v[24:27], a[36:39], a[156:159]\n"
"s_waitcnt lgkmcnt(7)\n"
"ds_read_b64_tr_b16 v[62:63], v167 offset:24576\n"
"v_cvt_pk_bf16_f32 v242, v172, v173\n"
"v_mfma_f32_16x16x32_bf16 a[160:163], v[32:35], a[32:35], a[160:163]\n"
"s_add_u32 s84, s84, s81\n"
"ds_read_b64_tr_b16 v[0:1], v160 offset:256\n"
"v_cvt_pk_bf16_f32 v243, v174, v175\n"
"v_mfma_f32_16x16x32_bf16 a[160:163], v[36:39], a[40:43], a[160:163]\n"
"ds_read_b64_tr_b16 v[2:3], v160 offset:8448\n"
"buffer_store_dwordx4 v[240:243], v227, s[20:23], s85 offen offset:0 sc1\n"
"v_mfma_f32_16x16x32_bf16 a[164:167], v[36:39], a[44:47], a[164:167]\n"
"ds_read_b64_tr_b16 v[4:5], v160 offset:16640\n"
"buffer_load_dwordx4 v208, s[24:27], s49 offen offset:0 lds\n"
"v_mfma_f32_16x16x32_bf16 a[164:167], v[32:35], a[36:39], a[164:167]\n"
"ds_read_b64_tr_b16 v[6:7], v160 offset:24832\n"
"ds_read_b64_tr_b16 v[8:9], v161 offset:256\n"
"v_mfma_f32_16x16x32_bf16 a[168:171], v[40:43], a[32:35], a[168:171]\n"
"buffer_load_dwordx4 v209, s[24:27], s49 offen offset:1024 lds\n"
"v_mfma_f32_16x16x32_bf16 a[168:171], v[44:47], a[40:43], a[168:171]\n"
"ds_read_b64_tr_b16 v[10:11], v161 offset:8448\n"
"buffer_load_dwordx4 v210, s[24:27], s49 offen offset:2048 lds\n"
"v_mfma_f32_16x16x32_bf16 a[172:175], v[44:47], a[44:47], a[172:175]\n"
"ds_read_b64_tr_b16 v[12:13], v161 offset:16640\n"
"buffer_store_dwordx4 v[80:83], v227, s[20:23], s84 offen offset:0 sc1\n"
"v_mfma_f32_16x16x32_bf16 a[172:175], v[40:43], a[36:39], a[172:175]\n"
"s_waitcnt lgkmcnt(7)\n"
"ds_read_b64_tr_b16 v[14:15], v161 offset:24832\n"
"v_mfma_f32_16x16x32_bf16 a[176:179], v[48:51], a[32:35], a[176:179]\n"
"ds_read_b64_tr_b16 v[16:17], v162 offset:256\n"
"buffer_load_dwordx4 v211, s[24:27], s49 offen offset:3072 lds\n"
"v_mfma_f32_16x16x32_bf16 a[176:179], v[52:55], a[40:43], a[176:179]\n"
"s_add_u32 m0, m0, 4096\n"
"ds_read_b64_tr_b16 v[18:19], v162 offset:8448\n"
"v_mfma_f32_16x16x32_bf16 a[180:183], v[52:55], a[44:47], a[180:183]\n"
"ds_read_b64_tr_b16 v[20:21], v162 offset:16640\n"
"v_mfma_f32_16x16x32_bf16 a[180:183], v[48:51], a[36:39], a[180:183]\n"
"ds_read_b64_tr_b16 v[22:23], v162 offset:24832\n"
"ds_read_b64_tr_b16 v[24:25], v163 offset:256\n"
"v_mfma_f32_16x16x32_bf16 a[184:187], v[56:59], a[32:35], a[184:187]\n"
"buffer_load_dwordx4 v212, s[24:27], s49 offen offset:0 lds\n"
"v_mfma_f32_16x16x32_bf16 a[184:187], v[60:63], a[40:43], a[184:187]\n"
"ds_read_b64_tr_b16 v[26:27], v163 offset:8448\n"
"v_mfma_f32_16x16x32_bf16 a[188:191], v[60:63], a[44:47], a[188:191]\n"
"ds_read_b64_tr_b16 v[28:29], v163 offset:16640\n"
"v_accvgpr_read_b32 v168, a104\n"
"v_mfma_f32_16x16x32_bf16 a[188:191], v[56:59], a[36:39], a[188:191]\n"
"s_waitcnt lgkmcnt(7)\n"
"ds_read_b64_tr_b16 v[30:31], v163 offset:24832\n"
"v_accvgpr_read_b32 v169, a105\n"
"v_mfma_f32_16x16x32_bf16 a[192:195], v[0:3], a[32:35], a[192:195]\n"
"ds_read_b64_tr_b16 v[32:33], v164 offset:256\n"
"ds_read_b64_tr_b16 v[34:35], v164 offset:8448\n"
"v_mfma_f32_16x16x32_bf16 a[192:195], v[4:7], a[40:43], a[192:195]\n"
"ds_read_b64_tr_b16 v[36:37], v164 offset:16640\n"
"ds_read_b64_tr_b16 v[38:39], v164 offset:24832\n"
"v_mfma_f32_16x16x32_bf16 a[196:199], v[4:7], a[44:47], a[196:199]\n"
"ds_read_b64_tr_b16 v[40:41], v165 offset:256\n"
"ds_read_b64_tr_b16 v[42:43], v165 offset:8448\n"
"v_mfma_f32_16x16x32_bf16 a[196:199], v[0:3], a[36:39], a[196:199]\n"
"ds_read_b64_tr_b16 v[44:45], v165 offset:16640\n"
"ds_read_b64_tr_b16 v[46:47], v165 offset:24832\n"
"v_mfma_f32_16x16x32_bf16 a[200:203], v[8:11], a[32:35], a[200:203]\n"
"ds_read_b64_tr_b16 v[48:49], v166 offset:256\n"
"ds_read_b64_tr_b16 v[50:51], v166 offset:8448\n"
"v_mfma_f32_16x16x32_bf16 a[200:203], v[12:15], a[40:43], a[200:203]\n"
"ds_read_b64_tr_b16 v[52:53], v166 offset:16640\n"
"ds_read_b64_tr_b16 v[54:55], v166 offset:24832\n"
"v_mfma_f32_16x16x32_bf16 a[204:207], v[12:15], a[44:47], a[204:207]\n"
"ds_read_b64_tr_b16 v[56:57], v167 offset:256\n"
"ds_read_b64_tr_b16 v[58:59], v167 offset:8448\n"
"v_mfma_f32_16x16x32_bf16 a[204:207], v[8:11], a[36:39], a[204:207]\n"
"s_waitcnt lgkmcnt(14)\n"
"ds_read_b64_tr_b16 v[60:61], v167 offset:16640\n"
"ds_read_b64_tr_b16 v[62:63], v167 offset:24832\n"
"v_mfma_f32_16x16x32_bf16 a[208:211], v[16:19], a[32:35], a[208:211]\n"
"buffer_load_dwordx4 v213, s[24:27], s49 offen offset:1024 lds\n"
"v_mfma_f32_16x16x32_bf16 a[208:211], v[20:23], a[40:43], a[208:211]\n"
"v_accvgpr_read_b32 v170, a106\n"
"v_accvgpr_read_b32 v171, a107\n"
"v_mfma_f32_16x16x32_bf16 a[212:215], v[20:23], a[44:47], a[212:215]\n"
"s_add_u32 s54, s50, s47\n"
"buffer_load_dwordx4 a[104:107], v191, s[20:23], s86 offen offset:2048 sc1\n"
"v_mfma_f32_16x16x32_bf16 a[212:215], v[16:19], a[36:39], a[212:215]\n"
"s_add_u32 s50, s50, 32768\n"
"v_accvgpr_read_b32 v172, a108\n"
"v_accvgpr_read_b32 v173, a109\n"
"v_mfma_f32_16x16x32_bf16 a[216:219], v[24:27], a[32:35], a[216:219]\n"
"s_cmp_lt_u32 s50, 163840\n"
"s_cselect_b32 s50, s50, 0\n"
"v_accvgpr_read_b32 v174, a110\n"
"v_mfma_f32_16x16x32_bf16 a[216:219], v[28:31], a[40:43], a[216:219]\n"
"v_add_u32 v160, s50, v192\n"
"v_add_u32 v161, s50, v193\n"
"v_mfma_f32_16x16x32_bf16 a[220:223], v[28:31], a[44:47], a[220:223]\n"
"s_waitcnt lgkmcnt(0)\n"
"v_add_u32 v162, s50, v194\n"
"v_add_u32 v163, s50, v195\n"
"v_mfma_f32_16x16x32_bf16 a[220:223], v[24:27], a[36:39], a[220:223]\n"
"s_waitcnt vmcnt(50)\n"
"s_barrier\n"
"v_mfma_f32_16x16x32_bf16 a[224:227], v[32:35], a[32:35], a[224:227]\n"
"ds_read_b64_tr_b16 v[0:1], v160 offset:0\n"
"v_add_u32 v164, s50, v196\n"
"v_mfma_f32_16x16x32_bf16 a[224:227], v[36:39], a[40:43], a[224:227]\n"
"ds_read_b64_tr_b16 v[2:3], v160 offset:8192\n"
"v_add_u32 v165, s50, v197\n"
"v_mfma_f32_16x16x32_bf16 a[228:231], v[36:39], a[44:47], a[228:231]\n"
"ds_read_b64_tr_b16 v[4:5], v160 offset:16384\n"
"v_add_u32 v166, s50, v198\n"
"v_mfma_f32_16x16x32_bf16 a[228:231], v[32:35], a[36:39], a[228:231]\n"
"ds_read_b64_tr_b16 v[6:7], v160 offset:24576\n"
"ds_read_b64_tr_b16 v[8:9], v161 offset:0\n"
"v_add_u32 v167, s50, v199\n"
"v_mfma_f32_16x16x32_bf16 a[232:235], v[40:43], a[32:35], a[232:235]\n"
"v_accvgpr_read_b32 v175, a111\n"
"v_cvt_pk_bf16_f32 v244, v168, v169\n"
"v_mfma_f32_16x16x32_bf16 a[232:235], v[44:47], a[40:43], a[232:235]\n"
"ds_read_b64_tr_b16 v[10:11], v161 offset:8192\n"
"buffer_load_dwordx4 a[108:111], v191, s[20:23], s86 offen offset:3072 sc1\n"
"v_mfma_f32_16x16x32_bf16 a[236:239], v[44:47], a[44:47], a[236:239]\n"
"ds_read_b64_tr_b16 v[12:13], v161 offset:16384\n"
"v_cvt_pk_bf16_f32 v245, v170, v171\n"
"v_mfma_f32_16x16x32_bf16 a[236:239], v[40:43], a[36:39], a[236:239]\n"
"ds_read_b64_tr_b16 v[14:15], v161 offset:24576\n"
"v_cvt_pk_bf16_f32 v246, v172, v173\n"
"v_mfma_f32_16x16x32_bf16 a[240:243], v[48:51], a[32:35], a[240:243]\n"
"ds_read_b64_tr_b16 v[16:17], v162 offset:0\n"
"v_cvt_pk_bf16_f32 v247, v174, v175\n"
"v_mfma_f32_16x16x32_bf16 a[240:243], v[52:55], a[40:43], a[240:243]\n"
"ds_read_b64_tr_b16 v[18:19], v162 offset:8192\n"
"buffer_store_dwordx4 v[244:247], v227, s[20:23], s85 offen offset:1024 sc1\n"
"v_mfma_f32_16x16x32_bf16 a[244:247], v[52:55], a[44:47], a[244:247]\n"
"ds_read_b64_tr_b16 v[20:21], v162 offset:16384\n"
"buffer_load_dwordx4 v214, s[24:27], s49 offen offset:2048 lds\n"
"v_mfma_f32_16x16x32_bf16 a[244:247], v[48:51], a[36:39], a[244:247]\n"
"ds_read_b64_tr_b16 v[22:23], v162 offset:24576\n"
"ds_read_b64_tr_b16 v[24:25], v163 offset:0\n"
"v_mfma_f32_16x16x32_bf16 a[248:251], v[56:59], a[32:35], a[248:251]\n"
"buffer_load_dwordx4 v215, s[24:27], s49 offen offset:3072 lds\n"
"v_mfma_f32_16x16x32_bf16 a[248:251], v[60:63], a[40:43], a[248:251]\n"
"ds_read_b64_tr_b16 v[26:27], v163 offset:8192\n"
"buffer_store_dwordx4 v[84:87], v227, s[20:23], s84 offen offset:1024 sc1\n"
"v_mfma_f32_16x16x32_bf16 a[252:255], v[60:63], a[44:47], a[252:255]\n"
"s_mov_b32 m0, s54\n"
"ds_read_b64_tr_b16 v[28:29], v163 offset:16384\n"
"v_mov_b32 v208, v184\n"
"v_mfma_f32_16x16x32_bf16 a[252:255], v[56:59], a[36:39], a[252:255]\n"
"s_waitcnt lgkmcnt(7)\n"
"ds_read_b64_tr_b16 v[30:31], v163 offset:24576\n"
"v_mov_b32 v209, v185\n"
"v_mov_b32 v210, v186\n"
"v_mov_b32 v211, v187\n"
"v_mov_b32 v212, v188\n"
"v_mov_b32 v213, v189\n"
"v_mov_b32 v214, v190\n"
"v_mov_b32 v215, 0x7f000000\n"
"v_mfma_f32_16x16x32_bf16 a[128:131], v[0:3], a[48:51], a[128:131]\n"
"ds_read_b64_tr_b16 v[32:33], v164 offset:0\n"
"v_readlane_b32 s24, v224, s48\n"
"v_mfma_f32_16x16x32_bf16 a[128:131], v[4:7], a[56:59], a[128:131]\n"
"ds_read_b64_tr_b16 v[34:35], v164 offset:8192\n"
"v_readlane_b32 s25, v225, s48\n"
"v_mfma_f32_16x16x32_bf16 a[132:135], v[4:7], a[60:63], a[132:135]\n"
"ds_read_b64_tr_b16 v[36:37], v164 offset:16384\n"
"v_readlane_b32 s26, v226, s48\n"
"v_mfma_f32_16x16x32_bf16 a[132:135], v[0:3], a[52:55], a[132:135]\n"
"s_add_u32 s48, s48, 1\n"
"ds_read_b64_tr_b16 v[38:39], v164 offset:24576\n"
"ds_read_b64_tr_b16 v[40:41], v165 offset:0\n"
"v_mfma_f32_16x16x32_bf16 a[136:139], v[8:11], a[48:51], a[136:139]\n"
"v_accvgpr_read_b32 v168, a112\n"
"v_accvgpr_read_b32 v169, a113\n"
"v_mfma_f32_16x16x32_bf16 a[136:139], v[12:15], a[56:59], a[136:139]\n"
"ds_read_b64_tr_b16 v[42:43], v165 offset:8192\n"
"v_accvgpr_read_b32 v170, a114\n"
"v_mfma_f32_16x16x32_bf16 a[140:143], v[12:15], a[60:63], a[140:143]\n"
"ds_read_b64_tr_b16 v[44:45], v165 offset:16384\n"
"v_accvgpr_read_b32 v171, a115\n"
"v_mfma_f32_16x16x32_bf16 a[140:143], v[8:11], a[52:55], a[140:143]\n"
"s_waitcnt lgkmcnt(7)\n"
"ds_read_b64_tr_b16 v[46:47], v165 offset:24576\n"
"v_accvgpr_read_b32 v172, a116\n"
"v_mfma_f32_16x16x32_bf16 a[144:147], v[16:19], a[48:51], a[144:147]\n"
"ds_read_b64_tr_b16 v[48:49], v166 offset:0\n"
"v_accvgpr_read_b32 v173, a117\n"
"v_mfma_f32_16x16x32_bf16 a[144:147], v[20:23], a[56:59], a[144:147]\n"
"ds_read_b64_tr_b16 v[50:51], v166 offset:8192\n"
"v_accvgpr_read_b32 v174, a118\n"
"v_mfma_f32_16x16x32_bf16 a[148:151], v[20:23], a[60:63], a[148:151]\n"
"ds_read_b64_tr_b16 v[52:53], v166 offset:16384\n"
"v_accvgpr_read_b32 v175, a119\n"
"v_mfma_f32_16x16x32_bf16 a[148:151], v[16:19], a[52:55], a[148:151]\n"
"ds_read_b64_tr_b16 v[54:55], v166 offset:24576\n"
"ds_read_b64_tr_b16 v[56:57], v167 offset:0\n"
"v_mfma_f32_16x16x32_bf16 a[152:155], v[24:27], a[48:51], a[152:155]\n"
"v_cvt_pk_bf16_f32 v248, v168, v169\n"
"v_cvt_pk_bf16_f32 v249, v170, v171\n"
"v_mfma_f32_16x16x32_bf16 a[152:155], v[28:31], a[56:59], a[152:155]\n"
"ds_read_b64_tr_b16 v[58:59], v167 offset:8192\n"
"v_cvt_pk_bf16_f32 v250, v172, v173\n"
"v_mfma_f32_16x16x32_bf16 a[156:159], v[28:31], a[60:63], a[156:159]\n"
"ds_read_b64_tr_b16 v[60:61], v167 offset:16384\n"
"v_cvt_pk_bf16_f32 v251, v174, v175\n"
"v_mfma_f32_16x16x32_bf16 a[156:159], v[24:27], a[52:55], a[156:159]\n"
"s_waitcnt lgkmcnt(7)\n"
"ds_read_b64_tr_b16 v[62:63], v167 offset:24576\n"
"buffer_store_dwordx4 v[248:251], v227, s[20:23], s85 offen offset:2048 sc1\n"
"v_mfma_f32_16x16x32_bf16 a[160:163], v[32:35], a[48:51], a[160:163]\n"
"ds_read_b64_tr_b16 v[0:1], v160 offset:256\n"
"buffer_load_dwordx4 v208, s[24:27], s49 offen offset:0 lds\n"
"v_mfma_f32_16x16x32_bf16 a[160:163], v[36:39], a[56:59], a[160:163]\n"
"ds_read_b64_tr_b16 v[2:3], v160 offset:8448\n"
"buffer_load_dwordx4 v209, s[24:27], s49 offen offset:1024 lds\n"
"v_mfma_f32_16x16x32_bf16 a[164:167], v[36:39], a[60:63], a[164:167]\n"
"ds_read_b64_tr_b16 v[4:5], v160 offset:16640\n"
"v_mfma_f32_16x16x32_bf16 a[164:167], v[32:35], a[52:55], a[164:167]\n"
"ds_read_b64_tr_b16 v[6:7], v160 offset:24832\n"
"ds_read_b64_tr_b16 v[8:9], v161 offset:256\n"
"v_mfma_f32_16x16x32_bf16 a[168:171], v[40:43], a[48:51], a[168:171]\n"
"buffer_load_dwordx4 v210, s[24:27], s49 offen offset:2048 lds\n"
"v_mfma_f32_16x16x32_bf16 a[168:171], v[44:47], a[56:59], a[168:171]\n"
"ds_read_b64_tr_b16 v[10:11], v161 offset:8448\n"
"buffer_store_dwordx4 v[88:91], v227, s[20:23], s84 offen offset:2048 sc1\n"
"v_mfma_f32_16x16x32_bf16 a[172:175], v[44:47], a[60:63], a[172:175]\n"
"ds_read_b64_tr_b16 v[12:13], v161 offset:16640\n"
"v_mfma_f32_16x16x32_bf16 a[172:175], v[40:43], a[52:55], a[172:175]\n"
"s_waitcnt lgkmcnt(7)\n"
"ds_read_b64_tr_b16 v[14:15], v161 offset:24832\n"
"v_mfma_f32_16x16x32_bf16 a[176:179], v[48:51], a[48:51], a[176:179]\n"
"ds_read_b64_tr_b16 v[16:17], v162 offset:256\n"
"buffer_load_dwordx4 v211, s[24:27], s49 offen offset:3072 lds\n"
"v_mfma_f32_16x16x32_bf16 a[176:179], v[52:55], a[56:59], a[176:179]\n"
"s_add_u32 m0, m0, 4096\n"
"ds_read_b64_tr_b16 v[18:19], v162 offset:8448\n"
"v_mfma_f32_16x16x32_bf16 a[180:183], v[52:55], a[60:63], a[180:183]\n"
"ds_read_b64_tr_b16 v[20:21], v162 offset:16640\n"
"v_mfma_f32_16x16x32_bf16 a[180:183], v[48:51], a[52:55], a[180:183]\n"
"ds_read_b64_tr_b16 v[22:23], v162 offset:24832\n"
"ds_read_b64_tr_b16 v[24:25], v163 offset:256\n"
"v_mfma_f32_16x16x32_bf16 a[184:187], v[56:59], a[48:51], a[184:187]\n"
"buffer_load_dwordx4 v212, s[24:27], s49 offen offset:0 lds\n"
"v_mfma_f32_16x16x32_bf16 a[184:187], v[60:63], a[56:59], a[184:187]\n"
"ds_read_b64_tr_b16 v[26:27], v163 offset:8448\n"
"v_mfma_f32_16x16x32_bf16 a[188:191], v[60:63], a[60:63], a[188:191]\n"
"ds_read_b64_tr_b16 v[28:29], v163 offset:16640\n"
"v_accvgpr_read_b32 v168, a120\n"
"v_mfma_f32_16x16x32_bf16 a[188:191], v[56:59], a[52:55], a[188:191]\n"
"s_waitcnt lgkmcnt(7)\n"
"ds_read_b64_tr_b16 v[30:31], v163 offset:24832\n"
"v_accvgpr_read_b32 v169, a121\n"
"v_mfma_f32_16x16x32_bf16 a[192:195], v[0:3], a[48:51], a[192:195]\n"
"ds_read_b64_tr_b16 v[32:33], v164 offset:256\n"
"ds_read_b64_tr_b16 v[34:35], v164 offset:8448\n"
"v_mfma_f32_16x16x32_bf16 a[192:195], v[4:7], a[56:59], a[192:195]\n"
"ds_read_b64_tr_b16 v[36:37], v164 offset:16640\n"
"ds_read_b64_tr_b16 v[38:39], v164 offset:24832\n"
"v_mfma_f32_16x16x32_bf16 a[196:199], v[4:7], a[60:63], a[196:199]\n"
"ds_read_b64_tr_b16 v[40:41], v165 offset:256\n"
"ds_read_b64_tr_b16 v[42:43], v165 offset:8448\n"
"v_mfma_f32_16x16x32_bf16 a[196:199], v[0:3], a[52:55], a[196:199]\n"
"ds_read_b64_tr_b16 v[44:45], v165 offset:16640\n"
"ds_read_b64_tr_b16 v[46:47], v165 offset:24832\n"
"v_mfma_f32_16x16x32_bf16 a[200:203], v[8:11], a[48:51], a[200:203]\n"
"ds_read_b64_tr_b16 v[48:49], v166 offset:256\n"
"ds_read_b64_tr_b16 v[50:51], v166 offset:8448\n"
"v_mfma_f32_16x16x32_bf16 a[200:203], v[12:15], a[56:59], a[200:203]\n"
"ds_read_b64_tr_b16 v[52:53], v166 offset:16640\n"
"ds_read_b64_tr_b16 v[54:55], v166 offset:24832\n"
"v_mfma_f32_16x16x32_bf16 a[204:207], v[12:15], a[60:63], a[204:207]\n"
"ds_read_b64_tr_b16 v[56:57], v167 offset:256\n"
"ds_read_b64_tr_b16 v[58:59], v167 offset:8448\n"
"v_mfma_f32_16x16x32_bf16 a[204:207], v[8:11], a[52:55], a[204:207]\n"
"s_waitcnt lgkmcnt(14)\n"
"ds_read_b64_tr_b16 v[60:61], v167 offset:16640\n"
"ds_read_b64_tr_b16 v[62:63], v167 offset:24832\n"
"v_mfma_f32_16x16x32_bf16 a[208:211], v[16:19], a[48:51], a[208:211]\n"
"buffer_load_dwordx4 v213, s[24:27], s49 offen offset:1024 lds\n"
"v_mfma_f32_16x16x32_bf16 a[208:211], v[20:23], a[56:59], a[208:211]\n"
"v_accvgpr_read_b32 v170, a122\n"
"v_accvgpr_read_b32 v171, a123\n"
"v_mfma_f32_16x16x32_bf16 a[212:215], v[20:23], a[60:63], a[212:215]\n"
"s_add_u32 s54, s50, s47\n"
"v_accvgpr_read_b32 v172, a124\n"
"v_accvgpr_read_b32 v173, a125\n"
"v_mfma_f32_16x16x32_bf16 a[212:215], v[16:19], a[52:55], a[212:215]\n"
"s_add_u32 s50, s50, 32768\n"
"v_accvgpr_read_b32 v174, a126\n"
"v_accvgpr_read_b32 v175, a127\n"
"v_mfma_f32_16x16x32_bf16 a[216:219], v[24:27], a[48:51], a[216:219]\n"
"s_cmp_lt_u32 s50, 163840\n"
"s_cselect_b32 s50, s50, 0\n"
"v_cvt_pk_bf16_f32 v252, v168, v169\n"
"v_mfma_f32_16x16x32_bf16 a[216:219], v[28:31], a[56:59], a[216:219]\n"
"v_add_u32 v160, s50, v192\n"
"v_add_u32 v161, s50, v193\n"
"v_mfma_f32_16x16x32_bf16 a[220:223], v[28:31], a[60:63], a[220:223]\n"
"s_waitcnt lgkmcnt(0)\n"
"v_add_u32 v162, s50, v194\n"
"v_add_u32 v163, s50, v195\n"
"v_mfma_f32_16x16x32_bf16 a[220:223], v[24:27], a[52:55], a[220:223]\n"
"s_waitcnt vmcnt(41)\n"
"s_barrier\n"
"v_mfma_f32_16x16x32_bf16 a[224:227], v[32:35], a[48:51], a[224:227]\n"
"ds_read_b64_tr_b16 v[0:1], v160 offset:0\n"
"v_add_u32 v164, s50, v196\n"
"v_mfma_f32_16x16x32_bf16 a[224:227], v[36:39], a[56:59], a[224:227]\n"
"ds_read_b64_tr_b16 v[2:3], v160 offset:8192\n"
"v_add_u32 v165, s50, v197\n"
"v_mfma_f32_16x16x32_bf16 a[228:231], v[36:39], a[60:63], a[228:231]\n"
"ds_read_b64_tr_b16 v[4:5], v160 offset:16384\n"
"v_add_u32 v166, s50, v198\n"
"v_mfma_f32_16x16x32_bf16 a[228:231], v[32:35], a[52:55], a[228:231]\n"
"ds_read_b64_tr_b16 v[6:7], v160 offset:24576\n"
"ds_read_b64_tr_b16 v[8:9], v161 offset:0\n"
"v_add_u32 v167, s50, v199\n"
"v_mfma_f32_16x16x32_bf16 a[232:235], v[40:43], a[48:51], a[232:235]\n"
"v_cvt_pk_bf16_f32 v253, v170, v171\n"
"v_cvt_pk_bf16_f32 v254, v172, v173\n"
"v_mfma_f32_16x16x32_bf16 a[232:235], v[44:47], a[56:59], a[232:235]\n"
"ds_read_b64_tr_b16 v[10:11], v161 offset:8192\n"
"v_cvt_pk_bf16_f32 v255, v174, v175\n"
"v_mfma_f32_16x16x32_bf16 a[236:239], v[44:47], a[60:63], a[236:239]\n"
"ds_read_b64_tr_b16 v[12:13], v161 offset:16384\n"
"buffer_store_dwordx4 v[252:255], v227, s[20:23], s85 offen offset:3072 sc1\n"
"v_mfma_f32_16x16x32_bf16 a[236:239], v[40:43], a[52:55], a[236:239]\n"
"ds_read_b64_tr_b16 v[14:15], v161 offset:24576\n"
"buffer_load_dwordx4 v214, s[24:27], s49 offen offset:2048 lds\n"
"v_mfma_f32_16x16x32_bf16 a[240:243], v[48:51], a[48:51], a[240:243]\n"
"ds_read_b64_tr_b16 v[16:17], v162 offset:0\n"
"buffer_load_dwordx4 v215, s[24:27], s49 offen offset:3072 lds\n"
"v_mfma_f32_16x16x32_bf16 a[240:243], v[52:55], a[56:59], a[240:243]\n"
"ds_read_b64_tr_b16 v[18:19], v162 offset:8192\n"
"buffer_store_dwordx4 v[92:95], v227, s[20:23], s84 offen offset:3072 sc1\n"
"v_mfma_f32_16x16x32_bf16 a[244:247], v[52:55], a[60:63], a[244:247]\n"
"s_mov_b32 m0, s54\n"
"ds_read_b64_tr_b16 v[20:21], v162 offset:16384\n"
"v_mfma_f32_16x16x32_bf16 a[244:247], v[48:51], a[52:55], a[244:247]\n"
"ds_read_b64_tr_b16 v[22:23], v162 offset:24576\n"
"ds_read_b64_tr_b16 v[24:25], v163 offset:0\n"
"v_mfma_f32_16x16x32_bf16 a[248:251], v[56:59], a[48:51], a[248:251]\n"
"v_mfma_f32_16x16x32_bf16 a[248:251], v[60:63], a[56:59], a[248:251]\n"
"ds_read_b64_tr_b16 v[26:27], v163 offset:8192\n"
"v_mfma_f32_16x16x32_bf16 a[252:255], v[60:63], a[60:63], a[252:255]\n"
"ds_read_b64_tr_b16 v[28:29], v163 offset:16384\n"
"v_mfma_f32_16x16x32_bf16 a[252:255], v[56:59], a[52:55], a[252:255]\n"
"s_waitcnt lgkmcnt(7)\n"
"ds_read_b64_tr_b16 v[30:31], v163 offset:24576\n"
"v_mfma_f32_16x16x32_bf16 a[128:131], v[0:3], a[64:67], a[128:131]\n"
"ds_read_b64_tr_b16 v[32:33], v164 offset:0\n"
"v_readlane_b32 s24, v224, s48\n"
"v_mfma_f32_16x16x32_bf16 a[128:131], v[4:7], a[72:75], a[128:131]\n"
"ds_read_b64_tr_b16 v[34:35], v164 offset:8192\n"
"v_readlane_b32 s25, v225, s48\n"
"v_mfma_f32_16x16x32_bf16 a[132:135], v[4:7], a[76:79], a[132:135]\n"
"ds_read_b64_tr_b16 v[36:37], v164 offset:16384\n"
"v_readlane_b32 s26, v226, s48\n"
"v_mfma_f32_16x16x32_bf16 a[132:135], v[0:3], a[68:71], a[132:135]\n"
"s_add_u32 s48, s48, 1\n"
"ds_read_b64_tr_b16 v[38:39], v164 offset:24576\n"
"ds_read_b64_tr_b16 v[40:41], v165 offset:0\n"
"v_mfma_f32_16x16x32_bf16 a[136:139], v[8:11], a[64:67], a[136:139]\n"
"s_add_u32 s84, s84, s81\n"
"v_lshlrev_b32 v64, 16, v128\n"
"v_mfma_f32_16x16x32_bf16 a[136:139], v[12:15], a[72:75], a[136:139]\n"
"ds_read_b64_tr_b16 v[42:43], v165 offset:8192\n"
"v_mfma_f32_16x16x32_bf16 a[140:143], v[12:15], a[76:79], a[140:143]\n"
"ds_read_b64_tr_b16 v[44:45], v165 offset:16384\n"
"v_and_b32 v65, 0xffff0000, v128\n"
"v_mfma_f32_16x16x32_bf16 a[140:143], v[8:11], a[68:71], a[140:143]\n"
"s_waitcnt lgkmcnt(7)\n"
"ds_read_b64_tr_b16 v[46:47], v165 offset:24576\n"
"v_mfma_f32_16x16x32_bf16 a[144:147], v[16:19], a[64:67], a[144:147]\n"
"ds_read_b64_tr_b16 v[48:49], v166 offset:0\n"
"buffer_store_dwordx4 v[96:99], v227, s[20:23], s84 offen offset:0 sc1\n"
"v_mfma_f32_16x16x32_bf16 a[144:147], v[20:23], a[72:75], a[144:147]\n"
"ds_read_b64_tr_b16 v[50:51], v166 offset:8192\n"
"buffer_load_dwordx4 v208, s[24:27], s49 offen offset:0 lds\n"
"v_mfma_f32_16x16x32_bf16 a[148:151], v[20:23], a[76:79], a[148:151]\n"
"ds_read_b64_tr_b16 v[52:53], v166 offset:16384\n"
"v_lshlrev_b32 v66, 16, v129\n"
"v_mfma_f32_16x16x32_bf16 a[148:151], v[16:19], a[68:71], a[148:151]\n"
"ds_read_b64_tr_b16 v[54:55], v166 offset:24576\n"
"ds_read_b64_tr_b16 v[56:57], v167 offset:0\n"
"v_mfma_f32_16x16x32_bf16 a[152:155], v[24:27], a[64:67], a[152:155]\n"
"buffer_load_dwordx4 v209, s[24:27], s49 offen offset:1024 lds\n"
"v_mfma_f32_16x16x32_bf16 a[152:155], v[28:31], a[72:75], a[152:155]\n"
"ds_read_b64_tr_b16 v[58:59], v167 offset:8192\n"
"v_and_b32 v67, 0xffff0000, v129\n"
"v_mfma_f32_16x16x32_bf16 a[156:159], v[28:31], a[76:79], a[156:159]\n"
"ds_read_b64_tr_b16 v[60:61], v167 offset:16384\n"
"v_lshlrev_b32 v68, 16, v128\n"
"v_mfma_f32_16x16x32_bf16 a[156:159], v[24:27], a[68:71], a[156:159]\n"
"s_waitcnt lgkmcnt(7)\n"
"ds_read_b64_tr_b16 v[62:63], v167 offset:24576\n"
"v_and_b32 v69, 0xffff0000, v128\n"
"v_mfma_f32_16x16x32_bf16 a[160:163], v[32:35], a[64:67], a[160:163]\n"
"ds_read_b64_tr_b16 v[0:1], v160 offset:256\n"
"v_mfma_f32_16x16x32_bf16 a[160:163], v[36:39], a[72:75], a[160:163]\n"
"ds_read_b64_tr_b16 v[2:3], v160 offset:8448\n"
"v_lshlrev_b32 v70, 16, v129\n"
"v_mfma_f32_16x16x32_bf16 a[164:167], v[36:39], a[76:79], a[164:167]\n"
"ds_read_b64_tr_b16 v[4:5], v160 offset:16640\n"
"v_mfma_f32_16x16x32_bf16 a[164:167], v[32:35], a[68:71], a[164:167]\n"
"ds_read_b64_tr_b16 v[6:7], v160 offset:24832\n"
"ds_read_b64_tr_b16 v[8:9], v161 offset:256\n"
"v_mfma_f32_16x16x32_bf16 a[168:171], v[40:43], a[64:67], a[168:171]\n"
"buffer_load_dwordx4 v210, s[24:27], s49 offen offset:2048 lds\n"
"v_mfma_f32_16x16x32_bf16 a[168:171], v[44:47], a[72:75], a[168:171]\n"
"ds_read_b64_tr_b16 v[10:11], v161 offset:8448\n"
"v_and_b32 v71, 0xffff0000, v129\n"
"v_mfma_f32_16x16x32_bf16 a[172:175], v[44:47], a[76:79], a[172:175]\n"
"ds_read_b64_tr_b16 v[12:13], v161 offset:16640\n"
"buffer_store_dwordx4 v[100:103], v227, s[20:23], s84 offen offset:1024 sc1\n"
"v_mfma_f32_16x16x32_bf16 a[172:175], v[40:43], a[68:71], a[172:175]\n"
"s_waitcnt lgkmcnt(7)\n"
"ds_read_b64_tr_b16 v[14:15], v161 offset:24832\n"
"v_lshlrev_b32 v72, 16, v130\n"
"v_mfma_f32_16x16x32_bf16 a[176:179], v[48:51], a[64:67], a[176:179]\n"
"ds_read_b64_tr_b16 v[16:17], v162 offset:256\n"
"buffer_load_dwordx4 v211, s[24:27], s49 offen offset:3072 lds\n"
"v_mfma_f32_16x16x32_bf16 a[176:179], v[52:55], a[72:75], a[176:179]\n"
"s_add_u32 m0, m0, 4096\n"
"ds_read_b64_tr_b16 v[18:19], v162 offset:8448\n"
"v_and_b32 v73, 0xffff0000, v130\n"
"v_mfma_f32_16x16x32_bf16 a[180:183], v[52:55], a[76:79], a[180:183]\n"
"ds_read_b64_tr_b16 v[20:21], v162 offset:16640\n"
"v_lshlrev_b32 v74, 16, v131\n"
"v_mfma_f32_16x16x32_bf16 a[180:183], v[48:51], a[68:71], a[180:183]\n"
"ds_read_b64_tr_b16 v[22:23], v162 offset:24832\n"
"ds_read_b64_tr_b16 v[24:25], v163 offset:256\n"
"v_mfma_f32_16x16x32_bf16 a[184:187], v[56:59], a[64:67], a[184:187]\n"
"buffer_load_dwordx4 v212, s[24:27], s49 offen offset:0 lds\n"
"v_mfma_f32_16x16x32_bf16 a[184:187], v[60:63], a[72:75], a[184:187]\n"
"ds_read_b64_tr_b16 v[26:27], v163 offset:8448\n"
"v_and_b32 v75, 0xffff0000, v131\n"
"v_mfma_f32_16x16x32_bf16 a[188:191], v[60:63], a[76:79], a[188:191]\n"
"ds_read_b64_tr_b16 v[28:29], v163 offset:16640\n"
"v_mfma_f32_16x16x32_bf16 a[188:191], v[56:59], a[68:71], a[188:191]\n"
"s_waitcnt lgkmcnt(7)\n"
"ds_read_b64_tr_b16 v[30:31], v163 offset:24832\n"
"v_lshlrev_b32 v76, 16, v130\n"
"v_mfma_f32_16x16x32_bf16 a[192:195], v[0:3], a[64:67], a[192:195]\n"
"ds_read_b64_tr_b16 v[32:33], v164 offset:256\n"
"ds_read_b64_tr_b16 v[34:35], v164 offset:8448\n"
"v_mfma_f32_16x16x32_bf16 a[192:195], v[4:7], a[72:75], a[192:195]\n"
"ds_read_b64_tr_b16 v[36:37], v164 offset:16640\n"
"ds_read_b64_tr_b16 v[38:39], v164 offset:24832\n"
"v_mfma_f32_16x16x32_bf16 a[196:199], v[4:7], a[76:79], a[196:199]\n"
"ds_read_b64_tr_b16 v[40:41], v165 offset:256\n"
"ds_read_b64_tr_b16 v[42:43], v165 offset:8448\n"
"v_mfma_f32_16x16x32_bf16 a[196:199], v[0:3], a[68:71], a[196:199]\n"
"ds_read_b64_tr_b16 v[44:45], v165 offset:16640\n"
"ds_read_b64_tr_b16 v[46:47], v165 offset:24832\n"
"v_mfma_f32_16x16x32_bf16 a[200:203], v[8:11], a[64:67], a[200:203]\n"
"ds_read_b64_tr_b16 v[48:49], v166 offset:256\n"
"ds_read_b64_tr_b16 v[50:51], v166 offset:8448\n"
"v_mfma_f32_16x16x32_bf16 a[200:203], v[12:15], a[72:75], a[200:203]\n"
"ds_read_b64_tr_b16 v[52:53], v166 offset:16640\n"
"ds_read_b64_tr_b16 v[54:55], v166 offset:24832\n"
"v_mfma_f32_16x16x32_bf16 a[204:207], v[12:15], a[76:79], a[204:207]\n"
"ds_read_b64_tr_b16 v[56:57], v167 offset:256\n"
"ds_read_b64_tr_b16 v[58:59], v167 offset:8448\n"
"v_mfma_f32_16x16x32_bf16 a[204:207], v[8:11], a[68:71], a[204:207]\n"
"s_waitcnt lgkmcnt(14)\n"
"ds_read_b64_tr_b16 v[60:61], v167 offset:16640\n"
"ds_read_b64_tr_b16 v[62:63], v167 offset:24832\n"
"v_mfma_f32_16x16x32_bf16 a[208:211], v[16:19], a[64:67], a[208:211]\n"
"buffer_load_dwordx4 v213, s[24:27], s49 offen offset:1024 lds\n"
"v_mfma_f32_16x16x32_bf16 a[208:211], v[20:23], a[72:75], a[208:211]\n"
"buffer_store_dwordx4 v[104:107], v227, s[20:23], s84 offen offset:2048 sc1\n"
"v_mfma_f32_16x16x32_bf16 a[212:215], v[20:23], a[76:79], a[212:215]\n"
"s_add_u32 s54, s50, s47\n"
"buffer_load_dwordx4 v214, s[24:27], s49 offen offset:2048 lds\n"
"v_mfma_f32_16x16x32_bf16 a[212:215], v[16:19], a[68:71], a[212:215]\n"
"s_add_u32 s50, s50, 32768\n"
"v_and_b32 v77, 0xffff0000, v130\n"
"v_lshlrev_b32 v78, 16, v131\n"
"v_mfma_f32_16x16x32_bf16 a[216:219], v[24:27], a[64:67], a[216:219]\n"
"s_cmp_lt_u32 s50, 163840\n"
"s_cselect_b32 s50, s50, 0\n"
"v_and_b32 v79, 0xffff0000, v131\n"
"v_mfma_f32_16x16x32_bf16 a[216:219], v[28:31], a[72:75], a[216:219]\n"
"v_add_u32 v160, s50, v192\n"
"v_add_u32 v161, s50, v193\n"
"v_mfma_f32_16x16x32_bf16 a[220:223], v[28:31], a[76:79], a[220:223]\n"
"s_waitcnt lgkmcnt(0)\n"
"v_add_u32 v162, s50, v194\n"
"v_add_u32 v163, s50, v195\n"
"v_mfma_f32_16x16x32_bf16 a[220:223], v[24:27], a[68:71], a[220:223]\n"
"s_waitcnt vmcnt(39)\n"
"s_barrier\n"
"v_mfma_f32_16x16x32_bf16 a[224:227], v[32:35], a[64:67], a[224:227]\n"
"ds_read_b64_tr_b16 v[0:1], v160 offset:0\n"
"v_add_u32 v164, s50, v196\n"
"v_mfma_f32_16x16x32_bf16 a[224:227], v[36:39], a[72:75], a[224:227]\n"
"ds_read_b64_tr_b16 v[2:3], v160 offset:8192\n"
"v_add_u32 v165, s50, v197\n"
"v_mfma_f32_16x16x32_bf16 a[228:231], v[36:39], a[76:79], a[228:231]\n"
"ds_read_b64_tr_b16 v[4:5], v160 offset:16384\n"
"v_add_u32 v166, s50, v198\n"
"v_mfma_f32_16x16x32_bf16 a[228:231], v[32:35], a[68:71], a[228:231]\n"
"ds_read_b64_tr_b16 v[6:7], v160 offset:24576\n"
"ds_read_b64_tr_b16 v[8:9], v161 offset:0\n"
"v_add_u32 v167, s50, v199\n"
"v_mfma_f32_16x16x32_bf16 a[232:235], v[40:43], a[64:67], a[232:235]\n"
"buffer_load_dwordx4 v215, s[24:27], s49 offen offset:3072 lds\n"
"v_mfma_f32_16x16x32_bf16 a[232:235], v[44:47], a[72:75], a[232:235]\n"
"ds_read_b64_tr_b16 v[10:11], v161 offset:8192\n"
"buffer_store_dwordx4 v[108:111], v227, s[20:23], s84 offen offset:3072 sc1\n"
"v_mfma_f32_16x16x32_bf16 a[236:239], v[44:47], a[76:79], a[236:239]\n"
"s_mov_b32 m0, s54\n"
"ds_read_b64_tr_b16 v[12:13], v161 offset:16384\n"
"v_lshlrev_b32 v80, 16, v132\n"
"v_mfma_f32_16x16x32_bf16 a[236:239], v[40:43], a[68:71], a[236:239]\n"
"ds_read_b64_tr_b16 v[14:15], v161 offset:24576\n"
"v_and_b32 v81, 0xffff0000, v132\n"
"v_mfma_f32_16x16x32_bf16 a[240:243], v[48:51], a[64:67], a[240:243]\n"
"ds_read_b64_tr_b16 v[16:17], v162 offset:0\n"
"v_lshlrev_b32 v82, 16, v133\n"
"v_mfma_f32_16x16x32_bf16 a[240:243], v[52:55], a[72:75], a[240:243]\n"
"ds_read_b64_tr_b16 v[18:19], v162 offset:8192\n"
"v_and_b32 v83, 0xffff0000, v133\n"
"v_mfma_f32_16x16x32_bf16 a[244:247], v[52:55], a[76:79], a[244:247]\n"
"ds_read_b64_tr_b16 v[20:21], v162 offset:16384\n"
"v_lshlrev_b32 v84, 16, v132\n"
"v_mfma_f32_16x16x32_bf16 a[244:247], v[48:51], a[68:71], a[244:247]\n"
"ds_read_b64_tr_b16 v[22:23], v162 offset:24576\n"
"ds_read_b64_tr_b16 v[24:25], v163 offset:0\n"
"v_mfma_f32_16x16x32_bf16 a[248:251], v[56:59], a[64:67], a[248:251]\n"
"v_and_b32 v85, 0xffff0000, v132\n"
"v_lshlrev_b32 v86, 16, v133\n"
"v_mfma_f32_16x16x32_bf16 a[248:251], v[60:63], a[72:75], a[248:251]\n"
"ds_read_b64_tr_b16 v[26:27], v163 offset:8192\n"
"v_and_b32 v87, 0xffff0000, v133\n"
"v_mfma_f32_16x16x32_bf16 a[252:255], v[60:63], a[76:79], a[252:255]\n"
"ds_read_b64_tr_b16 v[28:29], v163 offset:16384\n"
"v_mfma_f32_16x16x32_bf16 a[252:255], v[56:59], a[68:71], a[252:255]\n"
"s_waitcnt lgkmcnt(7)\n"
"ds_read_b64_tr_b16 v[30:31], v163 offset:24576\n"
"v_mfma_f32_16x16x32_bf16 a[128:131], v[0:3], a[80:83], a[128:131]\n"
"ds_read_b64_tr_b16 v[32:33], v164 offset:0\n"
"v_readlane_b32 s24, v224, s48\n"
"v_mfma_f32_16x16x32_bf16 a[128:131], v[4:7], a[88:91], a[128:131]\n"
"ds_read_b64_tr_b16 v[34:35], v164 offset:8192\n"
"v_readlane_b32 s25, v225, s48\n"
"v_mfma_f32_16x16x32_bf16 a[132:135], v[4:7], a[92:95], a[132:135]\n"
"ds_read_b64_tr_b16 v[36:37], v164 offset:16384\n"
"v_readlane_b32 s26, v226, s48\n"
"v_mfma_f32_16x16x32_bf16 a[132:135], v[0:3], a[84:87], a[132:135]\n"
"s_add_u32 s48, s48, 1\n"
"ds_read_b64_tr_b16 v[38:39], v164 offset:24576\n"
"ds_read_b64_tr_b16 v[40:41], v165 offset:0\n"
"v_mfma_f32_16x16x32_bf16 a[136:139], v[8:11], a[80:83], a[136:139]\n"
"s_add_u32 s84, s84, s81\n"
"v_lshlrev_b32 v88, 16, v134\n"
"v_mfma_f32_16x16x32_bf16 a[136:139], v[12:15], a[88:91], a[136:139]\n"
"ds_read_b64_tr_b16 v[42:43], v165 offset:8192\n"
"v_mfma_f32_16x16x32_bf16 a[140:143], v[12:15], a[92:95], a[140:143]\n"
"ds_read_b64_tr_b16 v[44:45], v165 offset:16384\n"
"v_and_b32 v89, 0xffff0000, v134\n"
"v_mfma_f32_16x16x32_bf16 a[140:143], v[8:11], a[84:87], a[140:143]\n"
"s_waitcnt lgkmcnt(7)\n"
"ds_read_b64_tr_b16 v[46:47], v165 offset:24576\n"
"v_mfma_f32_16x16x32_bf16 a[144:147], v[16:19], a[80:83], a[144:147]\n"
"ds_read_b64_tr_b16 v[48:49], v166 offset:0\n"
"buffer_store_dwordx4 v[112:115], v227, s[20:23], s84 offen offset:0 sc1\n"
"v_mfma_f32_16x16x32_bf16 a[144:147], v[20:23], a[88:91], a[144:147]\n"
"ds_read_b64_tr_b16 v[50:51], v166 offset:8192\n"
"buffer_load_dwordx4 v208, s[24:27], s49 offen offset:0 lds\n"
"v_mfma_f32_16x16x32_bf16 a[148:151], v[20:23], a[92:95], a[148:151]\n"
"ds_read_b64_tr_b16 v[52:53], v166 offset:16384\n"
"v_lshlrev_b32 v90, 16, v135\n"
"v_mfma_f32_16x16x32_bf16 a[148:151], v[16:19], a[84:87], a[148:151]\n"
"ds_read_b64_tr_b16 v[54:55], v166 offset:24576\n"
"ds_read_b64_tr_b16 v[56:57], v167 offset:0\n"
"v_mfma_f32_16x16x32_bf16 a[152:155], v[24:27], a[80:83], a[152:155]\n"
"buffer_load_dwordx4 v209, s[24:27], s49 offen offset:1024 lds\n"
"v_mfma_f32_16x16x32_bf16 a[152:155], v[28:31], a[88:91], a[152:155]\n"
"ds_read_b64_tr_b16 v[58:59], v167 offset:8192\n"
"v_and_b32 v91, 0xffff0000, v135\n"
"v_mfma_f32_16x16x32_bf16 a[156:159], v[28:31], a[92:95], a[156:159]\n"
"ds_read_b64_tr_b16 v[60:61], v167 offset:16384\n"
"v_lshlrev_b32 v92, 16, v134\n"
"v_mfma_f32_16x16x32_bf16 a[156:159], v[24:27], a[84:87], a[156:159]\n"
"s_waitcnt lgkmcnt(7)\n"
"ds_read_b64_tr_b16 v[62:63], v167 offset:24576\n"
"v_and_b32 v93, 0xffff0000, v134\n"
"v_mfma_f32_16x16x32_bf16 a[160:163], v[32:35], a[80:83], a[160:163]\n"
"ds_read_b64_tr_b16 v[0:1], v160 offset:256\n"
"v_mfma_f32_16x16x32_bf16 a[160:163], v[36:39], a[88:91], a[160:163]\n"
"ds_read_b64_tr_b16 v[2:3], v160 offset:8448\n"
"v_lshlrev_b32 v94, 16, v135\n"
"v_mfma_f32_16x16x32_bf16 a[164:167], v[36:39], a[92:95], a[164:167]\n"
"ds_read_b64_tr_b16 v[4:5], v160 offset:16640\n"
"v_mfma_f32_16x16x32_bf16 a[164:167], v[32:35], a[84:87], a[164:167]\n"
"ds_read_b64_tr_b16 v[6:7], v160 offset:24832\n"
"ds_read_b64_tr_b16 v[8:9], v161 offset:256\n"
"v_mfma_f32_16x16x32_bf16 a[168:171], v[40:43], a[80:83], a[168:171]\n"
"buffer_load_dwordx4 v210, s[24:27], s49 offen offset:2048 lds\n"
"v_mfma_f32_16x16x32_bf16 a[168:171], v[44:47], a[88:91], a[168:171]\n"
"ds_read_b64_tr_b16 v[10:11], v161 offset:8448\n"
"v_and_b32 v95, 0xffff0000, v135\n"
"v_mfma_f32_16x16x32_bf16 a[172:175], v[44:47], a[92:95], a[172:175]\n"
"ds_read_b64_tr_b16 v[12:13], v161 offset:16640\n"
"buffer_store_dwordx4 v[116:119], v227, s[20:23], s84 offen offset:1024 sc1\n"
"v_mfma_f32_16x16x32_bf16 a[172:175], v[40:43], a[84:87], a[172:175]\n"
"s_waitcnt lgkmcnt(7)\n"
"ds_read_b64_tr_b16 v[14:15], v161 offset:24832\n"
"v_lshlrev_b32 v96, 16, v136\n"
"v_mfma_f32_16x16x32_bf16 a[176:179], v[48:51], a[80:83], a[176:179]\n"
"ds_read_b64_tr_b16 v[16:17], v162 offset:256\n"
"buffer_load_dwordx4 v211, s[24:27], s49 offen offset:3072 lds\n"
"v_mfma_f32_16x16x32_bf16 a[176:179], v[52:55], a[88:91], a[176:179]\n"
"s_add_u32 m0, m0, 4096\n"
"ds_read_b64_tr_b16 v[18:19], v162 offset:8448\n"
"v_and_b32 v97, 0xffff0000, v136\n"
"v_mfma_f32_16x16x32_bf16 a[180:183], v[52:55], a[92:95], a[180:183]\n"
"ds_read_b64_tr_b16 v[20:21], v162 offset:16640\n"
"v_lshlrev_b32 v98, 16, v137\n"
"v_mfma_f32_16x16x32_bf16 a[180:183], v[48:51], a[84:87], a[180:183]\n"
"ds_read_b64_tr_b16 v[22:23], v162 offset:24832\n"
"ds_read_b64_tr_b16 v[24:25], v163 offset:256\n"
"v_mfma_f32_16x16x32_bf16 a[184:187], v[56:59], a[80:83], a[184:187]\n"
"buffer_load_dwordx4 v212, s[24:27], s49 offen offset:0 lds\n"
"v_mfma_f32_16x16x32_bf16 a[184:187], v[60:63], a[88:91], a[184:187]\n"
"ds_read_b64_tr_b16 v[26:27], v163 offset:8448\n"
"v_and_b32 v99, 0xffff0000, v137\n"
"v_mfma_f32_16x16x32_bf16 a[188:191], v[60:63], a[92:95], a[188:191]\n"
"ds_read_b64_tr_b16 v[28:29], v163 offset:16640\n"
"v_mfma_f32_16x16x32_bf16 a[188:191], v[56:59], a[84:87], a[188:191]\n"
"s_waitcnt lgkmcnt(7)\n"
"ds_read_b64_tr_b16 v[30:31], v163 offset:24832\n"
"v_lshlrev_b32 v100, 16, v136\n"
"v_mfma_f32_16x16x32_bf16 a[192:195], v[0:3], a[80:83], a[192:195]\n"
"ds_read_b64_tr_b16 v[32:33], v164 offset:256\n"
"ds_read_b64_tr_b16 v[34:35], v164 offset:8448\n"
"v_mfma_f32_16x16x32_bf16 a[192:195], v[4:7], a[88:91], a[192:195]\n"
"ds_read_b64_tr_b16 v[36:37], v164 offset:16640\n"
"ds_read_b64_tr_b16 v[38:39], v164 offset:24832\n"
"v_mfma_f32_16x16x32_bf16 a[196:199], v[4:7], a[92:95], a[196:199]\n"
"ds_read_b64_tr_b16 v[40:41], v165 offset:256\n"
"ds_read_b64_tr_b16 v[42:43], v165 offset:8448\n"
"v_mfma_f32_16x16x32_bf16 a[196:199], v[0:3], a[84:87], a[196:199]\n"
"ds_read_b64_tr_b16 v[44:45], v165 offset:16640\n"
"ds_read_b64_tr_b16 v[46:47], v165 offset:24832\n"
"v_mfma_f32_16x16x32_bf16 a[200:203], v[8:11], a[80:83], a[200:203]\n"
"ds_read_b64_tr_b16 v[48:49], v166 offset:256\n"
"ds_read_b64_tr_b16 v[50:51], v166 offset:8448\n"
"v_mfma_f32_16x16x32_bf16 a[200:203], v[12:15], a[88:91], a[200:203]\n"
"ds_read_b64_tr_b16 v[52:53], v166 offset:16640\n"
"ds_read_b64_tr_b16 v[54:55], v166 offset:24832\n"
"v_mfma_f32_16x16x32_bf16 a[204:207], v[12:15], a[92:95], a[204:207]\n"
"ds_read_b64_tr_b16 v[56:57], v167 offset:256\n"
"ds_read_b64_tr_b16 v[58:59], v167 offset:8448\n"
"v_mfma_f32_16x16x32_bf16 a[204:207], v[8:11], a[84:87], a[204:207]\n"
"s_waitcnt lgkmcnt(14)\n"
"ds_read_b64_tr_b16 v[60:61], v167 offset:16640\n"
"ds_read_b64_tr_b16 v[62:63], v167 offset:24832\n"
"v_mfma_f32_16x16x32_bf16 a[208:211], v[16:19], a[80:83], a[208:211]\n"
"buffer_load_dwordx4 v213, s[24:27], s49 offen offset:1024 lds\n"
"v_mfma_f32_16x16x32_bf16 a[208:211], v[20:23], a[88:91], a[208:211]\n"
"buffer_store_dwordx4 v[120:123], v227, s[20:23], s84 offen offset:2048 sc1\n"
"v_mfma_f32_16x16x32_bf16 a[212:215], v[20:23], a[92:95], a[212:215]\n"
"s_add_u32 s54, s50, s47\n"
"buffer_load_dwordx4 v214, s[24:27], s49 offen offset:2048 lds\n"
"v_mfma_f32_16x16x32_bf16 a[212:215], v[16:19], a[84:87], a[212:215]\n"
"s_add_u32 s50, s50, 32768\n"
"v_and_b32 v101, 0xffff0000, v136\n"
"v_lshlrev_b32 v102, 16, v137\n"
"v_mfma_f32_16x16x32_bf16 a[216:219], v[24:27], a[80:83], a[216:219]\n"
"s_cmp_lt_u32 s50, 163840\n"
"s_cselect_b32 s50, s50, 0\n"
"v_and_b32 v103, 0xffff0000, v137\n"
"v_mfma_f32_16x16x32_bf16 a[216:219], v[28:31], a[88:91], a[216:219]\n"
"v_add_u32 v160, s50, v192\n"
"v_add_u32 v161, s50, v193\n"
"v_mfma_f32_16x16x32_bf16 a[220:223], v[28:31], a[92:95], a[220:223]\n"
"s_waitcnt lgkmcnt(0)\n"
"v_add_u32 v162, s50, v194\n"
"v_add_u32 v163, s50, v195\n"
"v_mfma_f32_16x16x32_bf16 a[220:223], v[24:27], a[84:87], a[220:223]\n"
"s_waitcnt vmcnt(35)\n"
"s_barrier\n"
"v_mfma_f32_16x16x32_bf16 a[224:227], v[32:35], a[80:83], a[224:227]\n"
"ds_read_b64_tr_b16 v[0:1], v160 offset:0\n"
"v_add_u32 v164, s50, v196\n"
"v_mfma_f32_16x16x32_bf16 a[224:227], v[36:39], a[88:91], a[224:227]\n"
"ds_read_b64_tr_b16 v[2:3], v160 offset:8192\n"
"v_add_u32 v165, s50, v197\n"
"v_mfma_f32_16x16x32_bf16 a[228:231], v[36:39], a[92:95], a[228:231]\n"
"ds_read_b64_tr_b16 v[4:5], v160 offset:16384\n"
"v_add_u32 v166, s50, v198\n"
"v_mfma_f32_16x16x32_bf16 a[228:231], v[32:35], a[84:87], a[228:231]\n"
"ds_read_b64_tr_b16 v[6:7], v160 offset:24576\n"
"ds_read_b64_tr_b16 v[8:9], v161 offset:0\n"
"v_add_u32 v167, s50, v199\n"
"v_mfma_f32_16x16x32_bf16 a[232:235], v[40:43], a[80:83], a[232:235]\n"
"buffer_load_dwordx4 v215, s[24:27], s49 offen offset:3072 lds\n"
"v_mfma_f32_16x16x32_bf16 a[232:235], v[44:47], a[88:91], a[232:235]\n"
"ds_read_b64_tr_b16 v[10:11], v161 offset:8192\n"
"buffer_store_dwordx4 v[124:127], v227, s[20:23], s84 offen offset:3072 sc1\n"
"v_mfma_f32_16x16x32_bf16 a[236:239], v[44:47], a[92:95], a[236:239]\n"
"s_mov_b32 m0, s54\n"
"ds_read_b64_tr_b16 v[12:13], v161 offset:16384\n"
"v_lshlrev_b32 v104, 16, v138\n"
"v_mfma_f32_16x16x32_bf16 a[236:239], v[40:43], a[84:87], a[236:239]\n"
"ds_read_b64_tr_b16 v[14:15], v161 offset:24576\n"
"v_and_b32 v105, 0xffff0000, v138\n"
"v_mfma_f32_16x16x32_bf16 a[240:243], v[48:51], a[80:83], a[240:243]\n"
"ds_read_b64_tr_b16 v[16:17], v162 offset:0\n"
"v_lshlrev_b32 v106, 16, v139\n"
"v_mfma_f32_16x16x32_bf16 a[240:243], v[52:55], a[88:91], a[240:243]\n"
"ds_read_b64_tr_b16 v[18:19], v162 offset:8192\n"
"v_and_b32 v107, 0xffff0000, v139\n"
"v_mfma_f32_16x16x32_bf16 a[244:247], v[52:55], a[92:95], a[244:247]\n"
"ds_read_b64_tr_b16 v[20:21], v162 offset:16384\n"
"v_lshlrev_b32 v108, 16, v138\n"
"v_mfma_f32_16x16x32_bf16 a[244:247], v[48:51], a[84:87], a[244:247]\n"
"ds_read_b64_tr_b16 v[22:23], v162 offset:24576\n"
"ds_read_b64_tr_b16 v[24:25], v163 offset:0\n"
"v_mfma_f32_16x16x32_bf16 a[248:251], v[56:59], a[80:83], a[248:251]\n"
"v_and_b32 v109, 0xffff0000, v138\n"
"v_lshlrev_b32 v110, 16, v139\n"
"v_mfma_f32_16x16x32_bf16 a[248:251], v[60:63], a[88:91], a[248:251]\n"
"ds_read_b64_tr_b16 v[26:27], v163 offset:8192\n"
"v_and_b32 v111, 0xffff0000, v139\n"
"v_mfma_f32_16x16x32_bf16 a[252:255], v[60:63], a[92:95], a[252:255]\n"
"ds_read_b64_tr_b16 v[28:29], v163 offset:16384\n"
"v_mfma_f32_16x16x32_bf16 a[252:255], v[56:59], a[84:87], a[252:255]\n"
"s_waitcnt lgkmcnt(7)\n"
"ds_read_b64_tr_b16 v[30:31], v163 offset:24576\n"
"v_mfma_f32_16x16x32_bf16 a[128:131], v[0:3], a[96:99], a[128:131]\n"
"ds_read_b64_tr_b16 v[32:33], v164 offset:0\n"
"v_readlane_b32 s24, v224, s48\n"
"v_mfma_f32_16x16x32_bf16 a[128:131], v[4:7], a[104:107], a[128:131]\n"
"ds_read_b64_tr_b16 v[34:35], v164 offset:8192\n"
"v_readlane_b32 s25, v225, s48\n"
"v_mfma_f32_16x16x32_bf16 a[132:135], v[4:7], a[108:111], a[132:135]\n"
"ds_read_b64_tr_b16 v[36:37], v164 offset:16384\n"
"v_readlane_b32 s26, v226, s48\n"
"v_mfma_f32_16x16x32_bf16 a[132:135], v[0:3], a[100:103], a[132:135]\n"
"s_add_u32 s48, s48, 1\n"
"ds_read_b64_tr_b16 v[38:39], v164 offset:24576\n"
"ds_read_b64_tr_b16 v[40:41], v165 offset:0\n"
"v_mfma_f32_16x16x32_bf16 a[136:139], v[8:11], a[96:99], a[136:139]\n"
"v_lshlrev_b32 v112, 16, v140\n"
"v_mfma_f32_16x16x32_bf16 a[136:139], v[12:15], a[104:107], a[136:139]\n"
"ds_read_b64_tr_b16 v[42:43], v165 offset:8192\n"
"v_mfma_f32_16x16x32_bf16 a[140:143], v[12:15], a[108:111], a[140:143]\n"
"ds_read_b64_tr_b16 v[44:45], v165 offset:16384\n"
"v_and_b32 v113, 0xffff0000, v140\n"
"v_mfma_f32_16x16x32_bf16 a[140:143], v[8:11], a[100:103], a[140:143]\n"
"s_waitcnt lgkmcnt(7)\n"
"ds_read_b64_tr_b16 v[46:47], v165 offset:24576\n"
"v_mfma_f32_16x16x32_bf16 a[144:147], v[16:19], a[96:99], a[144:147]\n"
"ds_read_b64_tr_b16 v[48:49], v166 offset:0\n"
"v_lshlrev_b32 v114, 16, v141\n"
"v_mfma_f32_16x16x32_bf16 a[144:147], v[20:23], a[104:107], a[144:147]\n"
"ds_read_b64_tr_b16 v[50:51], v166 offset:8192\n"
"buffer_load_dwordx4 v208, s[24:27], s49 offen offset:0 lds\n"
"v_mfma_f32_16x16x32_bf16 a[148:151], v[20:23], a[108:111], a[148:151]\n"
"ds_read_b64_tr_b16 v[52:53], v166 offset:16384\n"
"v_and_b32 v115, 0xffff0000, v141\n"
"v_mfma_f32_16x16x32_bf16 a[148:151], v[16:19], a[100:103], a[148:151]\n"
"ds_read_b64_tr_b16 v[54:55], v166 offset:24576\n"
"ds_read_b64_tr_b16 v[56:57], v167 offset:0\n"
"v_mfma_f32_16x16x32_bf16 a[152:155], v[24:27], a[96:99], a[152:155]\n"
"buffer_load_dwordx4 v209, s[24:27], s49 offen offset:1024 lds\n"
"v_mfma_f32_16x16x32_bf16 a[152:155], v[28:31], a[104:107], a[152:155]\n"
"ds_read_b64_tr_b16 v[58:59], v167 offset:8192\n"
"v_lshlrev_b32 v116, 16, v140\n"
"v_mfma_f32_16x16x32_bf16 a[156:159], v[28:31], a[108:111], a[156:159]\n"
"ds_read_b64_tr_b16 v[60:61], v167 offset:16384\n"
"v_mfma_f32_16x16x32_bf16 a[156:159], v[24:27], a[100:103], a[156:159]\n"
"s_waitcnt lgkmcnt(7)\n"
"ds_read_b64_tr_b16 v[62:63], v167 offset:24576\n"
"v_and_b32 v117, 0xffff0000, v140\n"
"v_mfma_f32_16x16x32_bf16 a[160:163], v[32:35], a[96:99], a[160:163]\n"
"ds_read_b64_tr_b16 v[0:1], v160 offset:256\n"
"v_mfma_f32_16x16x32_bf16 a[160:163], v[36:39], a[104:107], a[160:163]\n"
"ds_read_b64_tr_b16 v[2:3], v160 offset:8448\n"
"v_lshlrev_b32 v118, 16, v141\n"
"v_mfma_f32_16x16x32_bf16 a[164:167], v[36:39], a[108:111], a[164:167]\n"
"ds_read_b64_tr_b16 v[4:5], v160 offset:16640\n"
"v_mfma_f32_16x16x32_bf16 a[164:167], v[32:35], a[100:103], a[164:167]\n"
"ds_read_b64_tr_b16 v[6:7], v160 offset:24832\n"
"ds_read_b64_tr_b16 v[8:9], v161 offset:256\n"
"v_mfma_f32_16x16x32_bf16 a[168:171], v[40:43], a[96:99], a[168:171]\n"
"buffer_load_dwordx4 v210, s[24:27], s49 offen offset:2048 lds\n"
"v_mfma_f32_16x16x32_bf16 a[168:171], v[44:47], a[104:107], a[168:171]\n"
"ds_read_b64_tr_b16 v[10:11], v161 offset:8448\n"
"v_and_b32 v119, 0xffff0000, v141\n"
"v_mfma_f32_16x16x32_bf16 a[172:175], v[44:47], a[108:111], a[172:175]\n"
"ds_read_b64_tr_b16 v[12:13], v161 offset:16640\n"
"v_mfma_f32_16x16x32_bf16 a[172:175], v[40:43], a[100:103], a[172:175]\n"
"s_waitcnt lgkmcnt(7)\n"
"ds_read_b64_tr_b16 v[14:15], v161 offset:24832\n"
"v_mfma_f32_16x16x32_bf16 a[176:179], v[48:51], a[96:99], a[176:179]\n"
"ds_read_b64_tr_b16 v[16:17], v162 offset:256\n"
"buffer_load_dwordx4 v211, s[24:27], s49 offen offset:3072 lds\n"
"v_mfma_f32_16x16x32_bf16 a[176:179], v[52:55], a[104:107], a[176:179]\n"
"s_add_u32 m0, m0, 4096\n"
"ds_read_b64_tr_b16 v[18:19], v162 offset:8448\n"
"v_mfma_f32_16x16x32_bf16 a[180:183], v[52:55], a[108:111], a[180:183]\n"
"ds_read_b64_tr_b16 v[20:21], v162 offset:16640\n"
"v_mfma_f32_16x16x32_bf16 a[180:183], v[48:51], a[100:103], a[180:183]\n"
"ds_read_b64_tr_b16 v[22:23], v162 offset:24832\n"
"ds_read_b64_tr_b16 v[24:25], v163 offset:256\n"
"v_mfma_f32_16x16x32_bf16 a[184:187], v[56:59], a[96:99], a[184:187]\n"
"buffer_load_dwordx4 v212, s[24:27], s49 offen offset:0 lds\n"
"v_mfma_f32_16x16x32_bf16 a[184:187], v[60:63], a[104:107], a[184:187]\n"
"ds_read_b64_tr_b16 v[26:27], v163 offset:8448\n"
"v_mfma_f32_16x16x32_bf16 a[188:191], v[60:63], a[108:111], a[188:191]\n"
"ds_read_b64_tr_b16 v[28:29], v163 offset:16640\n"
"v_mfma_f32_16x16x32_bf16 a[188:191], v[56:59], a[100:103], a[188:191]\n"
"s_waitcnt lgkmcnt(7)\n"
"ds_read_b64_tr_b16 v[30:31], v163 offset:24832\n"
"v_mfma_f32_16x16x32_bf16 a[192:195], v[0:3], a[96:99], a[192:195]\n"
"ds_read_b64_tr_b16 v[32:33], v164 offset:256\n"
"ds_read_b64_tr_b16 v[34:35], v164 offset:8448\n"
"v_mfma_f32_16x16x32_bf16 a[192:195], v[4:7], a[104:107], a[192:195]\n"
"ds_read_b64_tr_b16 v[36:37], v164 offset:16640\n"
"ds_read_b64_tr_b16 v[38:39], v164 offset:24832\n"
"v_mfma_f32_16x16x32_bf16 a[196:199], v[4:7], a[108:111], a[196:199]\n"
"ds_read_b64_tr_b16 v[40:41], v165 offset:256\n"
"ds_read_b64_tr_b16 v[42:43], v165 offset:8448\n"
"v_mfma_f32_16x16x32_bf16 a[196:199], v[0:3], a[100:103], a[196:199]\n"
"ds_read_b64_tr_b16 v[44:45], v165 offset:16640\n"
"ds_read_b64_tr_b16 v[46:47], v165 offset:24832\n"
"v_mfma_f32_16x16x32_bf16 a[200:203], v[8:11], a[96:99], a[200:203]\n"
"ds_read_b64_tr_b16 v[48:49], v166 offset:256\n"
"ds_read_b64_tr_b16 v[50:51], v166 offset:8448\n"
"v_mfma_f32_16x16x32_bf16 a[200:203], v[12:15], a[104:107], a[200:203]\n"
"ds_read_b64_tr_b16 v[52:53], v166 offset:16640\n"
"ds_read_b64_tr_b16 v[54:55], v166 offset:24832\n"
"v_mfma_f32_16x16x32_bf16 a[204:207], v[12:15], a[108:111], a[204:207]\n"
"ds_read_b64_tr_b16 v[56:57], v167 offset:256\n"
"ds_read_b64_tr_b16 v[58:59], v167 offset:8448\n"
"v_mfma_f32_16x16x32_bf16 a[204:207], v[8:11], a[100:103], a[204:207]\n"
"s_waitcnt lgkmcnt(14)\n"
"ds_read_b64_tr_b16 v[60:61], v167 offset:16640\n"
"ds_read_b64_tr_b16 v[62:63], v167 offset:24832\n"
"v_mfma_f32_16x16x32_bf16 a[208:211], v[16:19], a[96:99], a[208:211]\n"
"buffer_load_dwordx4 v213, s[24:27], s49 offen offset:1024 lds\n"
"v_mfma_f32_16x16x32_bf16 a[208:211], v[20:23], a[104:107], a[208:211]\n"
"buffer_load_dwordx4 v214, s[24:27], s49 offen offset:2048 lds\n"
"v_mfma_f32_16x16x32_bf16 a[212:215], v[20:23], a[108:111], a[212:215]\n"
"s_add_u32 s54, s50, s47\n"
"v_mfma_f32_16x16x32_bf16 a[212:215], v[16:19], a[100:103], a[212:215]\n"
"s_add_u32 s50, s50, 32768\n"
"v_mfma_f32_16x16x32_bf16 a[216:219], v[24:27], a[96:99], a[216:219]\n"
"s_cmp_lt_u32 s50, 163840\n"
"s_cselect_b32 s50, s50, 0\n"
"v_mfma_f32_16x16x32_bf16 a[216:219], v[28:31], a[104:107], a[216:219]\n"
"buffer_load_dwordx4 v215, s[24:27], s49 offen offset:3072 lds\n"
"v_mfma_f32_16x16x32_bf16 a[220:223], v[28:31], a[108:111], a[220:223]\n"
"s_waitcnt lgkmcnt(0)\n"
"s_mov_b32 m0, s54\n"
"v_add_u32 v232, s50, v228\n"
"v_mfma_f32_16x16x32_bf16 a[220:223], v[24:27], a[100:103], a[220:223]\n"
"s_waitcnt vmcnt(33)\n"
"s_barrier\n"
"v_mfma_f32_16x16x32_bf16 a[224:227], v[32:35], a[96:99], a[224:227]\n"
"ds_read_b64_tr_b16 v[0:1], v232 offset:0\n"
"v_mfma_f32_16x16x32_bf16 a[224:227], v[36:39], a[104:107], a[224:227]\n"
"ds_read_b64_tr_b16 v[2:3], v232 offset:3584\n"
"v_mfma_f32_16x16x32_bf16 a[228:231], v[36:39], a[108:111], a[228:231]\n"
"ds_read_b64_tr_b16 v[4:5], v232 offset:8192\n"
"v_add_u32 v208, 0, v201\n"
"v_mfma_f32_16x16x32_bf16 a[228:231], v[32:35], a[100:103], a[228:231]\n"
"ds_read_b64_tr_b16 v[6:7], v232 offset:11776\n"
"ds_read_b64_tr_b16 v[8:9], v232 offset:16384\n"
"v_mfma_f32_16x16x32_bf16 a[232:235], v[40:43], a[96:99], a[232:235]\n"
"v_add_u32 v209, 0, v201\n"
"v_add_u32 v210, 0, v201\n"
"v_mfma_f32_16x16x32_bf16 a[232:235], v[44:47], a[104:107], a[232:235]\n"
"ds_read_b64_tr_b16 v[10:11], v232 offset:19968\n"
"v_add_u32 v211, 0, v201\n"
"v_mfma_f32_16x16x32_bf16 a[236:239], v[44:47], a[108:111], a[236:239]\n"
"ds_read_b64_tr_b16 v[12:13], v232 offset:24576\n"
"v_add_u32 v212, 4096, v201\n"
"v_mfma_f32_16x16x32_bf16 a[236:239], v[40:43], a[100:103], a[236:239]\n"
"ds_read_b64_tr_b16 v[14:15], v232 offset:28160\n"
"v_add_u32 v213, 4096, v201\n"
"v_mfma_f32_16x16x32_bf16 a[240:243], v[48:51], a[96:99], a[240:243]\n"
"ds_read_b64_tr_b16 v[16:17], v232 offset:32\n"
"v_add_u32 v214, 4096, v201\n"
"v_mfma_f32_16x16x32_bf16 a[240:243], v[52:55], a[104:107], a[240:243]\n"
"ds_read_b64_tr_b16 v[18:19], v232 offset:3616\n"
"v_add_u32 v215, 4096, v201\n"
"v_mfma_f32_16x16x32_bf16 a[244:247], v[52:55], a[108:111], a[244:247]\n"
"s_mov_b32 s24, s20\n"
"ds_read_b64_tr_b16 v[20:21], v232 offset:8224\n"
"v_mfma_f32_16x16x32_bf16 a[244:247], v[48:51], a[100:103], a[244:247]\n"
"s_mov_b32 s25, s21\n"
"ds_read_b64_tr_b16 v[22:23], v232 offset:11808\n"
"ds_read_b64_tr_b16 v[24:25], v232 offset:16416\n"
"v_mfma_f32_16x16x32_bf16 a[248:251], v[56:59], a[96:99], a[248:251]\n"
"s_mov_b32 s26, s22\n"
"v_mfma_f32_16x16x32_bf16 a[248:251], v[60:63], a[104:107], a[248:251]\n"
"ds_read_b64_tr_b16 v[26:27], v232 offset:20000\n"
"v_mfma_f32_16x16x32_bf16 a[252:255], v[60:63], a[108:111], a[252:255]\n"
"ds_read_b64_tr_b16 v[28:29], v232 offset:24608\n"
"v_mfma_f32_16x16x32_bf16 a[252:255], v[56:59], a[100:103], a[252:255]\n"
"s_waitcnt lgkmcnt(7)\n"
"ds_read_b64_tr_b16 v[30:31], v232 offset:28192\n"
"v_mov_b32 v242, s57\n"
"v_mov_b32 v243, s57\n"
"v_accvgpr_read_b32 v128, a128\n"
"v_accvgpr_read_b32 v129, a129\n"
"v_accvgpr_read_b32 v130, a130\n"
"v_accvgpr_read_b32 v131, a131\n"
"v_accvgpr_read_b32 v132, a136\n"
"v_accvgpr_read_b32 v133, a137\n"
"v_accvgpr_read_b32 v134, a138\n"
"v_accvgpr_read_b32 v135, a139\n"
"v_pk_mul_f32 v[128:129], v[242:243], v[128:129]\n"
"v_pk_mul_f32 v[130:131], v[242:243], v[130:131]\n"
"v_pk_mul_f32 v[132:133], v[242:243], v[132:133]\n"
"v_pk_mul_f32 v[134:135], v[242:243], v[134:135]\n"
"v_cvt_pk_bf16_f32 v136, v128, v129\n"
"v_cvt_pk_bf16_f32 v137, v130, v131\n"
"v_cvt_pk_bf16_f32 v138, v132, v133\n"
"v_cvt_pk_bf16_f32 v139, v134, v135\n"
"s_add_u32 s54, s11, 0\n"
"buffer_store_dwordx4 v[136:139], v227, s[20:23], s54 offen offset:0 sc1\n"
"v_accvgpr_write_b32 a128, v136\n"
"v_accvgpr_write_b32 a129, v137\n"
"v_accvgpr_write_b32 a130, v138\n"
"v_accvgpr_write_b32 a131, v139\n"
"s_waitcnt vmcnt(9)\n"
"s_barrier\n"
"s_cmp_lg_u32 s47, 0\n"
"s_cbranch_scc1 L_kvf%=\n"
"v_mov_b32 v240, 1\n"
"s_mov_b64 exec, 1\n"
"buffer_store_dword v240, v231, s[20:23], s77 offen offset:0 sc1\n"
"s_mov_b64 exec, -1\n"
"s_nop 3\n"
"L_kvf%=:\n"
"v_accvgpr_read_b32 v140, a132\n"
"v_accvgpr_read_b32 v141, a133\n"
"v_accvgpr_read_b32 v142, a134\n"
"v_accvgpr_read_b32 v143, a135\n"
"v_accvgpr_read_b32 v144, a140\n"
"v_accvgpr_read_b32 v145, a141\n"
"v_accvgpr_read_b32 v146, a142\n"
"v_accvgpr_read_b32 v147, a143\n"
"v_pk_mul_f32 v[140:141], v[242:243], v[140:141]\n"
"v_pk_mul_f32 v[142:143], v[242:243], v[142:143]\n"
"v_pk_mul_f32 v[144:145], v[242:243], v[144:145]\n"
"v_pk_mul_f32 v[146:147], v[242:243], v[146:147]\n"
"v_cvt_pk_bf16_f32 v148, v140, v141\n"
"v_cvt_pk_bf16_f32 v149, v142, v143\n"
"v_cvt_pk_bf16_f32 v150, v144, v145\n"
"v_cvt_pk_bf16_f32 v151, v146, v147\n"
"buffer_store_dwordx4 v[148:151], v227, s[20:23], s54 offen offset:1024 sc1\n"
"v_accvgpr_write_b32 a132, v148\n"
"v_accvgpr_write_b32 a133, v149\n"
"v_accvgpr_write_b32 a134, v150\n"
"v_accvgpr_write_b32 a135, v151\n"
"v_accvgpr_read_b32 v152, a144\n"
"v_accvgpr_read_b32 v153, a145\n"
"v_accvgpr_read_b32 v154, a146\n"
"v_accvgpr_read_b32 v155, a147\n"
"v_accvgpr_read_b32 v156, a152\n"
"v_accvgpr_read_b32 v157, a153\n"
"v_accvgpr_read_b32 v158, a154\n"
"v_accvgpr_read_b32 v159, a155\n"
"v_pk_mul_f32 v[152:153], v[242:243], v[152:153]\n"
"v_pk_mul_f32 v[154:155], v[242:243], v[154:155]\n"
"v_pk_mul_f32 v[156:157], v[242:243], v[156:157]\n"
"v_pk_mul_f32 v[158:159], v[242:243], v[158:159]\n"
"v_cvt_pk_bf16_f32 v160, v152, v153\n"
"v_cvt_pk_bf16_f32 v161, v154, v155\n"
"v_cvt_pk_bf16_f32 v162, v156, v157\n"
"v_cvt_pk_bf16_f32 v163, v158, v159\n"
"buffer_store_dwordx4 v[160:163], v227, s[20:23], s54 offen offset:2048 sc1\n"
"v_accvgpr_write_b32 a136, v160\n"
"v_accvgpr_write_b32 a137, v161\n"
"v_accvgpr_write_b32 a138, v162\n"
"v_accvgpr_write_b32 a139, v163\n"
"v_accvgpr_read_b32 v164, a148\n"
"v_accvgpr_read_b32 v165, a149\n"
"v_accvgpr_read_b32 v166, a150\n"
"v_accvgpr_read_b32 v167, a151\n"
"v_accvgpr_read_b32 v168, a156\n"
"v_accvgpr_read_b32 v169, a157\n"
"v_accvgpr_read_b32 v170, a158\n"
"v_accvgpr_read_b32 v171, a159\n"
"v_pk_mul_f32 v[164:165], v[242:243], v[164:165]\n"
"v_pk_mul_f32 v[166:167], v[242:243], v[166:167]\n"
"v_pk_mul_f32 v[168:169], v[242:243], v[168:169]\n"
"v_pk_mul_f32 v[170:171], v[242:243], v[170:171]\n"
"v_cvt_pk_bf16_f32 v172, v164, v165\n"
"v_cvt_pk_bf16_f32 v173, v166, v167\n"
"v_cvt_pk_bf16_f32 v174, v168, v169\n"
"v_cvt_pk_bf16_f32 v175, v170, v171\n"
"buffer_store_dwordx4 v[172:175], v227, s[20:23], s54 offen offset:3072 sc1\n"
"v_accvgpr_write_b32 a140, v172\n"
"v_accvgpr_write_b32 a141, v173\n"
"v_accvgpr_write_b32 a142, v174\n"
"v_accvgpr_write_b32 a143, v175\n"
"v_accvgpr_read_b32 v128, a160\n"
"v_accvgpr_read_b32 v129, a161\n"
"v_accvgpr_read_b32 v130, a162\n"
"v_accvgpr_read_b32 v131, a163\n"
"v_accvgpr_read_b32 v132, a168\n"
"v_accvgpr_read_b32 v133, a169\n"
"v_accvgpr_read_b32 v134, a170\n"
"v_accvgpr_read_b32 v135, a171\n"
"v_pk_mul_f32 v[128:129], v[242:243], v[128:129]\n"
"v_pk_mul_f32 v[130:131], v[242:243], v[130:131]\n"
"v_pk_mul_f32 v[132:133], v[242:243], v[132:133]\n"
"v_pk_mul_f32 v[134:135], v[242:243], v[134:135]\n"
"v_cvt_pk_bf16_f32 v136, v128, v129\n"
"v_cvt_pk_bf16_f32 v137, v130, v131\n"
"v_cvt_pk_bf16_f32 v138, v132, v133\n"
"v_cvt_pk_bf16_f32 v139, v134, v135\n"
"s_add_u32 s54, s11, 4096\n"
"buffer_store_dwordx4 v[136:139], v227, s[20:23], s54 offen offset:0 sc1\n"
"v_accvgpr_write_b32 a144, v136\n"
"v_accvgpr_write_b32 a145, v137\n"
"v_accvgpr_write_b32 a146, v138\n"
"v_accvgpr_write_b32 a147, v139\n"
"v_accvgpr_read_b32 v140, a164\n"
"v_accvgpr_read_b32 v141, a165\n"
"v_accvgpr_read_b32 v142, a166\n"
"v_accvgpr_read_b32 v143, a167\n"
"v_accvgpr_read_b32 v144, a172\n"
"v_accvgpr_read_b32 v145, a173\n"
"v_accvgpr_read_b32 v146, a174\n"
"v_accvgpr_read_b32 v147, a175\n"
"v_pk_mul_f32 v[140:141], v[242:243], v[140:141]\n"
"v_pk_mul_f32 v[142:143], v[242:243], v[142:143]\n"
"v_pk_mul_f32 v[144:145], v[242:243], v[144:145]\n"
"v_pk_mul_f32 v[146:147], v[242:243], v[146:147]\n"
"v_cvt_pk_bf16_f32 v148, v140, v141\n"
"v_cvt_pk_bf16_f32 v149, v142, v143\n"
"v_cvt_pk_bf16_f32 v150, v144, v145\n"
"v_cvt_pk_bf16_f32 v151, v146, v147\n"
"buffer_store_dwordx4 v[148:151], v227, s[20:23], s54 offen offset:1024 sc1\n"
"v_accvgpr_write_b32 a148, v148\n"
"v_accvgpr_write_b32 a149, v149\n"
"v_accvgpr_write_b32 a150, v150\n"
"v_accvgpr_write_b32 a151, v151\n"
"v_accvgpr_read_b32 v152, a176\n"
"v_accvgpr_read_b32 v153, a177\n"
"v_accvgpr_read_b32 v154, a178\n"
"v_accvgpr_read_b32 v155, a179\n"
"v_accvgpr_read_b32 v156, a184\n"
"v_accvgpr_read_b32 v157, a185\n"
"v_accvgpr_read_b32 v158, a186\n"
"v_accvgpr_read_b32 v159, a187\n"
"v_pk_mul_f32 v[152:153], v[242:243], v[152:153]\n"
"v_pk_mul_f32 v[154:155], v[242:243], v[154:155]\n"
"v_pk_mul_f32 v[156:157], v[242:243], v[156:157]\n"
"v_pk_mul_f32 v[158:159], v[242:243], v[158:159]\n"
"v_cvt_pk_bf16_f32 v160, v152, v153\n"
"v_cvt_pk_bf16_f32 v161, v154, v155\n"
"v_cvt_pk_bf16_f32 v162, v156, v157\n"
"v_cvt_pk_bf16_f32 v163, v158, v159\n"
"buffer_store_dwordx4 v[160:163], v227, s[20:23], s54 offen offset:2048 sc1\n"
"v_accvgpr_write_b32 a152, v160\n"
"v_accvgpr_write_b32 a153, v161\n"
"v_accvgpr_write_b32 a154, v162\n"
"v_accvgpr_write_b32 a155, v163\n"
"v_accvgpr_read_b32 v164, a180\n"
"v_accvgpr_read_b32 v165, a181\n"
"v_accvgpr_read_b32 v166, a182\n"
"v_accvgpr_read_b32 v167, a183\n"
"v_accvgpr_read_b32 v168, a188\n"
"v_accvgpr_read_b32 v169, a189\n"
"v_accvgpr_read_b32 v170, a190\n"
"v_accvgpr_read_b32 v171, a191\n"
"v_pk_mul_f32 v[164:165], v[242:243], v[164:165]\n"
"v_pk_mul_f32 v[166:167], v[242:243], v[166:167]\n"
"v_pk_mul_f32 v[168:169], v[242:243], v[168:169]\n"
"v_pk_mul_f32 v[170:171], v[242:243], v[170:171]\n"
"v_cvt_pk_bf16_f32 v172, v164, v165\n"
"v_cvt_pk_bf16_f32 v173, v166, v167\n"
"v_cvt_pk_bf16_f32 v174, v168, v169\n"
"v_cvt_pk_bf16_f32 v175, v170, v171\n"
"buffer_store_dwordx4 v[172:175], v227, s[20:23], s54 offen offset:3072 sc1\n"
"v_accvgpr_write_b32 a156, v172\n"
"v_accvgpr_write_b32 a157, v173\n"
"v_accvgpr_write_b32 a158, v174\n"
"v_accvgpr_write_b32 a159, v175\n"
"v_accvgpr_read_b32 v128, a192\n"
"v_accvgpr_read_b32 v129, a193\n"
"v_accvgpr_read_b32 v130, a194\n"
"v_accvgpr_read_b32 v131, a195\n"
"v_accvgpr_read_b32 v132, a200\n"
"v_accvgpr_read_b32 v133, a201\n"
"v_accvgpr_read_b32 v134, a202\n"
"v_accvgpr_read_b32 v135, a203\n"
"v_pk_mul_f32 v[128:129], v[242:243], v[128:129]\n"
"v_pk_mul_f32 v[130:131], v[242:243], v[130:131]\n"
"v_pk_mul_f32 v[132:133], v[242:243], v[132:133]\n"
"v_pk_mul_f32 v[134:135], v[242:243], v[134:135]\n"
"v_cvt_pk_bf16_f32 v136, v128, v129\n"
"v_cvt_pk_bf16_f32 v137, v130, v131\n"
"v_cvt_pk_bf16_f32 v138, v132, v133\n"
"v_cvt_pk_bf16_f32 v139, v134, v135\n"
"s_add_u32 s54, s11, 8192\n"
"buffer_store_dwordx4 v[136:139], v227, s[20:23], s54 offen offset:0 sc1\n"
"v_accvgpr_write_b32 a160, v136\n"
"v_accvgpr_write_b32 a161, v137\n"
"v_accvgpr_write_b32 a162, v138\n"
"v_accvgpr_write_b32 a163, v139\n"
"v_accvgpr_read_b32 v140, a196\n"
"v_accvgpr_read_b32 v141, a197\n"
"v_accvgpr_read_b32 v142, a198\n"
"v_accvgpr_read_b32 v143, a199\n"
"v_accvgpr_read_b32 v144, a204\n"
"v_accvgpr_read_b32 v145, a205\n"
"v_accvgpr_read_b32 v146, a206\n"
"v_accvgpr_read_b32 v147, a207\n"
"v_pk_mul_f32 v[140:141], v[242:243], v[140:141]\n"
"v_pk_mul_f32 v[142:143], v[242:243], v[142:143]\n"
"v_pk_mul_f32 v[144:145], v[242:243], v[144:145]\n"
"v_pk_mul_f32 v[146:147], v[242:243], v[146:147]\n"
"v_cvt_pk_bf16_f32 v148, v140, v141\n"
"v_cvt_pk_bf16_f32 v149, v142, v143\n"
"v_cvt_pk_bf16_f32 v150, v144, v145\n"
"v_cvt_pk_bf16_f32 v151, v146, v147\n"
"buffer_store_dwordx4 v[148:151], v227, s[20:23], s54 offen offset:1024 sc1\n"
"v_accvgpr_write_b32 a164, v148\n"
"v_accvgpr_write_b32 a165, v149\n"
"v_accvgpr_write_b32 a166, v150\n"
"v_accvgpr_write_b32 a167, v151\n"
"v_accvgpr_read_b32 v152, a208\n"
"v_accvgpr_read_b32 v153, a209\n"
"v_accvgpr_read_b32 v154, a210\n"
"v_accvgpr_read_b32 v155, a211\n"
"v_accvgpr_read_b32 v156, a216\n"
"v_accvgpr_read_b32 v157, a217\n"
"v_accvgpr_read_b32 v158, a218\n"
"v_accvgpr_read_b32 v159, a219\n"
"v_pk_mul_f32 v[152:153], v[242:243], v[152:153]\n"
"v_pk_mul_f32 v[154:155], v[242:243], v[154:155]\n"
"v_pk_mul_f32 v[156:157], v[242:243], v[156:157]\n"
"v_pk_mul_f32 v[158:159], v[242:243], v[158:159]\n"
"v_cvt_pk_bf16_f32 v160, v152, v153\n"
"v_cvt_pk_bf16_f32 v161, v154, v155\n"
"v_cvt_pk_bf16_f32 v162, v156, v157\n"
"v_cvt_pk_bf16_f32 v163, v158, v159\n"
"buffer_store_dwordx4 v[160:163], v227, s[20:23], s54 offen offset:2048 sc1\n"
"v_accvgpr_write_b32 a168, v160\n"
"v_accvgpr_write_b32 a169, v161\n"
"v_accvgpr_write_b32 a170, v162\n"
"v_accvgpr_write_b32 a171, v163\n"
"v_accvgpr_read_b32 v164, a212\n"
"v_accvgpr_read_b32 v165, a213\n"
"v_accvgpr_read_b32 v166, a214\n"
"v_accvgpr_read_b32 v167, a215\n"
"v_accvgpr_read_b32 v168, a220\n"
"v_accvgpr_read_b32 v169, a221\n"
"v_accvgpr_read_b32 v170, a222\n"
"v_accvgpr_read_b32 v171, a223\n"
"v_pk_mul_f32 v[164:165], v[242:243], v[164:165]\n"
"v_pk_mul_f32 v[166:167], v[242:243], v[166:167]\n"
"v_pk_mul_f32 v[168:169], v[242:243], v[168:169]\n"
"v_pk_mul_f32 v[170:171], v[242:243], v[170:171]\n"
"v_cvt_pk_bf16_f32 v172, v164, v165\n"
"v_cvt_pk_bf16_f32 v173, v166, v167\n"
"v_cvt_pk_bf16_f32 v174, v168, v169\n"
"v_cvt_pk_bf16_f32 v175, v170, v171\n"
"buffer_store_dwordx4 v[172:175], v227, s[20:23], s54 offen offset:3072 sc1\n"
"v_accvgpr_write_b32 a172, v172\n"
"v_accvgpr_write_b32 a173, v173\n"
"v_accvgpr_write_b32 a174, v174\n"
"v_accvgpr_write_b32 a175, v175\n"
"v_accvgpr_read_b32 v128, a224\n"
"v_accvgpr_read_b32 v129, a225\n"
"v_accvgpr_read_b32 v130, a226\n"
"v_accvgpr_read_b32 v131, a227\n"
"v_accvgpr_read_b32 v132, a232\n"
"v_accvgpr_read_b32 v133, a233\n"
"v_accvgpr_read_b32 v134, a234\n"
"v_accvgpr_read_b32 v135, a235\n"
"v_pk_mul_f32 v[128:129], v[242:243], v[128:129]\n"
"v_pk_mul_f32 v[130:131], v[242:243], v[130:131]\n"
"v_pk_mul_f32 v[132:133], v[242:243], v[132:133]\n"
"v_pk_mul_f32 v[134:135], v[242:243], v[134:135]\n"
"v_cvt_pk_bf16_f32 v136, v128, v129\n"
"v_cvt_pk_bf16_f32 v137, v130, v131\n"
"v_cvt_pk_bf16_f32 v138, v132, v133\n"
"v_cvt_pk_bf16_f32 v139, v134, v135\n"
"s_add_u32 s54, s11, 12288\n"
"buffer_store_dwordx4 v[136:139], v227, s[20:23], s54 offen offset:0 sc1\n"
"v_accvgpr_write_b32 a176, v136\n"
"v_accvgpr_write_b32 a177, v137\n"
"v_accvgpr_write_b32 a178, v138\n"
"v_accvgpr_write_b32 a179, v139\n"
"v_accvgpr_read_b32 v140, a228\n"
"v_accvgpr_read_b32 v141, a229\n"
"v_accvgpr_read_b32 v142, a230\n"
"v_accvgpr_read_b32 v143, a231\n"
"v_accvgpr_read_b32 v144, a236\n"
"v_accvgpr_read_b32 v145, a237\n"
"v_accvgpr_read_b32 v146, a238\n"
"v_accvgpr_read_b32 v147, a239\n"
"v_pk_mul_f32 v[140:141], v[242:243], v[140:141]\n"
"v_pk_mul_f32 v[142:143], v[242:243], v[142:143]\n"
"v_pk_mul_f32 v[144:145], v[242:243], v[144:145]\n"
"v_pk_mul_f32 v[146:147], v[242:243], v[146:147]\n"
"v_cvt_pk_bf16_f32 v148, v140, v141\n"
"v_cvt_pk_bf16_f32 v149, v142, v143\n"
"v_cvt_pk_bf16_f32 v150, v144, v145\n"
"v_cvt_pk_bf16_f32 v151, v146, v147\n"
"buffer_store_dwordx4 v[148:151], v227, s[20:23], s54 offen offset:1024 sc1\n"
"v_accvgpr_write_b32 a180, v148\n"
"v_accvgpr_write_b32 a181, v149\n"
"v_accvgpr_write_b32 a182, v150\n"
"v_accvgpr_write_b32 a183, v151\n"
"v_accvgpr_read_b32 v152, a240\n"
"v_accvgpr_read_b32 v153, a241\n"
"v_accvgpr_read_b32 v154, a242\n"
"v_accvgpr_read_b32 v155, a243\n"
"v_accvgpr_read_b32 v156, a248\n"
"v_accvgpr_read_b32 v157, a249\n"
"v_accvgpr_read_b32 v158, a250\n"
"v_accvgpr_read_b32 v159, a251\n"
"v_pk_mul_f32 v[152:153], v[242:243], v[152:153]\n"
"v_pk_mul_f32 v[154:155], v[242:243], v[154:155]\n"
"v_pk_mul_f32 v[156:157], v[242:243], v[156:157]\n"
"v_pk_mul_f32 v[158:159], v[242:243], v[158:159]\n"
"v_cvt_pk_bf16_f32 v160, v152, v153\n"
"v_cvt_pk_bf16_f32 v161, v154, v155\n"
"v_cvt_pk_bf16_f32 v162, v156, v157\n"
"v_cvt_pk_bf16_f32 v163, v158, v159\n"
"buffer_store_dwordx4 v[160:163], v227, s[20:23], s54 offen offset:2048 sc1\n"
"v_accvgpr_write_b32 a184, v160\n"
"v_accvgpr_write_b32 a185, v161\n"
"v_accvgpr_write_b32 a186, v162\n"
"v_accvgpr_write_b32 a187, v163\n"
"v_accvgpr_read_b32 v164, a244\n"
"v_accvgpr_read_b32 v165, a245\n"
"v_accvgpr_read_b32 v166, a246\n"
"v_accvgpr_read_b32 v167, a247\n"
"v_accvgpr_read_b32 v168, a252\n"
"v_accvgpr_read_b32 v169, a253\n"
"v_accvgpr_read_b32 v170, a254\n"
"v_accvgpr_read_b32 v171, a255\n"
"v_pk_mul_f32 v[164:165], v[242:243], v[164:165]\n"
"v_pk_mul_f32 v[166:167], v[242:243], v[166:167]\n"
"v_pk_mul_f32 v[168:169], v[242:243], v[168:169]\n"
"v_pk_mul_f32 v[170:171], v[242:243], v[170:171]\n"
"v_cvt_pk_bf16_f32 v172, v164, v165\n"
"v_cvt_pk_bf16_f32 v173, v166, v167\n"
"v_cvt_pk_bf16_f32 v174, v168, v169\n"
"v_cvt_pk_bf16_f32 v175, v170, v171\n"
"buffer_store_dwordx4 v[172:175], v227, s[20:23], s54 offen offset:3072 sc1\n"
"v_accvgpr_write_b32 a188, v172\n"
"v_accvgpr_write_b32 a189, v173\n"
"v_accvgpr_write_b32 a190, v174\n"
"v_accvgpr_write_b32 a191, v175\n"
"v_mfma_f32_16x16x32_bf16 v[64:67], v[0:3], a[0:3], v[64:67]\n"
"ds_read_b64_tr_b16 v[32:33], v232 offset:64\n"
"v_mfma_f32_16x16x32_bf16 v[64:67], v[4:7], a[8:11], v[64:67]\n"
"ds_read_b64_tr_b16 v[34:35], v232 offset:3648\n"
"v_mfma_f32_16x16x32_bf16 v[64:67], v[8:11], a[16:19], v[64:67]\n"
"ds_read_b64_tr_b16 v[36:37], v232 offset:8256\n"
"v_accvgpr_mov_b32 a192, a128\n"
"v_mfma_f32_16x16x32_bf16 v[64:67], v[12:15], a[24:27], v[64:67]\n"
"ds_read_b64_tr_b16 v[38:39], v232 offset:11840\n"
"ds_read_b64_tr_b16 v[40:41], v232 offset:16448\n"
"v_mfma_f32_16x16x32_bf16 v[68:71], v[12:15], a[28:31], v[68:71]\n"
"v_accvgpr_mov_b32 a193, a129\n"
"v_mfma_f32_16x16x32_bf16 v[68:71], v[8:11], a[20:23], v[68:71]\n"
"ds_read_b64_tr_b16 v[42:43], v232 offset:20032\n"
"v_accvgpr_mov_b32 a194, a130\n"
"v_mfma_f32_16x16x32_bf16 v[68:71], v[4:7], a[12:15], v[68:71]\n"
"ds_read_b64_tr_b16 v[44:45], v232 offset:24640\n"
"v_accvgpr_mov_b32 a195, a131\n"
"v_mfma_f32_16x16x32_bf16 v[68:71], v[0:3], a[4:7], v[68:71]\n"
"s_waitcnt lgkmcnt(7)\n"
"ds_read_b64_tr_b16 v[46:47], v232 offset:28224\n"
"v_mfma_f32_16x16x32_bf16 v[72:75], v[16:19], a[0:3], v[72:75]\n"
"ds_read_b64_tr_b16 v[48:49], v232 offset:96\n"
"v_accvgpr_mov_b32 a196, a132\n"
"v_mfma_f32_16x16x32_bf16 v[72:75], v[20:23], a[8:11], v[72:75]\n"
"ds_read_b64_tr_b16 v[50:51], v232 offset:3680\n"
"v_accvgpr_mov_b32 a197, a133\n"
"v_mfma_f32_16x16x32_bf16 v[72:75], v[24:27], a[16:19], v[72:75]\n"
"ds_read_b64_tr_b16 v[52:53], v232 offset:8288\n"
"v_mfma_f32_16x16x32_bf16 v[72:75], v[28:31], a[24:27], v[72:75]\n"
"ds_read_b64_tr_b16 v[54:55], v232 offset:11872\n"
"ds_read_b64_tr_b16 v[56:57], v232 offset:16480\n"
"v_mfma_f32_16x16x32_bf16 v[76:79], v[28:31], a[28:31], v[76:79]\n"
"v_accvgpr_mov_b32 a198, a134\n"
"v_accvgpr_mov_b32 a199, a135\n"
"v_mfma_f32_16x16x32_bf16 v[76:79], v[24:27], a[20:23], v[76:79]\n"
"ds_read_b64_tr_b16 v[58:59], v232 offset:20064\n"
"v_mfma_f32_16x16x32_bf16 v[76:79], v[20:23], a[12:15], v[76:79]\n"
"ds_read_b64_tr_b16 v[60:61], v232 offset:24672\n"
"v_accvgpr_mov_b32 a200, a136\n"
"v_mfma_f32_16x16x32_bf16 v[76:79], v[16:19], a[4:7], v[76:79]\n"
"s_waitcnt lgkmcnt(7)\n"
"ds_read_b64_tr_b16 v[62:63], v232 offset:28256\n"
"v_mfma_f32_16x16x32_bf16 v[80:83], v[32:35], a[0:3], v[80:83]\n"
"ds_read_b64_tr_b16 v[0:1], v232 offset:128\n"
"v_accvgpr_mov_b32 a201, a137\n"
"v_mfma_f32_16x16x32_bf16 v[80:83], v[36:39], a[8:11], v[80:83]\n"
"ds_read_b64_tr_b16 v[2:3], v232 offset:3712\n"
"v_accvgpr_mov_b32 a202, a138\n"
"v_mfma_f32_16x16x32_bf16 v[80:83], v[40:43], a[16:19], v[80:83]\n"
"ds_read_b64_tr_b16 v[4:5], v232 offset:8320\n"
"v_mfma_f32_16x16x32_bf16 v[80:83], v[44:47], a[24:27], v[80:83]\n"
"ds_read_b64_tr_b16 v[6:7], v232 offset:11904\n"
"ds_read_b64_tr_b16 v[8:9], v232 offset:16512\n"
"v_mfma_f32_16x16x32_bf16 v[84:87], v[44:47], a[28:31], v[84:87]\n"
"v_accvgpr_mov_b32 a203, a139\n"
"v_accvgpr_mov_b32 a204, a140\n"
"v_mfma_f32_16x16x32_bf16 v[84:87], v[40:43], a[20:23], v[84:87]\n"
"ds_read_b64_tr_b16 v[10:11], v232 offset:20096\n"
"v_mfma_f32_16x16x32_bf16 v[84:87], v[36:39], a[12:15], v[84:87]\n"
"ds_read_b64_tr_b16 v[12:13], v232 offset:24704\n"
"v_accvgpr_mov_b32 a205, a141\n"
"v_mfma_f32_16x16x32_bf16 v[84:87], v[32:35], a[4:7], v[84:87]\n"
"s_waitcnt lgkmcnt(7)\n"
"ds_read_b64_tr_b16 v[14:15], v232 offset:28288\n"
"v_accvgpr_mov_b32 a206, a142\n"
"v_mfma_f32_16x16x32_bf16 v[88:91], v[48:51], a[0:3], v[88:91]\n"
"ds_read_b64_tr_b16 v[16:17], v232 offset:160\n"
"v_mfma_f32_16x16x32_bf16 v[88:91], v[52:55], a[8:11], v[88:91]\n"
"ds_read_b64_tr_b16 v[18:19], v232 offset:3744\n"
"v_accvgpr_mov_b32 a207, a143\n"
"v_mfma_f32_16x16x32_bf16 v[88:91], v[56:59], a[16:19], v[88:91]\n"
"ds_read_b64_tr_b16 v[20:21], v232 offset:8352\n"
"v_mfma_f32_16x16x32_bf16 v[88:91], v[60:63], a[24:27], v[88:91]\n"
"ds_read_b64_tr_b16 v[22:23], v232 offset:11936\n"
"ds_read_b64_tr_b16 v[24:25], v232 offset:16544\n"
"v_mfma_f32_16x16x32_bf16 v[92:95], v[60:63], a[28:31], v[92:95]\n"
"v_accvgpr_mov_b32 a208, a144\n"
"v_accvgpr_mov_b32 a209, a145\n"
"v_mfma_f32_16x16x32_bf16 v[92:95], v[56:59], a[20:23], v[92:95]\n"
"ds_read_b64_tr_b16 v[26:27], v232 offset:20128\n"
"v_mfma_f32_16x16x32_bf16 v[92:95], v[52:55], a[12:15], v[92:95]\n"
"ds_read_b64_tr_b16 v[28:29], v232 offset:24736\n"
"v_accvgpr_mov_b32 a210, a146\n"
"v_mfma_f32_16x16x32_bf16 v[92:95], v[48:51], a[4:7], v[92:95]\n"
"s_waitcnt lgkmcnt(7)\n"
"ds_read_b64_tr_b16 v[30:31], v232 offset:28320\n"
"v_accvgpr_mov_b32 a211, a147\n"
"v_mfma_f32_16x16x32_bf16 v[96:99], v[0:3], a[0:3], v[96:99]\n"
"ds_read_b64_tr_b16 v[32:33], v232 offset:192\n"
"ds_read_b64_tr_b16 v[34:35], v232 offset:3776\n"
"v_mfma_f32_16x16x32_bf16 v[96:99], v[4:7], a[8:11], v[96:99]\n"
"ds_read_b64_tr_b16 v[36:37], v232 offset:8384\n"
"ds_read_b64_tr_b16 v[38:39], v232 offset:11968\n"
"v_mfma_f32_16x16x32_bf16 v[96:99], v[8:11], a[16:19], v[96:99]\n"
"s_add_u32 s54, s50, s47\n"
"ds_read_b64_tr_b16 v[40:41], v232 offset:16576\n"
"ds_read_b64_tr_b16 v[42:43], v232 offset:20160\n"
"v_mfma_f32_16x16x32_bf16 v[96:99], v[12:15], a[24:27], v[96:99]\n"
"s_add_u32 s50, s50, 32768\n"
"ds_read_b64_tr_b16 v[44:45], v232 offset:24768\n"
"ds_read_b64_tr_b16 v[46:47], v232 offset:28352\n"
"v_mfma_f32_16x16x32_bf16 v[100:103], v[12:15], a[28:31], v[100:103]\n"
"s_cmp_lt_u32 s50, 163840\n"
"s_cselect_b32 s50, s50, 0\n"
"v_accvgpr_mov_b32 a212, a148\n"
"v_mfma_f32_16x16x32_bf16 v[100:103], v[8:11], a[20:23], v[100:103]\n"
"v_accvgpr_mov_b32 a213, a149\n"
"v_accvgpr_mov_b32 a214, a150\n"
"v_mfma_f32_16x16x32_bf16 v[100:103], v[4:7], a[12:15], v[100:103]\n"
"s_waitcnt lgkmcnt(0)\n"
"v_add_u32 v232, s50, v228\n"
"v_accvgpr_mov_b32 a215, a151\n"
"v_mfma_f32_16x16x32_bf16 v[100:103], v[0:3], a[4:7], v[100:103]\n"
"s_waitcnt vmcnt(0)\n"
"s_barrier\n"
"v_mfma_f32_16x16x32_bf16 v[104:107], v[16:19], a[0:3], v[104:107]\n"
"ds_read_b64_tr_b16 v[48:49], v232 offset:0\n"
"v_accvgpr_mov_b32 a216, a152\n"
"v_mfma_f32_16x16x32_bf16 v[104:107], v[20:23], a[8:11], v[104:107]\n"
"ds_read_b64_tr_b16 v[50:51], v232 offset:3584\n"
"v_accvgpr_mov_b32 a217, a153\n"
"v_mfma_f32_16x16x32_bf16 v[104:107], v[24:27], a[16:19], v[104:107]\n"
"ds_read_b64_tr_b16 v[52:53], v232 offset:8192\n"
"v_accvgpr_mov_b32 a218, a154\n"
"v_mfma_f32_16x16x32_bf16 v[104:107], v[28:31], a[24:27], v[104:107]\n"
"ds_read_b64_tr_b16 v[54:55], v232 offset:11776\n"
"ds_read_b64_tr_b16 v[56:57], v232 offset:16384\n"
"v_mfma_f32_16x16x32_bf16 v[108:111], v[28:31], a[28:31], v[108:111]\n"
"v_accvgpr_mov_b32 a219, a155\n"
"v_mfma_f32_16x16x32_bf16 v[108:111], v[24:27], a[20:23], v[108:111]\n"
"ds_read_b64_tr_b16 v[58:59], v232 offset:19968\n"
"v_accvgpr_mov_b32 a220, a156\n"
"v_mfma_f32_16x16x32_bf16 v[108:111], v[20:23], a[12:15], v[108:111]\n"
"ds_read_b64_tr_b16 v[60:61], v232 offset:24576\n"
"v_mfma_f32_16x16x32_bf16 v[108:111], v[16:19], a[4:7], v[108:111]\n"
"ds_read_b64_tr_b16 v[62:63], v232 offset:28160\n"
"v_accvgpr_mov_b32 a221, a157\n"
"v_mfma_f32_16x16x32_bf16 v[112:115], v[32:35], a[0:3], v[112:115]\n"
"ds_read_b64_tr_b16 v[0:1], v232 offset:32\n"
"v_accvgpr_mov_b32 a222, a158\n"
"v_mfma_f32_16x16x32_bf16 v[112:115], v[36:39], a[8:11], v[112:115]\n"
"ds_read_b64_tr_b16 v[2:3], v232 offset:3616\n"
"v_mfma_f32_16x16x32_bf16 v[112:115], v[40:43], a[16:19], v[112:115]\n"
"ds_read_b64_tr_b16 v[4:5], v232 offset:8224\n"
"v_accvgpr_mov_b32 a223, a159\n"
"v_mfma_f32_16x16x32_bf16 v[112:115], v[44:47], a[24:27], v[112:115]\n"
"ds_read_b64_tr_b16 v[6:7], v232 offset:11808\n"
"ds_read_b64_tr_b16 v[8:9], v232 offset:16416\n"
"v_mfma_f32_16x16x32_bf16 v[116:119], v[44:47], a[28:31], v[116:119]\n"
"v_mfma_f32_16x16x32_bf16 v[116:119], v[40:43], a[20:23], v[116:119]\n"
"ds_read_b64_tr_b16 v[10:11], v232 offset:20000\n"
"v_mfma_f32_16x16x32_bf16 v[116:119], v[36:39], a[12:15], v[116:119]\n"
"ds_read_b64_tr_b16 v[12:13], v232 offset:24608\n"
"v_mfma_f32_16x16x32_bf16 v[116:119], v[32:35], a[4:7], v[116:119]\n"
"s_waitcnt lgkmcnt(7)\n"
"ds_read_b64_tr_b16 v[14:15], v232 offset:28192\n"
"s_cmp_lg_u32 s37, 2\n"
"s_cbranch_scc1 L_sigq%=\n"
"s_cmp_lg_u32 s47, 0\n"
"s_cbranch_scc1 L_sigq%=\n"
"v_mov_b32 v240, 1\n"
"s_mov_b64 exec, 1\n"
"buffer_store_dword v240, v231, s[20:23], s77 offen offset:4 sc1\n"
"s_mov_b64 exec, -1\n"
"s_nop 3\n"
"L_sigq%=:\n"
"v_mfma_f32_16x16x32_bf16 v[64:67], v[48:51], a[32:35], v[64:67]\n"
"ds_read_b64_tr_b16 v[16:17], v232 offset:64\n"
"v_mfma_f32_16x16x32_bf16 v[64:67], v[52:55], a[40:43], v[64:67]\n"
"ds_read_b64_tr_b16 v[18:19], v232 offset:3648\n"
"v_mfma_f32_16x16x32_bf16 v[64:67], v[56:59], a[48:51], v[64:67]\n"
"ds_read_b64_tr_b16 v[20:21], v232 offset:8256\n"
"v_accvgpr_mov_b32 a224, a160\n"
"v_mfma_f32_16x16x32_bf16 v[64:67], v[60:63], a[56:59], v[64:67]\n"
"ds_read_b64_tr_b16 v[22:23], v232 offset:11840\n"
"ds_read_b64_tr_b16 v[24:25], v232 offset:16448\n"
"v_mfma_f32_16x16x32_bf16 v[68:71], v[60:63], a[60:63], v[68:71]\n"
"buffer_load_dword v239, v182, s[20:23], 0 offen offset:0 sc1\n"
"v_mfma_f32_16x16x32_bf16 v[68:71], v[56:59], a[52:55], v[68:71]\n"
"ds_read_b64_tr_b16 v[26:27], v232 offset:20032\n"
"v_accvgpr_mov_b32 a225, a161\n"
"v_mfma_f32_16x16x32_bf16 v[68:71], v[52:55], a[44:47], v[68:71]\n"
"ds_read_b64_tr_b16 v[28:29], v232 offset:24640\n"
"v_accvgpr_mov_b32 a226, a162\n"
"v_mfma_f32_16x16x32_bf16 v[68:71], v[48:51], a[36:39], v[68:71]\n"
"s_waitcnt lgkmcnt(7)\n"
"ds_read_b64_tr_b16 v[30:31], v232 offset:28224\n"
"v_accvgpr_mov_b32 a227, a163\n"
"v_mfma_f32_16x16x32_bf16 v[72:75], v[0:3], a[32:35], v[72:75]\n"
"ds_read_b64_tr_b16 v[32:33], v232 offset:96\n"
"v_accvgpr_mov_b32 a228, a164\n"
"v_mfma_f32_16x16x32_bf16 v[72:75], v[4:7], a[40:43], v[72:75]\n"
"ds_read_b64_tr_b16 v[34:35], v232 offset:3680\n"
"v_accvgpr_mov_b32 a229, a165\n"
"v_mfma_f32_16x16x32_bf16 v[72:75], v[8:11], a[48:51], v[72:75]\n"
"ds_read_b64_tr_b16 v[36:37], v232 offset:8288\n"
"v_mfma_f32_16x16x32_bf16 v[72:75], v[12:15], a[56:59], v[72:75]\n"
"ds_read_b64_tr_b16 v[38:39], v232 offset:11872\n"
"ds_read_b64_tr_b16 v[40:41], v232 offset:16480\n"
"v_mfma_f32_16x16x32_bf16 v[76:79], v[12:15], a[60:63], v[76:79]\n"
"v_accvgpr_mov_b32 a230, a166\n"
"v_accvgpr_mov_b32 a231, a167\n"
"v_mfma_f32_16x16x32_bf16 v[76:79], v[8:11], a[52:55], v[76:79]\n"
"ds_read_b64_tr_b16 v[42:43], v232 offset:20064\n"
"v_mfma_f32_16x16x32_bf16 v[76:79], v[4:7], a[44:47], v[76:79]\n"
"ds_read_b64_tr_b16 v[44:45], v232 offset:24672\n"
"v_accvgpr_mov_b32 a232, a168\n"
"v_mfma_f32_16x16x32_bf16 v[76:79], v[0:3], a[36:39], v[76:79]\n"
"s_waitcnt lgkmcnt(7)\n"
"ds_read_b64_tr_b16 v[46:47], v232 offset:28256\n"
"v_mfma_f32_16x16x32_bf16 v[80:83], v[16:19], a[32:35], v[80:83]\n"
"ds_read_b64_tr_b16 v[48:49], v232 offset:128\n"
"v_accvgpr_mov_b32 a233, a169\n"
"v_mfma_f32_16x16x32_bf16 v[80:83], v[20:23], a[40:43], v[80:83]\n"
"ds_read_b64_tr_b16 v[50:51], v232 offset:3712\n"
"v_accvgpr_mov_b32 a234, a170\n"
"v_mfma_f32_16x16x32_bf16 v[80:83], v[24:27], a[48:51], v[80:83]\n"
"ds_read_b64_tr_b16 v[52:53], v232 offset:8320\n"
"v_mfma_f32_16x16x32_bf16 v[80:83], v[28:31], a[56:59], v[80:83]\n"
"ds_read_b64_tr_b16 v[54:55], v232 offset:11904\n"
"ds_read_b64_tr_b16 v[56:57], v232 offset:16512\n"
"v_mfma_f32_16x16x32_bf16 v[84:87], v[28:31], a[60:63], v[84:87]\n"
"v_accvgpr_mov_b32 a235, a171\n"
"v_accvgpr_mov_b32 a236, a172\n"
"v_mfma_f32_16x16x32_bf16 v[84:87], v[24:27], a[52:55], v[84:87]\n"
"ds_read_b64_tr_b16 v[58:59], v232 offset:20096\n"
"v_mfma_f32_16x16x32_bf16 v[84:87], v[20:23], a[44:47], v[84:87]\n"
"ds_read_b64_tr_b16 v[60:61], v232 offset:24704\n"
"v_accvgpr_mov_b32 a237, a173\n"
"v_mfma_f32_16x16x32_bf16 v[84:87], v[16:19], a[36:39], v[84:87]\n"
"s_waitcnt lgkmcnt(7)\n"
"ds_read_b64_tr_b16 v[62:63], v232 offset:28288\n"
"v_accvgpr_mov_b32 a238, a174\n"
"v_mfma_f32_16x16x32_bf16 v[88:91], v[32:35], a[32:35], v[88:91]\n"
"ds_read_b64_tr_b16 v[0:1], v232 offset:160\n"
"v_mfma_f32_16x16x32_bf16 v[88:91], v[36:39], a[40:43], v[88:91]\n"
"ds_read_b64_tr_b16 v[2:3], v232 offset:3744\n"
"v_accvgpr_mov_b32 a239, a175\n"
"v_mfma_f32_16x16x32_bf16 v[88:91], v[40:43], a[48:51], v[88:91]\n"
"ds_read_b64_tr_b16 v[4:5], v232 offset:8352\n"
"v_mfma_f32_16x16x32_bf16 v[88:91], v[44:47], a[56:59], v[88:91]\n"
"ds_read_b64_tr_b16 v[6:7], v232 offset:11936\n"
"ds_read_b64_tr_b16 v[8:9], v232 offset:16544\n"
"v_mfma_f32_16x16x32_bf16 v[92:95], v[44:47], a[60:63], v[92:95]\n"
"v_accvgpr_mov_b32 a240, a176\n"
"v_accvgpr_mov_b32 a241, a177\n"
"v_mfma_f32_16x16x32_bf16 v[92:95], v[40:43], a[52:55], v[92:95]\n"
"ds_read_b64_tr_b16 v[10:11], v232 offset:20128\n"
"v_mfma_f32_16x16x32_bf16 v[92:95], v[36:39], a[44:47], v[92:95]\n"
"ds_read_b64_tr_b16 v[12:13], v232 offset:24736\n"
"v_accvgpr_mov_b32 a242, a178\n"
"v_mfma_f32_16x16x32_bf16 v[92:95], v[32:35], a[36:39], v[92:95]\n"
"s_waitcnt lgkmcnt(7)\n"
"ds_read_b64_tr_b16 v[14:15], v232 offset:28320\n"
"v_accvgpr_mov_b32 a243, a179\n"
"v_mfma_f32_16x16x32_bf16 v[96:99], v[48:51], a[32:35], v[96:99]\n"
"ds_read_b64_tr_b16 v[16:17], v232 offset:192\n"
"ds_read_b64_tr_b16 v[18:19], v232 offset:3776\n"
"v_mfma_f32_16x16x32_bf16 v[96:99], v[52:55], a[40:43], v[96:99]\n"
"ds_read_b64_tr_b16 v[20:21], v232 offset:8384\n"
"ds_read_b64_tr_b16 v[22:23], v232 offset:11968\n"
"v_mfma_f32_16x16x32_bf16 v[96:99], v[56:59], a[48:51], v[96:99]\n"
"s_add_u32 s54, s50, s47\n"
"ds_read_b64_tr_b16 v[24:25], v232 offset:16576\n"
"ds_read_b64_tr_b16 v[26:27], v232 offset:20160\n"
"v_mfma_f32_16x16x32_bf16 v[96:99], v[60:63], a[56:59], v[96:99]\n"
"s_add_u32 s50, s50, 32768\n"
"ds_read_b64_tr_b16 v[28:29], v232 offset:24768\n"
"ds_read_b64_tr_b16 v[30:31], v232 offset:28352\n"
"v_mfma_f32_16x16x32_bf16 v[100:103], v[60:63], a[60:63], v[100:103]\n"
"s_cmp_lt_u32 s50, 163840\n"
"s_cselect_b32 s50, s50, 0\n"
"v_accvgpr_mov_b32 a244, a180\n"
"v_mfma_f32_16x16x32_bf16 v[100:103], v[56:59], a[52:55], v[100:103]\n"
"v_accvgpr_mov_b32 a245, a181\n"
"v_accvgpr_mov_b32 a246, a182\n"
"v_mfma_f32_16x16x32_bf16 v[100:103], v[52:55], a[44:47], v[100:103]\n"
"s_waitcnt lgkmcnt(0)\n"
"v_add_u32 v232, s50, v228\n"
"v_accvgpr_mov_b32 a247, a183\n"
"v_mfma_f32_16x16x32_bf16 v[100:103], v[48:51], a[36:39], v[100:103]\n"
"s_waitcnt vmcnt(26)\n"
"s_barrier\n"
"v_mfma_f32_16x16x32_bf16 v[104:107], v[0:3], a[32:35], v[104:107]\n"
"ds_read_b64_tr_b16 v[32:33], v232 offset:0\n"
"v_accvgpr_mov_b32 a248, a184\n"
"v_mfma_f32_16x16x32_bf16 v[104:107], v[4:7], a[40:43], v[104:107]\n"
"ds_read_b64_tr_b16 v[34:35], v232 offset:3584\n"
"v_accvgpr_mov_b32 a249, a185\n"
"v_mfma_f32_16x16x32_bf16 v[104:107], v[8:11], a[48:51], v[104:107]\n"
"ds_read_b64_tr_b16 v[36:37], v232 offset:8192\n"
"v_accvgpr_mov_b32 a250, a186\n"
"v_mfma_f32_16x16x32_bf16 v[104:107], v[12:15], a[56:59], v[104:107]\n"
"ds_read_b64_tr_b16 v[38:39], v232 offset:11776\n"
"ds_read_b64_tr_b16 v[40:41], v232 offset:16384\n"
"v_mfma_f32_16x16x32_bf16 v[108:111], v[12:15], a[60:63], v[108:111]\n"
"v_accvgpr_mov_b32 a251, a187\n"
"v_mfma_f32_16x16x32_bf16 v[108:111], v[8:11], a[52:55], v[108:111]\n"
"ds_read_b64_tr_b16 v[42:43], v232 offset:19968\n"
"v_accvgpr_mov_b32 a252, a188\n"
"v_mfma_f32_16x16x32_bf16 v[108:111], v[4:7], a[44:47], v[108:111]\n"
"ds_read_b64_tr_b16 v[44:45], v232 offset:24576\n"
"v_mfma_f32_16x16x32_bf16 v[108:111], v[0:3], a[36:39], v[108:111]\n"
"ds_read_b64_tr_b16 v[46:47], v232 offset:28160\n"
"v_accvgpr_mov_b32 a253, a189\n"
"v_mfma_f32_16x16x32_bf16 v[112:115], v[16:19], a[32:35], v[112:115]\n"
"ds_read_b64_tr_b16 v[48:49], v232 offset:32\n"
"v_accvgpr_mov_b32 a254, a190\n"
"v_mfma_f32_16x16x32_bf16 v[112:115], v[20:23], a[40:43], v[112:115]\n"
"ds_read_b64_tr_b16 v[50:51], v232 offset:3616\n"
"v_mfma_f32_16x16x32_bf16 v[112:115], v[24:27], a[48:51], v[112:115]\n"
"ds_read_b64_tr_b16 v[52:53], v232 offset:8224\n"
"v_accvgpr_mov_b32 a255, a191\n"
"v_mfma_f32_16x16x32_bf16 v[112:115], v[28:31], a[56:59], v[112:115]\n"
"ds_read_b64_tr_b16 v[54:55], v232 offset:11808\n"
"ds_read_b64_tr_b16 v[56:57], v232 offset:16416\n"
"v_mfma_f32_16x16x32_bf16 v[116:119], v[28:31], a[60:63], v[116:119]\n"
"v_mfma_f32_16x16x32_bf16 v[116:119], v[24:27], a[52:55], v[116:119]\n"
"ds_read_b64_tr_b16 v[58:59], v232 offset:20000\n"
"v_mfma_f32_16x16x32_bf16 v[116:119], v[20:23], a[44:47], v[116:119]\n"
"ds_read_b64_tr_b16 v[60:61], v232 offset:24608\n"
"v_mfma_f32_16x16x32_bf16 v[116:119], v[16:19], a[36:39], v[116:119]\n"
"s_waitcnt lgkmcnt(7)\n"
"ds_read_b64_tr_b16 v[62:63], v232 offset:28192\n"
"s_waitcnt vmcnt(0)\n"
"s_mov_b32 s55, 0\n"
"L_kvw%=_chk:\n"
"v_cmp_ne_u32 vcc, 0, v239\n"
"s_nop 0\n"
"s_cmp_eq_u64 vcc, -1\n"
"s_cbranch_scc1 L_kvw%=_ok\n"
"s_sleep 2\n"
"buffer_load_dword v239, v182, s[20:23], 0 offen offset:0 sc1\n"
"s_add_u32 s55, s55, 1\n"
"s_waitcnt vmcnt(0)\n"
"s_cmp_lt_u32 s55, 500000\n"
"s_cbranch_scc1 L_kvw%=_chk\n"
"L_kvw%=_ok:\n"
"v_mfma_f32_16x16x32_bf16 v[64:67], v[32:35], a[64:67], v[64:67]\n"
"s_add_u32 m0, s47, 0\n"
"ds_read_b64_tr_b16 v[0:1], v232 offset:64\n"
"v_mfma_f32_16x16x32_bf16 v[64:67], v[36:39], a[72:75], v[64:67]\n"
"ds_read_b64_tr_b16 v[2:3], v232 offset:3648\n"
"v_readlane_b32 s49, v224, s48\n"
"v_mfma_f32_16x16x32_bf16 v[64:67], v[40:43], a[80:83], v[64:67]\n"
"ds_read_b64_tr_b16 v[4:5], v232 offset:8256\n"
"v_mfma_f32_16x16x32_bf16 v[64:67], v[44:47], a[88:91], v[64:67]\n"
"ds_read_b64_tr_b16 v[6:7], v232 offset:11840\n"
"ds_read_b64_tr_b16 v[8:9], v232 offset:16448\n"
"v_mfma_f32_16x16x32_bf16 v[68:71], v[44:47], a[92:95], v[68:71]\n"
"buffer_load_dword v239, v231, s[20:23], s45 offen offset:0 sc1\n"
"v_mfma_f32_16x16x32_bf16 v[68:71], v[40:43], a[84:87], v[68:71]\n"
"s_add_u32 s48, s48, 1\n"
"ds_read_b64_tr_b16 v[10:11], v232 offset:20032\n"
"v_mfma_f32_16x16x32_bf16 v[68:71], v[36:39], a[76:79], v[68:71]\n"
"ds_read_b64_tr_b16 v[12:13], v232 offset:24640\n"
"v_mfma_f32_16x16x32_bf16 v[68:71], v[32:35], a[68:71], v[68:71]\n"
"s_waitcnt lgkmcnt(7)\n"
"ds_read_b64_tr_b16 v[14:15], v232 offset:28224\n"
"v_mfma_f32_16x16x32_bf16 v[72:75], v[48:51], a[64:67], v[72:75]\n"
"ds_read_b64_tr_b16 v[16:17], v232 offset:96\n"
"v_mfma_f32_16x16x32_bf16 v[72:75], v[52:55], a[72:75], v[72:75]\n"
"ds_read_b64_tr_b16 v[18:19], v232 offset:3680\n"
"v_mfma_f32_16x16x32_bf16 v[72:75], v[56:59], a[80:83], v[72:75]\n"
"ds_read_b64_tr_b16 v[20:21], v232 offset:8288\n"
"buffer_load_dwordx4 v208, s[24:27], s49 offen offset:0 sc1 lds\n"
"v_mfma_f32_16x16x32_bf16 v[72:75], v[60:63], a[88:91], v[72:75]\n"
"ds_read_b64_tr_b16 v[22:23], v232 offset:11872\n"
"ds_read_b64_tr_b16 v[24:25], v232 offset:16480\n"
"v_mfma_f32_16x16x32_bf16 v[76:79], v[60:63], a[92:95], v[76:79]\n"
"buffer_load_dwordx4 v209, s[24:27], s49 offen offset:1024 sc1 lds\n"
"v_mfma_f32_16x16x32_bf16 v[76:79], v[56:59], a[84:87], v[76:79]\n"
"ds_read_b64_tr_b16 v[26:27], v232 offset:20064\n"
"v_mfma_f32_16x16x32_bf16 v[76:79], v[52:55], a[76:79], v[76:79]\n"
"ds_read_b64_tr_b16 v[28:29], v232 offset:24672\n"
"buffer_load_dwordx4 v210, s[24:27], s49 offen offset:2048 sc1 lds\n"
"v_mfma_f32_16x16x32_bf16 v[76:79], v[48:51], a[68:71], v[76:79]\n"
"s_waitcnt lgkmcnt(7)\n"
"ds_read_b64_tr_b16 v[30:31], v232 offset:28256\n"
"v_mfma_f32_16x16x32_bf16 v[80:83], v[0:3], a[64:67], v[80:83]\n"
"ds_read_b64_tr_b16 v[32:33], v232 offset:128\n"
"buffer_load_dwordx4 v211, s[24:27], s49 offen offset:3072 sc1 lds\n"
"v_mfma_f32_16x16x32_bf16 v[80:83], v[4:7], a[72:75], v[80:83]\n"
"s_add_u32 m0, m0, 4096\n"
"ds_read_b64_tr_b16 v[34:35], v232 offset:3712\n"
"v_mfma_f32_16x16x32_bf16 v[80:83], v[8:11], a[80:83], v[80:83]\n"
"ds_read_b64_tr_b16 v[36:37], v232 offset:8320\n"
"buffer_load_dwordx4 v212, s[24:27], s49 offen offset:0 sc1 lds\n"
"v_mfma_f32_16x16x32_bf16 v[80:83], v[12:15], a[88:91], v[80:83]\n"
"ds_read_b64_tr_b16 v[38:39], v232 offset:11904\n"
"ds_read_b64_tr_b16 v[40:41], v232 offset:16512\n"
"v_mfma_f32_16x16x32_bf16 v[84:87], v[12:15], a[92:95], v[84:87]\n"
"buffer_load_dwordx4 v213, s[24:27], s49 offen offset:1024 sc1 lds\n"
"v_mfma_f32_16x16x32_bf16 v[84:87], v[8:11], a[84:87], v[84:87]\n"
"ds_read_b64_tr_b16 v[42:43], v232 offset:20096\n"
"v_mfma_f32_16x16x32_bf16 v[84:87], v[4:7], a[76:79], v[84:87]\n"
"ds_read_b64_tr_b16 v[44:45], v232 offset:24704\n"
"buffer_load_dwordx4 v214, s[24:27], s49 offen offset:2048 sc1 lds\n"
"v_mfma_f32_16x16x32_bf16 v[84:87], v[0:3], a[68:71], v[84:87]\n"
"s_waitcnt lgkmcnt(7)\n"
"ds_read_b64_tr_b16 v[46:47], v232 offset:28288\n"
"v_mfma_f32_16x16x32_bf16 v[88:91], v[16:19], a[64:67], v[88:91]\n"
"ds_read_b64_tr_b16 v[48:49], v232 offset:160\n"
"buffer_load_dwordx4 v215, s[24:27], s49 offen offset:3072 sc1 lds\n"
"v_mfma_f32_16x16x32_bf16 v[88:91], v[20:23], a[72:75], v[88:91]\n"
"s_add_u32 m0, s47, 32768\n"
"ds_read_b64_tr_b16 v[50:51], v232 offset:3744\n"
"v_readlane_b32 s49, v224, s48\n"
"v_mfma_f32_16x16x32_bf16 v[88:91], v[24:27], a[80:83], v[88:91]\n"
"ds_read_b64_tr_b16 v[52:53], v232 offset:8352\n"
"v_mfma_f32_16x16x32_bf16 v[88:91], v[28:31], a[88:91], v[88:91]\n"
"ds_read_b64_tr_b16 v[54:55], v232 offset:11936\n"
"ds_read_b64_tr_b16 v[56:57], v232 offset:16544\n"
"v_mfma_f32_16x16x32_bf16 v[92:95], v[28:31], a[92:95], v[92:95]\n"
"s_add_u32 s48, s48, 1\n"
"v_mfma_f32_16x16x32_bf16 v[92:95], v[24:27], a[84:87], v[92:95]\n"
"ds_read_b64_tr_b16 v[58:59], v232 offset:20128\n"
"v_mfma_f32_16x16x32_bf16 v[92:95], v[20:23], a[76:79], v[92:95]\n"
"ds_read_b64_tr_b16 v[60:61], v232 offset:24736\n"
"v_mfma_f32_16x16x32_bf16 v[92:95], v[16:19], a[68:71], v[92:95]\n"
"s_waitcnt lgkmcnt(7)\n"
"ds_read_b64_tr_b16 v[62:63], v232 offset:28320\n"
"v_mfma_f32_16x16x32_bf16 v[96:99], v[32:35], a[64:67], v[96:99]\n"
"ds_read_b64_tr_b16 v[0:1], v232 offset:192\n"
"ds_read_b64_tr_b16 v[2:3], v232 offset:3776\n"
"v_mfma_f32_16x16x32_bf16 v[96:99], v[36:39], a[72:75], v[96:99]\n"
"ds_read_b64_tr_b16 v[4:5], v232 offset:8384\n"
"ds_read_b64_tr_b16 v[6:7], v232 offset:11968\n"
"v_mfma_f32_16x16x32_bf16 v[96:99], v[40:43], a[80:83], v[96:99]\n"
"s_add_u32 s54, s50, s47\n"
"ds_read_b64_tr_b16 v[8:9], v232 offset:16576\n"
"ds_read_b64_tr_b16 v[10:11], v232 offset:20160\n"
"v_mfma_f32_16x16x32_bf16 v[96:99], v[44:47], a[88:91], v[96:99]\n"
"s_add_u32 s50, s50, 32768\n"
"ds_read_b64_tr_b16 v[12:13], v232 offset:24768\n"
"ds_read_b64_tr_b16 v[14:15], v232 offset:28352\n"
"v_mfma_f32_16x16x32_bf16 v[100:103], v[44:47], a[92:95], v[100:103]\n"
"s_cmp_lt_u32 s50, 163840\n"
"s_cselect_b32 s50, s50, 0\n"
"v_mfma_f32_16x16x32_bf16 v[100:103], v[40:43], a[84:87], v[100:103]\n"
"buffer_load_dwordx4 v208, s[24:27], s49 offen offset:0 sc1 lds\n"
"v_mfma_f32_16x16x32_bf16 v[100:103], v[36:39], a[76:79], v[100:103]\n"
"s_waitcnt lgkmcnt(0)\n"
"v_add_u32 v232, s50, v228\n"
"v_mfma_f32_16x16x32_bf16 v[100:103], v[32:35], a[68:71], v[100:103]\n"
"s_waitcnt vmcnt(27)\n"
"s_barrier\n"
"v_mfma_f32_16x16x32_bf16 v[104:107], v[48:51], a[64:67], v[104:107]\n"
"ds_read_b64_tr_b16 v[16:17], v232 offset:0\n"
"buffer_load_dwordx4 v209, s[24:27], s49 offen offset:1024 sc1 lds\n"
"v_mfma_f32_16x16x32_bf16 v[104:107], v[52:55], a[72:75], v[104:107]\n"
"ds_read_b64_tr_b16 v[18:19], v232 offset:3584\n"
"buffer_load_dwordx4 v210, s[24:27], s49 offen offset:2048 sc1 lds\n"
"v_mfma_f32_16x16x32_bf16 v[104:107], v[56:59], a[80:83], v[104:107]\n"
"ds_read_b64_tr_b16 v[20:21], v232 offset:8192\n"
"v_mfma_f32_16x16x32_bf16 v[104:107], v[60:63], a[88:91], v[104:107]\n"
"ds_read_b64_tr_b16 v[22:23], v232 offset:11776\n"
"ds_read_b64_tr_b16 v[24:25], v232 offset:32\n"
"v_mfma_f32_16x16x32_bf16 v[108:111], v[60:63], a[92:95], v[108:111]\n"
"buffer_load_dwordx4 v211, s[24:27], s49 offen offset:3072 sc1 lds\n"
"v_mfma_f32_16x16x32_bf16 v[108:111], v[56:59], a[84:87], v[108:111]\n"
"s_add_u32 m0, m0, 4096\n"
"ds_read_b64_tr_b16 v[26:27], v232 offset:3616\n"
"v_mfma_f32_16x16x32_bf16 v[108:111], v[52:55], a[76:79], v[108:111]\n"
"ds_read_b64_tr_b16 v[28:29], v232 offset:8224\n"
"buffer_load_dwordx4 v212, s[24:27], s49 offen offset:0 sc1 lds\n"
"v_mfma_f32_16x16x32_bf16 v[108:111], v[48:51], a[68:71], v[108:111]\n"
"ds_read_b64_tr_b16 v[30:31], v232 offset:11808\n"
"v_mfma_f32_16x16x32_bf16 v[112:115], v[0:3], a[64:67], v[112:115]\n"
"ds_read_b64_tr_b16 v[32:33], v232 offset:64\n"
"v_mfma_f32_16x16x32_bf16 v[112:115], v[4:7], a[72:75], v[112:115]\n"
"ds_read_b64_tr_b16 v[34:35], v232 offset:3648\n"
"buffer_load_dwordx4 v213, s[24:27], s49 offen offset:1024 sc1 lds\n"
"v_mfma_f32_16x16x32_bf16 v[112:115], v[8:11], a[80:83], v[112:115]\n"
"ds_read_b64_tr_b16 v[36:37], v232 offset:8256\n"
"v_mfma_f32_16x16x32_bf16 v[112:115], v[12:15], a[88:91], v[112:115]\n"
"ds_read_b64_tr_b16 v[38:39], v232 offset:11840\n"
"ds_read_b64_tr_b16 v[40:41], v232 offset:96\n"
"v_mfma_f32_16x16x32_bf16 v[116:119], v[12:15], a[92:95], v[116:119]\n"
"buffer_load_dwordx4 v214, s[24:27], s49 offen offset:2048 sc1 lds\n"
"v_mfma_f32_16x16x32_bf16 v[116:119], v[8:11], a[84:87], v[116:119]\n"
"ds_read_b64_tr_b16 v[42:43], v232 offset:3680\n"
"v_mfma_f32_16x16x32_bf16 v[116:119], v[4:7], a[76:79], v[116:119]\n"
"ds_read_b64_tr_b16 v[44:45], v232 offset:8288\n"
"v_mfma_f32_16x16x32_bf16 v[116:119], v[0:3], a[68:71], v[116:119]\n"
"s_waitcnt lgkmcnt(7)\n"
"ds_read_b64_tr_b16 v[46:47], v232 offset:11872\n"
"buffer_load_dwordx4 v215, s[24:27], s49 offen offset:3072 sc1 lds\n"
"s_cmp_lg_u32 s37, 3\n"
"s_cbranch_scc1 L_noldq%=\n"
"s_waitcnt vmcnt(16)\n"
"s_mov_b32 s55, 0\n"
"L_qw%=_chk:\n"
"v_cmp_ne_u32 vcc, 0, v239\n"
"s_nop 0\n"
"s_cmp_eq_u64 vcc, -1\n"
"s_cbranch_scc1 L_qw%=_ok\n"
"s_sleep 2\n"
"buffer_load_dword v239, v231, s[20:23], s45 offen offset:0 sc1\n"
"s_add_u32 s55, s55, 1\n"
"s_waitcnt vmcnt(0)\n"
"s_cmp_lt_u32 s55, 500000\n"
"s_cbranch_scc1 L_qw%=_chk\n"
"L_qw%=_ok:\n"
"s_branch L_w13b%=\n"
"L_noldq%=:\n"
"v_mfma_f32_16x16x32_bf16 v[64:67], v[16:19], a[96:99], v[64:67]\n"
"ds_read_b64_tr_b16 v[48:49], v232 offset:128\n"
"ds_read_b64_tr_b16 v[50:51], v232 offset:3712\n"
"v_mfma_f32_16x16x32_bf16 v[64:67], v[20:23], a[104:107], v[64:67]\n"
"ds_read_b64_tr_b16 v[52:53], v232 offset:8320\n"
"ds_read_b64_tr_b16 v[54:55], v232 offset:11904\n"
"v_mfma_f32_16x16x32_bf16 v[68:71], v[20:23], a[108:111], v[68:71]\n"
"ds_read_b64_tr_b16 v[56:57], v232 offset:160\n"
"ds_read_b64_tr_b16 v[58:59], v232 offset:3744\n"
"v_mfma_f32_16x16x32_bf16 v[68:71], v[16:19], a[100:103], v[68:71]\n"
"ds_read_b64_tr_b16 v[60:61], v232 offset:8352\n"
"ds_read_b64_tr_b16 v[62:63], v232 offset:11936\n"
"v_mfma_f32_16x16x32_bf16 v[72:75], v[24:27], a[96:99], v[72:75]\n"
"ds_read_b64_tr_b16 v[0:1], v232 offset:192\n"
"ds_read_b64_tr_b16 v[2:3], v232 offset:3776\n"
"v_mfma_f32_16x16x32_bf16 v[72:75], v[28:31], a[104:107], v[72:75]\n"
"ds_read_b64_tr_b16 v[4:5], v232 offset:8384\n"
"ds_read_b64_tr_b16 v[6:7], v232 offset:11968\n"
"v_mfma_f32_16x16x32_bf16 v[76:79], v[28:31], a[108:111], v[76:79]\n"
"s_add_u32 s54, s50, s47\n"
"v_mfma_f32_16x16x32_bf16 v[76:79], v[24:27], a[100:103], v[76:79]\n"
"s_waitcnt lgkmcnt(12)\n"
"s_add_u32 s50, s50, 32768\n"
"v_mfma_f32_16x16x32_bf16 v[80:83], v[32:35], a[96:99], v[80:83]\n"
"s_cmp_lt_u32 s50, 163840\n"
"s_cselect_b32 s50, s50, 0\n"
"v_mfma_f32_16x16x32_bf16 v[80:83], v[36:39], a[104:107], v[80:83]\n"
"v_mfma_f32_16x16x32_bf16 v[84:87], v[36:39], a[108:111], v[84:87]\n"
"v_mfma_f32_16x16x32_bf16 v[84:87], v[32:35], a[100:103], v[84:87]\n"
"v_mfma_f32_16x16x32_bf16 v[88:91], v[40:43], a[96:99], v[88:91]\n"
"v_mfma_f32_16x16x32_bf16 v[88:91], v[44:47], a[104:107], v[88:91]\n"
"v_mfma_f32_16x16x32_bf16 v[92:95], v[44:47], a[108:111], v[92:95]\n"
"v_mfma_f32_16x16x32_bf16 v[92:95], v[40:43], a[100:103], v[92:95]\n"
"s_waitcnt lgkmcnt(4)\n"
"v_mfma_f32_16x16x32_bf16 v[96:99], v[48:51], a[96:99], v[96:99]\n"
"v_mfma_f32_16x16x32_bf16 v[96:99], v[52:55], a[104:107], v[96:99]\n"
"v_mfma_f32_16x16x32_bf16 v[100:103], v[52:55], a[108:111], v[100:103]\n"
"v_mfma_f32_16x16x32_bf16 v[100:103], v[48:51], a[100:103], v[100:103]\n"
"v_mfma_f32_16x16x32_bf16 v[104:107], v[56:59], a[96:99], v[104:107]\n"
"v_mfma_f32_16x16x32_bf16 v[104:107], v[60:63], a[104:107], v[104:107]\n"
"v_mfma_f32_16x16x32_bf16 v[108:111], v[60:63], a[108:111], v[108:111]\n"
"v_mfma_f32_16x16x32_bf16 v[108:111], v[56:59], a[100:103], v[108:111]\n"
"s_waitcnt lgkmcnt(0)\n"
"v_mfma_f32_16x16x32_bf16 v[112:115], v[0:3], a[96:99], v[112:115]\n"
"v_mfma_f32_16x16x32_bf16 v[112:115], v[4:7], a[104:107], v[112:115]\n"
"v_mfma_f32_16x16x32_bf16 v[116:119], v[4:7], a[108:111], v[116:119]\n"
"v_mfma_f32_16x16x32_bf16 v[116:119], v[0:3], a[100:103], v[116:119]\n"
"L_w1end%=:\n"
"v_mov_b32 v252, 0\n"
"v_mov_b32 v253, 0\n"
"v_mov_b32 v254, 0\n"
"v_mov_b32 v255, 0\n"
"s_nop 1\n"
"v_mfma_f32_32x32x16_bf16 a[0:15], v[252:255], v[252:255], 0\n"
"s_add_u32 m0, s47, 65536\n"
"v_readlane_b32 s49, v224, s48\n"
"v_max_f32 v64, 0, v64\n"
"v_max_f32 v65, 0, v65\n"
"v_max_f32 v66, 0, v66\n"
"v_max_f32 v67, 0, v67\n"
"v_mul_f32 v64, v64, v64\n"
"v_mul_f32 v65, v65, v65\n"
"v_mul_f32 v66, v66, v66\n"
"v_mul_f32 v67, v67, v67\n"
"v_cvt_pk_bf16_f32 v176, v64, v65\n"
"v_cvt_pk_bf16_f32 v177, v66, v67\n"
"s_add_u32 s48, s48, 1\n"
"buffer_load_dwordx4 v208, s[24:27], s49 offen offset:0 sc1 lds\n"
"v_max_f32 v68, 0, v68\n"
"v_max_f32 v69, 0, v69\n"
"v_max_f32 v70, 0, v70\n"
"v_max_f32 v71, 0, v71\n"
"v_mul_f32 v68, v68, v68\n"
"v_mul_f32 v69, v69, v69\n"
"v_mul_f32 v70, v70, v70\n"
"v_mul_f32 v71, v71, v71\n"
"v_cvt_pk_bf16_f32 v192, v68, v69\n"
"v_cvt_pk_bf16_f32 v193, v70, v71\n"
"v_mfma_f32_32x32x16_bf16 a[16:31], v[252:255], v[252:255], 0\n"
"buffer_load_dwordx4 v209, s[24:27], s49 offen offset:1024 sc1 lds\n"
"buffer_load_dwordx4 v210, s[24:27], s49 offen offset:2048 sc1 lds\n"
"v_max_f32 v72, 0, v72\n"
"v_max_f32 v73, 0, v73\n"
"v_max_f32 v74, 0, v74\n"
"v_max_f32 v75, 0, v75\n"
"v_mul_f32 v72, v72, v72\n"
"v_mul_f32 v73, v73, v73\n"
"v_mul_f32 v74, v74, v74\n"
"v_mul_f32 v75, v75, v75\n"
"v_cvt_pk_bf16_f32 v178, v72, v73\n"
"v_cvt_pk_bf16_f32 v179, v74, v75\n"
"buffer_load_dwordx4 v211, s[24:27], s49 offen offset:3072 sc1 lds\n"
"s_add_u32 m0, m0, 4096\n"
"v_max_f32 v76, 0, v76\n"
"v_max_f32 v77, 0, v77\n"
"v_max_f32 v78, 0, v78\n"
"v_max_f32 v79, 0, v79\n"
"v_mul_f32 v76, v76, v76\n"
"v_mul_f32 v77, v77, v77\n"
"v_mul_f32 v78, v78, v78\n"
"v_mul_f32 v79, v79, v79\n"
"v_cvt_pk_bf16_f32 v194, v76, v77\n"
"v_cvt_pk_bf16_f32 v195, v78, v79\n"
"v_mfma_f32_32x32x16_bf16 a[32:47], v[252:255], v[252:255], 0\n"
"buffer_load_dwordx4 v212, s[24:27], s49 offen offset:0 sc1 lds\n"
"buffer_load_dwordx4 v213, s[24:27], s49 offen offset:1024 sc1 lds\n"
"v_max_f32 v80, 0, v80\n"
"v_max_f32 v81, 0, v81\n"
"v_max_f32 v82, 0, v82\n"
"v_max_f32 v83, 0, v83\n"
"v_mul_f32 v80, v80, v80\n"
"v_mul_f32 v81, v81, v81\n"
"v_mul_f32 v82, v82, v82\n"
"v_mul_f32 v83, v83, v83\n"
"v_cvt_pk_bf16_f32 v180, v80, v81\n"
"v_cvt_pk_bf16_f32 v181, v82, v83\n"
"buffer_load_dwordx4 v214, s[24:27], s49 offen offset:2048 sc1 lds\n"
"buffer_load_dwordx4 v215, s[24:27], s49 offen offset:3072 sc1 lds\n"
"v_max_f32 v84, 0, v84\n"
"v_max_f32 v85, 0, v85\n"
"v_max_f32 v86, 0, v86\n"
"v_max_f32 v87, 0, v87\n"
"v_mul_f32 v84, v84, v84\n"
"v_mul_f32 v85, v85, v85\n"
"v_mul_f32 v86, v86, v86\n"
"v_mul_f32 v87, v87, v87\n"
"v_cvt_pk_bf16_f32 v196, v84, v85\n"
"v_cvt_pk_bf16_f32 v197, v86, v87\n"
"v_mfma_f32_32x32x16_bf16 a[48:63], v[252:255], v[252:255], 0\n"
"s_add_u32 m0, s47, 98304\n"
"v_readlane_b32 s49, v224, s48\n"
"v_max_f32 v88, 0, v88\n"
"v_max_f32 v89, 0, v89\n"
"v_max_f32 v90, 0, v90\n"
"v_max_f32 v91, 0, v91\n"
"v_mul_f32 v88, v88, v88\n"
"v_mul_f32 v89, v89, v89\n"
"v_mul_f32 v90, v90, v90\n"
"v_mul_f32 v91, v91, v91\n"
"v_cvt_pk_bf16_f32 v182, v88, v89\n"
"v_cvt_pk_bf16_f32 v183, v90, v91\n"
"s_add_u32 s48, s48, 1\n"
"buffer_load_dwordx4 v208, s[24:27], s49 offen offset:0 sc1 lds\n"
"v_max_f32 v92, 0, v92\n"
"v_max_f32 v93, 0, v93\n"
"v_max_f32 v94, 0, v94\n"
"v_max_f32 v95, 0, v95\n"
"v_mul_f32 v92, v92, v92\n"
"v_mul_f32 v93, v93, v93\n"
"v_mul_f32 v94, v94, v94\n"
"v_mul_f32 v95, v95, v95\n"
"v_cvt_pk_bf16_f32 v198, v92, v93\n"
"v_cvt_pk_bf16_f32 v199, v94, v95\n"
"v_mfma_f32_32x32x16_bf16 a[64:79], v[252:255], v[252:255], 0\n"
"buffer_load_dwordx4 v209, s[24:27], s49 offen offset:1024 sc1 lds\n"
"buffer_load_dwordx4 v210, s[24:27], s49 offen offset:2048 sc1 lds\n"
"v_max_f32 v96, 0, v96\n"
"v_max_f32 v97, 0, v97\n"
"v_max_f32 v98, 0, v98\n"
"v_max_f32 v99, 0, v99\n"
"v_mul_f32 v96, v96, v96\n"
"v_mul_f32 v97, v97, v97\n"
"v_mul_f32 v98, v98, v98\n"
"v_mul_f32 v99, v99, v99\n"
"v_cvt_pk_bf16_f32 v184, v96, v97\n"
"v_cvt_pk_bf16_f32 v185, v98, v99\n"
"buffer_load_dwordx4 v211, s[24:27], s49 offen offset:3072 sc1 lds\n"
"s_add_u32 m0, m0, 4096\n"
"v_max_f32 v100, 0, v100\n"
"v_max_f32 v101, 0, v101\n"
"v_max_f32 v102, 0, v102\n"
"v_max_f32 v103, 0, v103\n"
"v_mul_f32 v100, v100, v100\n"
"v_mul_f32 v101, v101, v101\n"
"v_mul_f32 v102, v102, v102\n"
"v_mul_f32 v103, v103, v103\n"
"v_cvt_pk_bf16_f32 v200, v100, v101\n"
"v_cvt_pk_bf16_f32 v201, v102, v103\n"
"v_mfma_f32_32x32x16_bf16 a[80:95], v[252:255], v[252:255], 0\n"
"buffer_load_dwordx4 v212, s[24:27], s49 offen offset:0 sc1 lds\n"
"buffer_load_dwordx4 v213, s[24:27], s49 offen offset:1024 sc1 lds\n"
"v_max_f32 v104, 0, v104\n"
"v_max_f32 v105, 0, v105\n"
"v_max_f32 v106, 0, v106\n"
"v_max_f32 v107, 0, v107\n"
"v_mul_f32 v104, v104, v104\n"
"v_mul_f32 v105, v105, v105\n"
"v_mul_f32 v106, v106, v106\n"
"v_mul_f32 v107, v107, v107\n"
"v_cvt_pk_bf16_f32 v186, v104, v105\n"
"v_cvt_pk_bf16_f32 v187, v106, v107\n"
"buffer_load_dwordx4 v214, s[24:27], s49 offen offset:2048 sc1 lds\n"
"buffer_load_dwordx4 v215, s[24:27], s49 offen offset:3072 sc1 lds\n"
"v_max_f32 v108, 0, v108\n"
"v_max_f32 v109, 0, v109\n"
"v_max_f32 v110, 0, v110\n"
"v_max_f32 v111, 0, v111\n"
"v_mul_f32 v108, v108, v108\n"
"v_mul_f32 v109, v109, v109\n"
"v_mul_f32 v110, v110, v110\n"
"v_mul_f32 v111, v111, v111\n"
"v_cvt_pk_bf16_f32 v202, v108, v109\n"
"v_cvt_pk_bf16_f32 v203, v110, v111\n"
"v_mfma_f32_32x32x16_bf16 a[96:111], v[252:255], v[252:255], 0\n"
"v_max_f32 v112, 0, v112\n"
"v_max_f32 v113, 0, v113\n"
"v_max_f32 v114, 0, v114\n"
"v_max_f32 v115, 0, v115\n"
"v_mul_f32 v112, v112, v112\n"
"v_mul_f32 v113, v113, v113\n"
"v_mul_f32 v114, v114, v114\n"
"v_mul_f32 v115, v115, v115\n"
"v_cvt_pk_bf16_f32 v188, v112, v113\n"
"v_cvt_pk_bf16_f32 v189, v114, v115\n"
"v_max_f32 v116, 0, v116\n"
"v_max_f32 v117, 0, v117\n"
"v_max_f32 v118, 0, v118\n"
"v_max_f32 v119, 0, v119\n"
"v_mul_f32 v116, v116, v116\n"
"v_mul_f32 v117, v117, v117\n"
"v_mul_f32 v118, v118, v118\n"
"v_mul_f32 v119, v119, v119\n"
"v_cvt_pk_bf16_f32 v204, v116, v117\n"
"v_cvt_pk_bf16_f32 v205, v118, v119\n"
"v_mfma_f32_32x32x16_bf16 a[112:127], v[252:255], v[252:255], 0\n"
"v_mov_b32 v190, 0\n"
"v_mov_b32 v191, 0\n"
"v_mov_b32 v206, 0\n"
"v_mov_b32 v207, 0\n"
"s_add_u32 m0, s47, 131072\n"
"v_add_u32 v232, s50, v227\n"
"s_waitcnt vmcnt(16)\n"
"s_waitcnt lgkmcnt(0)\n"
"s_barrier\n"
"ds_read_b128 v[0:3], v232 offset:0\n"
"ds_read_b128 v[4:7], v232 offset:1024\n"
"ds_read_b128 v[8:11], v232 offset:2048\n"
"ds_read_b128 v[12:15], v232 offset:3072\n"
"ds_read_b128 v[16:19], v232 offset:4096\n"
"ds_read_b128 v[20:23], v232 offset:5120\n"
"ds_read_b128 v[24:27], v232 offset:6144\n"
"ds_read_b128 v[28:31], v232 offset:7168\n"
"s_mov_b32 s51, s38\n"
"s_mov_b32 s52, s39\n"
"L_seg%=:\n"
"v_mov_b32 v237, 0\n"
"v_mov_b32 v233, 0\n"
"v_mov_b32 v238, 0\n"
"v_mov_b32 v234, 0\n"
"s_cmp_eq_u32 s52, 0\n"
"s_cbranch_scc1 L_m0%=\n"
"v_mov_b32 v96, 0\n"
"v_mov_b32 v97, 0\n"
"v_mov_b32 v98, 0\n"
"v_mov_b32 v99, 0\n"
"v_mov_b32 v100, 0\n"
"v_mov_b32 v101, 0\n"
"v_mov_b32 v102, 0\n"
"v_mov_b32 v103, 0\n"
"v_mov_b32 v104, 0\n"
"v_mov_b32 v105, 0\n"
"v_mov_b32 v106, 0\n"
"v_mov_b32 v107, 0\n"
"v_mov_b32 v108, 0\n"
"v_mov_b32 v109, 0\n"
"v_mov_b32 v110, 0\n"
"v_mov_b32 v111, 0\n"
"v_mov_b32 v112, 0\n"
"v_mov_b32 v113, 0\n"
"v_mov_b32 v114, 0\n"
"v_mov_b32 v115, 0\n"
"v_mov_b32 v116, 0\n"
"v_mov_b32 v117, 0\n"
"v_mov_b32 v118, 0\n"
"v_mov_b32 v119, 0\n"
"v_mov_b32 v120, 0\n"
"v_mov_b32 v121, 0\n"
"v_mov_b32 v122, 0\n"
"v_mov_b32 v123, 0\n"
"v_mov_b32 v124, 0\n"
"v_mov_b32 v125, 0\n"
"v_mov_b32 v126, 0\n"
"v_mov_b32 v127, 0\n"
"L_m0r%=:\n"
"s_waitcnt lgkmcnt(0)\n"
"v_mfma_f32_16x16x32_bf16 v[64:67], v[0:3], a[128:131], v[96:99]\n"
"s_mov_b32 s58, 0xff7fffff\n"
"ds_read_b128 v[32:35], v232 offset:8192\n"
"v_mfma_f32_16x16x32_bf16 v[64:67], v[4:7], a[136:139], v[64:67]\n"
"s_mov_b32 s59, 0x37800000\n"
"ds_read_b128 v[36:39], v232 offset:9216\n"
"v_readlane_b32 s49, v224, s48\n"
"v_mfma_f32_16x16x32_bf16 v[64:67], v[8:11], a[144:147], v[64:67]\n"
"s_cmp_ge_u32 s48, s42\n"
"s_cbranch_scc1 L_sw0%=_go\n"
"L_sw0%=_ret:\n"
"v_mfma_f32_16x16x32_bf16 v[64:67], v[12:15], a[152:155], v[64:67]\n"
"s_add_u32 s52, s52, 1\n"
"ds_read_b128 v[40:43], v232 offset:10240\n"
"v_mfma_f32_16x16x32_bf16 v[64:67], v[16:19], a[160:163], v[64:67]\n"
"s_add_u32 s48, s48, 1\n"
"ds_read_b128 v[44:47], v232 offset:11264\n"
"v_mfma_f32_16x16x32_bf16 v[64:67], v[20:23], a[168:171], v[64:67]\n"
"v_mfma_f32_16x16x32_bf16 v[64:67], v[24:27], a[176:179], v[64:67]\n"
"ds_read_b128 v[48:51], v232 offset:12288\n"
"v_mfma_f32_16x16x32_bf16 v[64:67], v[28:31], a[184:187], v[64:67]\n"
"ds_read_b128 v[52:55], v232 offset:13312\n"
"v_mfma_f32_16x16x32_bf16 v[68:71], v[28:31], a[188:191], v[100:103]\n"
"buffer_load_dwordx4 v208, s[24:27], s49 offen offset:0 sc1 lds\n"
"v_mfma_f32_16x16x32_bf16 v[68:71], v[24:27], a[180:183], v[68:71]\n"
"ds_read_b128 v[56:59], v232 offset:14336\n"
"v_mfma_f32_16x16x32_bf16 v[68:71], v[20:23], a[172:175], v[68:71]\n"
"ds_read_b128 v[60:63], v232 offset:15360\n"
"v_mfma_f32_16x16x32_bf16 v[68:71], v[16:19], a[164:167], v[68:71]\n"
"buffer_load_dwordx4 v209, s[24:27], s49 offen offset:1024 sc1 lds\n"
"v_mfma_f32_16x16x32_bf16 v[68:71], v[12:15], a[156:159], v[68:71]\n"
"v_exp_f32 v144, v64\n"
"v_mfma_f32_16x16x32_bf16 v[68:71], v[8:11], a[148:151], v[68:71]\n"
"v_exp_f32 v145, v65\n"
"v_mfma_f32_16x16x32_bf16 v[68:71], v[4:7], a[140:143], v[68:71]\n"
"v_exp_f32 v146, v66\n"
"v_mfma_f32_16x16x32_bf16 v[68:71], v[0:3], a[132:135], v[68:71]\n"
"s_waitcnt lgkmcnt(4)\n"
"v_exp_f32 v147, v67\n"
"v_mfma_f32_16x16x32_bf16 v[72:75], v[32:35], a[128:131], v[104:107]\n"
"ds_read_b128 v[0:3], v232 offset:16384\n"
"v_add_f32 v235, v144, v145\n"
"v_mfma_f32_16x16x32_bf16 v[72:75], v[36:39], a[136:139], v[72:75]\n"
"ds_read_b128 v[4:7], v232 offset:17408\n"
"v_add_f32 v235, v235, v146\n"
"v_mfma_f32_16x16x32_bf16 v[72:75], v[40:43], a[144:147], v[72:75]\n"
"buffer_load_dwordx4 v210, s[24:27], s49 offen offset:2048 sc1 lds\n"
"v_mfma_f32_16x16x32_bf16 v[72:75], v[44:47], a[152:155], v[72:75]\n"
"s_waitcnt lgkmcnt(2)\n"
"ds_read_b128 v[8:11], v232 offset:18432\n"
"v_add_f32 v235, v235, v147\n"
"v_mfma_f32_16x16x32_bf16 v[72:75], v[48:51], a[160:163], v[72:75]\n"
"ds_read_b128 v[12:15], v232 offset:19456\n"
"v_mfma_f32_16x16x32_bf16 v[72:75], v[52:55], a[168:171], v[72:75]\n"
"buffer_load_dwordx4 v211, s[24:27], s49 offen offset:3072 sc1 lds\n"
"v_mfma_f32_16x16x32_bf16 v[72:75], v[56:59], a[176:179], v[72:75]\n"
"ds_read_b128 v[16:19], v232 offset:20480\n"
"v_mfma_f32_16x16x32_bf16 v[72:75], v[60:63], a[184:187], v[72:75]\n"
"ds_read_b128 v[20:23], v232 offset:21504\n"
"v_mfma_f32_16x16x32_bf16 v[76:79], v[60:63], a[188:191], v[108:111]\n"
"s_add_u32 m0, m0, 4096\n"
"v_exp_f32 v148, v68\n"
"v_mfma_f32_16x16x32_bf16 v[76:79], v[56:59], a[180:183], v[76:79]\n"
"ds_read_b128 v[24:27], v232 offset:22528\n"
"v_mfma_f32_16x16x32_bf16 v[76:79], v[52:55], a[172:175], v[76:79]\n"
"ds_read_b128 v[28:31], v232 offset:23552\n"
"v_mfma_f32_16x16x32_bf16 v[76:79], v[48:51], a[164:167], v[76:79]\n"
"buffer_load_dwordx4 v212, s[24:27], s49 offen offset:0 sc1 lds\n"
"v_mfma_f32_16x16x32_bf16 v[76:79], v[44:47], a[156:159], v[76:79]\n"
"v_exp_f32 v149, v69\n"
"v_mfma_f32_16x16x32_bf16 v[76:79], v[40:43], a[148:151], v[76:79]\n"
"v_exp_f32 v150, v70\n"
"v_mfma_f32_16x16x32_bf16 v[76:79], v[36:39], a[140:143], v[76:79]\n"
"v_exp_f32 v151, v71\n"
"v_mfma_f32_16x16x32_bf16 v[76:79], v[32:35], a[132:135], v[76:79]\n"
"s_waitcnt lgkmcnt(4)\n"
"v_exp_f32 v152, v72\n"
"v_mfma_f32_16x16x32_bf16 v[80:83], v[0:3], a[128:131], v[112:115]\n"
"ds_read_b128 v[32:35], v232 offset:24576\n"
"v_add_f32 v236, v148, v149\n"
"v_mfma_f32_16x16x32_bf16 v[80:83], v[4:7], a[136:139], v[80:83]\n"
"ds_read_b128 v[36:39], v232 offset:25600\n"
"v_add_f32 v236, v236, v150\n"
"v_mfma_f32_16x16x32_bf16 v[80:83], v[8:11], a[144:147], v[80:83]\n"
"ds_read_b128 v[40:43], v232 offset:26624\n"
"v_add_f32 v236, v236, v151\n"
"v_mfma_f32_16x16x32_bf16 v[80:83], v[12:15], a[152:155], v[80:83]\n"
"s_waitcnt lgkmcnt(3)\n"
"ds_read_b128 v[44:47], v232 offset:27648\n"
"buffer_load_dwordx4 v213, s[24:27], s49 offen offset:1024 sc1 lds\n"
"v_mfma_f32_16x16x32_bf16 v[80:83], v[16:19], a[160:163], v[80:83]\n"
"ds_read_b128 v[48:51], v232 offset:28672\n"
"v_add_f32 v235, v235, v152\n"
"v_mfma_f32_16x16x32_bf16 v[80:83], v[20:23], a[168:171], v[80:83]\n"
"ds_read_b128 v[52:55], v232 offset:29696\n"
"v_mfma_f32_16x16x32_bf16 v[80:83], v[24:27], a[176:179], v[80:83]\n"
"ds_read_b128 v[56:59], v232 offset:30720\n"
"v_mfma_f32_16x16x32_bf16 v[80:83], v[28:31], a[184:187], v[80:83]\n"
"ds_read_b128 v[60:63], v232 offset:31744\n"
"v_mfma_f32_16x16x32_bf16 v[84:87], v[28:31], a[188:191], v[116:119]\n"
"buffer_load_dwordx4 v214, s[24:27], s49 offen offset:2048 sc1 lds\n"
"v_mfma_f32_16x16x32_bf16 v[84:87], v[24:27], a[180:183], v[84:87]\n"
"buffer_load_dwordx4 v215, s[24:27], s49 offen offset:3072 sc1 lds\n"
"v_mfma_f32_16x16x32_bf16 v[84:87], v[20:23], a[172:175], v[84:87]\n"
"s_add_u32 s54, s50, s47\n"
"v_exp_f32 v153, v73\n"
"v_mfma_f32_16x16x32_bf16 v[84:87], v[16:19], a[164:167], v[84:87]\n"
"s_add_u32 s50, s50, 32768\n"
"v_exp_f32 v154, v74\n"
"v_mfma_f32_16x16x32_bf16 v[84:87], v[12:15], a[156:159], v[84:87]\n"
"s_cmp_lt_u32 s50, 163840\n"
"s_cselect_b32 s50, s50, 0\n"
"v_add_f32 v235, v235, v153\n"
"v_mfma_f32_16x16x32_bf16 v[84:87], v[8:11], a[148:151], v[84:87]\n"
"s_mov_b32 m0, s54\n"
"v_exp_f32 v155, v75\n"
"v_mfma_f32_16x16x32_bf16 v[84:87], v[4:7], a[140:143], v[84:87]\n"
"s_waitcnt lgkmcnt(0)\n"
"v_add_u32 v232, s50, v227\n"
"v_add_f32 v235, v235, v154\n"
"v_mfma_f32_16x16x32_bf16 v[84:87], v[0:3], a[132:135], v[84:87]\n"
"s_waitcnt vmcnt(24)\n"
"s_barrier\n"
"v_mfma_f32_16x16x32_bf16 v[88:91], v[32:35], a[128:131], v[120:123]\n"
"ds_read_b128 v[0:3], v232 offset:0\n"
"v_add_f32 v235, v235, v155\n"
"v_mfma_f32_16x16x32_bf16 v[88:91], v[36:39], a[136:139], v[88:91]\n"
"ds_read_b128 v[4:7], v232 offset:1024\n"
"v_mfma_f32_16x16x32_bf16 v[88:91], v[40:43], a[144:147], v[88:91]\n"
"v_exp_f32 v156, v76\n"
"v_mfma_f32_16x16x32_bf16 v[88:91], v[44:47], a[152:155], v[88:91]\n"
"ds_read_b128 v[8:11], v232 offset:2048\n"
"v_add_f32 v236, v236, v156\n"
"v_mfma_f32_16x16x32_bf16 v[88:91], v[48:51], a[160:163], v[88:91]\n"
"ds_read_b128 v[12:15], v232 offset:3072\n"
"v_mfma_f32_16x16x32_bf16 v[88:91], v[52:55], a[168:171], v[88:91]\n"
"v_exp_f32 v157, v77\n"
"v_mfma_f32_16x16x32_bf16 v[88:91], v[56:59], a[176:179], v[88:91]\n"
"ds_read_b128 v[16:19], v232 offset:4096\n"
"v_add_f32 v236, v236, v157\n"
"v_mfma_f32_16x16x32_bf16 v[88:91], v[60:63], a[184:187], v[88:91]\n"
"ds_read_b128 v[20:23], v232 offset:5120\n"
"v_mfma_f32_16x16x32_bf16 v[92:95], v[60:63], a[188:191], v[124:127]\n"
"v_exp_f32 v158, v78\n"
"v_mfma_f32_16x16x32_bf16 v[92:95], v[56:59], a[180:183], v[92:95]\n"
"ds_read_b128 v[24:27], v232 offset:6144\n"
"v_add_f32 v236, v236, v158\n"
"v_mfma_f32_16x16x32_bf16 v[92:95], v[52:55], a[172:175], v[92:95]\n"
"ds_read_b128 v[28:31], v232 offset:7168\n"
"v_mfma_f32_16x16x32_bf16 v[92:95], v[48:51], a[164:167], v[92:95]\n"
"v_exp_f32 v159, v79\n"
"v_mfma_f32_16x16x32_bf16 v[92:95], v[44:47], a[156:159], v[92:95]\n"
"v_exp_f32 v160, v80\n"
"v_mfma_f32_16x16x32_bf16 v[92:95], v[40:43], a[148:151], v[92:95]\n"
"v_exp_f32 v161, v81\n"
"v_mfma_f32_16x16x32_bf16 v[92:95], v[36:39], a[140:143], v[92:95]\n"
"v_exp_f32 v162, v82\n"
"v_mfma_f32_16x16x32_bf16 v[92:95], v[32:35], a[132:135], v[92:95]\n"
"s_waitcnt lgkmcnt(4)\n"
"v_exp_f32 v163, v83\n"
"v_add_f32 v236, v236, v159\n"
"v_add_f32 v235, v235, v160\n"
"v_add_f32 v235, v235, v161\n"
"v_add_f32 v235, v235, v162\n"
"v_add_f32 v235, v235, v163\n"
"v_exp_f32 v164, v84\n"
"v_exp_f32 v165, v85\n"
"v_exp_f32 v166, v86\n"
"v_exp_f32 v167, v87\n"
"v_add_f32 v236, v236, v164\n"
"v_add_f32 v236, v236, v165\n"
"v_add_f32 v236, v236, v166\n"
"v_add_f32 v236, v236, v167\n"
"v_exp_f32 v168, v88\n"
"v_exp_f32 v169, v89\n"
"v_exp_f32 v170, v90\n"
"v_exp_f32 v171, v91\n"
"v_add_f32 v235, v235, v168\n"
"v_add_f32 v235, v235, v169\n"
"v_add_f32 v235, v235, v170\n"
"v_add_f32 v235, v235, v171\n"
"v_exp_f32 v172, v92\n"
"v_exp_f32 v173, v93\n"
"v_exp_f32 v174, v94\n"
"v_exp_f32 v175, v95\n"
"v_add_f32 v236, v236, v172\n"
"v_add_f32 v236, v236, v173\n"
"v_add_f32 v236, v236, v174\n"
"v_add_f32 v236, v236, v175\n"
"v_max_f32 v240, v235, v236\n"
"v_mov_b32 v242, v235\n"
"v_mov_b32 v243, v236\n"
"v_cmp_lt_f32 s[92:93], s56, v240\n"
"s_nop 0\n"
"v_permlane32_swap_b32 v242, v243\n"
"v_add_f32 v242, v242, v243\n"
"v_mov_b32 v243, v242\n"
"s_nop 1\n"
"v_permlane16_swap_b32 v242, v243\n"
"v_add_f32 v242, v242, v243\n"
"v_cmp_gt_f32 vcc, s59, v242\n"
"s_nop 0\n"
"s_or_b64 vcc, vcc, s[92:93]\n"
"s_cmp_lg_u32 s53, 1\n"
"s_cbranch_scc1 L_nosig%=\n"
"s_waitcnt vmcnt(8)\n"
"s_barrier\n"
"s_cmp_lg_u32 s47, 0\n"
"s_cbranch_scc1 L_sigp%=\n"
"v_mov_b32 v240, 1\n"
"s_mov_b64 exec, 1\n"
"buffer_store_dword v240, v231, s[20:23], s77 offen offset:8 sc1\n"
"s_mov_b64 exec, -1\n"
"s_nop 3\n"
"L_sigp%=:\n"
"L_nosig%=:\n"
"s_nop 1\n"
"s_branch L_tail%=\n"
".p2align 6\n"
"L_loop%=:\n"
"v_mfma_f32_16x16x32_bf16 v[64:67], v[0:3], a[128:131], v[96:99]\n"
"ds_read_b128 v[32:35], v232 offset:8192\n"
"v_add_f32 v233, v233, v235\n"
"v_mfma_f32_16x16x32_bf16 v[64:67], v[4:7], a[136:139], v[64:67]\n"
"ds_read_b128 v[36:39], v232 offset:9216\n"
"v_readlane_b32 s49, v224, s48\n"
"v_mfma_f32_16x16x32_bf16 v[64:67], v[8:11], a[144:147], v[64:67]\n"
"s_cmp_ge_u32 s48, s42\n"
"s_cbranch_scc1 L_sw1%=_go\n"
"L_sw1%=_ret:\n"
"v_mfma_f32_16x16x32_bf16 v[64:67], v[12:15], a[152:155], v[64:67]\n"
"s_waitcnt lgkmcnt(2)\n"
"ds_read_b128 v[40:43], v232 offset:10240\n"
"v_add_f32 v234, v234, v236\n"
"v_mfma_f32_16x16x32_bf16 v[64:67], v[16:19], a[160:163], v[64:67]\n"
"s_add_u32 s48, s48, 1\n"
"ds_read_b128 v[44:47], v232 offset:11264\n"
"v_cvt_pk_bf16_f32 v128, v144, v145\n"
"v_mfma_f32_16x16x32_bf16 v[64:67], v[20:23], a[168:171], v[64:67]\n"
"v_cvt_pk_bf16_f32 v129, v146, v147\n"
"v_cvt_pk_bf16_f32 v136, v148, v149\n"
"v_mfma_f32_16x16x32_bf16 v[64:67], v[24:27], a[176:179], v[64:67]\n"
"ds_read_b128 v[48:51], v232 offset:12288\n"
"v_cvt_pk_bf16_f32 v137, v150, v151\n"
"v_mfma_f32_16x16x32_bf16 v[64:67], v[28:31], a[184:187], v[64:67]\n"
"ds_read_b128 v[52:55], v232 offset:13312\n"
"v_cvt_pk_bf16_f32 v130, v152, v153\n"
"v_mfma_f32_16x16x32_bf16 v[68:71], v[28:31], a[188:191], v[100:103]\n"
"buffer_load_dwordx4 v208, s[24:27], s49 offen offset:0 sc1 lds\n"
"v_mfma_f32_16x16x32_bf16 v[68:71], v[24:27], a[180:183], v[68:71]\n"
"ds_read_b128 v[56:59], v232 offset:14336\n"
"v_cvt_pk_bf16_f32 v131, v154, v155\n"
"v_mfma_f32_16x16x32_bf16 v[68:71], v[20:23], a[172:175], v[68:71]\n"
"ds_read_b128 v[60:63], v232 offset:15360\n"
"v_mfma_f32_16x16x32_bf16 v[68:71], v[16:19], a[164:167], v[68:71]\n"
"buffer_load_dwordx4 v209, s[24:27], s49 offen offset:1024 sc1 lds\n"
"v_mfma_f32_16x16x32_bf16 v[68:71], v[12:15], a[156:159], v[68:71]\n"
"v_exp_f32 v144, v64\n"
"v_mfma_f32_16x16x32_bf16 v[68:71], v[8:11], a[148:151], v[68:71]\n"
"v_exp_f32 v145, v65\n"
"v_mfma_f32_16x16x32_bf16 v[68:71], v[4:7], a[140:143], v[68:71]\n"
"v_exp_f32 v146, v66\n"
"v_mfma_f32_16x16x32_bf16 v[68:71], v[0:3], a[132:135], v[68:71]\n"
"s_waitcnt lgkmcnt(4)\n"
"v_exp_f32 v147, v67\n"
"v_mfma_f32_16x16x32_bf16 v[72:75], v[32:35], a[128:131], v[104:107]\n"
"ds_read_b128 v[0:3], v232 offset:16384\n"
"v_cvt_pk_bf16_f32 v138, v156, v157\n"
"v_mfma_f32_16x16x32_bf16 v[72:75], v[36:39], a[136:139], v[72:75]\n"
"ds_read_b128 v[4:7], v232 offset:17408\n"
"v_cvt_pk_bf16_f32 v139, v158, v159\n"
"v_mfma_f32_16x16x32_bf16 v[72:75], v[40:43], a[144:147], v[72:75]\n"
"buffer_load_dwordx4 v210, s[24:27], s49 offen offset:2048 sc1 lds\n"
"v_mfma_f32_16x16x32_bf16 v[72:75], v[44:47], a[152:155], v[72:75]\n"
"s_waitcnt lgkmcnt(2)\n"
"ds_read_b128 v[8:11], v232 offset:18432\n"
"v_cvt_pk_bf16_f32 v132, v160, v161\n"
"v_mfma_f32_16x16x32_bf16 v[72:75], v[48:51], a[160:163], v[72:75]\n"
"ds_read_b128 v[12:15], v232 offset:19456\n"
"v_cvt_pk_bf16_f32 v133, v162, v163\n"
"v_mfma_f32_16x16x32_bf16 v[72:75], v[52:55], a[168:171], v[72:75]\n"
"buffer_load_dwordx4 v211, s[24:27], s49 offen offset:3072 sc1 lds\n"
"v_mfma_f32_16x16x32_bf16 v[72:75], v[56:59], a[176:179], v[72:75]\n"
"ds_read_b128 v[16:19], v232 offset:20480\n"
"v_cvt_pk_bf16_f32 v140, v164, v165\n"
"v_mfma_f32_16x16x32_bf16 v[72:75], v[60:63], a[184:187], v[72:75]\n"
"ds_read_b128 v[20:23], v232 offset:21504\n"
"v_add_f32 v235, v144, v145\n"
"v_mfma_f32_16x16x32_bf16 v[76:79], v[60:63], a[188:191], v[108:111]\n"
"s_add_u32 m0, m0, 4096\n"
"v_exp_f32 v148, v68\n"
"v_mfma_f32_16x16x32_bf16 v[76:79], v[56:59], a[180:183], v[76:79]\n"
"ds_read_b128 v[24:27], v232 offset:22528\n"
"v_cvt_pk_bf16_f32 v141, v166, v167\n"
"v_mfma_f32_16x16x32_bf16 v[76:79], v[52:55], a[172:175], v[76:79]\n"
"ds_read_b128 v[28:31], v232 offset:23552\n"
"v_add_f32 v235, v235, v146\n"
"v_mfma_f32_16x16x32_bf16 v[76:79], v[48:51], a[164:167], v[76:79]\n"
"buffer_load_dwordx4 v212, s[24:27], s49 offen offset:0 sc1 lds\n"
"v_mfma_f32_16x16x32_bf16 v[76:79], v[44:47], a[156:159], v[76:79]\n"
"v_add_f32 v235, v235, v147\n"
"v_mfma_f32_16x16x32_bf16 v[76:79], v[40:43], a[148:151], v[76:79]\n"
"v_exp_f32 v149, v69\n"
"v_mfma_f32_16x16x32_bf16 v[76:79], v[36:39], a[140:143], v[76:79]\n"
"v_exp_f32 v150, v70\n"
"v_mfma_f32_16x16x32_bf16 v[76:79], v[32:35], a[132:135], v[76:79]\n"
"s_waitcnt lgkmcnt(4)\n"
"v_exp_f32 v151, v71\n"
"v_mfma_f32_16x16x32_bf16 v[80:83], v[0:3], a[128:131], v[112:115]\n"
"ds_read_b128 v[32:35], v232 offset:24576\n"
"v_cvt_pk_bf16_f32 v134, v168, v169\n"
"v_mfma_f32_16x16x32_bf16 v[80:83], v[4:7], a[136:139], v[80:83]\n"
"ds_read_b128 v[36:39], v232 offset:25600\n"
"v_cvt_pk_bf16_f32 v135, v170, v171\n"
"v_mfma_f32_16x16x32_bf16 v[80:83], v[8:11], a[144:147], v[80:83]\n"
"ds_read_b128 v[40:43], v232 offset:26624\n"
"v_cvt_pk_bf16_f32 v142, v172, v173\n"
"v_mfma_f32_16x16x32_bf16 v[80:83], v[12:15], a[152:155], v[80:83]\n"
"s_waitcnt lgkmcnt(3)\n"
"ds_read_b128 v[44:47], v232 offset:27648\n"
"v_add_f32 v236, v148, v149\n"
"v_mfma_f32_16x16x32_bf16 v[80:83], v[16:19], a[160:163], v[80:83]\n"
"ds_read_b128 v[48:51], v232 offset:28672\n"
"v_cvt_pk_bf16_f32 v143, v174, v175\n"
"v_mfma_f32_16x16x32_bf16 v[80:83], v[20:23], a[168:171], v[80:83]\n"
"ds_read_b128 v[52:55], v232 offset:29696\n"
"v_add_f32 v236, v236, v150\n"
"v_mfma_f32_16x16x32_bf16 v[80:83], v[24:27], a[176:179], v[80:83]\n"
"ds_read_b128 v[56:59], v232 offset:30720\n"
"v_add_f32 v236, v236, v151\n"
"v_mfma_f32_16x16x32_bf16 v[80:83], v[28:31], a[184:187], v[80:83]\n"
"ds_read_b128 v[60:63], v232 offset:31744\n"
"v_mfma_f32_16x16x32_bf16 v[84:87], v[28:31], a[188:191], v[116:119]\n"
"buffer_load_dwordx4 v213, s[24:27], s49 offen offset:1024 sc1 lds\n"
"v_mfma_f32_16x16x32_bf16 v[84:87], v[24:27], a[180:183], v[84:87]\n"
"buffer_load_dwordx4 v214, s[24:27], s49 offen offset:2048 sc1 lds\n"
"v_mfma_f32_16x16x32_bf16 v[84:87], v[20:23], a[172:175], v[84:87]\n"
"s_add_u32 s54, s50, s47\n"
"v_exp_f32 v152, v72\n"
"v_mfma_f32_16x16x32_bf16 v[84:87], v[16:19], a[164:167], v[84:87]\n"
"s_add_u32 s50, s50, 32768\n"
"v_exp_f32 v153, v73\n"
"v_mfma_f32_16x16x32_bf16 v[84:87], v[12:15], a[156:159], v[84:87]\n"
"s_cmp_lt_u32 s50, 163840\n"
"s_cselect_b32 s50, s50, 0\n"
"s_mov_b32 s58, 0\n"
"v_mfma_f32_16x16x32_bf16 v[84:87], v[8:11], a[148:151], v[84:87]\n"
"buffer_load_dwordx4 v215, s[24:27], s49 offen offset:3072 sc1 lds\n"
"v_mfma_f32_16x16x32_bf16 v[84:87], v[4:7], a[140:143], v[84:87]\n"
"s_waitcnt lgkmcnt(0)\n"
"s_mov_b32 m0, s54\n"
"v_add_u32 v232, s50, v227\n"
"v_mfma_f32_16x16x32_bf16 v[84:87], v[0:3], a[132:135], v[84:87]\n"
"s_waitcnt vmcnt(24)\n"
"s_barrier\n"
"v_mfma_f32_16x16x32_bf16 v[88:91], v[32:35], a[128:131], v[120:123]\n"
"ds_read_b128 v[0:3], v232 offset:0\n"
"v_add_f32 v235, v235, v152\n"
"v_mfma_f32_16x16x32_bf16 v[88:91], v[36:39], a[136:139], v[88:91]\n"
"ds_read_b128 v[4:7], v232 offset:1024\n"
"v_add_f32 v235, v235, v153\n"
"v_mfma_f32_16x16x32_bf16 v[88:91], v[40:43], a[144:147], v[88:91]\n"
"v_exp_f32 v154, v74\n"
"v_mfma_f32_16x16x32_bf16 v[88:91], v[44:47], a[152:155], v[88:91]\n"
"ds_read_b128 v[8:11], v232 offset:2048\n"
"v_add_f32 v235, v235, v154\n"
"v_mfma_f32_16x16x32_bf16 v[88:91], v[48:51], a[160:163], v[88:91]\n"
"ds_read_b128 v[12:15], v232 offset:3072\n"
"v_mfma_f32_16x16x32_bf16 v[88:91], v[52:55], a[168:171], v[88:91]\n"
"v_exp_f32 v155, v75\n"
"v_mfma_f32_16x16x32_bf16 v[88:91], v[56:59], a[176:179], v[88:91]\n"
"ds_read_b128 v[16:19], v232 offset:4096\n"
"v_add_f32 v235, v235, v155\n"
"v_mfma_f32_16x16x32_bf16 v[88:91], v[60:63], a[184:187], v[88:91]\n"
"ds_read_b128 v[20:23], v232 offset:5120\n"
"v_mfma_f32_16x16x32_bf16 v[92:95], v[60:63], a[188:191], v[124:127]\n"
"v_exp_f32 v156, v76\n"
"v_mfma_f32_16x16x32_bf16 v[92:95], v[56:59], a[180:183], v[92:95]\n"
"ds_read_b128 v[24:27], v232 offset:6144\n"
"v_mfma_f32_16x16x32_bf16 v[92:95], v[52:55], a[172:175], v[92:95]\n"
"ds_read_b128 v[28:31], v232 offset:7168\n"
"v_mfma_f32_16x16x32_bf16 v[92:95], v[48:51], a[164:167], v[92:95]\n"
"v_exp_f32 v157, v77\n"
"v_mfma_f32_16x16x32_bf16 v[92:95], v[44:47], a[156:159], v[92:95]\n"
"v_exp_f32 v158, v78\n"
"v_mfma_f32_16x16x32_bf16 v[92:95], v[40:43], a[148:151], v[92:95]\n"
"v_exp_f32 v159, v79\n"
"v_mfma_f32_16x16x32_bf16 v[92:95], v[36:39], a[140:143], v[92:95]\n"
"v_mfma_f32_16x16x32_bf16 v[92:95], v[32:35], a[132:135], v[92:95]\n"
"s_waitcnt lgkmcnt(4)\n"
"v_mfma_f32_16x16x32_bf16 a[0:3], v[0:3], v[128:131], a[0:3]\n"
"ds_read_b128 v[32:35], v232 offset:8192\n"
"v_add_f32 v236, v236, v156\n"
"v_mfma_f32_16x16x32_bf16 a[0:3], v[4:7], v[132:135], a[0:3]\n"
"ds_read_b128 v[36:39], v232 offset:9216\n"
"v_readlane_b32 s49, v224, s48\n"
"v_mfma_f32_16x16x32_bf16 a[4:7], v[4:7], v[140:143], a[4:7]\n"
"s_cmp_ge_u32 s48, s42\n"
"s_cbranch_scc1 L_sw2%=_go\n"
"L_sw2%=_ret:\n"
"v_mfma_f32_16x16x32_bf16 a[4:7], v[0:3], v[136:139], a[4:7]\n"
"ds_read_b128 v[40:43], v232 offset:10240\n"
"v_add_f32 v236, v236, v157\n"
"v_mfma_f32_16x16x32_bf16 a[8:11], v[8:11], v[128:131], a[8:11]\n"
"s_add_u32 s48, s48, 1\n"
"ds_read_b128 v[44:47], v232 offset:11264\n"
"v_add_f32 v236, v236, v158\n"
"v_mfma_f32_16x16x32_bf16 a[8:11], v[12:15], v[132:135], a[8:11]\n"
"buffer_load_dword v239, v231, s[20:23], s45 offen offset:0 sc1\n"
"v_mfma_f32_16x16x32_bf16 a[12:15], v[12:15], v[140:143], a[12:15]\n"
"ds_read_b128 v[48:51], v232 offset:12288\n"
"v_add_f32 v236, v236, v159\n"
"v_mfma_f32_16x16x32_bf16 a[12:15], v[8:11], v[136:139], a[12:15]\n"
"s_waitcnt lgkmcnt(5)\n"
"ds_read_b128 v[52:55], v232 offset:13312\n"
"v_mfma_f32_16x16x32_bf16 a[16:19], v[16:19], v[128:131], a[16:19]\n"
"buffer_load_dwordx4 v208, s[24:27], s49 offen offset:0 sc1 lds\n"
"v_mfma_f32_16x16x32_bf16 a[16:19], v[20:23], v[132:135], a[16:19]\n"
"ds_read_b128 v[56:59], v232 offset:14336\n"
"v_mfma_f32_16x16x32_bf16 a[20:23], v[20:23], v[140:143], a[20:23]\n"
"ds_read_b128 v[60:63], v232 offset:15360\n"
"v_mfma_f32_16x16x32_bf16 a[20:23], v[16:19], v[136:139], a[20:23]\n"
"buffer_load_dwordx4 v209, s[24:27], s49 offen offset:1024 sc1 lds\n"
"v_mfma_f32_16x16x32_bf16 a[24:27], v[24:27], v[128:131], a[24:27]\n"
"v_exp_f32 v160, v80\n"
"v_mfma_f32_16x16x32_bf16 a[24:27], v[28:31], v[132:135], a[24:27]\n"
"v_exp_f32 v161, v81\n"
"v_mfma_f32_16x16x32_bf16 a[28:31], v[28:31], v[140:143], a[28:31]\n"
"v_exp_f32 v162, v82\n"
"v_mfma_f32_16x16x32_bf16 a[28:31], v[24:27], v[136:139], a[28:31]\n"
"s_waitcnt lgkmcnt(4)\n"
"v_exp_f32 v163, v83\n"
"v_mfma_f32_16x16x32_bf16 a[32:35], v[32:35], v[128:131], a[32:35]\n"
"ds_read_b128 v[0:3], v232 offset:16384\n"
"v_add_f32 v235, v235, v160\n"
"v_mfma_f32_16x16x32_bf16 a[32:35], v[36:39], v[132:135], a[32:35]\n"
"ds_read_b128 v[4:7], v232 offset:17408\n"
"v_add_f32 v235, v235, v161\n"
"v_mfma_f32_16x16x32_bf16 a[36:39], v[36:39], v[140:143], a[36:39]\n"
"buffer_load_dwordx4 v210, s[24:27], s49 offen offset:2048 sc1 lds\n"
"v_mfma_f32_16x16x32_bf16 a[36:39], v[32:35], v[136:139], a[36:39]\n"
"ds_read_b128 v[8:11], v232 offset:18432\n"
"v_add_f32 v235, v235, v162\n"
"v_mfma_f32_16x16x32_bf16 a[40:43], v[40:43], v[128:131], a[40:43]\n"
"ds_read_b128 v[12:15], v232 offset:19456\n"
"v_add_f32 v235, v235, v163\n"
"v_mfma_f32_16x16x32_bf16 a[40:43], v[44:47], v[132:135], a[40:43]\n"
"buffer_load_dwordx4 v211, s[24:27], s49 offen offset:3072 sc1 lds\n"
"v_mfma_f32_16x16x32_bf16 a[44:47], v[44:47], v[140:143], a[44:47]\n"
"ds_read_b128 v[16:19], v232 offset:20480\n"
"v_mfma_f32_16x16x32_bf16 a[44:47], v[40:43], v[136:139], a[44:47]\n"
"s_waitcnt lgkmcnt(5)\n"
"ds_read_b128 v[20:23], v232 offset:21504\n"
"v_mfma_f32_16x16x32_bf16 a[48:51], v[48:51], v[128:131], a[48:51]\n"
"s_add_u32 m0, m0, 4096\n"
"v_exp_f32 v164, v84\n"
"v_mfma_f32_16x16x32_bf16 a[48:51], v[52:55], v[132:135], a[48:51]\n"
"ds_read_b128 v[24:27], v232 offset:22528\n"
"v_add_f32 v236, v236, v164\n"
"v_mfma_f32_16x16x32_bf16 a[52:55], v[52:55], v[140:143], a[52:55]\n"
"ds_read_b128 v[28:31], v232 offset:23552\n"
"v_mfma_f32_16x16x32_bf16 a[52:55], v[48:51], v[136:139], a[52:55]\n"
"buffer_load_dwordx4 v212, s[24:27], s49 offen offset:0 sc1 lds\n"
"v_mfma_f32_16x16x32_bf16 a[56:59], v[56:59], v[128:131], a[56:59]\n"
"v_exp_f32 v165, v85\n"
"v_mfma_f32_16x16x32_bf16 a[56:59], v[60:63], v[132:135], a[56:59]\n"
"v_exp_f32 v166, v86\n"
"v_mfma_f32_16x16x32_bf16 a[60:63], v[60:63], v[140:143], a[60:63]\n"
"v_exp_f32 v167, v87\n"
"v_mfma_f32_16x16x32_bf16 a[60:63], v[56:59], v[136:139], a[60:63]\n"
"s_waitcnt lgkmcnt(4)\n"
"v_exp_f32 v168, v88\n"
"v_mfma_f32_16x16x32_bf16 a[64:67], v[0:3], v[128:131], a[64:67]\n"
"ds_read_b128 v[32:35], v232 offset:24576\n"
"v_add_f32 v236, v236, v165\n"
"v_mfma_f32_16x16x32_bf16 a[64:67], v[4:7], v[132:135], a[64:67]\n"
"ds_read_b128 v[36:39], v232 offset:25600\n"
"v_add_f32 v236, v236, v166\n"
"v_mfma_f32_16x16x32_bf16 a[68:71], v[4:7], v[140:143], a[68:71]\n"
"ds_read_b128 v[40:43], v232 offset:26624\n"
"v_add_f32 v236, v236, v167\n"
"v_mfma_f32_16x16x32_bf16 a[68:71], v[0:3], v[136:139], a[68:71]\n"
"ds_read_b128 v[44:47], v232 offset:27648\n"
"v_add_f32 v235, v235, v168\n"
"v_mfma_f32_16x16x32_bf16 a[72:75], v[8:11], v[128:131], a[72:75]\n"
"ds_read_b128 v[48:51], v232 offset:28672\n"
"v_mfma_f32_16x16x32_bf16 a[72:75], v[12:15], v[132:135], a[72:75]\n"
"ds_read_b128 v[52:55], v232 offset:29696\n"
"v_mfma_f32_16x16x32_bf16 a[76:79], v[12:15], v[140:143], a[76:79]\n"
"ds_read_b128 v[56:59], v232 offset:30720\n"
"v_mfma_f32_16x16x32_bf16 a[76:79], v[8:11], v[136:139], a[76:79]\n"
"s_waitcnt lgkmcnt(7)\n"
"ds_read_b128 v[60:63], v232 offset:31744\n"
"v_mfma_f32_16x16x32_bf16 a[80:83], v[16:19], v[128:131], a[80:83]\n"
"buffer_load_dwordx4 v213, s[24:27], s49 offen offset:1024 sc1 lds\n"
"v_mfma_f32_16x16x32_bf16 a[80:83], v[20:23], v[132:135], a[80:83]\n"
"buffer_load_dwordx4 v214, s[24:27], s49 offen offset:2048 sc1 lds\n"
"v_mfma_f32_16x16x32_bf16 a[84:87], v[20:23], v[140:143], a[84:87]\n"
"s_add_u32 s54, s50, s47\n"
"v_exp_f32 v169, v89\n"
"v_mfma_f32_16x16x32_bf16 a[84:87], v[16:19], v[136:139], a[84:87]\n"
"s_add_u32 s50, s50, 32768\n"
"v_exp_f32 v170, v90\n"
"v_mfma_f32_16x16x32_bf16 a[88:91], v[24:27], v[128:131], a[88:91]\n"
"s_cmp_lt_u32 s50, 163840\n"
"s_cselect_b32 s50, s50, 0\n"
"v_add_f32 v235, v235, v169\n"
"v_mfma_f32_16x16x32_bf16 a[88:91], v[28:31], v[132:135], a[88:91]\n"
"buffer_load_dwordx4 v215, s[24:27], s49 offen offset:3072 sc1 lds\n"
"v_mfma_f32_16x16x32_bf16 a[92:95], v[28:31], v[140:143], a[92:95]\n"
"s_waitcnt lgkmcnt(0)\n"
"v_add_u32 v232, s50, v227\n"
"v_add_f32 v235, v235, v170\n"
"v_mfma_f32_16x16x32_bf16 a[92:95], v[24:27], v[136:139], a[92:95]\n"
"s_waitcnt vmcnt(25)\n"
"s_barrier\n"
"v_mfma_f32_16x16x32_bf16 a[96:99], v[32:35], v[128:131], a[96:99]\n"
"s_mov_b32 m0, s54\n"
"ds_read_b128 v[0:3], v232 offset:0\n"
"v_mfma_f32_16x16x32_bf16 a[96:99], v[36:39], v[132:135], a[96:99]\n"
"ds_read_b128 v[4:7], v232 offset:1024\n"
"v_mfma_f32_16x16x32_bf16 a[100:103], v[36:39], v[140:143], a[100:103]\n"
"s_add_u32 s52, s52, 1\n"
"v_exp_f32 v171, v91\n"
"v_mfma_f32_16x16x32_bf16 a[100:103], v[32:35], v[136:139], a[100:103]\n"
"s_sub_u32 s51, s51, 1\n"
"ds_read_b128 v[8:11], v232 offset:2048\n"
"v_add_f32 v235, v235, v171\n"
"v_mfma_f32_16x16x32_bf16 a[104:107], v[40:43], v[128:131], a[104:107]\n"
"ds_read_b128 v[12:15], v232 offset:3072\n"
"v_mfma_f32_16x16x32_bf16 a[104:107], v[44:47], v[132:135], a[104:107]\n"
"v_exp_f32 v172, v92\n"
"v_mfma_f32_16x16x32_bf16 a[108:111], v[44:47], v[140:143], a[108:111]\n"
"ds_read_b128 v[16:19], v232 offset:4096\n"
"v_add_f32 v236, v236, v172\n"
"v_mfma_f32_16x16x32_bf16 a[108:111], v[40:43], v[136:139], a[108:111]\n"
"ds_read_b128 v[20:23], v232 offset:5120\n"
"v_mfma_f32_16x16x32_bf16 a[112:115], v[48:51], v[128:131], a[112:115]\n"
"v_exp_f32 v173, v93\n"
"v_mfma_f32_16x16x32_bf16 a[112:115], v[52:55], v[132:135], a[112:115]\n"
"ds_read_b128 v[24:27], v232 offset:6144\n"
"v_add_f32 v236, v236, v173\n"
"v_mfma_f32_16x16x32_bf16 a[116:119], v[52:55], v[140:143], a[116:119]\n"
"s_cmp_lt_u32 s52, 2\n"
"s_cselect_b32 s55, 0, s51\n"
"ds_read_b128 v[28:31], v232 offset:7168\n"
"v_mfma_f32_16x16x32_bf16 a[116:119], v[48:51], v[136:139], a[116:119]\n"
"v_exp_f32 v174, v94\n"
"v_mfma_f32_16x16x32_bf16 a[120:123], v[56:59], v[128:131], a[120:123]\n"
"v_exp_f32 v175, v95\n"
"v_mfma_f32_16x16x32_bf16 a[120:123], v[60:63], v[132:135], a[120:123]\n"
"v_add_f32 v236, v236, v174\n"
"v_add_f32 v236, v236, v175\n"
"v_mfma_f32_16x16x32_bf16 a[124:127], v[60:63], v[140:143], a[124:127]\n"
"v_max_f32 v240, v235, v236\n"
"v_cmp_lt_f32 vcc, s56, v240\n"
"v_mfma_f32_16x16x32_bf16 a[124:127], v[56:59], v[136:139], a[124:127]\n"
"s_waitcnt lgkmcnt(4)\n"
"s_cmp_eq_u64 vcc, 0\n"
"s_cselect_b32 s55, s55, 0\n"
"s_cmp_lg_u32 s55, 0\n"
"s_cbranch_scc1 L_loop%=\n"
"L_tail%=:\n"
"s_cbranch_vccnz L_resc%=\n"
"s_cmp_lt_u32 s52, 2\n"
"s_cbranch_scc1 L_mask%=\n"
"L_ret%=:\n"
"s_cmp_lg_u32 s51, 0\n"
"s_cbranch_scc1 L_loop%=\n"
"v_cvt_pk_bf16_f32 v136, v148, v149\n"
"v_cvt_pk_bf16_f32 v137, v150, v151\n"
"v_cvt_pk_bf16_f32 v138, v156, v157\n"
"v_cvt_pk_bf16_f32 v139, v158, v159\n"
"v_cvt_pk_bf16_f32 v140, v164, v165\n"
"v_cvt_pk_bf16_f32 v141, v166, v167\n"
"v_cvt_pk_bf16_f32 v142, v172, v173\n"
"v_cvt_pk_bf16_f32 v143, v174, v175\n"
"s_cmp_eq_u32 s37, 2\n"
"s_cbranch_scc1 L_comb%=\n"
"v_mfma_f32_16x16x32_bf16 a[4:7], v[0:3], v[136:139], a[4:7]\n"
"ds_read_b128 v[32:35], v232 offset:8192\n"
"v_readlane_b32 s49, v224, s48\n"
"v_mfma_f32_16x16x32_bf16 a[4:7], v[4:7], v[140:143], a[4:7]\n"
"s_cmp_ge_u32 s48, s42\n"
"s_cbranch_scc1 L_sw3%=_go\n"
"L_sw3%=_ret:\n"
"ds_read_b128 v[36:39], v232 offset:9216\n"
"v_mfma_f32_16x16x32_bf16 a[12:15], v[8:11], v[136:139], a[12:15]\n"
"ds_read_b128 v[40:43], v232 offset:10240\n"
"v_add_f32 v233, v233, v235\n"
"v_mfma_f32_16x16x32_bf16 a[12:15], v[12:15], v[140:143], a[12:15]\n"
"s_add_u32 s48, s48, 1\n"
"ds_read_b128 v[44:47], v232 offset:11264\n"
"v_add_f32 v234, v234, v236\n"
"s_waitcnt lgkmcnt(4)\n"
"v_mfma_f32_16x16x32_bf16 a[20:23], v[16:19], v[136:139], a[20:23]\n"
"ds_read_b128 v[48:51], v232 offset:12288\n"
"v_mov_b32 v244, v233\n"
"v_mfma_f32_16x16x32_bf16 a[20:23], v[20:23], v[140:143], a[20:23]\n"
"ds_read_b128 v[52:55], v232 offset:13312\n"
"v_mov_b32 v245, v234\n"
"v_mfma_f32_16x16x32_bf16 a[28:31], v[24:27], v[136:139], a[28:31]\n"
"ds_read_b128 v[56:59], v232 offset:14336\n"
"v_permlane32_swap_b32 v244, v233\n"
"v_mfma_f32_16x16x32_bf16 a[28:31], v[28:31], v[140:143], a[28:31]\n"
"ds_read_b128 v[60:63], v232 offset:15360\n"
"v_permlane32_swap_b32 v245, v234\n"
"s_waitcnt lgkmcnt(7)\n"
"v_mfma_f32_16x16x32_bf16 a[36:39], v[32:35], v[136:139], a[36:39]\n"
"ds_read_b128 v[0:3], v232 offset:16384\n"
"v_add_f32 v233, v233, v244\n"
"s_waitcnt lgkmcnt(7)\n"
"v_mfma_f32_16x16x32_bf16 a[36:39], v[36:39], v[140:143], a[36:39]\n"
"ds_read_b128 v[4:7], v232 offset:17408\n"
"buffer_load_dwordx4 v208, s[24:27], s49 offen offset:0 sc1 lds\n"
"s_waitcnt lgkmcnt(7)\n"
"v_mfma_f32_16x16x32_bf16 a[44:47], v[40:43], v[136:139], a[44:47]\n"
"ds_read_b128 v[8:11], v232 offset:18432\n"
"v_add_f32 v234, v234, v245\n"
"s_waitcnt lgkmcnt(7)\n"
"v_mfma_f32_16x16x32_bf16 a[44:47], v[44:47], v[140:143], a[44:47]\n"
"ds_read_b128 v[12:15], v232 offset:19456\n"
"v_mov_b32 v244, v233\n"
"s_waitcnt lgkmcnt(6)\n"
"v_mfma_f32_16x16x32_bf16 a[52:55], v[48:51], v[136:139], a[52:55]\n"
"ds_read_b128 v[16:19], v232 offset:20480\n"
"buffer_load_dwordx4 v209, s[24:27], s49 offen offset:1024 sc1 lds\n"
"v_mfma_f32_16x16x32_bf16 a[52:55], v[52:55], v[140:143], a[52:55]\n"
"ds_read_b128 v[20:23], v232 offset:21504\n"
"v_mov_b32 v245, v234\n"
"s_waitcnt lgkmcnt(6)\n"
"v_mfma_f32_16x16x32_bf16 a[60:63], v[56:59], v[136:139], a[60:63]\n"
"ds_read_b128 v[24:27], v232 offset:22528\n"
"v_permlane16_swap_b32 v244, v233\n"
"v_mfma_f32_16x16x32_bf16 a[60:63], v[60:63], v[140:143], a[60:63]\n"
"ds_read_b128 v[28:31], v232 offset:23552\n"
"buffer_load_dwordx4 v210, s[24:27], s49 offen offset:2048 sc1 lds\n"
"s_waitcnt lgkmcnt(6)\n"
"v_mfma_f32_16x16x32_bf16 a[68:71], v[0:3], v[136:139], a[68:71]\n"
"ds_read_b128 v[32:35], v232 offset:24576\n"
"v_permlane16_swap_b32 v245, v234\n"
"v_mfma_f32_16x16x32_bf16 a[68:71], v[4:7], v[140:143], a[68:71]\n"
"ds_read_b128 v[36:39], v232 offset:25600\n"
"v_add_f32 v233, v233, v244\n"
"s_waitcnt lgkmcnt(7)\n"
"v_mfma_f32_16x16x32_bf16 a[76:79], v[8:11], v[136:139], a[76:79]\n"
"ds_read_b128 v[40:43], v232 offset:26624\n"
"buffer_load_dwordx4 v211, s[24:27], s49 offen offset:3072 sc1 lds\n"
"s_waitcnt lgkmcnt(7)\n"
"v_mfma_f32_16x16x32_bf16 a[76:79], v[12:15], v[140:143], a[76:79]\n"
"s_add_u32 m0, m0, 4096\n"
"ds_read_b128 v[44:47], v232 offset:27648\n"
"v_add_f32 v234, v234, v245\n"
"s_waitcnt lgkmcnt(6)\n"
"v_mfma_f32_16x16x32_bf16 a[84:87], v[16:19], v[136:139], a[84:87]\n"
"ds_read_b128 v[48:51], v232 offset:28672\n"
"v_mfma_f32_16x16x32_bf16 a[84:87], v[20:23], v[140:143], a[84:87]\n"
"ds_read_b128 v[52:55], v232 offset:29696\n"
"buffer_load_dwordx4 v212, s[24:27], s49 offen offset:0 sc1 lds\n"
"s_waitcnt lgkmcnt(6)\n"
"v_mfma_f32_16x16x32_bf16 a[92:95], v[24:27], v[136:139], a[92:95]\n"
"ds_read_b128 v[56:59], v232 offset:30720\n"
"v_mfma_f32_16x16x32_bf16 a[92:95], v[28:31], v[140:143], a[92:95]\n"
"ds_read_b128 v[60:63], v232 offset:31744\n"
"s_waitcnt lgkmcnt(7)\n"
"v_mfma_f32_16x16x32_bf16 a[100:103], v[32:35], v[136:139], a[100:103]\n"
"s_add_u32 s54, s50, s47\n"
"s_add_u32 s50, s50, 32768\n"
"s_waitcnt lgkmcnt(6)\n"
"v_mfma_f32_16x16x32_bf16 a[100:103], v[36:39], v[140:143], a[100:103]\n"
"s_cmp_lt_u32 s50, 163840\n"
"s_cselect_b32 s50, s50, 0\n"
"s_waitcnt lgkmcnt(5)\n"
"v_mfma_f32_16x16x32_bf16 a[108:111], v[40:43], v[136:139], a[108:111]\n"
"buffer_load_dwordx4 v213, s[24:27], s49 offen offset:1024 sc1 lds\n"
"s_waitcnt lgkmcnt(4)\n"
"v_mfma_f32_16x16x32_bf16 a[108:111], v[44:47], v[140:143], a[108:111]\n"
"s_waitcnt lgkmcnt(0)\n"
"s_waitcnt vmcnt(22)\n"
"s_barrier\n"
"v_mfma_f32_16x16x32_bf16 a[116:119], v[48:51], v[136:139], a[116:119]\n"
"buffer_load_dwordx4 v214, s[24:27], s49 offen offset:2048 sc1 lds\n"
"v_mfma_f32_16x16x32_bf16 a[116:119], v[52:55], v[140:143], a[116:119]\n"
"buffer_load_dwordx4 v215, s[24:27], s49 offen offset:3072 sc1 lds\n"
"v_mfma_f32_16x16x32_bf16 a[124:127], v[56:59], v[136:139], a[124:127]\n"
"s_mov_b32 m0, s54\n"
"v_rcp_f32 v235, v233\n"
"v_mfma_f32_16x16x32_bf16 a[124:127], v[60:63], v[140:143], a[124:127]\n"
"v_rcp_f32 v236, v234\n"
"s_sub_u32 s54, s37, s53\n"
"s_cmp_eq_u32 s54, 3\n"
"s_cbranch_scc1 L_pub%=\n"
"v_add_u32 v232, s50, v228\n"
"ds_read_b64_tr_b16 v[0:1], v232 offset:0\n"
"ds_read_b64_tr_b16 v[2:3], v232 offset:3584\n"
"ds_read_b64_tr_b16 v[4:5], v232 offset:8192\n"
"ds_read_b64_tr_b16 v[6:7], v232 offset:11776\n"
"ds_read_b64_tr_b16 v[8:9], v232 offset:16384\n"
"ds_read_b64_tr_b16 v[10:11], v232 offset:19968\n"
"ds_read_b64_tr_b16 v[12:13], v232 offset:24576\n"
"ds_read_b64_tr_b16 v[14:15], v232 offset:28160\n"
"ds_read_b64_tr_b16 v[16:17], v232 offset:32\n"
"ds_read_b64_tr_b16 v[18:19], v232 offset:3616\n"
"ds_read_b64_tr_b16 v[20:21], v232 offset:8224\n"
"ds_read_b64_tr_b16 v[22:23], v232 offset:11808\n"
"ds_read_b64_tr_b16 v[24:25], v232 offset:16416\n"
"ds_read_b64_tr_b16 v[26:27], v232 offset:20000\n"
"ds_read_b64_tr_b16 v[28:29], v232 offset:24608\n"
"ds_read_b64_tr_b16 v[30:31], v232 offset:28192\n"
"s_branch L_epi%=\n"
"L_pub%=:\n"
"v_add_u32 v232, s50, v227\n"
"ds_read_b128 v[0:3], v232 offset:0\n"
"ds_read_b128 v[4:7], v232 offset:1024\n"
"ds_read_b128 v[8:11], v232 offset:2048\n"
"ds_read_b128 v[12:15], v232 offset:3072\n"
"ds_read_b128 v[16:19], v232 offset:4096\n"
"ds_read_b128 v[20:23], v232 offset:5120\n"
"ds_read_b128 v[24:27], v232 offset:6144\n"
"ds_read_b128 v[28:31], v232 offset:7168\n"
"v_mov_b32 v244, v237\n"
"v_mov_b32 v245, v233\n"
"v_mov_b32 v246, v238\n"
"v_mov_b32 v247, v234\n"
"v_mov_b32 v252, 0\n"
"v_mov_b32 v253, 0\n"
"v_mov_b32 v254, 0\n"
"v_mov_b32 v255, 0\n"
"buffer_store_dwordx4 v[244:247], v227, s[20:23], s44 offen offset:0 sc1\n"
"v_accvgpr_read_b32 v128, a0\n"
"v_accvgpr_read_b32 v129, a1\n"
"v_accvgpr_read_b32 v130, a2\n"
"v_accvgpr_read_b32 v131, a3\n"
"v_accvgpr_read_b32 v132, a8\n"
"v_accvgpr_read_b32 v133, a9\n"
"v_accvgpr_read_b32 v134, a10\n"
"v_accvgpr_read_b32 v135, a11\n"
"v_cvt_pk_bf16_f32 v136, v128, v129\n"
"v_cvt_pk_bf16_f32 v137, v130, v131\n"
"v_cvt_pk_bf16_f32 v138, v132, v133\n"
"v_cvt_pk_bf16_f32 v139, v134, v135\n"
"s_add_u32 s54, s43, 46137344\n"
"buffer_store_dwordx4 v[136:139], v227, s[20:23], s54 offen offset:0 sc1\n"
"v_accvgpr_mov_b32 a128, a192\n"
"v_accvgpr_mov_b32 a129, a193\n"
"v_accvgpr_mov_b32 a130, a194\n"
"v_accvgpr_mov_b32 a131, a195\n"
"v_accvgpr_read_b32 v140, a4\n"
"v_accvgpr_read_b32 v141, a5\n"
"v_accvgpr_read_b32 v142, a6\n"
"v_accvgpr_read_b32 v143, a7\n"
"v_accvgpr_read_b32 v144, a12\n"
"v_accvgpr_read_b32 v145, a13\n"
"v_accvgpr_read_b32 v146, a14\n"
"v_accvgpr_read_b32 v147, a15\n"
"v_mfma_f32_32x32x16_bf16 a[0:15], v[252:255], v[252:255], 0\n"
"v_cvt_pk_bf16_f32 v148, v140, v141\n"
"v_cvt_pk_bf16_f32 v149, v142, v143\n"
"v_cvt_pk_bf16_f32 v150, v144, v145\n"
"v_cvt_pk_bf16_f32 v151, v146, v147\n"
"s_add_u32 s54, s43, 46145536\n"
"buffer_store_dwordx4 v[148:151], v227, s[20:23], s54 offen offset:0 sc1\n"
"v_accvgpr_mov_b32 a132, a196\n"
"v_accvgpr_mov_b32 a133, a197\n"
"v_accvgpr_mov_b32 a134, a198\n"
"v_accvgpr_mov_b32 a135, a199\n"
"v_accvgpr_read_b32 v152, a16\n"
"v_accvgpr_read_b32 v153, a17\n"
"v_accvgpr_read_b32 v154, a18\n"
"v_accvgpr_read_b32 v155, a19\n"
"v_accvgpr_read_b32 v156, a24\n"
"v_accvgpr_read_b32 v157, a25\n"
"v_accvgpr_read_b32 v158, a26\n"
"v_accvgpr_read_b32 v159, a27\n"
"v_cvt_pk_bf16_f32 v160, v152, v153\n"
"v_cvt_pk_bf16_f32 v161, v154, v155\n"
"v_cvt_pk_bf16_f32 v162, v156, v157\n"
"v_cvt_pk_bf16_f32 v163, v158, v159\n"
"s_add_u32 s54, s43, 46138368\n"
"buffer_store_dwordx4 v[160:163], v227, s[20:23], s54 offen offset:0 sc1\n"
"v_accvgpr_mov_b32 a136, a200\n"
"v_accvgpr_mov_b32 a137, a201\n"
"v_accvgpr_mov_b32 a138, a202\n"
"v_accvgpr_mov_b32 a139, a203\n"
"v_accvgpr_read_b32 v164, a20\n"
"v_accvgpr_read_b32 v165, a21\n"
"v_accvgpr_read_b32 v166, a22\n"
"v_accvgpr_read_b32 v167, a23\n"
"v_accvgpr_read_b32 v168, a28\n"
"v_accvgpr_read_b32 v169, a29\n"
"v_accvgpr_read_b32 v170, a30\n"
"v_accvgpr_read_b32 v171, a31\n"
"v_mfma_f32_32x32x16_bf16 a[16:31], v[252:255], v[252:255], 0\n"
"v_cvt_pk_bf16_f32 v172, v164, v165\n"
"v_cvt_pk_bf16_f32 v173, v166, v167\n"
"v_cvt_pk_bf16_f32 v174, v168, v169\n"
"v_cvt_pk_bf16_f32 v175, v170, v171\n"
"s_add_u32 s54, s43, 46146560\n"
"buffer_store_dwordx4 v[172:175], v227, s[20:23], s54 offen offset:0 sc1\n"
"v_accvgpr_mov_b32 a140, a204\n"
"v_accvgpr_mov_b32 a141, a205\n"
"v_accvgpr_mov_b32 a142, a206\n"
"v_accvgpr_mov_b32 a143, a207\n"
"v_accvgpr_read_b32 v128, a32\n"
"v_accvgpr_read_b32 v129, a33\n"
"v_accvgpr_read_b32 v130, a34\n"
"v_accvgpr_read_b32 v131, a35\n"
"v_accvgpr_read_b32 v132, a40\n"
"v_accvgpr_read_b32 v133, a41\n"
"v_accvgpr_read_b32 v134, a42\n"
"v_accvgpr_read_b32 v135, a43\n"
"v_cvt_pk_bf16_f32 v136, v128, v129\n"
"v_cvt_pk_bf16_f32 v137, v130, v131\n"
"v_cvt_pk_bf16_f32 v138, v132, v133\n"
"v_cvt_pk_bf16_f32 v139, v134, v135\n"
"s_add_u32 s54, s43, 46139392\n"
"buffer_store_dwordx4 v[136:139], v227, s[20:23], s54 offen offset:0 sc1\n"
"v_accvgpr_mov_b32 a144, a208\n"
"v_accvgpr_mov_b32 a145, a209\n"
"v_accvgpr_mov_b32 a146, a210\n"
"v_accvgpr_mov_b32 a147, a211\n"
"v_accvgpr_read_b32 v140, a36\n"
"v_accvgpr_read_b32 v141, a37\n"
"v_accvgpr_read_b32 v142, a38\n"
"v_accvgpr_read_b32 v143, a39\n"
"v_accvgpr_read_b32 v144, a44\n"
"v_accvgpr_read_b32 v145, a45\n"
"v_accvgpr_read_b32 v146, a46\n"
"v_accvgpr_read_b32 v147, a47\n"
"v_mfma_f32_32x32x16_bf16 a[32:47], v[252:255], v[252:255], 0\n"
"v_cvt_pk_bf16_f32 v148, v140, v141\n"
"v_cvt_pk_bf16_f32 v149, v142, v143\n"
"v_cvt_pk_bf16_f32 v150, v144, v145\n"
"v_cvt_pk_bf16_f32 v151, v146, v147\n"
"s_add_u32 s54, s43, 46147584\n"
"buffer_store_dwordx4 v[148:151], v227, s[20:23], s54 offen offset:0 sc1\n"
"v_accvgpr_mov_b32 a148, a212\n"
"v_accvgpr_mov_b32 a149, a213\n"
"v_accvgpr_mov_b32 a150, a214\n"
"v_accvgpr_mov_b32 a151, a215\n"
"v_accvgpr_read_b32 v152, a48\n"
"v_accvgpr_read_b32 v153, a49\n"
"v_accvgpr_read_b32 v154, a50\n"
"v_accvgpr_read_b32 v155, a51\n"
"v_accvgpr_read_b32 v156, a56\n"
"v_accvgpr_read_b32 v157, a57\n"
"v_accvgpr_read_b32 v158, a58\n"
"v_accvgpr_read_b32 v159, a59\n"
"v_cvt_pk_bf16_f32 v160, v152, v153\n"
"v_cvt_pk_bf16_f32 v161, v154, v155\n"
"v_cvt_pk_bf16_f32 v162, v156, v157\n"
"v_cvt_pk_bf16_f32 v163, v158, v159\n"
"s_add_u32 s54, s43, 46140416\n"
"buffer_store_dwordx4 v[160:163], v227, s[20:23], s54 offen offset:0 sc1\n"
"v_accvgpr_mov_b32 a152, a216\n"
"v_accvgpr_mov_b32 a153, a217\n"
"v_accvgpr_mov_b32 a154, a218\n"
"v_accvgpr_mov_b32 a155, a219\n"
"v_accvgpr_read_b32 v164, a52\n"
"v_accvgpr_read_b32 v165, a53\n"
"v_accvgpr_read_b32 v166, a54\n"
"v_accvgpr_read_b32 v167, a55\n"
"v_accvgpr_read_b32 v168, a60\n"
"v_accvgpr_read_b32 v169, a61\n"
"v_accvgpr_read_b32 v170, a62\n"
"v_accvgpr_read_b32 v171, a63\n"
"v_mfma_f32_32x32x16_bf16 a[48:63], v[252:255], v[252:255], 0\n"
"v_cvt_pk_bf16_f32 v172, v164, v165\n"
"v_cvt_pk_bf16_f32 v173, v166, v167\n"
"v_cvt_pk_bf16_f32 v174, v168, v169\n"
"v_cvt_pk_bf16_f32 v175, v170, v171\n"
"s_add_u32 s54, s43, 46148608\n"
"buffer_store_dwordx4 v[172:175], v227, s[20:23], s54 offen offset:0 sc1\n"
"v_accvgpr_mov_b32 a156, a220\n"
"v_accvgpr_mov_b32 a157, a221\n"
"v_accvgpr_mov_b32 a158, a222\n"
"v_accvgpr_mov_b32 a159, a223\n"
"v_accvgpr_read_b32 v128, a64\n"
"v_accvgpr_read_b32 v129, a65\n"
"v_accvgpr_read_b32 v130, a66\n"
"v_accvgpr_read_b32 v131, a67\n"
"v_accvgpr_read_b32 v132, a72\n"
"v_accvgpr_read_b32 v133, a73\n"
"v_accvgpr_read_b32 v134, a74\n"
"v_accvgpr_read_b32 v135, a75\n"
"v_cvt_pk_bf16_f32 v136, v128, v129\n"
"v_cvt_pk_bf16_f32 v137, v130, v131\n"
"v_cvt_pk_bf16_f32 v138, v132, v133\n"
"v_cvt_pk_bf16_f32 v139, v134, v135\n"
"s_add_u32 s54, s43, 46141440\n"
"buffer_store_dwordx4 v[136:139], v227, s[20:23], s54 offen offset:0 sc1\n"
"v_accvgpr_mov_b32 a160, a224\n"
"v_accvgpr_mov_b32 a161, a225\n"
"v_accvgpr_mov_b32 a162, a226\n"
"v_accvgpr_mov_b32 a163, a227\n"
"v_accvgpr_read_b32 v140, a68\n"
"v_accvgpr_read_b32 v141, a69\n"
"v_accvgpr_read_b32 v142, a70\n"
"v_accvgpr_read_b32 v143, a71\n"
"v_accvgpr_read_b32 v144, a76\n"
"v_accvgpr_read_b32 v145, a77\n"
"v_accvgpr_read_b32 v146, a78\n"
"v_accvgpr_read_b32 v147, a79\n"
"v_mfma_f32_32x32x16_bf16 a[64:79], v[252:255], v[252:255], 0\n"
"v_cvt_pk_bf16_f32 v148, v140, v141\n"
"v_cvt_pk_bf16_f32 v149, v142, v143\n"
"v_cvt_pk_bf16_f32 v150, v144, v145\n"
"v_cvt_pk_bf16_f32 v151, v146, v147\n"
"s_add_u32 s54, s43, 46149632\n"
"buffer_store_dwordx4 v[148:151], v227, s[20:23], s54 offen offset:0 sc1\n"
"v_accvgpr_mov_b32 a164, a228\n"
"v_accvgpr_mov_b32 a165, a229\n"
"v_accvgpr_mov_b32 a166, a230\n"
"v_accvgpr_mov_b32 a167, a231\n"
"v_accvgpr_read_b32 v152, a80\n"
"v_accvgpr_read_b32 v153, a81\n"
"v_accvgpr_read_b32 v154, a82\n"
"v_accvgpr_read_b32 v155, a83\n"
"v_accvgpr_read_b32 v156, a88\n"
"v_accvgpr_read_b32 v157, a89\n"
"v_accvgpr_read_b32 v158, a90\n"
"v_accvgpr_read_b32 v159, a91\n"
"v_cvt_pk_bf16_f32 v160, v152, v153\n"
"v_cvt_pk_bf16_f32 v161, v154, v155\n"
"v_cvt_pk_bf16_f32 v162, v156, v157\n"
"v_cvt_pk_bf16_f32 v163, v158, v159\n"
"s_add_u32 s54, s43, 46142464\n"
"buffer_store_dwordx4 v[160:163], v227, s[20:23], s54 offen offset:0 sc1\n"
"v_accvgpr_mov_b32 a168, a232\n"
"v_accvgpr_mov_b32 a169, a233\n"
"v_accvgpr_mov_b32 a170, a234\n"
"v_accvgpr_mov_b32 a171, a235\n"
"v_accvgpr_read_b32 v164, a84\n"
"v_accvgpr_read_b32 v165, a85\n"
"v_accvgpr_read_b32 v166, a86\n"
"v_accvgpr_read_b32 v167, a87\n"
"v_accvgpr_read_b32 v168, a92\n"
"v_accvgpr_read_b32 v169, a93\n"
"v_accvgpr_read_b32 v170, a94\n"
"v_accvgpr_read_b32 v171, a95\n"
"v_mfma_f32_32x32x16_bf16 a[80:95], v[252:255], v[252:255], 0\n"
"v_cvt_pk_bf16_f32 v172, v164, v165\n"
"v_cvt_pk_bf16_f32 v173, v166, v167\n"
"v_cvt_pk_bf16_f32 v174, v168, v169\n"
"v_cvt_pk_bf16_f32 v175, v170, v171\n"
"s_add_u32 s54, s43, 46150656\n"
"buffer_store_dwordx4 v[172:175], v227, s[20:23], s54 offen offset:0 sc1\n"
"v_accvgpr_mov_b32 a172, a236\n"
"v_accvgpr_mov_b32 a173, a237\n"
"v_accvgpr_mov_b32 a174, a238\n"
"v_accvgpr_mov_b32 a175, a239\n"
"v_accvgpr_read_b32 v128, a96\n"
"v_accvgpr_read_b32 v129, a97\n"
"v_accvgpr_read_b32 v130, a98\n"
"v_accvgpr_read_b32 v131, a99\n"
"v_accvgpr_read_b32 v132, a104\n"
"v_accvgpr_read_b32 v133, a105\n"
"v_accvgpr_read_b32 v134, a106\n"
"v_accvgpr_read_b32 v135, a107\n"
"v_cvt_pk_bf16_f32 v136, v128, v129\n"
"v_cvt_pk_bf16_f32 v137, v130, v131\n"
"v_cvt_pk_bf16_f32 v138, v132, v133\n"
"v_cvt_pk_bf16_f32 v139, v134, v135\n"
"s_add_u32 s54, s43, 46143488\n"
"buffer_store_dwordx4 v[136:139], v227, s[20:23], s54 offen offset:0 sc1\n"
"v_accvgpr_mov_b32 a176, a240\n"
"v_accvgpr_mov_b32 a177, a241\n"
"v_accvgpr_mov_b32 a178, a242\n"
"v_accvgpr_mov_b32 a179, a243\n"
"v_accvgpr_read_b32 v140, a100\n"
"v_accvgpr_read_b32 v141, a101\n"
"v_accvgpr_read_b32 v142, a102\n"
"v_accvgpr_read_b32 v143, a103\n"
"v_accvgpr_read_b32 v144, a108\n"
"v_accvgpr_read_b32 v145, a109\n"
"v_accvgpr_read_b32 v146, a110\n"
"v_accvgpr_read_b32 v147, a111\n"
"v_mfma_f32_32x32x16_bf16 a[96:111], v[252:255], v[252:255], 0\n"
"v_cvt_pk_bf16_f32 v148, v140, v141\n"
"v_cvt_pk_bf16_f32 v149, v142, v143\n"
"v_cvt_pk_bf16_f32 v150, v144, v145\n"
"v_cvt_pk_bf16_f32 v151, v146, v147\n"
"s_add_u32 s54, s43, 46151680\n"
"buffer_store_dwordx4 v[148:151], v227, s[20:23], s54 offen offset:0 sc1\n"
"v_accvgpr_mov_b32 a180, a244\n"
"v_accvgpr_mov_b32 a181, a245\n"
"v_accvgpr_mov_b32 a182, a246\n"
"v_accvgpr_mov_b32 a183, a247\n"
"v_accvgpr_read_b32 v152, a112\n"
"v_accvgpr_read_b32 v153, a113\n"
"v_accvgpr_read_b32 v154, a114\n"
"v_accvgpr_read_b32 v155, a115\n"
"v_accvgpr_read_b32 v156, a120\n"
"v_accvgpr_read_b32 v157, a121\n"
"v_accvgpr_read_b32 v158, a122\n"
"v_accvgpr_read_b32 v159, a123\n"
"v_cvt_pk_bf16_f32 v160, v152, v153\n"
"v_cvt_pk_bf16_f32 v161, v154, v155\n"
"v_cvt_pk_bf16_f32 v162, v156, v157\n"
"v_cvt_pk_bf16_f32 v163, v158, v159\n"
"s_add_u32 s54, s43, 46144512\n"
"buffer_store_dwordx4 v[160:163], v227, s[20:23], s54 offen offset:0 sc1\n"
"v_accvgpr_mov_b32 a184, a248\n"
"v_accvgpr_mov_b32 a185, a249\n"
"v_accvgpr_mov_b32 a186, a250\n"
"v_accvgpr_mov_b32 a187, a251\n"
"v_accvgpr_read_b32 v164, a116\n"
"v_accvgpr_read_b32 v165, a117\n"
"v_accvgpr_read_b32 v166, a118\n"
"v_accvgpr_read_b32 v167, a119\n"
"v_accvgpr_read_b32 v168, a124\n"
"v_accvgpr_read_b32 v169, a125\n"
"v_accvgpr_read_b32 v170, a126\n"
"v_accvgpr_read_b32 v171, a127\n"
"v_mfma_f32_32x32x16_bf16 a[112:127], v[252:255], v[252:255], 0\n"
"v_cvt_pk_bf16_f32 v172, v164, v165\n"
"v_cvt_pk_bf16_f32 v173, v166, v167\n"
"v_cvt_pk_bf16_f32 v174, v168, v169\n"
"v_cvt_pk_bf16_f32 v175, v170, v171\n"
"s_add_u32 s54, s43, 46152704\n"
"buffer_store_dwordx4 v[172:175], v227, s[20:23], s54 offen offset:0 sc1\n"
"v_accvgpr_mov_b32 a188, a252\n"
"v_accvgpr_mov_b32 a189, a253\n"
"v_accvgpr_mov_b32 a190, a254\n"
"v_accvgpr_mov_b32 a191, a255\n"
"s_mov_b32 s51, s40\n"
"s_mov_b32 s52, s41\n"
"s_mov_b32 s53, 1\n"
"s_waitcnt lgkmcnt(0)\n"
"s_nop 3\n"
"s_branch L_seg%=\n"
"L_comb%=:\n"
"v_cvt_pk_bf16_f32 v128, v144, v145\n"
"v_cvt_pk_bf16_f32 v129, v146, v147\n"
"v_cvt_pk_bf16_f32 v130, v152, v153\n"
"v_cvt_pk_bf16_f32 v131, v154, v155\n"
"v_cvt_pk_bf16_f32 v132, v160, v161\n"
"v_cvt_pk_bf16_f32 v133, v162, v163\n"
"v_cvt_pk_bf16_f32 v134, v168, v169\n"
"v_cvt_pk_bf16_f32 v135, v170, v171\n"
"s_waitcnt vmcnt(8)\n"
"s_mov_b32 s55, 0\n"
"L_pw%=_chk:\n"
"v_cmp_ne_u32 vcc, 0, v239\n"
"s_nop 0\n"
"s_cmp_eq_u64 vcc, -1\n"
"s_cbranch_scc1 L_pw%=_ok\n"
"s_sleep 2\n"
"buffer_load_dword v239, v231, s[20:23], s45 offen offset:0 sc1\n"
"s_add_u32 s55, s55, 1\n"
"s_waitcnt vmcnt(0)\n"
"s_cmp_lt_u32 s55, 500000\n"
"s_cbranch_scc1 L_pw%=_chk\n"
"L_pw%=_ok:\n"
"v_mfma_f32_16x16x32_bf16 a[0:3], v[0:3], v[128:131], a[0:3]\n"
"ds_read_b128 v[32:35], v232 offset:8192\n"
"v_mfma_f32_16x16x32_bf16 a[0:3], v[4:7], v[132:135], a[0:3]\n"
"s_add_u32 s55, s43, 46137344\n"
"ds_read_b128 v[36:39], v232 offset:9216\n"
"v_readlane_b32 s49, v224, s48\n"
"v_mfma_f32_16x16x32_bf16 a[4:7], v[4:7], v[140:143], a[4:7]\n"
"s_cmp_ge_u32 s48, s42\n"
"s_cbranch_scc1 L_sw4%=_go\n"
"L_sw4%=_ret:\n"
"v_mfma_f32_16x16x32_bf16 a[4:7], v[0:3], v[136:139], a[4:7]\n"
"ds_read_b128 v[40:43], v232 offset:10240\n"
"v_add_f32 v233, v233, v235\n"
"v_mfma_f32_16x16x32_bf16 a[8:11], v[8:11], v[128:131], a[8:11]\n"
"ds_read_b128 v[44:47], v232 offset:11264\n"
"buffer_load_dwordx4 v[248:251], v227, s[20:23], s44 offen offset:0 sc1\n"
"v_mfma_f32_16x16x32_bf16 a[8:11], v[12:15], v[132:135], a[8:11]\n"
"buffer_load_dwordx4 a[128:131], v227, s[20:23], s55 offen offset:0 sc1\n"
"v_mfma_f32_16x16x32_bf16 a[12:15], v[12:15], v[140:143], a[12:15]\n"
"s_add_u32 s48, s48, 1\n"
"ds_read_b128 v[48:51], v232 offset:12288\n"
"v_add_f32 v234, v234, v236\n"
"v_mfma_f32_16x16x32_bf16 a[12:15], v[8:11], v[136:139], a[12:15]\n"
"s_waitcnt lgkmcnt(5)\n"
"ds_read_b128 v[52:55], v232 offset:13312\n"
"v_mov_b32 v244, v233\n"
"v_mfma_f32_16x16x32_bf16 a[16:19], v[16:19], v[128:131], a[16:19]\n"
"buffer_load_dwordx4 a[132:135], v227, s[20:23], s55 offen offset:1024 sc1\n"
"v_mfma_f32_16x16x32_bf16 a[16:19], v[20:23], v[132:135], a[16:19]\n"
"ds_read_b128 v[56:59], v232 offset:14336\n"
"v_mov_b32 v245, v234\n"
"v_mfma_f32_16x16x32_bf16 a[20:23], v[20:23], v[140:143], a[20:23]\n"
"ds_read_b128 v[60:63], v232 offset:15360\n"
"buffer_load_dwordx4 a[136:139], v227, s[20:23], s55 offen offset:2048 sc1\n"
"v_mfma_f32_16x16x32_bf16 a[20:23], v[16:19], v[136:139], a[20:23]\n"
"buffer_load_dwordx4 v208, s[24:27], s49 offen offset:0 sc1 lds\n"
"v_mfma_f32_16x16x32_bf16 a[24:27], v[24:27], v[128:131], a[24:27]\n"
"buffer_load_dwordx4 a[140:143], v227, s[20:23], s55 offen offset:3072 sc1\n"
"v_mfma_f32_16x16x32_bf16 a[24:27], v[28:31], v[132:135], a[24:27]\n"
"s_add_u32 s55, s43, 46141440\n"
"v_permlane32_swap_b32 v244, v233\n"
"v_permlane32_swap_b32 v245, v234\n"
"v_mfma_f32_16x16x32_bf16 a[28:31], v[28:31], v[140:143], a[28:31]\n"
"buffer_load_dwordx4 a[144:147], v227, s[20:23], s55 offen offset:0 sc1\n"
"v_mfma_f32_16x16x32_bf16 a[28:31], v[24:27], v[136:139], a[28:31]\n"
"s_waitcnt lgkmcnt(4)\n"
"buffer_load_dwordx4 v209, s[24:27], s49 offen offset:1024 sc1 lds\n"
"v_mfma_f32_16x16x32_bf16 a[32:35], v[32:35], v[128:131], a[32:35]\n"
"ds_read_b128 v[0:3], v232 offset:16384\n"
"buffer_load_dwordx4 a[148:151], v227, s[20:23], s55 offen offset:1024 sc1\n"
"v_mfma_f32_16x16x32_bf16 a[32:35], v[36:39], v[132:135], a[32:35]\n"
"ds_read_b128 v[4:7], v232 offset:17408\n"
"v_add_f32 v233, v233, v244\n"
"v_mfma_f32_16x16x32_bf16 a[36:39], v[36:39], v[140:143], a[36:39]\n"
"buffer_load_dwordx4 a[152:155], v227, s[20:23], s55 offen offset:2048 sc1\n"
"v_mfma_f32_16x16x32_bf16 a[36:39], v[32:35], v[136:139], a[36:39]\n"
"ds_read_b128 v[8:11], v232 offset:18432\n"
"v_add_f32 v234, v234, v245\n"
"v_mfma_f32_16x16x32_bf16 a[40:43], v[40:43], v[128:131], a[40:43]\n"
"ds_read_b128 v[12:15], v232 offset:19456\n"
"buffer_load_dwordx4 a[156:159], v227, s[20:23], s55 offen offset:3072 sc1\n"
"v_mfma_f32_16x16x32_bf16 a[40:43], v[44:47], v[132:135], a[40:43]\n"
"buffer_load_dwordx4 v210, s[24:27], s49 offen offset:2048 sc1 lds\n"
"v_mfma_f32_16x16x32_bf16 a[44:47], v[44:47], v[140:143], a[44:47]\n"
"s_add_u32 s55, s43, 46145536\n"
"ds_read_b128 v[16:19], v232 offset:20480\n"
"v_mov_b32 v244, v233\n"
"v_mfma_f32_16x16x32_bf16 a[44:47], v[40:43], v[136:139], a[44:47]\n"
"s_waitcnt lgkmcnt(5)\n"
"ds_read_b128 v[20:23], v232 offset:21504\n"
"buffer_load_dwordx4 a[160:163], v227, s[20:23], s55 offen offset:0 sc1\n"
"v_mfma_f32_16x16x32_bf16 a[48:51], v[48:51], v[128:131], a[48:51]\n"
"buffer_load_dwordx4 a[164:167], v227, s[20:23], s55 offen offset:1024 sc1\n"
"v_mfma_f32_16x16x32_bf16 a[48:51], v[52:55], v[132:135], a[48:51]\n"
"ds_read_b128 v[24:27], v232 offset:22528\n"
"buffer_load_dwordx4 v211, s[24:27], s49 offen offset:3072 sc1 lds\n"
"v_mfma_f32_16x16x32_bf16 a[52:55], v[52:55], v[140:143], a[52:55]\n"
"ds_read_b128 v[28:31], v232 offset:23552\n"
"buffer_load_dwordx4 a[168:171], v227, s[20:23], s55 offen offset:2048 sc1\n"
"v_mfma_f32_16x16x32_bf16 a[52:55], v[48:51], v[136:139], a[52:55]\n"
"buffer_load_dwordx4 a[172:175], v227, s[20:23], s55 offen offset:3072 sc1\n"
"v_mfma_f32_16x16x32_bf16 a[56:59], v[56:59], v[128:131], a[56:59]\n"
"s_add_u32 m0, m0, 4096\n"
"v_mov_b32 v245, v234\n"
"v_permlane16_swap_b32 v244, v233\n"
"v_mfma_f32_16x16x32_bf16 a[56:59], v[60:63], v[132:135], a[56:59]\n"
"s_add_u32 s55, s43, 46149632\n"
"v_permlane16_swap_b32 v245, v234\n"
"v_add_f32 v233, v233, v244\n"
"v_mfma_f32_16x16x32_bf16 a[60:63], v[60:63], v[140:143], a[60:63]\n"
"buffer_load_dwordx4 a[176:179], v227, s[20:23], s55 offen offset:0 sc1\n"
"v_mfma_f32_16x16x32_bf16 a[60:63], v[56:59], v[136:139], a[60:63]\n"
"s_waitcnt lgkmcnt(4)\n"
"buffer_load_dwordx4 v212, s[24:27], s49 offen offset:0 sc1 lds\n"
"v_mfma_f32_16x16x32_bf16 a[64:67], v[0:3], v[128:131], a[64:67]\n"
"ds_read_b128 v[32:35], v232 offset:24576\n"
"buffer_load_dwordx4 a[180:183], v227, s[20:23], s55 offen offset:1024 sc1\n"
"v_mfma_f32_16x16x32_bf16 a[64:67], v[4:7], v[132:135], a[64:67]\n"
"ds_read_b128 v[36:39], v232 offset:25600\n"
"v_add_f32 v234, v234, v245\n"
"v_mfma_f32_16x16x32_bf16 a[68:71], v[4:7], v[140:143], a[68:71]\n"
"ds_read_b128 v[40:43], v232 offset:26624\n"
"buffer_load_dwordx4 a[184:187], v227, s[20:23], s55 offen offset:2048 sc1\n"
"v_mfma_f32_16x16x32_bf16 a[68:71], v[0:3], v[136:139], a[68:71]\n"
"ds_read_b128 v[44:47], v232 offset:27648\n"
"buffer_load_dwordx4 v213, s[24:27], s49 offen offset:1024 sc1 lds\n"
"v_mfma_f32_16x16x32_bf16 a[72:75], v[8:11], v[128:131], a[72:75]\n"
"ds_read_b128 v[48:51], v232 offset:28672\n"
"buffer_load_dwordx4 a[188:191], v227, s[20:23], s55 offen offset:3072 sc1\n"
"v_mfma_f32_16x16x32_bf16 a[72:75], v[12:15], v[132:135], a[72:75]\n"
"ds_read_b128 v[52:55], v232 offset:29696\n"
"v_bfe_u32 v64, v227, 4, 4\n"
"v_mfma_f32_16x16x32_bf16 a[76:79], v[12:15], v[140:143], a[76:79]\n"
"ds_read_b128 v[56:59], v232 offset:30720\n"
"v_lshrrev_b32 v65, 8, v227\n"
"v_mfma_f32_16x16x32_bf16 a[76:79], v[8:11], v[136:139], a[76:79]\n"
"s_waitcnt lgkmcnt(7)\n"
"ds_read_b128 v[60:63], v232 offset:31744\n"
"v_mov_b32 v70, 0\n"
"v_mfma_f32_16x16x32_bf16 a[80:83], v[16:19], v[128:131], a[80:83]\n"
"buffer_load_dwordx4 v214, s[24:27], s49 offen offset:2048 sc1 lds\n"
"v_mfma_f32_16x16x32_bf16 a[80:83], v[20:23], v[132:135], a[80:83]\n"
"buffer_load_dwordx4 v215, s[24:27], s49 offen offset:3072 sc1 lds\n"
"v_mfma_f32_16x16x32_bf16 a[84:87], v[20:23], v[140:143], a[84:87]\n"
"s_add_u32 s54, s50, s47\n"
"v_mov_b32 v71, 0\n"
"v_mov_b32 v74, 0\n"
"v_mfma_f32_16x16x32_bf16 a[84:87], v[16:19], v[136:139], a[84:87]\n"
"s_add_u32 s50, s50, 32768\n"
"v_mov_b32 v75, 0\n"
"v_and_b32 v66, 1, v64\n"
"v_mfma_f32_16x16x32_bf16 a[88:91], v[24:27], v[128:131], a[88:91]\n"
"s_cmp_lt_u32 s50, 163840\n"
"s_cselect_b32 s50, s50, 0\n"
"v_lshlrev_b32 v66, 4, v66\n"
"v_mfma_f32_16x16x32_bf16 a[88:91], v[28:31], v[132:135], a[88:91]\n"
"s_mov_b32 m0, s54\n"
"v_mov_b32 v67, 0x3f80\n"
"v_lshlrev_b32 v67, v66, v67\n"
"v_mfma_f32_16x16x32_bf16 a[92:95], v[28:31], v[140:143], a[92:95]\n"
"s_waitcnt lgkmcnt(0)\n"
"v_add_u32 v232, s50, v228\n"
"v_lshrrev_b32 v66, 1, v64\n"
"v_mfma_f32_16x16x32_bf16 a[92:95], v[24:27], v[136:139], a[92:95]\n"
"s_waitcnt vmcnt(41)\n"
"s_barrier\n"
"v_mfma_f32_16x16x32_bf16 a[96:99], v[32:35], v[128:131], a[96:99]\n"
"ds_read_b64_tr_b16 v[0:1], v232 offset:0\n"
"v_lshlrev_b32 v68, 1, v65\n"
"v_mfma_f32_16x16x32_bf16 a[96:99], v[36:39], v[132:135], a[96:99]\n"
"ds_read_b64_tr_b16 v[2:3], v232 offset:3584\n"
"v_cmp_eq_u32 vcc, v66, v68\n"
"v_mfma_f32_16x16x32_bf16 a[100:103], v[36:39], v[140:143], a[100:103]\n"
"ds_read_b64_tr_b16 v[4:5], v232 offset:8192\n"
"v_cndmask_b32 v72, 0, v67, vcc\n"
"v_mfma_f32_16x16x32_bf16 a[100:103], v[32:35], v[136:139], a[100:103]\n"
"ds_read_b64_tr_b16 v[6:7], v232 offset:11776\n"
"ds_read_b64_tr_b16 v[8:9], v232 offset:16384\n"
"v_mfma_f32_16x16x32_bf16 a[104:107], v[40:43], v[128:131], a[104:107]\n"
"v_add_u32 v68, 1, v68\n"
"v_cmp_eq_u32 vcc, v66, v68\n"
"v_mfma_f32_16x16x32_bf16 a[104:107], v[44:47], v[132:135], a[104:107]\n"
"ds_read_b64_tr_b16 v[10:11], v232 offset:19968\n"
"v_cndmask_b32 v73, 0, v67, vcc\n"
"v_mfma_f32_16x16x32_bf16 a[108:111], v[44:47], v[140:143], a[108:111]\n"
"ds_read_b64_tr_b16 v[12:13], v232 offset:24576\n"
"v_mfma_f32_16x16x32_bf16 a[108:111], v[40:43], v[136:139], a[108:111]\n"
"ds_read_b64_tr_b16 v[14:15], v232 offset:28160\n"
"v_mfma_f32_16x16x32_bf16 a[112:115], v[48:51], v[128:131], a[112:115]\n"
"ds_read_b64_tr_b16 v[16:17], v232 offset:32\n"
"v_mfma_f32_16x16x32_bf16 a[112:115], v[52:55], v[132:135], a[112:115]\n"
"ds_read_b64_tr_b16 v[18:19], v232 offset:3616\n"
"v_mfma_f32_16x16x32_bf16 a[116:119], v[52:55], v[140:143], a[116:119]\n"
"ds_read_b64_tr_b16 v[20:21], v232 offset:8224\n"
"v_mfma_f32_16x16x32_bf16 a[116:119], v[48:51], v[136:139], a[116:119]\n"
"ds_read_b64_tr_b16 v[22:23], v232 offset:11808\n"
"ds_read_b64_tr_b16 v[24:25], v232 offset:16416\n"
"v_mfma_f32_16x16x32_bf16 a[120:123], v[56:59], v[128:131], a[120:123]\n"
"v_mfma_f32_16x16x32_bf16 a[120:123], v[60:63], v[132:135], a[120:123]\n"
"ds_read_b64_tr_b16 v[26:27], v232 offset:20000\n"
"v_mfma_f32_16x16x32_bf16 a[124:127], v[60:63], v[140:143], a[124:127]\n"
"ds_read_b64_tr_b16 v[28:29], v232 offset:24608\n"
"v_mfma_f32_16x16x32_bf16 a[124:127], v[56:59], v[136:139], a[124:127]\n"
"s_waitcnt lgkmcnt(7)\n"
"ds_read_b64_tr_b16 v[30:31], v232 offset:28192\n"
"s_waitcnt vmcnt(2)\n"
"v_cmp_neq_f32 s[92:93], v237, v248\n"
"v_cmp_neq_f32 vcc, v238, v250\n"
"s_nop 0\n"
"s_or_b64 vcc, vcc, s[92:93]\n"
"s_cbranch_vccnz L_eq%=\n"
"L_eqr%=:\n"
"v_mfma_f32_16x16x32_bf16 a[0:3], v[72:75], a[128:131], a[0:3]\n"
"v_add_f32 v233, v233, v249\n"
"v_mfma_f32_16x16x32_bf16 a[8:11], v[70:73], a[128:131], a[8:11]\n"
"v_rcp_f32 v235, v233\n"
"v_mfma_f32_16x16x32_bf16 a[4:7], v[72:75], a[160:163], a[4:7]\n"
"v_add_f32 v234, v234, v251\n"
"v_mfma_f32_16x16x32_bf16 a[12:15], v[70:73], a[160:163], a[12:15]\n"
"v_rcp_f32 v236, v234\n"
"v_mfma_f32_16x16x32_bf16 a[16:19], v[72:75], a[132:135], a[16:19]\n"
"v_mfma_f32_16x16x32_bf16 a[24:27], v[70:73], a[132:135], a[24:27]\n"
"v_mfma_f32_16x16x32_bf16 a[20:23], v[72:75], a[164:167], a[20:23]\n"
"v_mfma_f32_16x16x32_bf16 a[28:31], v[70:73], a[164:167], a[28:31]\n"
"v_mfma_f32_16x16x32_bf16 a[32:35], v[72:75], a[136:139], a[32:35]\n"
"v_mfma_f32_16x16x32_bf16 a[40:43], v[70:73], a[136:139], a[40:43]\n"
"v_mfma_f32_16x16x32_bf16 a[36:39], v[72:75], a[168:171], a[36:39]\n"
"v_mfma_f32_16x16x32_bf16 a[44:47], v[70:73], a[168:171], a[44:47]\n"
"v_mfma_f32_16x16x32_bf16 a[48:51], v[72:75], a[140:143], a[48:51]\n"
"v_mfma_f32_16x16x32_bf16 a[56:59], v[70:73], a[140:143], a[56:59]\n"
"v_mfma_f32_16x16x32_bf16 a[52:55], v[72:75], a[172:175], a[52:55]\n"
"v_mfma_f32_16x16x32_bf16 a[60:63], v[70:73], a[172:175], a[60:63]\n"
"v_mfma_f32_16x16x32_bf16 a[64:67], v[72:75], a[144:147], a[64:67]\n"
"v_mfma_f32_16x16x32_bf16 a[72:75], v[70:73], a[144:147], a[72:75]\n"
"v_mfma_f32_16x16x32_bf16 a[68:71], v[72:75], a[176:179], a[68:71]\n"
"v_mfma_f32_16x16x32_bf16 a[76:79], v[70:73], a[176:179], a[76:79]\n"
"v_mfma_f32_16x16x32_bf16 a[80:83], v[72:75], a[148:151], a[80:83]\n"
"v_mfma_f32_16x16x32_bf16 a[88:91], v[70:73], a[148:151], a[88:91]\n"
"v_mfma_f32_16x16x32_bf16 a[84:87], v[72:75], a[180:183], a[84:87]\n"
"v_mfma_f32_16x16x32_bf16 a[92:95], v[70:73], a[180:183], a[92:95]\n"
"v_mfma_f32_16x16x32_bf16 a[96:99], v[72:75], a[152:155], a[96:99]\n"
"v_mfma_f32_16x16x32_bf16 a[104:107], v[70:73], a[152:155], a[104:107]\n"
"v_mfma_f32_16x16x32_bf16 a[100:103], v[72:75], a[184:187], a[100:103]\n"
"v_mfma_f32_16x16x32_bf16 a[108:111], v[70:73], a[184:187], a[108:111]\n"
"v_mfma_f32_16x16x32_bf16 a[112:115], v[72:75], a[156:159], a[112:115]\n"
"v_mfma_f32_16x16x32_bf16 a[120:123], v[70:73], a[156:159], a[120:123]\n"
"v_mfma_f32_16x16x32_bf16 a[116:119], v[72:75], a[188:191], a[116:119]\n"
"v_mfma_f32_16x16x32_bf16 a[124:127], v[70:73], a[188:191], a[124:127]\n"
"L_epi%=:\n"
"s_waitcnt lgkmcnt(0)\n"
"s_nop 1\n"
"v_mfma_f32_16x16x32_bf16 v[64:67], v[0:3], v[176:179], 0\n"
"ds_read_b64_tr_b16 v[32:33], v232 offset:64\n"
"v_readlane_b32 s24, v224, s48\n"
"v_mfma_f32_16x16x32_bf16 v[64:67], v[4:7], v[180:183], v[64:67]\n"
"ds_read_b64_tr_b16 v[34:35], v232 offset:3648\n"
"v_readlane_b32 s25, v225, s48\n"
"v_mfma_f32_16x16x32_bf16 v[64:67], v[8:11], v[184:187], v[64:67]\n"
"ds_read_b64_tr_b16 v[36:37], v232 offset:8256\n"
"v_readlane_b32 s26, v226, s48\n"
"v_accvgpr_read_b32 v128, a0\n"
"v_accvgpr_read_b32 v129, a1\n"
"v_accvgpr_read_b32 v130, a2\n"
"v_accvgpr_read_b32 v131, a3\n"
"v_accvgpr_read_b32 v132, a8\n"
"v_mfma_f32_16x16x32_bf16 v[64:67], v[12:15], v[188:191], v[64:67]\n"
"s_add_u32 s48, s48, 1\n"
"ds_read_b64_tr_b16 v[38:39], v232 offset:11840\n"
"ds_read_b64_tr_b16 v[40:41], v232 offset:16448\n"
"v_accvgpr_read_b32 v133, a9\n"
"v_accvgpr_read_b32 v134, a10\n"
"v_accvgpr_read_b32 v135, a11\n"
"v_mul_f32 v128, v235, v128\n"
"v_mul_f32 v129, v235, v129\n"
"v_mfma_f32_16x16x32_bf16 v[68:71], v[12:15], v[204:207], 0\n"
"v_mul_f32 v130, v235, v130\n"
"v_mul_f32 v131, v235, v131\n"
"v_mul_f32 v132, v235, v132\n"
"v_mul_f32 v133, v235, v133\n"
"v_mul_f32 v134, v235, v134\n"
"v_mul_f32 v135, v235, v135\n"
"v_cvt_pk_bf16_f32 v136, v128, v129\n"
"v_mfma_f32_16x16x32_bf16 v[68:71], v[8:11], v[200:203], v[68:71]\n"
"ds_read_b64_tr_b16 v[42:43], v232 offset:20032\n"
"v_cvt_pk_bf16_f32 v137, v130, v131\n"
"v_cvt_pk_bf16_f32 v138, v132, v133\n"
"v_cvt_pk_bf16_f32 v139, v134, v135\n"
"v_accvgpr_write_b32 a192, v136\n"
"v_accvgpr_write_b32 a193, v137\n"
"v_accvgpr_write_b32 a194, v138\n"
"v_mfma_f32_16x16x32_bf16 v[68:71], v[4:7], v[196:199], v[68:71]\n"
"ds_read_b64_tr_b16 v[44:45], v232 offset:24640\n"
"v_accvgpr_write_b32 a195, v139\n"
"v_mfma_f32_16x16x32_bf16 v[68:71], v[0:3], v[192:195], v[68:71]\n"
"ds_read_b64_tr_b16 v[46:47], v232 offset:28224\n"
"v_mfma_f32_16x16x32_bf16 v[72:75], v[16:19], v[176:179], 0\n"
"ds_read_b64_tr_b16 v[48:49], v232 offset:96\n"
"v_mfma_f32_16x16x32_bf16 v[72:75], v[20:23], v[180:183], v[72:75]\n"
"ds_read_b64_tr_b16 v[50:51], v232 offset:3680\n"
"buffer_load_dwordx4 v208, s[24:27], s49 offen offset:0 lds\n"
"v_mfma_f32_16x16x32_bf16 v[72:75], v[24:27], v[184:187], v[72:75]\n"
"ds_read_b64_tr_b16 v[52:53], v232 offset:8288\n"
"v_accvgpr_read_b32 v140, a4\n"
"v_accvgpr_read_b32 v141, a5\n"
"v_accvgpr_read_b32 v142, a6\n"
"v_accvgpr_read_b32 v143, a7\n"
"v_accvgpr_read_b32 v144, a12\n"
"v_accvgpr_read_b32 v145, a13\n"
"v_mfma_f32_16x16x32_bf16 v[72:75], v[28:31], v[188:191], v[72:75]\n"
"ds_read_b64_tr_b16 v[54:55], v232 offset:11872\n"
"ds_read_b64_tr_b16 v[56:57], v232 offset:16480\n"
"v_accvgpr_read_b32 v146, a14\n"
"v_accvgpr_read_b32 v147, a15\n"
"v_mul_f32 v140, v236, v140\n"
"v_mul_f32 v141, v236, v141\n"
"v_mul_f32 v142, v236, v142\n"
"v_mfma_f32_16x16x32_bf16 v[76:79], v[28:31], v[204:207], 0\n"
"buffer_load_dwordx4 v209, s[24:27], s49 offen offset:1024 lds\n"
"v_mfma_f32_16x16x32_bf16 v[76:79], v[24:27], v[200:203], v[76:79]\n"
"ds_read_b64_tr_b16 v[58:59], v232 offset:20064\n"
"v_mul_f32 v143, v236, v143\n"
"v_mul_f32 v144, v236, v144\n"
"v_mul_f32 v145, v236, v145\n"
"v_mul_f32 v146, v236, v146\n"
"v_mul_f32 v147, v236, v147\n"
"v_cvt_pk_bf16_f32 v148, v140, v141\n"
"v_mfma_f32_16x16x32_bf16 v[76:79], v[20:23], v[196:199], v[76:79]\n"
"ds_read_b64_tr_b16 v[60:61], v232 offset:24672\n"
"v_cvt_pk_bf16_f32 v149, v142, v143\n"
"v_cvt_pk_bf16_f32 v150, v144, v145\n"
"v_cvt_pk_bf16_f32 v151, v146, v147\n"
"v_accvgpr_write_b32 a224, v148\n"
"v_accvgpr_write_b32 a225, v149\n"
"v_accvgpr_write_b32 a226, v150\n"
"v_mfma_f32_16x16x32_bf16 v[76:79], v[16:19], v[192:195], v[76:79]\n"
"s_waitcnt lgkmcnt(7)\n"
"ds_read_b64_tr_b16 v[62:63], v232 offset:28256\n"
"v_accvgpr_write_b32 a227, v151\n"
"v_accvgpr_read_b32 v152, a16\n"
"v_accvgpr_read_b32 v153, a17\n"
"v_accvgpr_read_b32 v154, a18\n"
"v_accvgpr_read_b32 v155, a19\n"
"v_accvgpr_read_b32 v156, a24\n"
"v_mfma_f32_16x16x32_bf16 v[80:83], v[32:35], v[176:179], 0\n"
"ds_read_b64_tr_b16 v[0:1], v232 offset:128\n"
"v_accvgpr_read_b32 v157, a25\n"
"v_accvgpr_read_b32 v158, a26\n"
"v_accvgpr_read_b32 v159, a27\n"
"v_mul_f32 v152, v235, v152\n"
"v_mul_f32 v153, v235, v153\n"
"v_mul_f32 v154, v235, v154\n"
"v_mfma_f32_16x16x32_bf16 v[80:83], v[36:39], v[180:183], v[80:83]\n"
"ds_read_b64_tr_b16 v[2:3], v232 offset:3712\n"
"buffer_load_dwordx4 v210, s[24:27], s49 offen offset:2048 lds\n"
"v_mfma_f32_16x16x32_bf16 v[80:83], v[40:43], v[184:187], v[80:83]\n"
"ds_read_b64_tr_b16 v[4:5], v232 offset:8320\n"
"v_mul_f32 v155, v235, v155\n"
"v_mul_f32 v156, v235, v156\n"
"v_mul_f32 v157, v235, v157\n"
"v_mul_f32 v158, v235, v158\n"
"v_mul_f32 v159, v235, v159\n"
"v_cvt_pk_bf16_f32 v160, v152, v153\n"
"v_mfma_f32_16x16x32_bf16 v[80:83], v[44:47], v[188:191], v[80:83]\n"
"ds_read_b64_tr_b16 v[6:7], v232 offset:11904\n"
"ds_read_b64_tr_b16 v[8:9], v232 offset:16512\n"
"v_cvt_pk_bf16_f32 v161, v154, v155\n"
"v_cvt_pk_bf16_f32 v162, v156, v157\n"
"v_cvt_pk_bf16_f32 v163, v158, v159\n"
"v_accvgpr_write_b32 a196, v160\n"
"v_accvgpr_write_b32 a197, v161\n"
"v_mfma_f32_16x16x32_bf16 v[84:87], v[44:47], v[204:207], 0\n"
"buffer_load_dwordx4 v211, s[24:27], s49 offen offset:3072 lds\n"
"v_mfma_f32_16x16x32_bf16 v[84:87], v[40:43], v[200:203], v[84:87]\n"
"s_add_u32 m0, m0, 4096\n"
"ds_read_b64_tr_b16 v[10:11], v232 offset:20096\n"
"v_accvgpr_write_b32 a198, v162\n"
"v_accvgpr_write_b32 a199, v163\n"
"v_mfma_f32_16x16x32_bf16 v[84:87], v[36:39], v[196:199], v[84:87]\n"
"ds_read_b64_tr_b16 v[12:13], v232 offset:24704\n"
"v_accvgpr_read_b32 v164, a20\n"
"v_accvgpr_read_b32 v165, a21\n"
"v_accvgpr_read_b32 v166, a22\n"
"v_accvgpr_read_b32 v167, a23\n"
"v_accvgpr_read_b32 v168, a28\n"
"v_accvgpr_read_b32 v169, a29\n"
"v_mfma_f32_16x16x32_bf16 v[84:87], v[32:35], v[192:195], v[84:87]\n"
"s_waitcnt lgkmcnt(7)\n"
"ds_read_b64_tr_b16 v[14:15], v232 offset:28288\n"
"v_accvgpr_read_b32 v170, a30\n"
"v_accvgpr_read_b32 v171, a31\n"
"v_mul_f32 v164, v236, v164\n"
"v_mul_f32 v165, v236, v165\n"
"v_mul_f32 v166, v236, v166\n"
"v_mul_f32 v167, v236, v167\n"
"v_mfma_f32_16x16x32_bf16 v[88:91], v[48:51], v[176:179], 0\n"
"ds_read_b64_tr_b16 v[16:17], v232 offset:160\n"
"v_mul_f32 v168, v236, v168\n"
"v_mul_f32 v169, v236, v169\n"
"v_mul_f32 v170, v236, v170\n"
"v_mul_f32 v171, v236, v171\n"
"v_cvt_pk_bf16_f32 v172, v164, v165\n"
"v_cvt_pk_bf16_f32 v173, v166, v167\n"
"v_mfma_f32_16x16x32_bf16 v[88:91], v[52:55], v[180:183], v[88:91]\n"
"ds_read_b64_tr_b16 v[18:19], v232 offset:3744\n"
"buffer_load_dwordx4 v212, s[24:27], s49 offen offset:0 lds\n"
"v_mfma_f32_16x16x32_bf16 v[88:91], v[56:59], v[184:187], v[88:91]\n"
"ds_read_b64_tr_b16 v[20:21], v232 offset:8352\n"
"v_cvt_pk_bf16_f32 v174, v168, v169\n"
"v_cvt_pk_bf16_f32 v175, v170, v171\n"
"v_accvgpr_write_b32 a228, v172\n"
"v_accvgpr_write_b32 a229, v173\n"
"v_accvgpr_write_b32 a230, v174\n"
"v_accvgpr_write_b32 a231, v175\n"
"v_mfma_f32_16x16x32_bf16 v[88:91], v[60:63], v[188:191], v[88:91]\n"
"ds_read_b64_tr_b16 v[22:23], v232 offset:11936\n"
"ds_read_b64_tr_b16 v[24:25], v232 offset:16544\n"
"v_mfma_f32_16x16x32_bf16 v[92:95], v[60:63], v[204:207], 0\n"
"buffer_load_dwordx4 v213, s[24:27], s49 offen offset:1024 lds\n"
"v_mfma_f32_16x16x32_bf16 v[92:95], v[56:59], v[200:203], v[92:95]\n"
"ds_read_b64_tr_b16 v[26:27], v232 offset:20128\n"
"v_accvgpr_read_b32 v128, a32\n"
"v_accvgpr_read_b32 v129, a33\n"
"v_accvgpr_read_b32 v130, a34\n"
"v_accvgpr_read_b32 v131, a35\n"
"v_accvgpr_read_b32 v132, a40\n"
"v_accvgpr_read_b32 v133, a41\n"
"v_mfma_f32_16x16x32_bf16 v[92:95], v[52:55], v[196:199], v[92:95]\n"
"ds_read_b64_tr_b16 v[28:29], v232 offset:24736\n"
"v_accvgpr_read_b32 v134, a42\n"
"v_accvgpr_read_b32 v135, a43\n"
"v_mul_f32 v128, v235, v128\n"
"v_mul_f32 v129, v235, v129\n"
"v_mul_f32 v130, v235, v130\n"
"v_mul_f32 v131, v235, v131\n"
"v_mfma_f32_16x16x32_bf16 v[92:95], v[48:51], v[192:195], v[92:95]\n"
"s_waitcnt lgkmcnt(7)\n"
"ds_read_b64_tr_b16 v[30:31], v232 offset:28320\n"
"v_mul_f32 v132, v235, v132\n"
"v_mul_f32 v133, v235, v133\n"
"v_mul_f32 v134, v235, v134\n"
"v_mul_f32 v135, v235, v135\n"
"v_cvt_pk_bf16_f32 v136, v128, v129\n"
"v_cvt_pk_bf16_f32 v137, v130, v131\n"
"v_mfma_f32_16x16x32_bf16 v[96:99], v[0:3], v[176:179], 0\n"
"ds_read_b64_tr_b16 v[32:33], v232 offset:192\n"
"ds_read_b64_tr_b16 v[34:35], v232 offset:3776\n"
"v_cvt_pk_bf16_f32 v138, v132, v133\n"
"v_cvt_pk_bf16_f32 v139, v134, v135\n"
"v_accvgpr_write_b32 a200, v136\n"
"v_accvgpr_write_b32 a201, v137\n"
"v_accvgpr_write_b32 a202, v138\n"
"v_mfma_f32_16x16x32_bf16 v[96:99], v[4:7], v[180:183], v[96:99]\n"
"ds_read_b64_tr_b16 v[36:37], v232 offset:8384\n"
"ds_read_b64_tr_b16 v[38:39], v232 offset:11968\n"
"v_accvgpr_write_b32 a203, v139\n"
"v_mfma_f32_16x16x32_bf16 v[96:99], v[8:11], v[184:187], v[96:99]\n"
"s_add_u32 s54, s50, s47\n"
"ds_read_b64_tr_b16 v[40:41], v232 offset:16576\n"
"ds_read_b64_tr_b16 v[42:43], v232 offset:20160\n"
"v_mfma_f32_16x16x32_bf16 v[96:99], v[12:15], v[188:191], v[96:99]\n"
"s_add_u32 s50, s50, 32768\n"
"ds_read_b64_tr_b16 v[44:45], v232 offset:24768\n"
"ds_read_b64_tr_b16 v[46:47], v232 offset:28352\n"
"v_accvgpr_read_b32 v140, a36\n"
"v_accvgpr_read_b32 v141, a37\n"
"v_accvgpr_read_b32 v142, a38\n"
"v_accvgpr_read_b32 v143, a39\n"
"v_accvgpr_read_b32 v144, a44\n"
"v_mfma_f32_16x16x32_bf16 v[100:103], v[12:15], v[204:207], 0\n"
"s_cmp_lt_u32 s50, 163840\n"
"s_cselect_b32 s50, s50, 0\n"
"v_accvgpr_read_b32 v145, a45\n"
"v_accvgpr_read_b32 v146, a46\n"
"v_accvgpr_read_b32 v147, a47\n"
"v_mul_f32 v140, v236, v140\n"
"v_mul_f32 v141, v236, v141\n"
"v_mul_f32 v142, v236, v142\n"
"v_mfma_f32_16x16x32_bf16 v[100:103], v[8:11], v[200:203], v[100:103]\n"
"buffer_load_dwordx4 v214, s[24:27], s49 offen offset:2048 lds\n"
"v_mfma_f32_16x16x32_bf16 v[100:103], v[4:7], v[196:199], v[100:103]\n"
"s_waitcnt lgkmcnt(0)\n"
"v_add_u32 v232, s50, v228\n"
"v_mul_f32 v143, v236, v143\n"
"v_mul_f32 v144, v236, v144\n"
"v_mul_f32 v145, v236, v145\n"
"v_mul_f32 v146, v236, v146\n"
"v_mul_f32 v147, v236, v147\n"
"v_cvt_pk_bf16_f32 v148, v140, v141\n"
"v_mfma_f32_16x16x32_bf16 v[100:103], v[0:3], v[192:195], v[100:103]\n"
"s_waitcnt vmcnt(23)\n"
"s_barrier\n"
"v_cvt_pk_bf16_f32 v149, v142, v143\n"
"v_cvt_pk_bf16_f32 v150, v144, v145\n"
"v_cvt_pk_bf16_f32 v151, v146, v147\n"
"v_accvgpr_write_b32 a232, v148\n"
"v_accvgpr_write_b32 a233, v149\n"
"v_mfma_f32_16x16x32_bf16 v[104:107], v[16:19], v[176:179], 0\n"
"ds_read_b64_tr_b16 v[48:49], v232 offset:0\n"
"buffer_load_dwordx4 v215, s[24:27], s49 offen offset:3072 lds\n"
"v_mfma_f32_16x16x32_bf16 v[104:107], v[20:23], v[180:183], v[104:107]\n"
"s_mov_b32 m0, s54\n"
"ds_read_b64_tr_b16 v[50:51], v232 offset:3584\n"
"v_accvgpr_write_b32 a234, v150\n"
"v_accvgpr_write_b32 a235, v151\n"
"v_accvgpr_read_b32 v152, a48\n"
"v_accvgpr_read_b32 v153, a49\n"
"v_accvgpr_read_b32 v154, a50\n"
"v_accvgpr_read_b32 v155, a51\n"
"v_mfma_f32_16x16x32_bf16 v[104:107], v[24:27], v[184:187], v[104:107]\n"
"ds_read_b64_tr_b16 v[52:53], v232 offset:8192\n"
"v_accvgpr_read_b32 v156, a56\n"
"v_accvgpr_read_b32 v157, a57\n"
"v_accvgpr_read_b32 v158, a58\n"
"v_accvgpr_read_b32 v159, a59\n"
"v_mul_f32 v152, v235, v152\n"
"v_mul_f32 v153, v235, v153\n"
"v_mfma_f32_16x16x32_bf16 v[104:107], v[28:31], v[188:191], v[104:107]\n"
"ds_read_b64_tr_b16 v[54:55], v232 offset:11776\n"
"ds_read_b64_tr_b16 v[56:57], v232 offset:16384\n"
"v_mul_f32 v154, v235, v154\n"
"v_mul_f32 v155, v235, v155\n"
"v_mul_f32 v156, v235, v156\n"
"v_mul_f32 v157, v235, v157\n"
"v_mul_f32 v158, v235, v158\n"
"v_mfma_f32_16x16x32_bf16 v[108:111], v[28:31], v[204:207], 0\n"
"v_mul_f32 v159, v235, v159\n"
"v_cvt_pk_bf16_f32 v160, v152, v153\n"
"v_cvt_pk_bf16_f32 v161, v154, v155\n"
"v_cvt_pk_bf16_f32 v162, v156, v157\n"
"v_cvt_pk_bf16_f32 v163, v158, v159\n"
"v_accvgpr_write_b32 a204, v160\n"
"v_accvgpr_write_b32 a205, v161\n"
"v_mfma_f32_16x16x32_bf16 v[108:111], v[24:27], v[200:203], v[108:111]\n"
"ds_read_b64_tr_b16 v[58:59], v232 offset:19968\n"
"v_accvgpr_write_b32 a206, v162\n"
"v_accvgpr_write_b32 a207, v163\n"
"v_mfma_f32_16x16x32_bf16 v[108:111], v[20:23], v[196:199], v[108:111]\n"
"ds_read_b64_tr_b16 v[60:61], v232 offset:24576\n"
"v_mfma_f32_16x16x32_bf16 v[108:111], v[16:19], v[192:195], v[108:111]\n"
"ds_read_b64_tr_b16 v[62:63], v232 offset:28160\n"
"v_mfma_f32_16x16x32_bf16 v[112:115], v[32:35], v[176:179], 0\n"
"ds_read_b64_tr_b16 v[0:1], v232 offset:32\n"
"v_accvgpr_read_b32 v164, a52\n"
"v_accvgpr_read_b32 v165, a53\n"
"v_accvgpr_read_b32 v166, a54\n"
"v_accvgpr_read_b32 v167, a55\n"
"v_accvgpr_read_b32 v168, a60\n"
"v_accvgpr_read_b32 v169, a61\n"
"v_mfma_f32_16x16x32_bf16 v[112:115], v[36:39], v[180:183], v[112:115]\n"
"ds_read_b64_tr_b16 v[2:3], v232 offset:3616\n"
"v_accvgpr_read_b32 v170, a62\n"
"v_accvgpr_read_b32 v171, a63\n"
"v_mul_f32 v164, v236, v164\n"
"v_mul_f32 v165, v236, v165\n"
"v_mul_f32 v166, v236, v166\n"
"v_mul_f32 v167, v236, v167\n"
"v_mfma_f32_16x16x32_bf16 v[112:115], v[40:43], v[184:187], v[112:115]\n"
"ds_read_b64_tr_b16 v[4:5], v232 offset:8224\n"
"v_mul_f32 v168, v236, v168\n"
"v_mul_f32 v169, v236, v169\n"
"v_mul_f32 v170, v236, v170\n"
"v_mul_f32 v171, v236, v171\n"
"v_cvt_pk_bf16_f32 v172, v164, v165\n"
"v_cvt_pk_bf16_f32 v173, v166, v167\n"
"v_mfma_f32_16x16x32_bf16 v[112:115], v[44:47], v[188:191], v[112:115]\n"
"ds_read_b64_tr_b16 v[6:7], v232 offset:11808\n"
"ds_read_b64_tr_b16 v[8:9], v232 offset:16416\n"
"v_cvt_pk_bf16_f32 v174, v168, v169\n"
"v_cvt_pk_bf16_f32 v175, v170, v171\n"
"v_accvgpr_write_b32 a236, v172\n"
"v_accvgpr_write_b32 a237, v173\n"
"v_accvgpr_write_b32 a238, v174\n"
"v_mfma_f32_16x16x32_bf16 v[116:119], v[44:47], v[204:207], 0\n"
"v_accvgpr_write_b32 a239, v175\n"
"v_mfma_f32_16x16x32_bf16 v[116:119], v[40:43], v[200:203], v[116:119]\n"
"ds_read_b64_tr_b16 v[10:11], v232 offset:20000\n"
"v_mfma_f32_16x16x32_bf16 v[116:119], v[36:39], v[196:199], v[116:119]\n"
"ds_read_b64_tr_b16 v[12:13], v232 offset:24608\n"
"v_mfma_f32_16x16x32_bf16 v[116:119], v[32:35], v[192:195], v[116:119]\n"
"s_waitcnt lgkmcnt(7)\n"
"ds_read_b64_tr_b16 v[14:15], v232 offset:28192\n"
"v_mfma_f32_16x16x32_bf16 v[64:67], v[48:51], a[192:195], v[64:67]\n"
"ds_read_b64_tr_b16 v[16:17], v232 offset:64\n"
"v_readlane_b32 s24, v224, s48\n"
"v_mfma_f32_16x16x32_bf16 v[64:67], v[52:55], a[196:199], v[64:67]\n"
"ds_read_b64_tr_b16 v[18:19], v232 offset:3648\n"
"v_readlane_b32 s25, v225, s48\n"
"v_mfma_f32_16x16x32_bf16 v[64:67], v[56:59], a[200:203], v[64:67]\n"
"ds_read_b64_tr_b16 v[20:21], v232 offset:8256\n"
"v_readlane_b32 s26, v226, s48\n"
"v_accvgpr_read_b32 v128, a64\n"
"v_accvgpr_read_b32 v129, a65\n"
"v_accvgpr_read_b32 v130, a66\n"
"v_accvgpr_read_b32 v131, a67\n"
"v_accvgpr_read_b32 v132, a72\n"
"v_mfma_f32_16x16x32_bf16 v[64:67], v[60:63], a[204:207], v[64:67]\n"
"s_add_u32 s48, s48, 1\n"
"ds_read_b64_tr_b16 v[22:23], v232 offset:11840\n"
"ds_read_b64_tr_b16 v[24:25], v232 offset:16448\n"
"v_accvgpr_read_b32 v133, a73\n"
"v_accvgpr_read_b32 v134, a74\n"
"v_accvgpr_read_b32 v135, a75\n"
"v_mul_f32 v128, v235, v128\n"
"v_mul_f32 v129, v235, v129\n"
"v_mfma_f32_16x16x32_bf16 v[68:71], v[60:63], a[236:239], v[68:71]\n"
"v_mul_f32 v130, v235, v130\n"
"v_mul_f32 v131, v235, v131\n"
"v_mul_f32 v132, v235, v132\n"
"v_mul_f32 v133, v235, v133\n"
"v_mul_f32 v134, v235, v134\n"
"v_mul_f32 v135, v235, v135\n"
"v_cvt_pk_bf16_f32 v136, v128, v129\n"
"v_mfma_f32_16x16x32_bf16 v[68:71], v[56:59], a[232:235], v[68:71]\n"
"ds_read_b64_tr_b16 v[26:27], v232 offset:20032\n"
"v_cvt_pk_bf16_f32 v137, v130, v131\n"
"v_cvt_pk_bf16_f32 v138, v132, v133\n"
"v_cvt_pk_bf16_f32 v139, v134, v135\n"
"v_accvgpr_write_b32 a208, v136\n"
"v_accvgpr_write_b32 a209, v137\n"
"v_accvgpr_write_b32 a210, v138\n"
"v_mfma_f32_16x16x32_bf16 v[68:71], v[52:55], a[228:231], v[68:71]\n"
"ds_read_b64_tr_b16 v[28:29], v232 offset:24640\n"
"v_accvgpr_write_b32 a211, v139\n"
"v_mfma_f32_16x16x32_bf16 v[68:71], v[48:51], a[224:227], v[68:71]\n"
"s_waitcnt lgkmcnt(7)\n"
"ds_read_b64_tr_b16 v[30:31], v232 offset:28224\n"
"v_mfma_f32_16x16x32_bf16 v[72:75], v[0:3], a[192:195], v[72:75]\n"
"ds_read_b64_tr_b16 v[32:33], v232 offset:96\n"
"v_mfma_f32_16x16x32_bf16 v[72:75], v[4:7], a[196:199], v[72:75]\n"
"ds_read_b64_tr_b16 v[34:35], v232 offset:3680\n"
"buffer_load_dwordx4 v208, s[24:27], s49 offen offset:0 lds\n"
"v_mfma_f32_16x16x32_bf16 v[72:75], v[8:11], a[200:203], v[72:75]\n"
"ds_read_b64_tr_b16 v[36:37], v232 offset:8288\n"
"v_accvgpr_read_b32 v140, a68\n"
"v_accvgpr_read_b32 v141, a69\n"
"v_accvgpr_read_b32 v142, a70\n"
"v_accvgpr_read_b32 v143, a71\n"
"v_accvgpr_read_b32 v144, a76\n"
"v_accvgpr_read_b32 v145, a77\n"
"v_mfma_f32_16x16x32_bf16 v[72:75], v[12:15], a[204:207], v[72:75]\n"
"ds_read_b64_tr_b16 v[38:39], v232 offset:11872\n"
"ds_read_b64_tr_b16 v[40:41], v232 offset:16480\n"
"v_accvgpr_read_b32 v146, a78\n"
"v_accvgpr_read_b32 v147, a79\n"
"v_mul_f32 v140, v236, v140\n"
"v_mul_f32 v141, v236, v141\n"
"v_mul_f32 v142, v236, v142\n"
"v_mfma_f32_16x16x32_bf16 v[76:79], v[12:15], a[236:239], v[76:79]\n"
"buffer_load_dwordx4 v209, s[24:27], s49 offen offset:1024 lds\n"
"v_mfma_f32_16x16x32_bf16 v[76:79], v[8:11], a[232:235], v[76:79]\n"
"ds_read_b64_tr_b16 v[42:43], v232 offset:20064\n"
"v_mul_f32 v143, v236, v143\n"
"v_mul_f32 v144, v236, v144\n"
"v_mul_f32 v145, v236, v145\n"
"v_mul_f32 v146, v236, v146\n"
"v_mul_f32 v147, v236, v147\n"
"v_cvt_pk_bf16_f32 v148, v140, v141\n"
"v_mfma_f32_16x16x32_bf16 v[76:79], v[4:7], a[228:231], v[76:79]\n"
"ds_read_b64_tr_b16 v[44:45], v232 offset:24672\n"
"v_cvt_pk_bf16_f32 v149, v142, v143\n"
"v_cvt_pk_bf16_f32 v150, v144, v145\n"
"v_cvt_pk_bf16_f32 v151, v146, v147\n"
"v_accvgpr_write_b32 a240, v148\n"
"v_accvgpr_write_b32 a241, v149\n"
"v_accvgpr_write_b32 a242, v150\n"
"v_mfma_f32_16x16x32_bf16 v[76:79], v[0:3], a[224:227], v[76:79]\n"
"s_waitcnt lgkmcnt(7)\n"
"ds_read_b64_tr_b16 v[46:47], v232 offset:28256\n"
"v_accvgpr_write_b32 a243, v151\n"
"v_accvgpr_read_b32 v152, a80\n"
"v_accvgpr_read_b32 v153, a81\n"
"v_accvgpr_read_b32 v154, a82\n"
"v_accvgpr_read_b32 v155, a83\n"
"v_accvgpr_read_b32 v156, a88\n"
"v_mfma_f32_16x16x32_bf16 v[80:83], v[16:19], a[192:195], v[80:83]\n"
"ds_read_b64_tr_b16 v[48:49], v232 offset:128\n"
"v_accvgpr_read_b32 v157, a89\n"
"v_accvgpr_read_b32 v158, a90\n"
"v_accvgpr_read_b32 v159, a91\n"
"v_mul_f32 v152, v235, v152\n"
"v_mul_f32 v153, v235, v153\n"
"v_mul_f32 v154, v235, v154\n"
"v_mfma_f32_16x16x32_bf16 v[80:83], v[20:23], a[196:199], v[80:83]\n"
"ds_read_b64_tr_b16 v[50:51], v232 offset:3712\n"
"buffer_load_dwordx4 v210, s[24:27], s49 offen offset:2048 lds\n"
"v_mfma_f32_16x16x32_bf16 v[80:83], v[24:27], a[200:203], v[80:83]\n"
"ds_read_b64_tr_b16 v[52:53], v232 offset:8320\n"
"v_mul_f32 v155, v235, v155\n"
"v_mul_f32 v156, v235, v156\n"
"v_mul_f32 v157, v235, v157\n"
"v_mul_f32 v158, v235, v158\n"
"v_mul_f32 v159, v235, v159\n"
"v_cvt_pk_bf16_f32 v160, v152, v153\n"
"v_mfma_f32_16x16x32_bf16 v[80:83], v[28:31], a[204:207], v[80:83]\n"
"ds_read_b64_tr_b16 v[54:55], v232 offset:11904\n"
"ds_read_b64_tr_b16 v[56:57], v232 offset:16512\n"
"v_cvt_pk_bf16_f32 v161, v154, v155\n"
"v_cvt_pk_bf16_f32 v162, v156, v157\n"
"v_cvt_pk_bf16_f32 v163, v158, v159\n"
"v_accvgpr_write_b32 a212, v160\n"
"v_accvgpr_write_b32 a213, v161\n"
"v_mfma_f32_16x16x32_bf16 v[84:87], v[28:31], a[236:239], v[84:87]\n"
"buffer_load_dwordx4 v211, s[24:27], s49 offen offset:3072 lds\n"
"v_mfma_f32_16x16x32_bf16 v[84:87], v[24:27], a[232:235], v[84:87]\n"
"s_add_u32 m0, m0, 4096\n"
"ds_read_b64_tr_b16 v[58:59], v232 offset:20096\n"
"v_accvgpr_write_b32 a214, v162\n"
"v_accvgpr_write_b32 a215, v163\n"
"v_mfma_f32_16x16x32_bf16 v[84:87], v[20:23], a[228:231], v[84:87]\n"
"ds_read_b64_tr_b16 v[60:61], v232 offset:24704\n"
"v_accvgpr_read_b32 v164, a84\n"
"v_accvgpr_read_b32 v165, a85\n"
"v_accvgpr_read_b32 v166, a86\n"
"v_accvgpr_read_b32 v167, a87\n"
"v_accvgpr_read_b32 v168, a92\n"
"v_accvgpr_read_b32 v169, a93\n"
"v_mfma_f32_16x16x32_bf16 v[84:87], v[16:19], a[224:227], v[84:87]\n"
"s_waitcnt lgkmcnt(7)\n"
"ds_read_b64_tr_b16 v[62:63], v232 offset:28288\n"
"v_accvgpr_read_b32 v170, a94\n"
"v_accvgpr_read_b32 v171, a95\n"
"v_mul_f32 v164, v236, v164\n"
"v_mul_f32 v165, v236, v165\n"
"v_mul_f32 v166, v236, v166\n"
"v_mul_f32 v167, v236, v167\n"
"v_mfma_f32_16x16x32_bf16 v[88:91], v[32:35], a[192:195], v[88:91]\n"
"ds_read_b64_tr_b16 v[0:1], v232 offset:160\n"
"v_mul_f32 v168, v236, v168\n"
"v_mul_f32 v169, v236, v169\n"
"v_mul_f32 v170, v236, v170\n"
"v_mul_f32 v171, v236, v171\n"
"v_cvt_pk_bf16_f32 v172, v164, v165\n"
"v_cvt_pk_bf16_f32 v173, v166, v167\n"
"v_mfma_f32_16x16x32_bf16 v[88:91], v[36:39], a[196:199], v[88:91]\n"
"ds_read_b64_tr_b16 v[2:3], v232 offset:3744\n"
"buffer_load_dwordx4 v212, s[24:27], s49 offen offset:0 lds\n"
"v_mfma_f32_16x16x32_bf16 v[88:91], v[40:43], a[200:203], v[88:91]\n"
"ds_read_b64_tr_b16 v[4:5], v232 offset:8352\n"
"v_cvt_pk_bf16_f32 v174, v168, v169\n"
"v_cvt_pk_bf16_f32 v175, v170, v171\n"
"v_accvgpr_write_b32 a244, v172\n"
"v_accvgpr_write_b32 a245, v173\n"
"v_accvgpr_write_b32 a246, v174\n"
"v_accvgpr_write_b32 a247, v175\n"
"v_mfma_f32_16x16x32_bf16 v[88:91], v[44:47], a[204:207], v[88:91]\n"
"ds_read_b64_tr_b16 v[6:7], v232 offset:11936\n"
"ds_read_b64_tr_b16 v[8:9], v232 offset:16544\n"
"v_mfma_f32_16x16x32_bf16 v[92:95], v[44:47], a[236:239], v[92:95]\n"
"buffer_load_dwordx4 v213, s[24:27], s49 offen offset:1024 lds\n"
"v_mfma_f32_16x16x32_bf16 v[92:95], v[40:43], a[232:235], v[92:95]\n"
"ds_read_b64_tr_b16 v[10:11], v232 offset:20128\n"
"v_accvgpr_read_b32 v128, a96\n"
"v_accvgpr_read_b32 v129, a97\n"
"v_accvgpr_read_b32 v130, a98\n"
"v_accvgpr_read_b32 v131, a99\n"
"v_accvgpr_read_b32 v132, a104\n"
"v_accvgpr_read_b32 v133, a105\n"
"v_mfma_f32_16x16x32_bf16 v[92:95], v[36:39], a[228:231], v[92:95]\n"
"ds_read_b64_tr_b16 v[12:13], v232 offset:24736\n"
"v_accvgpr_read_b32 v134, a106\n"
"v_accvgpr_read_b32 v135, a107\n"
"v_mul_f32 v128, v235, v128\n"
"v_mul_f32 v129, v235, v129\n"
"v_mul_f32 v130, v235, v130\n"
"v_mul_f32 v131, v235, v131\n"
"v_mfma_f32_16x16x32_bf16 v[92:95], v[32:35], a[224:227], v[92:95]\n"
"s_waitcnt lgkmcnt(7)\n"
"ds_read_b64_tr_b16 v[14:15], v232 offset:28320\n"
"v_mul_f32 v132, v235, v132\n"
"v_mul_f32 v133, v235, v133\n"
"v_mul_f32 v134, v235, v134\n"
"v_mul_f32 v135, v235, v135\n"
"v_cvt_pk_bf16_f32 v136, v128, v129\n"
"v_cvt_pk_bf16_f32 v137, v130, v131\n"
"v_mfma_f32_16x16x32_bf16 v[96:99], v[48:51], a[192:195], v[96:99]\n"
"ds_read_b64_tr_b16 v[16:17], v232 offset:192\n"
"ds_read_b64_tr_b16 v[18:19], v232 offset:3776\n"
"v_cvt_pk_bf16_f32 v138, v132, v133\n"
"v_cvt_pk_bf16_f32 v139, v134, v135\n"
"v_accvgpr_write_b32 a216, v136\n"
"v_accvgpr_write_b32 a217, v137\n"
"v_accvgpr_write_b32 a218, v138\n"
"v_mfma_f32_16x16x32_bf16 v[96:99], v[52:55], a[196:199], v[96:99]\n"
"ds_read_b64_tr_b16 v[20:21], v232 offset:8384\n"
"ds_read_b64_tr_b16 v[22:23], v232 offset:11968\n"
"v_accvgpr_write_b32 a219, v139\n"
"v_mfma_f32_16x16x32_bf16 v[96:99], v[56:59], a[200:203], v[96:99]\n"
"s_add_u32 s54, s50, s47\n"
"ds_read_b64_tr_b16 v[24:25], v232 offset:16576\n"
"ds_read_b64_tr_b16 v[26:27], v232 offset:20160\n"
"v_mfma_f32_16x16x32_bf16 v[96:99], v[60:63], a[204:207], v[96:99]\n"
"s_add_u32 s50, s50, 32768\n"
"ds_read_b64_tr_b16 v[28:29], v232 offset:24768\n"
"ds_read_b64_tr_b16 v[30:31], v232 offset:28352\n"
"v_accvgpr_read_b32 v140, a100\n"
"v_accvgpr_read_b32 v141, a101\n"
"v_accvgpr_read_b32 v142, a102\n"
"v_accvgpr_read_b32 v143, a103\n"
"v_accvgpr_read_b32 v144, a108\n"
"v_mfma_f32_16x16x32_bf16 v[100:103], v[60:63], a[236:239], v[100:103]\n"
"s_cmp_lt_u32 s50, 163840\n"
"s_cselect_b32 s50, s50, 0\n"
"v_accvgpr_read_b32 v145, a109\n"
"v_accvgpr_read_b32 v146, a110\n"
"v_accvgpr_read_b32 v147, a111\n"
"v_mul_f32 v140, v236, v140\n"
"v_mul_f32 v141, v236, v141\n"
"v_mul_f32 v142, v236, v142\n"
"v_mfma_f32_16x16x32_bf16 v[100:103], v[56:59], a[232:235], v[100:103]\n"
"buffer_load_dwordx4 v214, s[24:27], s49 offen offset:2048 lds\n"
"v_mfma_f32_16x16x32_bf16 v[100:103], v[52:55], a[228:231], v[100:103]\n"
"s_waitcnt lgkmcnt(0)\n"
"v_add_u32 v232, s50, v228\n"
"v_mul_f32 v143, v236, v143\n"
"v_mul_f32 v144, v236, v144\n"
"v_mul_f32 v145, v236, v145\n"
"v_mul_f32 v146, v236, v146\n"
"v_mul_f32 v147, v236, v147\n"
"v_cvt_pk_bf16_f32 v148, v140, v141\n"
"v_mfma_f32_16x16x32_bf16 v[100:103], v[48:51], a[224:227], v[100:103]\n"
"s_waitcnt vmcnt(23)\n"
"s_barrier\n"
"v_cvt_pk_bf16_f32 v149, v142, v143\n"
"v_cvt_pk_bf16_f32 v150, v144, v145\n"
"v_cvt_pk_bf16_f32 v151, v146, v147\n"
"v_accvgpr_write_b32 a248, v148\n"
"v_accvgpr_write_b32 a249, v149\n"
"v_mfma_f32_16x16x32_bf16 v[104:107], v[0:3], a[192:195], v[104:107]\n"
"ds_read_b64_tr_b16 v[32:33], v232 offset:0\n"
"buffer_load_dwordx4 v215, s[24:27], s49 offen offset:3072 lds\n"
"v_mfma_f32_16x16x32_bf16 v[104:107], v[4:7], a[196:199], v[104:107]\n"
"s_mov_b32 m0, s54\n"
"ds_read_b64_tr_b16 v[34:35], v232 offset:3584\n"
"v_accvgpr_write_b32 a250, v150\n"
"v_accvgpr_write_b32 a251, v151\n"
"v_accvgpr_read_b32 v152, a112\n"
"v_accvgpr_read_b32 v153, a113\n"
"v_accvgpr_read_b32 v154, a114\n"
"v_accvgpr_read_b32 v155, a115\n"
"v_mfma_f32_16x16x32_bf16 v[104:107], v[8:11], a[200:203], v[104:107]\n"
"ds_read_b64_tr_b16 v[36:37], v232 offset:8192\n"
"v_accvgpr_read_b32 v156, a120\n"
"v_accvgpr_read_b32 v157, a121\n"
"v_accvgpr_read_b32 v158, a122\n"
"v_accvgpr_read_b32 v159, a123\n"
"v_mul_f32 v152, v235, v152\n"
"v_mul_f32 v153, v235, v153\n"
"v_mfma_f32_16x16x32_bf16 v[104:107], v[12:15], a[204:207], v[104:107]\n"
"ds_read_b64_tr_b16 v[38:39], v232 offset:11776\n"
"ds_read_b64_tr_b16 v[40:41], v232 offset:16384\n"
"v_mul_f32 v154, v235, v154\n"
"v_mul_f32 v155, v235, v155\n"
"v_mul_f32 v156, v235, v156\n"
"v_mul_f32 v157, v235, v157\n"
"v_mul_f32 v158, v235, v158\n"
"v_mfma_f32_16x16x32_bf16 v[108:111], v[12:15], a[236:239], v[108:111]\n"
"v_mul_f32 v159, v235, v159\n"
"v_cvt_pk_bf16_f32 v160, v152, v153\n"
"v_cvt_pk_bf16_f32 v161, v154, v155\n"
"v_cvt_pk_bf16_f32 v162, v156, v157\n"
"v_cvt_pk_bf16_f32 v163, v158, v159\n"
"v_accvgpr_write_b32 a220, v160\n"
"v_accvgpr_write_b32 a221, v161\n"
"v_mfma_f32_16x16x32_bf16 v[108:111], v[8:11], a[232:235], v[108:111]\n"
"ds_read_b64_tr_b16 v[42:43], v232 offset:19968\n"
"v_accvgpr_write_b32 a222, v162\n"
"v_accvgpr_write_b32 a223, v163\n"
"v_mfma_f32_16x16x32_bf16 v[108:111], v[4:7], a[228:231], v[108:111]\n"
"ds_read_b64_tr_b16 v[44:45], v232 offset:24576\n"
"v_mfma_f32_16x16x32_bf16 v[108:111], v[0:3], a[224:227], v[108:111]\n"
"ds_read_b64_tr_b16 v[46:47], v232 offset:28160\n"
"v_mfma_f32_16x16x32_bf16 v[112:115], v[16:19], a[192:195], v[112:115]\n"
"ds_read_b64_tr_b16 v[48:49], v232 offset:32\n"
"v_accvgpr_read_b32 v164, a116\n"
"v_accvgpr_read_b32 v165, a117\n"
"v_accvgpr_read_b32 v166, a118\n"
"v_accvgpr_read_b32 v167, a119\n"
"v_accvgpr_read_b32 v168, a124\n"
"v_accvgpr_read_b32 v169, a125\n"
"v_mfma_f32_16x16x32_bf16 v[112:115], v[20:23], a[196:199], v[112:115]\n"
"ds_read_b64_tr_b16 v[50:51], v232 offset:3616\n"
"v_accvgpr_read_b32 v170, a126\n"
"v_accvgpr_read_b32 v171, a127\n"
"v_mul_f32 v164, v236, v164\n"
"v_mul_f32 v165, v236, v165\n"
"v_mul_f32 v166, v236, v166\n"
"v_mul_f32 v167, v236, v167\n"
"v_mfma_f32_16x16x32_bf16 v[112:115], v[24:27], a[200:203], v[112:115]\n"
"ds_read_b64_tr_b16 v[52:53], v232 offset:8224\n"
"v_mul_f32 v168, v236, v168\n"
"v_mul_f32 v169, v236, v169\n"
"v_mul_f32 v170, v236, v170\n"
"v_mul_f32 v171, v236, v171\n"
"v_cvt_pk_bf16_f32 v172, v164, v165\n"
"v_cvt_pk_bf16_f32 v173, v166, v167\n"
"v_mfma_f32_16x16x32_bf16 v[112:115], v[28:31], a[204:207], v[112:115]\n"
"ds_read_b64_tr_b16 v[54:55], v232 offset:11808\n"
"ds_read_b64_tr_b16 v[56:57], v232 offset:16416\n"
"v_cvt_pk_bf16_f32 v174, v168, v169\n"
"v_cvt_pk_bf16_f32 v175, v170, v171\n"
"v_accvgpr_write_b32 a252, v172\n"
"v_accvgpr_write_b32 a253, v173\n"
"v_accvgpr_write_b32 a254, v174\n"
"v_mfma_f32_16x16x32_bf16 v[116:119], v[28:31], a[236:239], v[116:119]\n"
"v_accvgpr_write_b32 a255, v175\n"
"v_mfma_f32_16x16x32_bf16 v[116:119], v[24:27], a[232:235], v[116:119]\n"
"ds_read_b64_tr_b16 v[58:59], v232 offset:20000\n"
"v_mfma_f32_16x16x32_bf16 v[116:119], v[20:23], a[228:231], v[116:119]\n"
"ds_read_b64_tr_b16 v[60:61], v232 offset:24608\n"
"v_mfma_f32_16x16x32_bf16 v[116:119], v[16:19], a[224:227], v[116:119]\n"
"s_waitcnt lgkmcnt(7)\n"
"ds_read_b64_tr_b16 v[62:63], v232 offset:28192\n"
"v_mfma_f32_16x16x32_bf16 v[64:67], v[32:35], a[208:211], v[64:67]\n"
"ds_read_b64_tr_b16 v[0:1], v232 offset:64\n"
"v_readlane_b32 s24, v224, s48\n"
"v_mfma_f32_16x16x32_bf16 v[64:67], v[36:39], a[212:215], v[64:67]\n"
"ds_read_b64_tr_b16 v[2:3], v232 offset:3648\n"
"v_readlane_b32 s25, v225, s48\n"
"v_mfma_f32_16x16x32_bf16 v[64:67], v[40:43], a[216:219], v[64:67]\n"
"ds_read_b64_tr_b16 v[4:5], v232 offset:8256\n"
"v_readlane_b32 s26, v226, s48\n"
"v_mfma_f32_16x16x32_bf16 v[64:67], v[44:47], a[220:223], v[64:67]\n"
"s_add_u32 s48, s48, 1\n"
"ds_read_b64_tr_b16 v[6:7], v232 offset:11840\n"
"ds_read_b64_tr_b16 v[8:9], v232 offset:16448\n"
"v_mfma_f32_16x16x32_bf16 v[68:71], v[44:47], a[252:255], v[68:71]\n"
"v_bfe_u32 v240, v227, 6, 2\n"
"v_mfma_f32_16x16x32_bf16 v[68:71], v[40:43], a[248:251], v[68:71]\n"
"ds_read_b64_tr_b16 v[10:11], v232 offset:20032\n"
"v_mfma_f32_16x16x32_bf16 v[68:71], v[36:39], a[244:247], v[68:71]\n"
"ds_read_b64_tr_b16 v[12:13], v232 offset:24640\n"
"v_mul_u32_u24 v229, 7168, v240\n"
"v_mfma_f32_16x16x32_bf16 v[68:71], v[32:35], a[240:243], v[68:71]\n"
"s_waitcnt lgkmcnt(7)\n"
"ds_read_b64_tr_b16 v[14:15], v232 offset:28224\n"
"v_mfma_f32_16x16x32_bf16 v[72:75], v[48:51], a[208:211], v[72:75]\n"
"ds_read_b64_tr_b16 v[16:17], v232 offset:96\n"
"v_bfe_u32 v240, v227, 4, 2\n"
"v_mfma_f32_16x16x32_bf16 v[72:75], v[52:55], a[212:215], v[72:75]\n"
"ds_read_b64_tr_b16 v[18:19], v232 offset:3680\n"
"buffer_load_dwordx4 v208, s[24:27], s49 offen offset:0 lds\n"
"v_mfma_f32_16x16x32_bf16 v[72:75], v[56:59], a[216:219], v[72:75]\n"
"ds_read_b64_tr_b16 v[20:21], v232 offset:8288\n"
"v_lshl_add_u32 v229, v240, 6, v229\n"
"v_mfma_f32_16x16x32_bf16 v[72:75], v[60:63], a[220:223], v[72:75]\n"
"ds_read_b64_tr_b16 v[22:23], v232 offset:11872\n"
"ds_read_b64_tr_b16 v[24:25], v232 offset:16480\n"
"v_mfma_f32_16x16x32_bf16 v[76:79], v[60:63], a[252:255], v[76:79]\n"
"buffer_load_dwordx4 v209, s[24:27], s49 offen offset:1024 lds\n"
"v_mfma_f32_16x16x32_bf16 v[76:79], v[56:59], a[248:251], v[76:79]\n"
"ds_read_b64_tr_b16 v[26:27], v232 offset:20064\n"
"v_lshrrev_b32 v240, 8, v227\n"
"v_mfma_f32_16x16x32_bf16 v[76:79], v[52:55], a[244:247], v[76:79]\n"
"ds_read_b64_tr_b16 v[28:29], v232 offset:24672\n"
"v_lshl_add_u32 v229, v240, 4, v229\n"
"v_mfma_f32_16x16x32_bf16 v[76:79], v[48:51], a[240:243], v[76:79]\n"
"s_waitcnt lgkmcnt(7)\n"
"ds_read_b64_tr_b16 v[30:31], v232 offset:28256\n"
"v_mfma_f32_16x16x32_bf16 v[80:83], v[0:3], a[208:211], v[80:83]\n"
"ds_read_b64_tr_b16 v[32:33], v232 offset:128\n"
"v_mfma_f32_16x16x32_bf16 v[80:83], v[4:7], a[212:215], v[80:83]\n"
"ds_read_b64_tr_b16 v[34:35], v232 offset:3712\n"
"buffer_load_dwordx4 v210, s[24:27], s49 offen offset:2048 lds\n"
"v_mfma_f32_16x16x32_bf16 v[80:83], v[8:11], a[216:219], v[80:83]\n"
"ds_read_b64_tr_b16 v[36:37], v232 offset:8320\n"
"v_mfma_f32_16x16x32_bf16 v[80:83], v[12:15], a[220:223], v[80:83]\n"
"ds_read_b64_tr_b16 v[38:39], v232 offset:11904\n"
"ds_read_b64_tr_b16 v[40:41], v232 offset:16512\n"
"v_mfma_f32_16x16x32_bf16 v[84:87], v[12:15], a[252:255], v[84:87]\n"
"buffer_load_dwordx4 v211, s[24:27], s49 offen offset:3072 lds\n"
"v_mfma_f32_16x16x32_bf16 v[84:87], v[8:11], a[248:251], v[84:87]\n"
"s_add_u32 m0, m0, 4096\n"
"ds_read_b64_tr_b16 v[42:43], v232 offset:20096\n"
"v_mfma_f32_16x16x32_bf16 v[84:87], v[4:7], a[244:247], v[84:87]\n"
"ds_read_b64_tr_b16 v[44:45], v232 offset:24704\n"
"v_mfma_f32_16x16x32_bf16 v[84:87], v[0:3], a[240:243], v[84:87]\n"
"s_waitcnt lgkmcnt(7)\n"
"ds_read_b64_tr_b16 v[46:47], v232 offset:28288\n"
"v_mfma_f32_16x16x32_bf16 v[88:91], v[16:19], a[208:211], v[88:91]\n"
"ds_read_b64_tr_b16 v[48:49], v232 offset:160\n"
"v_mfma_f32_16x16x32_bf16 v[88:91], v[20:23], a[212:215], v[88:91]\n"
"ds_read_b64_tr_b16 v[50:51], v232 offset:3744\n"
"buffer_load_dwordx4 v212, s[24:27], s49 offen offset:0 lds\n"
"v_mfma_f32_16x16x32_bf16 v[88:91], v[24:27], a[216:219], v[88:91]\n"
"ds_read_b64_tr_b16 v[52:53], v232 offset:8352\n"
"v_mfma_f32_16x16x32_bf16 v[88:91], v[28:31], a[220:223], v[88:91]\n"
"ds_read_b64_tr_b16 v[54:55], v232 offset:11936\n"
"ds_read_b64_tr_b16 v[56:57], v232 offset:16544\n"
"v_mfma_f32_16x16x32_bf16 v[92:95], v[28:31], a[252:255], v[92:95]\n"
"buffer_load_dwordx4 v213, s[24:27], s49 offen offset:1024 lds\n"
"v_mfma_f32_16x16x32_bf16 v[92:95], v[24:27], a[248:251], v[92:95]\n"
"ds_read_b64_tr_b16 v[58:59], v232 offset:20128\n"
"v_mfma_f32_16x16x32_bf16 v[92:95], v[20:23], a[244:247], v[92:95]\n"
"ds_read_b64_tr_b16 v[60:61], v232 offset:24736\n"
"v_mfma_f32_16x16x32_bf16 v[92:95], v[16:19], a[240:243], v[92:95]\n"
"s_waitcnt lgkmcnt(7)\n"
"ds_read_b64_tr_b16 v[62:63], v232 offset:28320\n"
"v_mfma_f32_16x16x32_bf16 v[96:99], v[32:35], a[208:211], v[96:99]\n"
"ds_read_b64_tr_b16 v[0:1], v232 offset:192\n"
"ds_read_b64_tr_b16 v[2:3], v232 offset:3776\n"
"v_mfma_f32_16x16x32_bf16 v[96:99], v[36:39], a[212:215], v[96:99]\n"
"ds_read_b64_tr_b16 v[4:5], v232 offset:8384\n"
"ds_read_b64_tr_b16 v[6:7], v232 offset:11968\n"
"v_mfma_f32_16x16x32_bf16 v[96:99], v[40:43], a[216:219], v[96:99]\n"
"s_add_u32 s54, s50, s47\n"
"ds_read_b64_tr_b16 v[8:9], v232 offset:16576\n"
"ds_read_b64_tr_b16 v[10:11], v232 offset:20160\n"
"v_mfma_f32_16x16x32_bf16 v[96:99], v[44:47], a[220:223], v[96:99]\n"
"s_add_u32 s50, s50, 32768\n"
"ds_read_b64_tr_b16 v[12:13], v232 offset:24768\n"
"ds_read_b64_tr_b16 v[14:15], v232 offset:28352\n"
"v_mfma_f32_16x16x32_bf16 v[100:103], v[44:47], a[252:255], v[100:103]\n"
"s_cmp_lt_u32 s50, 163840\n"
"s_cselect_b32 s50, s50, 0\n"
"v_mfma_f32_16x16x32_bf16 v[100:103], v[40:43], a[248:251], v[100:103]\n"
"buffer_load_dwordx4 v214, s[24:27], s49 offen offset:2048 lds\n"
"v_mfma_f32_16x16x32_bf16 v[100:103], v[36:39], a[244:247], v[100:103]\n"
"s_waitcnt lgkmcnt(0)\n"
"v_add_u32 v232, s50, v228\n"
"v_mfma_f32_16x16x32_bf16 v[100:103], v[32:35], a[240:243], v[100:103]\n"
"s_waitcnt vmcnt(23)\n"
"s_barrier\n"
"v_mfma_f32_16x16x32_bf16 v[104:107], v[48:51], a[208:211], v[104:107]\n"
"ds_read_b64_tr_b16 v[16:17], v232 offset:0\n"
"buffer_load_dwordx4 v215, s[24:27], s49 offen offset:3072 lds\n"
"v_mfma_f32_16x16x32_bf16 v[104:107], v[52:55], a[212:215], v[104:107]\n"
"s_mov_b32 m0, s54\n"
"ds_read_b64_tr_b16 v[18:19], v232 offset:3584\n"
"v_mfma_f32_16x16x32_bf16 v[104:107], v[56:59], a[216:219], v[104:107]\n"
"ds_read_b64_tr_b16 v[20:21], v232 offset:8192\n"
"v_mfma_f32_16x16x32_bf16 v[104:107], v[60:63], a[220:223], v[104:107]\n"
"ds_read_b64_tr_b16 v[22:23], v232 offset:11776\n"
"ds_read_b64_tr_b16 v[24:25], v232 offset:16384\n"
"v_mfma_f32_16x16x32_bf16 v[108:111], v[60:63], a[252:255], v[108:111]\n"
"v_mfma_f32_16x16x32_bf16 v[108:111], v[56:59], a[248:251], v[108:111]\n"
"ds_read_b64_tr_b16 v[26:27], v232 offset:19968\n"
"v_mfma_f32_16x16x32_bf16 v[108:111], v[52:55], a[244:247], v[108:111]\n"
"ds_read_b64_tr_b16 v[28:29], v232 offset:24576\n"
"v_mfma_f32_16x16x32_bf16 v[108:111], v[48:51], a[240:243], v[108:111]\n"
"ds_read_b64_tr_b16 v[30:31], v232 offset:28160\n"
"v_mfma_f32_16x16x32_bf16 v[112:115], v[0:3], a[208:211], v[112:115]\n"
"ds_read_b64_tr_b16 v[32:33], v232 offset:32\n"
"v_mfma_f32_16x16x32_bf16 v[112:115], v[4:7], a[212:215], v[112:115]\n"
"ds_read_b64_tr_b16 v[34:35], v232 offset:3616\n"
"v_mfma_f32_16x16x32_bf16 v[112:115], v[8:11], a[216:219], v[112:115]\n"
"ds_read_b64_tr_b16 v[36:37], v232 offset:8224\n"
"v_mfma_f32_16x16x32_bf16 v[112:115], v[12:15], a[220:223], v[112:115]\n"
"ds_read_b64_tr_b16 v[38:39], v232 offset:11808\n"
"ds_read_b64_tr_b16 v[40:41], v232 offset:16416\n"
"v_mfma_f32_16x16x32_bf16 v[116:119], v[12:15], a[252:255], v[116:119]\n"
"v_mfma_f32_16x16x32_bf16 v[116:119], v[8:11], a[248:251], v[116:119]\n"
"ds_read_b64_tr_b16 v[42:43], v232 offset:20000\n"
"v_mfma_f32_16x16x32_bf16 v[116:119], v[4:7], a[244:247], v[116:119]\n"
"ds_read_b64_tr_b16 v[44:45], v232 offset:24608\n"
"v_mfma_f32_16x16x32_bf16 v[116:119], v[0:3], a[240:243], v[116:119]\n"
"s_waitcnt lgkmcnt(7)\n"
"ds_read_b64_tr_b16 v[46:47], v232 offset:28192\n"
"v_mfma_f32_16x16x32_bf16 v[120:123], v[16:19], v[176:179], 0\n"
"ds_read_b64_tr_b16 v[48:49], v232 offset:64\n"
"v_readlane_b32 s24, v224, s48\n"
"v_mfma_f32_16x16x32_bf16 v[120:123], v[20:23], v[180:183], v[120:123]\n"
"ds_read_b64_tr_b16 v[50:51], v232 offset:3648\n"
"v_readlane_b32 s25, v225, s48\n"
"v_mfma_f32_16x16x32_bf16 v[120:123], v[24:27], v[184:187], v[120:123]\n"
"ds_read_b64_tr_b16 v[52:53], v232 offset:8256\n"
"v_readlane_b32 s26, v226, s48\n"
"v_mfma_f32_16x16x32_bf16 v[120:123], v[28:31], v[188:191], v[120:123]\n"
"s_add_u32 s48, s48, 1\n"
"ds_read_b64_tr_b16 v[54:55], v232 offset:11840\n"
"ds_read_b64_tr_b16 v[56:57], v232 offset:16448\n"
"v_mfma_f32_16x16x32_bf16 v[124:127], v[28:31], v[204:207], 0\n"
"v_cvt_pk_bf16_f32 v240, v64, v65\n"
"v_mfma_f32_16x16x32_bf16 v[124:127], v[24:27], v[200:203], v[124:127]\n"
"ds_read_b64_tr_b16 v[58:59], v232 offset:20032\n"
"v_mfma_f32_16x16x32_bf16 v[124:127], v[20:23], v[196:199], v[124:127]\n"
"ds_read_b64_tr_b16 v[60:61], v232 offset:24640\n"
"v_cvt_pk_bf16_f32 v241, v66, v67\n"
"v_mfma_f32_16x16x32_bf16 v[124:127], v[16:19], v[192:195], v[124:127]\n"
"s_waitcnt lgkmcnt(7)\n"
"ds_read_b64_tr_b16 v[62:63], v232 offset:28224\n"
"v_mfma_f32_16x16x32_bf16 v[128:131], v[32:35], v[176:179], 0\n"
"ds_read_b64_tr_b16 v[0:1], v232 offset:96\n"
"v_cvt_pk_bf16_f32 v242, v68, v69\n"
"v_mfma_f32_16x16x32_bf16 v[128:131], v[36:39], v[180:183], v[128:131]\n"
"ds_read_b64_tr_b16 v[2:3], v232 offset:3680\n"
"buffer_load_dwordx4 v208, s[24:27], s49 offen offset:0 lds\n"
"v_mfma_f32_16x16x32_bf16 v[128:131], v[40:43], v[184:187], v[128:131]\n"
"ds_read_b64_tr_b16 v[4:5], v232 offset:8288\n"
"v_cvt_pk_bf16_f32 v243, v70, v71\n"
"v_mfma_f32_16x16x32_bf16 v[128:131], v[44:47], v[188:191], v[128:131]\n"
"ds_read_b64_tr_b16 v[6:7], v232 offset:11872\n"
"ds_read_b64_tr_b16 v[8:9], v232 offset:16480\n"
"v_mfma_f32_16x16x32_bf16 v[132:135], v[44:47], v[204:207], 0\n"
"buffer_load_dwordx4 v209, s[24:27], s49 offen offset:1024 lds\n"
"v_mfma_f32_16x16x32_bf16 v[132:135], v[40:43], v[200:203], v[132:135]\n"
"s_add_u32 s53, s46, 0\n"
"ds_read_b64_tr_b16 v[10:11], v232 offset:20064\n"
"v_mfma_f32_16x16x32_bf16 v[132:135], v[36:39], v[196:199], v[132:135]\n"
"ds_read_b64_tr_b16 v[12:13], v232 offset:24672\n"
"v_mfma_f32_16x16x32_bf16 v[132:135], v[32:35], v[192:195], v[132:135]\n"
"s_waitcnt lgkmcnt(7)\n"
"ds_read_b64_tr_b16 v[14:15], v232 offset:28256\n"
"v_mfma_f32_16x16x32_bf16 v[136:139], v[48:51], v[176:179], 0\n"
"ds_read_b64_tr_b16 v[16:17], v232 offset:128\n"
"v_mfma_f32_16x16x32_bf16 v[136:139], v[52:55], v[180:183], v[136:139]\n"
"ds_read_b64_tr_b16 v[18:19], v232 offset:3712\n"
"buffer_load_dwordx4 v210, s[24:27], s49 offen offset:2048 lds\n"
"v_mfma_f32_16x16x32_bf16 v[136:139], v[56:59], v[184:187], v[136:139]\n"
"ds_read_b64_tr_b16 v[20:21], v232 offset:8320\n"
"buffer_store_dwordx4 v[240:243], v229, s[20:23], s53 offen offset:0 sc1\n"
"v_mfma_f32_16x16x32_bf16 v[136:139], v[60:63], v[188:191], v[136:139]\n"
"ds_read_b64_tr_b16 v[22:23], v232 offset:11904\n"
"ds_read_b64_tr_b16 v[24:25], v232 offset:16512\n"
"v_mfma_f32_16x16x32_bf16 v[140:143], v[60:63], v[204:207], 0\n"
"buffer_load_dwordx4 v211, s[24:27], s49 offen offset:3072 lds\n"
"v_mfma_f32_16x16x32_bf16 v[140:143], v[56:59], v[200:203], v[140:143]\n"
"s_add_u32 m0, m0, 4096\n"
"ds_read_b64_tr_b16 v[26:27], v232 offset:20096\n"
"v_cvt_pk_bf16_f32 v244, v72, v73\n"
"v_mfma_f32_16x16x32_bf16 v[140:143], v[52:55], v[196:199], v[140:143]\n"
"ds_read_b64_tr_b16 v[28:29], v232 offset:24704\n"
"v_cvt_pk_bf16_f32 v245, v74, v75\n"
"v_mfma_f32_16x16x32_bf16 v[140:143], v[48:51], v[192:195], v[140:143]\n"
"s_waitcnt lgkmcnt(7)\n"
"ds_read_b64_tr_b16 v[30:31], v232 offset:28288\n"
"v_mfma_f32_16x16x32_bf16 v[144:147], v[0:3], v[176:179], 0\n"
"ds_read_b64_tr_b16 v[32:33], v232 offset:160\n"
"v_cvt_pk_bf16_f32 v246, v76, v77\n"
"v_mfma_f32_16x16x32_bf16 v[144:147], v[4:7], v[180:183], v[144:147]\n"
"ds_read_b64_tr_b16 v[34:35], v232 offset:3744\n"
"buffer_load_dwordx4 v212, s[24:27], s49 offen offset:0 lds\n"
"v_mfma_f32_16x16x32_bf16 v[144:147], v[8:11], v[184:187], v[144:147]\n"
"ds_read_b64_tr_b16 v[36:37], v232 offset:8352\n"
"v_cvt_pk_bf16_f32 v247, v78, v79\n"
"v_mfma_f32_16x16x32_bf16 v[144:147], v[12:15], v[188:191], v[144:147]\n"
"ds_read_b64_tr_b16 v[38:39], v232 offset:11936\n"
"ds_read_b64_tr_b16 v[40:41], v232 offset:16544\n"
"v_mfma_f32_16x16x32_bf16 v[148:151], v[12:15], v[204:207], 0\n"
"buffer_load_dwordx4 v213, s[24:27], s49 offen offset:1024 lds\n"
"v_mfma_f32_16x16x32_bf16 v[148:151], v[8:11], v[200:203], v[148:151]\n"
"ds_read_b64_tr_b16 v[42:43], v232 offset:20128\n"
"v_mfma_f32_16x16x32_bf16 v[148:151], v[4:7], v[196:199], v[148:151]\n"
"ds_read_b64_tr_b16 v[44:45], v232 offset:24736\n"
"v_mfma_f32_16x16x32_bf16 v[148:151], v[0:3], v[192:195], v[148:151]\n"
"s_waitcnt lgkmcnt(7)\n"
"ds_read_b64_tr_b16 v[46:47], v232 offset:28320\n"
"v_mfma_f32_16x16x32_bf16 v[152:155], v[16:19], v[176:179], 0\n"
"ds_read_b64_tr_b16 v[48:49], v232 offset:192\n"
"ds_read_b64_tr_b16 v[50:51], v232 offset:3776\n"
"v_mfma_f32_16x16x32_bf16 v[152:155], v[20:23], v[180:183], v[152:155]\n"
"ds_read_b64_tr_b16 v[52:53], v232 offset:8384\n"
"ds_read_b64_tr_b16 v[54:55], v232 offset:11968\n"
"v_mfma_f32_16x16x32_bf16 v[152:155], v[24:27], v[184:187], v[152:155]\n"
"s_add_u32 s54, s50, s47\n"
"ds_read_b64_tr_b16 v[56:57], v232 offset:16576\n"
"ds_read_b64_tr_b16 v[58:59], v232 offset:20160\n"
"v_mfma_f32_16x16x32_bf16 v[152:155], v[28:31], v[188:191], v[152:155]\n"
"s_add_u32 s50, s50, 32768\n"
"ds_read_b64_tr_b16 v[60:61], v232 offset:24768\n"
"ds_read_b64_tr_b16 v[62:63], v232 offset:28352\n"
"v_mfma_f32_16x16x32_bf16 v[156:159], v[28:31], v[204:207], 0\n"
"s_cmp_lt_u32 s50, 163840\n"
"s_cselect_b32 s50, s50, 0\n"
"v_cvt_pk_bf16_f32 v248, v80, v81\n"
"v_mfma_f32_16x16x32_bf16 v[156:159], v[24:27], v[200:203], v[156:159]\n"
"buffer_store_dwordx4 v[244:247], v229, s[20:23], s53 offen offset:256 sc1\n"
"v_mfma_f32_16x16x32_bf16 v[156:159], v[20:23], v[196:199], v[156:159]\n"
"s_waitcnt lgkmcnt(0)\n"
"v_add_u32 v232, s50, v228\n"
"v_cvt_pk_bf16_f32 v249, v82, v83\n"
"v_mfma_f32_16x16x32_bf16 v[156:159], v[16:19], v[192:195], v[156:159]\n"
"s_waitcnt vmcnt(24)\n"
"s_barrier\n"
"v_mfma_f32_16x16x32_bf16 v[160:163], v[32:35], v[176:179], 0\n"
"ds_read_b64_tr_b16 v[0:1], v232 offset:0\n"
"buffer_load_dwordx4 v214, s[24:27], s49 offen offset:2048 lds\n"
"v_mfma_f32_16x16x32_bf16 v[160:163], v[36:39], v[180:183], v[160:163]\n"
"ds_read_b64_tr_b16 v[2:3], v232 offset:3584\n"
"buffer_load_dwordx4 v215, s[24:27], s49 offen offset:3072 lds\n"
"v_mfma_f32_16x16x32_bf16 v[160:163], v[40:43], v[184:187], v[160:163]\n"
"s_mov_b32 m0, s54\n"
"ds_read_b64_tr_b16 v[4:5], v232 offset:8192\n"
"v_cvt_pk_bf16_f32 v250, v84, v85\n"
"v_mfma_f32_16x16x32_bf16 v[160:163], v[44:47], v[188:191], v[160:163]\n"
"ds_read_b64_tr_b16 v[6:7], v232 offset:11776\n"
"ds_read_b64_tr_b16 v[8:9], v232 offset:16384\n"
"v_mfma_f32_16x16x32_bf16 v[164:167], v[44:47], v[204:207], 0\n"
"v_cvt_pk_bf16_f32 v251, v86, v87\n"
"v_mfma_f32_16x16x32_bf16 v[164:167], v[40:43], v[200:203], v[164:167]\n"
"ds_read_b64_tr_b16 v[10:11], v232 offset:19968\n"
"v_mfma_f32_16x16x32_bf16 v[164:167], v[36:39], v[196:199], v[164:167]\n"
"ds_read_b64_tr_b16 v[12:13], v232 offset:24576\n"
"v_mfma_f32_16x16x32_bf16 v[164:167], v[32:35], v[192:195], v[164:167]\n"
"ds_read_b64_tr_b16 v[14:15], v232 offset:28160\n"
"v_mfma_f32_16x16x32_bf16 v[168:171], v[48:51], v[176:179], 0\n"
"ds_read_b64_tr_b16 v[16:17], v232 offset:32\n"
"buffer_store_dwordx4 v[248:251], v229, s[20:23], s53 offen offset:512 sc1\n"
"v_mfma_f32_16x16x32_bf16 v[168:171], v[52:55], v[180:183], v[168:171]\n"
"ds_read_b64_tr_b16 v[18:19], v232 offset:3616\n"
"v_mfma_f32_16x16x32_bf16 v[168:171], v[56:59], v[184:187], v[168:171]\n"
"ds_read_b64_tr_b16 v[20:21], v232 offset:8224\n"
"v_mfma_f32_16x16x32_bf16 v[168:171], v[60:63], v[188:191], v[168:171]\n"
"ds_read_b64_tr_b16 v[22:23], v232 offset:11808\n"
"ds_read_b64_tr_b16 v[24:25], v232 offset:16416\n"
"v_mfma_f32_16x16x32_bf16 v[172:175], v[60:63], v[204:207], 0\n"
"v_mfma_f32_16x16x32_bf16 v[172:175], v[56:59], v[200:203], v[172:175]\n"
"ds_read_b64_tr_b16 v[26:27], v232 offset:20000\n"
"v_mfma_f32_16x16x32_bf16 v[172:175], v[52:55], v[196:199], v[172:175]\n"
"ds_read_b64_tr_b16 v[28:29], v232 offset:24608\n"
"v_mfma_f32_16x16x32_bf16 v[172:175], v[48:51], v[192:195], v[172:175]\n"
"s_waitcnt lgkmcnt(7)\n"
"ds_read_b64_tr_b16 v[30:31], v232 offset:28192\n"
"v_mfma_f32_16x16x32_bf16 v[120:123], v[0:3], a[192:195], v[120:123]\n"
"ds_read_b64_tr_b16 v[32:33], v232 offset:64\n"
"v_readlane_b32 s24, v224, s48\n"
"v_mfma_f32_16x16x32_bf16 v[120:123], v[4:7], a[196:199], v[120:123]\n"
"ds_read_b64_tr_b16 v[34:35], v232 offset:3648\n"
"v_readlane_b32 s25, v225, s48\n"
"v_mfma_f32_16x16x32_bf16 v[120:123], v[8:11], a[200:203], v[120:123]\n"
"ds_read_b64_tr_b16 v[36:37], v232 offset:8256\n"
"v_readlane_b32 s26, v226, s48\n"
"v_mfma_f32_16x16x32_bf16 v[120:123], v[12:15], a[204:207], v[120:123]\n"
"s_add_u32 s48, s48, 1\n"
"ds_read_b64_tr_b16 v[38:39], v232 offset:11840\n"
"ds_read_b64_tr_b16 v[40:41], v232 offset:16448\n"
"v_mfma_f32_16x16x32_bf16 v[124:127], v[12:15], a[236:239], v[124:127]\n"
"v_cvt_pk_bf16_f32 v252, v88, v89\n"
"v_mfma_f32_16x16x32_bf16 v[124:127], v[8:11], a[232:235], v[124:127]\n"
"ds_read_b64_tr_b16 v[42:43], v232 offset:20032\n"
"v_mfma_f32_16x16x32_bf16 v[124:127], v[4:7], a[228:231], v[124:127]\n"
"ds_read_b64_tr_b16 v[44:45], v232 offset:24640\n"
"v_cvt_pk_bf16_f32 v253, v90, v91\n"
"v_mfma_f32_16x16x32_bf16 v[124:127], v[0:3], a[224:227], v[124:127]\n"
"s_waitcnt lgkmcnt(7)\n"
"ds_read_b64_tr_b16 v[46:47], v232 offset:28224\n"
"v_mfma_f32_16x16x32_bf16 v[128:131], v[16:19], a[192:195], v[128:131]\n"
"ds_read_b64_tr_b16 v[48:49], v232 offset:96\n"
"v_cvt_pk_bf16_f32 v254, v92, v93\n"
"v_mfma_f32_16x16x32_bf16 v[128:131], v[20:23], a[196:199], v[128:131]\n"
"ds_read_b64_tr_b16 v[50:51], v232 offset:3680\n"
"buffer_load_dwordx4 v208, s[24:27], s49 offen offset:0 lds\n"
"v_mfma_f32_16x16x32_bf16 v[128:131], v[24:27], a[200:203], v[128:131]\n"
"ds_read_b64_tr_b16 v[52:53], v232 offset:8288\n"
"v_cvt_pk_bf16_f32 v255, v94, v95\n"
"v_mfma_f32_16x16x32_bf16 v[128:131], v[28:31], a[204:207], v[128:131]\n"
"ds_read_b64_tr_b16 v[54:55], v232 offset:11872\n"
"ds_read_b64_tr_b16 v[56:57], v232 offset:16480\n"
"v_mfma_f32_16x16x32_bf16 v[132:135], v[28:31], a[236:239], v[132:135]\n"
"buffer_load_dwordx4 v209, s[24:27], s49 offen offset:1024 lds\n"
"v_mfma_f32_16x16x32_bf16 v[132:135], v[24:27], a[232:235], v[132:135]\n"
"ds_read_b64_tr_b16 v[58:59], v232 offset:20064\n"
"v_mfma_f32_16x16x32_bf16 v[132:135], v[20:23], a[228:231], v[132:135]\n"
"ds_read_b64_tr_b16 v[60:61], v232 offset:24672\n"
"v_mfma_f32_16x16x32_bf16 v[132:135], v[16:19], a[224:227], v[132:135]\n"
"s_waitcnt lgkmcnt(7)\n"
"ds_read_b64_tr_b16 v[62:63], v232 offset:28256\n"
"v_mfma_f32_16x16x32_bf16 v[136:139], v[32:35], a[192:195], v[136:139]\n"
"ds_read_b64_tr_b16 v[0:1], v232 offset:128\n"
"buffer_store_dwordx4 v[252:255], v229, s[20:23], s53 offen offset:768 sc1\n"
"v_mfma_f32_16x16x32_bf16 v[136:139], v[36:39], a[196:199], v[136:139]\n"
"ds_read_b64_tr_b16 v[2:3], v232 offset:3712\n"
"buffer_load_dwordx4 v210, s[24:27], s49 offen offset:2048 lds\n"
"v_mfma_f32_16x16x32_bf16 v[136:139], v[40:43], a[200:203], v[136:139]\n"
"ds_read_b64_tr_b16 v[4:5], v232 offset:8320\n"
"v_mfma_f32_16x16x32_bf16 v[136:139], v[44:47], a[204:207], v[136:139]\n"
"ds_read_b64_tr_b16 v[6:7], v232 offset:11904\n"
"ds_read_b64_tr_b16 v[8:9], v232 offset:16512\n"
"v_mfma_f32_16x16x32_bf16 v[140:143], v[44:47], a[236:239], v[140:143]\n"
"buffer_load_dwordx4 v211, s[24:27], s49 offen offset:3072 lds\n"
"v_mfma_f32_16x16x32_bf16 v[140:143], v[40:43], a[232:235], v[140:143]\n"
"s_add_u32 m0, m0, 4096\n"
"ds_read_b64_tr_b16 v[10:11], v232 offset:20096\n"
"v_mfma_f32_16x16x32_bf16 v[140:143], v[36:39], a[228:231], v[140:143]\n"
"ds_read_b64_tr_b16 v[12:13], v232 offset:24704\n"
"v_mfma_f32_16x16x32_bf16 v[140:143], v[32:35], a[224:227], v[140:143]\n"
"s_waitcnt lgkmcnt(7)\n"
"ds_read_b64_tr_b16 v[14:15], v232 offset:28288\n"
"v_mfma_f32_16x16x32_bf16 v[144:147], v[48:51], a[192:195], v[144:147]\n"
"ds_read_b64_tr_b16 v[16:17], v232 offset:160\n"
"v_mfma_f32_16x16x32_bf16 v[144:147], v[52:55], a[196:199], v[144:147]\n"
"ds_read_b64_tr_b16 v[18:19], v232 offset:3744\n"
"buffer_load_dwordx4 v212, s[24:27], s49 offen offset:0 lds\n"
"v_mfma_f32_16x16x32_bf16 v[144:147], v[56:59], a[200:203], v[144:147]\n"
"ds_read_b64_tr_b16 v[20:21], v232 offset:8352\n"
"v_mfma_f32_16x16x32_bf16 v[144:147], v[60:63], a[204:207], v[144:147]\n"
"ds_read_b64_tr_b16 v[22:23], v232 offset:11936\n"
"ds_read_b64_tr_b16 v[24:25], v232 offset:16544\n"
"v_mfma_f32_16x16x32_bf16 v[148:151], v[60:63], a[236:239], v[148:151]\n"
"buffer_load_dwordx4 v213, s[24:27], s49 offen offset:1024 lds\n"
"v_mfma_f32_16x16x32_bf16 v[148:151], v[56:59], a[232:235], v[148:151]\n"
"ds_read_b64_tr_b16 v[26:27], v232 offset:20128\n"
"v_cvt_pk_bf16_f32 v240, v96, v97\n"
"v_mfma_f32_16x16x32_bf16 v[148:151], v[52:55], a[228:231], v[148:151]\n"
"ds_read_b64_tr_b16 v[28:29], v232 offset:24736\n"
"v_cvt_pk_bf16_f32 v241, v98, v99\n"
"v_mfma_f32_16x16x32_bf16 v[148:151], v[48:51], a[224:227], v[148:151]\n"
"s_waitcnt lgkmcnt(7)\n"
"ds_read_b64_tr_b16 v[30:31], v232 offset:28320\n"
"v_mfma_f32_16x16x32_bf16 v[152:155], v[0:3], a[192:195], v[152:155]\n"
"ds_read_b64_tr_b16 v[32:33], v232 offset:192\n"
"ds_read_b64_tr_b16 v[34:35], v232 offset:3776\n"
"v_mfma_f32_16x16x32_bf16 v[152:155], v[4:7], a[196:199], v[152:155]\n"
"ds_read_b64_tr_b16 v[36:37], v232 offset:8384\n"
"ds_read_b64_tr_b16 v[38:39], v232 offset:11968\n"
"v_mfma_f32_16x16x32_bf16 v[152:155], v[8:11], a[200:203], v[152:155]\n"
"s_add_u32 s54, s50, s47\n"
"ds_read_b64_tr_b16 v[40:41], v232 offset:16576\n"
"ds_read_b64_tr_b16 v[42:43], v232 offset:20160\n"
"v_mfma_f32_16x16x32_bf16 v[152:155], v[12:15], a[204:207], v[152:155]\n"
"s_add_u32 s50, s50, 32768\n"
"ds_read_b64_tr_b16 v[44:45], v232 offset:24768\n"
"ds_read_b64_tr_b16 v[46:47], v232 offset:28352\n"
"v_mfma_f32_16x16x32_bf16 v[156:159], v[12:15], a[236:239], v[156:159]\n"
"s_cmp_lt_u32 s50, 163840\n"
"s_cselect_b32 s50, s50, 0\n"
"v_cvt_pk_bf16_f32 v242, v100, v101\n"
"v_mfma_f32_16x16x32_bf16 v[156:159], v[8:11], a[232:235], v[156:159]\n"
"buffer_load_dwordx4 v214, s[24:27], s49 offen offset:2048 lds\n"
"v_mfma_f32_16x16x32_bf16 v[156:159], v[4:7], a[228:231], v[156:159]\n"
"s_waitcnt lgkmcnt(0)\n"
"v_add_u32 v232, s50, v228\n"
"v_cvt_pk_bf16_f32 v243, v102, v103\n"
"v_mfma_f32_16x16x32_bf16 v[156:159], v[0:3], a[224:227], v[156:159]\n"
"s_waitcnt vmcnt(27)\n"
"s_barrier\n"
"v_mfma_f32_16x16x32_bf16 v[160:163], v[16:19], a[192:195], v[160:163]\n"
"ds_read_b64_tr_b16 v[48:49], v232 offset:0\n"
"buffer_load_dwordx4 v215, s[24:27], s49 offen offset:3072 lds\n"
"v_mfma_f32_16x16x32_bf16 v[160:163], v[20:23], a[196:199], v[160:163]\n"
"ds_read_b64_tr_b16 v[50:51], v232 offset:3584\n"
"buffer_store_dwordx4 v[240:243], v229, s[20:23], s53 offen offset:1024 sc1\n"
"v_mfma_f32_16x16x32_bf16 v[160:163], v[24:27], a[200:203], v[160:163]\n"
"s_mov_b32 m0, s54\n"
"ds_read_b64_tr_b16 v[52:53], v232 offset:8192\n"
"v_mfma_f32_16x16x32_bf16 v[160:163], v[28:31], a[204:207], v[160:163]\n"
"ds_read_b64_tr_b16 v[54:55], v232 offset:11776\n"
"ds_read_b64_tr_b16 v[56:57], v232 offset:16384\n"
"v_mfma_f32_16x16x32_bf16 v[164:167], v[28:31], a[236:239], v[164:167]\n"
"v_mfma_f32_16x16x32_bf16 v[164:167], v[24:27], a[232:235], v[164:167]\n"
"ds_read_b64_tr_b16 v[58:59], v232 offset:19968\n"
"v_mfma_f32_16x16x32_bf16 v[164:167], v[20:23], a[228:231], v[164:167]\n"
"ds_read_b64_tr_b16 v[60:61], v232 offset:24576\n"
"v_mfma_f32_16x16x32_bf16 v[164:167], v[16:19], a[224:227], v[164:167]\n"
"ds_read_b64_tr_b16 v[62:63], v232 offset:28160\n"
"v_mfma_f32_16x16x32_bf16 v[168:171], v[32:35], a[192:195], v[168:171]\n"
"ds_read_b64_tr_b16 v[0:1], v232 offset:32\n"
"v_mfma_f32_16x16x32_bf16 v[168:171], v[36:39], a[196:199], v[168:171]\n"
"ds_read_b64_tr_b16 v[2:3], v232 offset:3616\n"
"v_mfma_f32_16x16x32_bf16 v[168:171], v[40:43], a[200:203], v[168:171]\n"
"ds_read_b64_tr_b16 v[4:5], v232 offset:8224\n"
"v_mfma_f32_16x16x32_bf16 v[168:171], v[44:47], a[204:207], v[168:171]\n"
"ds_read_b64_tr_b16 v[6:7], v232 offset:11808\n"
"ds_read_b64_tr_b16 v[8:9], v232 offset:16416\n"
"v_mfma_f32_16x16x32_bf16 v[172:175], v[44:47], a[236:239], v[172:175]\n"
"v_mfma_f32_16x16x32_bf16 v[172:175], v[40:43], a[232:235], v[172:175]\n"
"ds_read_b64_tr_b16 v[10:11], v232 offset:20000\n"
"v_mfma_f32_16x16x32_bf16 v[172:175], v[36:39], a[228:231], v[172:175]\n"
"ds_read_b64_tr_b16 v[12:13], v232 offset:24608\n"
"v_mfma_f32_16x16x32_bf16 v[172:175], v[32:35], a[224:227], v[172:175]\n"
"s_waitcnt lgkmcnt(7)\n"
"ds_read_b64_tr_b16 v[14:15], v232 offset:28192\n"
"v_mfma_f32_16x16x32_bf16 v[120:123], v[48:51], a[208:211], v[120:123]\n"
"ds_read_b64_tr_b16 v[16:17], v232 offset:64\n"
"v_readlane_b32 s24, v224, s48\n"
"v_mfma_f32_16x16x32_bf16 v[120:123], v[52:55], a[212:215], v[120:123]\n"
"ds_read_b64_tr_b16 v[18:19], v232 offset:3648\n"
"v_readlane_b32 s25, v225, s48\n"
"v_mfma_f32_16x16x32_bf16 v[120:123], v[56:59], a[216:219], v[120:123]\n"
"ds_read_b64_tr_b16 v[20:21], v232 offset:8256\n"
"v_readlane_b32 s26, v226, s48\n"
"v_mfma_f32_16x16x32_bf16 v[120:123], v[60:63], a[220:223], v[120:123]\n"
"s_add_u32 s48, s48, 1\n"
"ds_read_b64_tr_b16 v[22:23], v232 offset:11840\n"
"ds_read_b64_tr_b16 v[24:25], v232 offset:16448\n"
"v_mfma_f32_16x16x32_bf16 v[124:127], v[60:63], a[252:255], v[124:127]\n"
"v_cvt_pk_bf16_f32 v244, v104, v105\n"
"v_mfma_f32_16x16x32_bf16 v[124:127], v[56:59], a[248:251], v[124:127]\n"
"ds_read_b64_tr_b16 v[26:27], v232 offset:20032\n"
"v_mfma_f32_16x16x32_bf16 v[124:127], v[52:55], a[244:247], v[124:127]\n"
"ds_read_b64_tr_b16 v[28:29], v232 offset:24640\n"
"v_cvt_pk_bf16_f32 v245, v106, v107\n"
"v_mfma_f32_16x16x32_bf16 v[124:127], v[48:51], a[240:243], v[124:127]\n"
"s_waitcnt lgkmcnt(7)\n"
"ds_read_b64_tr_b16 v[30:31], v232 offset:28224\n"
"v_mfma_f32_16x16x32_bf16 v[128:131], v[0:3], a[208:211], v[128:131]\n"
"ds_read_b64_tr_b16 v[32:33], v232 offset:96\n"
"v_cvt_pk_bf16_f32 v246, v108, v109\n"
"v_mfma_f32_16x16x32_bf16 v[128:131], v[4:7], a[212:215], v[128:131]\n"
"ds_read_b64_tr_b16 v[34:35], v232 offset:3680\n"
"buffer_load_dwordx4 v208, s[24:27], s49 offen offset:0 lds\n"
"v_mfma_f32_16x16x32_bf16 v[128:131], v[8:11], a[216:219], v[128:131]\n"
"ds_read_b64_tr_b16 v[36:37], v232 offset:8288\n"
"v_cvt_pk_bf16_f32 v247, v110, v111\n"
"v_mfma_f32_16x16x32_bf16 v[128:131], v[12:15], a[220:223], v[128:131]\n"
"ds_read_b64_tr_b16 v[38:39], v232 offset:11872\n"
"ds_read_b64_tr_b16 v[40:41], v232 offset:16480\n"
"v_mfma_f32_16x16x32_bf16 v[132:135], v[12:15], a[252:255], v[132:135]\n"
"buffer_load_dwordx4 v209, s[24:27], s49 offen offset:1024 lds\n"
"v_mfma_f32_16x16x32_bf16 v[132:135], v[8:11], a[248:251], v[132:135]\n"
"ds_read_b64_tr_b16 v[42:43], v232 offset:20064\n"
"v_mfma_f32_16x16x32_bf16 v[132:135], v[4:7], a[244:247], v[132:135]\n"
"ds_read_b64_tr_b16 v[44:45], v232 offset:24672\n"
"v_mfma_f32_16x16x32_bf16 v[132:135], v[0:3], a[240:243], v[132:135]\n"
"s_waitcnt lgkmcnt(7)\n"
"ds_read_b64_tr_b16 v[46:47], v232 offset:28256\n"
"v_mfma_f32_16x16x32_bf16 v[136:139], v[16:19], a[208:211], v[136:139]\n"
"ds_read_b64_tr_b16 v[48:49], v232 offset:128\n"
"buffer_store_dwordx4 v[244:247], v229, s[20:23], s53 offen offset:1280 sc1\n"
"v_mfma_f32_16x16x32_bf16 v[136:139], v[20:23], a[212:215], v[136:139]\n"
"ds_read_b64_tr_b16 v[50:51], v232 offset:3712\n"
"buffer_load_dwordx4 v210, s[24:27], s49 offen offset:2048 lds\n"
"v_mfma_f32_16x16x32_bf16 v[136:139], v[24:27], a[216:219], v[136:139]\n"
"ds_read_b64_tr_b16 v[52:53], v232 offset:8320\n"
"v_mfma_f32_16x16x32_bf16 v[136:139], v[28:31], a[220:223], v[136:139]\n"
"ds_read_b64_tr_b16 v[54:55], v232 offset:11904\n"
"ds_read_b64_tr_b16 v[56:57], v232 offset:16512\n"
"v_mfma_f32_16x16x32_bf16 v[140:143], v[28:31], a[252:255], v[140:143]\n"
"buffer_load_dwordx4 v211, s[24:27], s49 offen offset:3072 lds\n"
"v_mfma_f32_16x16x32_bf16 v[140:143], v[24:27], a[248:251], v[140:143]\n"
"s_add_u32 m0, m0, 4096\n"
"ds_read_b64_tr_b16 v[58:59], v232 offset:20096\n"
"v_mfma_f32_16x16x32_bf16 v[140:143], v[20:23], a[244:247], v[140:143]\n"
"ds_read_b64_tr_b16 v[60:61], v232 offset:24704\n"
"v_mfma_f32_16x16x32_bf16 v[140:143], v[16:19], a[240:243], v[140:143]\n"
"s_waitcnt lgkmcnt(7)\n"
"ds_read_b64_tr_b16 v[62:63], v232 offset:28288\n"
"v_mfma_f32_16x16x32_bf16 v[144:147], v[32:35], a[208:211], v[144:147]\n"
"ds_read_b64_tr_b16 v[0:1], v232 offset:160\n"
"v_mfma_f32_16x16x32_bf16 v[144:147], v[36:39], a[212:215], v[144:147]\n"
"ds_read_b64_tr_b16 v[2:3], v232 offset:3744\n"
"buffer_load_dwordx4 v212, s[24:27], s49 offen offset:0 lds\n"
"v_mfma_f32_16x16x32_bf16 v[144:147], v[40:43], a[216:219], v[144:147]\n"
"ds_read_b64_tr_b16 v[4:5], v232 offset:8352\n"
"v_mfma_f32_16x16x32_bf16 v[144:147], v[44:47], a[220:223], v[144:147]\n"
"ds_read_b64_tr_b16 v[6:7], v232 offset:11936\n"
"ds_read_b64_tr_b16 v[8:9], v232 offset:16544\n"
"v_mfma_f32_16x16x32_bf16 v[148:151], v[44:47], a[252:255], v[148:151]\n"
"buffer_load_dwordx4 v213, s[24:27], s49 offen offset:1024 lds\n"
"v_mfma_f32_16x16x32_bf16 v[148:151], v[40:43], a[248:251], v[148:151]\n"
"ds_read_b64_tr_b16 v[10:11], v232 offset:20128\n"
"v_cvt_pk_bf16_f32 v248, v112, v113\n"
"v_mfma_f32_16x16x32_bf16 v[148:151], v[36:39], a[244:247], v[148:151]\n"
"ds_read_b64_tr_b16 v[12:13], v232 offset:24736\n"
"v_cvt_pk_bf16_f32 v249, v114, v115\n"
"v_mfma_f32_16x16x32_bf16 v[148:151], v[32:35], a[240:243], v[148:151]\n"
"s_waitcnt lgkmcnt(7)\n"
"ds_read_b64_tr_b16 v[14:15], v232 offset:28320\n"
"v_mfma_f32_16x16x32_bf16 v[152:155], v[48:51], a[208:211], v[152:155]\n"
"ds_read_b64_tr_b16 v[16:17], v232 offset:192\n"
"ds_read_b64_tr_b16 v[18:19], v232 offset:3776\n"
"v_mfma_f32_16x16x32_bf16 v[152:155], v[52:55], a[212:215], v[152:155]\n"
"ds_read_b64_tr_b16 v[20:21], v232 offset:8384\n"
"ds_read_b64_tr_b16 v[22:23], v232 offset:11968\n"
"v_mfma_f32_16x16x32_bf16 v[152:155], v[56:59], a[216:219], v[152:155]\n"
"s_add_u32 s54, s50, s47\n"
"ds_read_b64_tr_b16 v[24:25], v232 offset:16576\n"
"ds_read_b64_tr_b16 v[26:27], v232 offset:20160\n"
"v_mfma_f32_16x16x32_bf16 v[152:155], v[60:63], a[220:223], v[152:155]\n"
"s_add_u32 s50, s50, 32768\n"
"ds_read_b64_tr_b16 v[28:29], v232 offset:24768\n"
"ds_read_b64_tr_b16 v[30:31], v232 offset:28352\n"
"v_mfma_f32_16x16x32_bf16 v[156:159], v[60:63], a[252:255], v[156:159]\n"
"s_cmp_lt_u32 s50, 163840\n"
"s_cselect_b32 s50, s50, 0\n"
"v_cvt_pk_bf16_f32 v250, v116, v117\n"
"v_mfma_f32_16x16x32_bf16 v[156:159], v[56:59], a[248:251], v[156:159]\n"
"buffer_load_dwordx4 v214, s[24:27], s49 offen offset:2048 lds\n"
"v_mfma_f32_16x16x32_bf16 v[156:159], v[52:55], a[244:247], v[156:159]\n"
"s_waitcnt lgkmcnt(0)\n"
"v_add_u32 v232, s50, v228\n"
"v_cvt_pk_bf16_f32 v251, v118, v119\n"
"v_mfma_f32_16x16x32_bf16 v[156:159], v[48:51], a[240:243], v[156:159]\n"
"s_waitcnt vmcnt(29)\n"
"s_barrier\n"
"v_mfma_f32_16x16x32_bf16 v[160:163], v[0:3], a[208:211], v[160:163]\n"
"ds_read_b64_tr_b16 v[32:33], v232 offset:0\n"
"buffer_load_dwordx4 v215, s[24:27], s49 offen offset:3072 lds\n"
"v_mfma_f32_16x16x32_bf16 v[160:163], v[4:7], a[212:215], v[160:163]\n"
"ds_read_b64_tr_b16 v[34:35], v232 offset:3584\n"
"buffer_store_dwordx4 v[248:251], v229, s[20:23], s53 offen offset:1536 sc1\n"
"v_mfma_f32_16x16x32_bf16 v[160:163], v[8:11], a[216:219], v[160:163]\n"
"s_mov_b32 m0, s54\n"
"ds_read_b64_tr_b16 v[36:37], v232 offset:8192\n"
"v_mfma_f32_16x16x32_bf16 v[160:163], v[12:15], a[220:223], v[160:163]\n"
"ds_read_b64_tr_b16 v[38:39], v232 offset:11776\n"
"ds_read_b64_tr_b16 v[40:41], v232 offset:16384\n"
"v_mfma_f32_16x16x32_bf16 v[164:167], v[12:15], a[252:255], v[164:167]\n"
"v_mfma_f32_16x16x32_bf16 v[164:167], v[8:11], a[248:251], v[164:167]\n"
"ds_read_b64_tr_b16 v[42:43], v232 offset:19968\n"
"v_mfma_f32_16x16x32_bf16 v[164:167], v[4:7], a[244:247], v[164:167]\n"
"ds_read_b64_tr_b16 v[44:45], v232 offset:24576\n"
"v_mfma_f32_16x16x32_bf16 v[164:167], v[0:3], a[240:243], v[164:167]\n"
"ds_read_b64_tr_b16 v[46:47], v232 offset:28160\n"
"v_mfma_f32_16x16x32_bf16 v[168:171], v[16:19], a[208:211], v[168:171]\n"
"ds_read_b64_tr_b16 v[48:49], v232 offset:32\n"
"v_mfma_f32_16x16x32_bf16 v[168:171], v[20:23], a[212:215], v[168:171]\n"
"ds_read_b64_tr_b16 v[50:51], v232 offset:3616\n"
"v_mfma_f32_16x16x32_bf16 v[168:171], v[24:27], a[216:219], v[168:171]\n"
"ds_read_b64_tr_b16 v[52:53], v232 offset:8224\n"
"v_mfma_f32_16x16x32_bf16 v[168:171], v[28:31], a[220:223], v[168:171]\n"
"ds_read_b64_tr_b16 v[54:55], v232 offset:11808\n"
"ds_read_b64_tr_b16 v[56:57], v232 offset:16416\n"
"v_mfma_f32_16x16x32_bf16 v[172:175], v[28:31], a[252:255], v[172:175]\n"
"v_mfma_f32_16x16x32_bf16 v[172:175], v[24:27], a[248:251], v[172:175]\n"
"ds_read_b64_tr_b16 v[58:59], v232 offset:20000\n"
"v_mfma_f32_16x16x32_bf16 v[172:175], v[20:23], a[244:247], v[172:175]\n"
"ds_read_b64_tr_b16 v[60:61], v232 offset:24608\n"
"v_mfma_f32_16x16x32_bf16 v[172:175], v[16:19], a[240:243], v[172:175]\n"
"s_waitcnt lgkmcnt(7)\n"
"ds_read_b64_tr_b16 v[62:63], v232 offset:28192\n"
"v_mfma_f32_16x16x32_bf16 v[64:67], v[32:35], v[176:179], 0\n"
"ds_read_b64_tr_b16 v[0:1], v232 offset:64\n"
"v_readlane_b32 s24, v224, s48\n"
"v_mfma_f32_16x16x32_bf16 v[64:67], v[36:39], v[180:183], v[64:67]\n"
"ds_read_b64_tr_b16 v[2:3], v232 offset:3648\n"
"v_readlane_b32 s25, v225, s48\n"
"v_mfma_f32_16x16x32_bf16 v[64:67], v[40:43], v[184:187], v[64:67]\n"
"ds_read_b64_tr_b16 v[4:5], v232 offset:8256\n"
"v_readlane_b32 s26, v226, s48\n"
"v_mfma_f32_16x16x32_bf16 v[64:67], v[44:47], v[188:191], v[64:67]\n"
"s_add_u32 s48, s48, 1\n"
"ds_read_b64_tr_b16 v[6:7], v232 offset:11840\n"
"ds_read_b64_tr_b16 v[8:9], v232 offset:16448\n"
"v_mfma_f32_16x16x32_bf16 v[68:71], v[44:47], v[204:207], 0\n"
"v_cvt_pk_bf16_f32 v240, v120, v121\n"
"v_mfma_f32_16x16x32_bf16 v[68:71], v[40:43], v[200:203], v[68:71]\n"
"ds_read_b64_tr_b16 v[10:11], v232 offset:20032\n"
"v_mfma_f32_16x16x32_bf16 v[68:71], v[36:39], v[196:199], v[68:71]\n"
"ds_read_b64_tr_b16 v[12:13], v232 offset:24640\n"
"v_cvt_pk_bf16_f32 v241, v122, v123\n"
"v_mfma_f32_16x16x32_bf16 v[68:71], v[32:35], v[192:195], v[68:71]\n"
"s_waitcnt lgkmcnt(7)\n"
"ds_read_b64_tr_b16 v[14:15], v232 offset:28224\n"
"v_mfma_f32_16x16x32_bf16 v[72:75], v[48:51], v[176:179], 0\n"
"ds_read_b64_tr_b16 v[16:17], v232 offset:96\n"
"v_cvt_pk_bf16_f32 v242, v124, v125\n"
"v_mfma_f32_16x16x32_bf16 v[72:75], v[52:55], v[180:183], v[72:75]\n"
"ds_read_b64_tr_b16 v[18:19], v232 offset:3680\n"
"buffer_load_dwordx4 v208, s[24:27], s49 offen offset:0 lds\n"
"v_mfma_f32_16x16x32_bf16 v[72:75], v[56:59], v[184:187], v[72:75]\n"
"ds_read_b64_tr_b16 v[20:21], v232 offset:8288\n"
"v_cvt_pk_bf16_f32 v243, v126, v127\n"
"v_mfma_f32_16x16x32_bf16 v[72:75], v[60:63], v[188:191], v[72:75]\n"
"ds_read_b64_tr_b16 v[22:23], v232 offset:11872\n"
"ds_read_b64_tr_b16 v[24:25], v232 offset:16480\n"
"v_mfma_f32_16x16x32_bf16 v[76:79], v[60:63], v[204:207], 0\n"
"buffer_load_dwordx4 v209, s[24:27], s49 offen offset:1024 lds\n"
"v_mfma_f32_16x16x32_bf16 v[76:79], v[56:59], v[200:203], v[76:79]\n"
"s_add_u32 s53, s46, 1792\n"
"ds_read_b64_tr_b16 v[26:27], v232 offset:20064\n"
"v_mfma_f32_16x16x32_bf16 v[76:79], v[52:55], v[196:199], v[76:79]\n"
"ds_read_b64_tr_b16 v[28:29], v232 offset:24672\n"
"v_mfma_f32_16x16x32_bf16 v[76:79], v[48:51], v[192:195], v[76:79]\n"
"s_waitcnt lgkmcnt(7)\n"
"ds_read_b64_tr_b16 v[30:31], v232 offset:28256\n"
"v_mfma_f32_16x16x32_bf16 v[80:83], v[0:3], v[176:179], 0\n"
"ds_read_b64_tr_b16 v[32:33], v232 offset:128\n"
"v_mfma_f32_16x16x32_bf16 v[80:83], v[4:7], v[180:183], v[80:83]\n"
"ds_read_b64_tr_b16 v[34:35], v232 offset:3712\n"
"buffer_load_dwordx4 v210, s[24:27], s49 offen offset:2048 lds\n"
"v_mfma_f32_16x16x32_bf16 v[80:83], v[8:11], v[184:187], v[80:83]\n"
"ds_read_b64_tr_b16 v[36:37], v232 offset:8320\n"
"buffer_store_dwordx4 v[240:243], v229, s[20:23], s53 offen offset:0 sc1\n"
"v_mfma_f32_16x16x32_bf16 v[80:83], v[12:15], v[188:191], v[80:83]\n"
"ds_read_b64_tr_b16 v[38:39], v232 offset:11904\n"
"ds_read_b64_tr_b16 v[40:41], v232 offset:16512\n"
"v_mfma_f32_16x16x32_bf16 v[84:87], v[12:15], v[204:207], 0\n"
"buffer_load_dwordx4 v211, s[24:27], s49 offen offset:3072 lds\n"
"v_mfma_f32_16x16x32_bf16 v[84:87], v[8:11], v[200:203], v[84:87]\n"
"s_add_u32 m0, m0, 4096\n"
"ds_read_b64_tr_b16 v[42:43], v232 offset:20096\n"
"v_cvt_pk_bf16_f32 v244, v128, v129\n"
"v_mfma_f32_16x16x32_bf16 v[84:87], v[4:7], v[196:199], v[84:87]\n"
"ds_read_b64_tr_b16 v[44:45], v232 offset:24704\n"
"v_cvt_pk_bf16_f32 v245, v130, v131\n"
"v_mfma_f32_16x16x32_bf16 v[84:87], v[0:3], v[192:195], v[84:87]\n"
"s_waitcnt lgkmcnt(7)\n"
"ds_read_b64_tr_b16 v[46:47], v232 offset:28288\n"
"v_mfma_f32_16x16x32_bf16 v[88:91], v[16:19], v[176:179], 0\n"
"ds_read_b64_tr_b16 v[48:49], v232 offset:160\n"
"v_cvt_pk_bf16_f32 v246, v132, v133\n"
"v_mfma_f32_16x16x32_bf16 v[88:91], v[20:23], v[180:183], v[88:91]\n"
"ds_read_b64_tr_b16 v[50:51], v232 offset:3744\n"
"buffer_load_dwordx4 v212, s[24:27], s49 offen offset:0 lds\n"
"v_mfma_f32_16x16x32_bf16 v[88:91], v[24:27], v[184:187], v[88:91]\n"
"ds_read_b64_tr_b16 v[52:53], v232 offset:8352\n"
"v_cvt_pk_bf16_f32 v247, v134, v135\n"
"v_mfma_f32_16x16x32_bf16 v[88:91], v[28:31], v[188:191], v[88:91]\n"
"ds_read_b64_tr_b16 v[54:55], v232 offset:11936\n"
"ds_read_b64_tr_b16 v[56:57], v232 offset:16544\n"
"v_mfma_f32_16x16x32_bf16 v[92:95], v[28:31], v[204:207], 0\n"
"buffer_load_dwordx4 v213, s[24:27], s49 offen offset:1024 lds\n"
"v_mfma_f32_16x16x32_bf16 v[92:95], v[24:27], v[200:203], v[92:95]\n"
"ds_read_b64_tr_b16 v[58:59], v232 offset:20128\n"
"v_mfma_f32_16x16x32_bf16 v[92:95], v[20:23], v[196:199], v[92:95]\n"
"ds_read_b64_tr_b16 v[60:61], v232 offset:24736\n"
"v_mfma_f32_16x16x32_bf16 v[92:95], v[16:19], v[192:195], v[92:95]\n"
"s_waitcnt lgkmcnt(7)\n"
"ds_read_b64_tr_b16 v[62:63], v232 offset:28320\n"
"v_mfma_f32_16x16x32_bf16 v[96:99], v[32:35], v[176:179], 0\n"
"ds_read_b64_tr_b16 v[0:1], v232 offset:192\n"
"ds_read_b64_tr_b16 v[2:3], v232 offset:3776\n"
"v_mfma_f32_16x16x32_bf16 v[96:99], v[36:39], v[180:183], v[96:99]\n"
"ds_read_b64_tr_b16 v[4:5], v232 offset:8384\n"
"ds_read_b64_tr_b16 v[6:7], v232 offset:11968\n"
"v_mfma_f32_16x16x32_bf16 v[96:99], v[40:43], v[184:187], v[96:99]\n"
"s_add_u32 s54, s50, s47\n"
"ds_read_b64_tr_b16 v[8:9], v232 offset:16576\n"
"ds_read_b64_tr_b16 v[10:11], v232 offset:20160\n"
"v_mfma_f32_16x16x32_bf16 v[96:99], v[44:47], v[188:191], v[96:99]\n"
"s_add_u32 s50, s50, 32768\n"
"ds_read_b64_tr_b16 v[12:13], v232 offset:24768\n"
"ds_read_b64_tr_b16 v[14:15], v232 offset:28352\n"
"v_mfma_f32_16x16x32_bf16 v[100:103], v[44:47], v[204:207], 0\n"
"s_cmp_lt_u32 s50, 163840\n"
"s_cselect_b32 s50, s50, 0\n"
"v_cvt_pk_bf16_f32 v248, v136, v137\n"
"v_mfma_f32_16x16x32_bf16 v[100:103], v[40:43], v[200:203], v[100:103]\n"
"buffer_store_dwordx4 v[244:247], v229, s[20:23], s53 offen offset:256 sc1\n"
"v_mfma_f32_16x16x32_bf16 v[100:103], v[36:39], v[196:199], v[100:103]\n"
"s_waitcnt lgkmcnt(0)\n"
"v_add_u32 v232, s50, v228\n"
"v_cvt_pk_bf16_f32 v249, v138, v139\n"
"v_mfma_f32_16x16x32_bf16 v[100:103], v[32:35], v[192:195], v[100:103]\n"
"s_waitcnt vmcnt(29)\n"
"s_barrier\n"
"v_mfma_f32_16x16x32_bf16 v[104:107], v[48:51], v[176:179], 0\n"
"ds_read_b64_tr_b16 v[16:17], v232 offset:0\n"
"buffer_load_dwordx4 v214, s[24:27], s49 offen offset:2048 lds\n"
"v_mfma_f32_16x16x32_bf16 v[104:107], v[52:55], v[180:183], v[104:107]\n"
"ds_read_b64_tr_b16 v[18:19], v232 offset:3584\n"
"buffer_load_dwordx4 v215, s[24:27], s49 offen offset:3072 lds\n"
"v_mfma_f32_16x16x32_bf16 v[104:107], v[56:59], v[184:187], v[104:107]\n"
"s_mov_b32 m0, s54\n"
"ds_read_b64_tr_b16 v[20:21], v232 offset:8192\n"
"v_cvt_pk_bf16_f32 v250, v140, v141\n"
"v_mfma_f32_16x16x32_bf16 v[104:107], v[60:63], v[188:191], v[104:107]\n"
"ds_read_b64_tr_b16 v[22:23], v232 offset:11776\n"
"ds_read_b64_tr_b16 v[24:25], v232 offset:16384\n"
"v_mfma_f32_16x16x32_bf16 v[108:111], v[60:63], v[204:207], 0\n"
"v_cvt_pk_bf16_f32 v251, v142, v143\n"
"v_mfma_f32_16x16x32_bf16 v[108:111], v[56:59], v[200:203], v[108:111]\n"
"ds_read_b64_tr_b16 v[26:27], v232 offset:19968\n"
"v_mfma_f32_16x16x32_bf16 v[108:111], v[52:55], v[196:199], v[108:111]\n"
"ds_read_b64_tr_b16 v[28:29], v232 offset:24576\n"
"v_mfma_f32_16x16x32_bf16 v[108:111], v[48:51], v[192:195], v[108:111]\n"
"ds_read_b64_tr_b16 v[30:31], v232 offset:28160\n"
"v_mfma_f32_16x16x32_bf16 v[112:115], v[0:3], v[176:179], 0\n"
"ds_read_b64_tr_b16 v[32:33], v232 offset:32\n"
"buffer_store_dwordx4 v[248:251], v229, s[20:23], s53 offen offset:512 sc1\n"
"v_mfma_f32_16x16x32_bf16 v[112:115], v[4:7], v[180:183], v[112:115]\n"
"ds_read_b64_tr_b16 v[34:35], v232 offset:3616\n"
"v_mfma_f32_16x16x32_bf16 v[112:115], v[8:11], v[184:187], v[112:115]\n"
"ds_read_b64_tr_b16 v[36:37], v232 offset:8224\n"
"v_mfma_f32_16x16x32_bf16 v[112:115], v[12:15], v[188:191], v[112:115]\n"
"ds_read_b64_tr_b16 v[38:39], v232 offset:11808\n"
"ds_read_b64_tr_b16 v[40:41], v232 offset:16416\n"
"v_mfma_f32_16x16x32_bf16 v[116:119], v[12:15], v[204:207], 0\n"
"v_mfma_f32_16x16x32_bf16 v[116:119], v[8:11], v[200:203], v[116:119]\n"
"ds_read_b64_tr_b16 v[42:43], v232 offset:20000\n"
"v_mfma_f32_16x16x32_bf16 v[116:119], v[4:7], v[196:199], v[116:119]\n"
"ds_read_b64_tr_b16 v[44:45], v232 offset:24608\n"
"v_mfma_f32_16x16x32_bf16 v[116:119], v[0:3], v[192:195], v[116:119]\n"
"s_waitcnt lgkmcnt(7)\n"
"ds_read_b64_tr_b16 v[46:47], v232 offset:28192\n"
"v_mfma_f32_16x16x32_bf16 v[64:67], v[16:19], a[192:195], v[64:67]\n"
"ds_read_b64_tr_b16 v[48:49], v232 offset:64\n"
"v_readlane_b32 s24, v224, s48\n"
"v_mfma_f32_16x16x32_bf16 v[64:67], v[20:23], a[196:199], v[64:67]\n"
"ds_read_b64_tr_b16 v[50:51], v232 offset:3648\n"
"v_readlane_b32 s25, v225, s48\n"
"v_mfma_f32_16x16x32_bf16 v[64:67], v[24:27], a[200:203], v[64:67]\n"
"ds_read_b64_tr_b16 v[52:53], v232 offset:8256\n"
"v_readlane_b32 s26, v226, s48\n"
"v_mfma_f32_16x16x32_bf16 v[64:67], v[28:31], a[204:207], v[64:67]\n"
"s_add_u32 s48, s48, 1\n"
"ds_read_b64_tr_b16 v[54:55], v232 offset:11840\n"
"ds_read_b64_tr_b16 v[56:57], v232 offset:16448\n"
"v_mfma_f32_16x16x32_bf16 v[68:71], v[28:31], a[236:239], v[68:71]\n"
"v_cvt_pk_bf16_f32 v252, v144, v145\n"
"v_mfma_f32_16x16x32_bf16 v[68:71], v[24:27], a[232:235], v[68:71]\n"
"ds_read_b64_tr_b16 v[58:59], v232 offset:20032\n"
"v_mfma_f32_16x16x32_bf16 v[68:71], v[20:23], a[228:231], v[68:71]\n"
"ds_read_b64_tr_b16 v[60:61], v232 offset:24640\n"
"v_cvt_pk_bf16_f32 v253, v146, v147\n"
"v_mfma_f32_16x16x32_bf16 v[68:71], v[16:19], a[224:227], v[68:71]\n"
"s_waitcnt lgkmcnt(7)\n"
"ds_read_b64_tr_b16 v[62:63], v232 offset:28224\n"
"v_mfma_f32_16x16x32_bf16 v[72:75], v[32:35], a[192:195], v[72:75]\n"
"ds_read_b64_tr_b16 v[0:1], v232 offset:96\n"
"v_cvt_pk_bf16_f32 v254, v148, v149\n"
"v_mfma_f32_16x16x32_bf16 v[72:75], v[36:39], a[196:199], v[72:75]\n"
"ds_read_b64_tr_b16 v[2:3], v232 offset:3680\n"
"buffer_load_dwordx4 v208, s[24:27], s49 offen offset:0 lds\n"
"v_mfma_f32_16x16x32_bf16 v[72:75], v[40:43], a[200:203], v[72:75]\n"
"ds_read_b64_tr_b16 v[4:5], v232 offset:8288\n"
"v_cvt_pk_bf16_f32 v255, v150, v151\n"
"v_mfma_f32_16x16x32_bf16 v[72:75], v[44:47], a[204:207], v[72:75]\n"
"ds_read_b64_tr_b16 v[6:7], v232 offset:11872\n"
"ds_read_b64_tr_b16 v[8:9], v232 offset:16480\n"
"v_mfma_f32_16x16x32_bf16 v[76:79], v[44:47], a[236:239], v[76:79]\n"
"buffer_load_dwordx4 v209, s[24:27], s49 offen offset:1024 lds\n"
"v_mfma_f32_16x16x32_bf16 v[76:79], v[40:43], a[232:235], v[76:79]\n"
"ds_read_b64_tr_b16 v[10:11], v232 offset:20064\n"
"v_mfma_f32_16x16x32_bf16 v[76:79], v[36:39], a[228:231], v[76:79]\n"
"ds_read_b64_tr_b16 v[12:13], v232 offset:24672\n"
"v_mfma_f32_16x16x32_bf16 v[76:79], v[32:35], a[224:227], v[76:79]\n"
"s_waitcnt lgkmcnt(7)\n"
"ds_read_b64_tr_b16 v[14:15], v232 offset:28256\n"
"v_mfma_f32_16x16x32_bf16 v[80:83], v[48:51], a[192:195], v[80:83]\n"
"ds_read_b64_tr_b16 v[16:17], v232 offset:128\n"
"buffer_store_dwordx4 v[252:255], v229, s[20:23], s53 offen offset:768 sc1\n"
"v_mfma_f32_16x16x32_bf16 v[80:83], v[52:55], a[196:199], v[80:83]\n"
"ds_read_b64_tr_b16 v[18:19], v232 offset:3712\n"
"buffer_load_dwordx4 v210, s[24:27], s49 offen offset:2048 lds\n"
"v_mfma_f32_16x16x32_bf16 v[80:83], v[56:59], a[200:203], v[80:83]\n"
"ds_read_b64_tr_b16 v[20:21], v232 offset:8320\n"
"v_mfma_f32_16x16x32_bf16 v[80:83], v[60:63], a[204:207], v[80:83]\n"
"ds_read_b64_tr_b16 v[22:23], v232 offset:11904\n"
"ds_read_b64_tr_b16 v[24:25], v232 offset:16512\n"
"v_mfma_f32_16x16x32_bf16 v[84:87], v[60:63], a[236:239], v[84:87]\n"
"buffer_load_dwordx4 v211, s[24:27], s49 offen offset:3072 lds\n"
"v_mfma_f32_16x16x32_bf16 v[84:87], v[56:59], a[232:235], v[84:87]\n"
"s_add_u32 m0, m0, 4096\n"
"ds_read_b64_tr_b16 v[26:27], v232 offset:20096\n"
"v_mfma_f32_16x16x32_bf16 v[84:87], v[52:55], a[228:231], v[84:87]\n"
"ds_read_b64_tr_b16 v[28:29], v232 offset:24704\n"
"v_mfma_f32_16x16x32_bf16 v[84:87], v[48:51], a[224:227], v[84:87]\n"
"s_waitcnt lgkmcnt(7)\n"
"ds_read_b64_tr_b16 v[30:31], v232 offset:28288\n"
"v_mfma_f32_16x16x32_bf16 v[88:91], v[0:3], a[192:195], v[88:91]\n"
"ds_read_b64_tr_b16 v[32:33], v232 offset:160\n"
"v_mfma_f32_16x16x32_bf16 v[88:91], v[4:7], a[196:199], v[88:91]\n"
"ds_read_b64_tr_b16 v[34:35], v232 offset:3744\n"
"buffer_load_dwordx4 v212, s[24:27], s49 offen offset:0 lds\n"
"v_mfma_f32_16x16x32_bf16 v[88:91], v[8:11], a[200:203], v[88:91]\n"
"ds_read_b64_tr_b16 v[36:37], v232 offset:8352\n"
"v_mfma_f32_16x16x32_bf16 v[88:91], v[12:15], a[204:207], v[88:91]\n"
"ds_read_b64_tr_b16 v[38:39], v232 offset:11936\n"
"ds_read_b64_tr_b16 v[40:41], v232 offset:16544\n"
"v_mfma_f32_16x16x32_bf16 v[92:95], v[12:15], a[236:239], v[92:95]\n"
"buffer_load_dwordx4 v213, s[24:27], s49 offen offset:1024 lds\n"
"v_mfma_f32_16x16x32_bf16 v[92:95], v[8:11], a[232:235], v[92:95]\n"
"ds_read_b64_tr_b16 v[42:43], v232 offset:20128\n"
"v_cvt_pk_bf16_f32 v240, v152, v153\n"
"v_mfma_f32_16x16x32_bf16 v[92:95], v[4:7], a[228:231], v[92:95]\n"
"ds_read_b64_tr_b16 v[44:45], v232 offset:24736\n"
"v_cvt_pk_bf16_f32 v241, v154, v155\n"
"v_mfma_f32_16x16x32_bf16 v[92:95], v[0:3], a[224:227], v[92:95]\n"
"s_waitcnt lgkmcnt(7)\n"
"ds_read_b64_tr_b16 v[46:47], v232 offset:28320\n"
"v_mfma_f32_16x16x32_bf16 v[96:99], v[16:19], a[192:195], v[96:99]\n"
"ds_read_b64_tr_b16 v[48:49], v232 offset:192\n"
"ds_read_b64_tr_b16 v[50:51], v232 offset:3776\n"
"v_mfma_f32_16x16x32_bf16 v[96:99], v[20:23], a[196:199], v[96:99]\n"
"ds_read_b64_tr_b16 v[52:53], v232 offset:8384\n"
"ds_read_b64_tr_b16 v[54:55], v232 offset:11968\n"
"v_mfma_f32_16x16x32_bf16 v[96:99], v[24:27], a[200:203], v[96:99]\n"
"s_add_u32 s54, s50, s47\n"
"ds_read_b64_tr_b16 v[56:57], v232 offset:16576\n"
"ds_read_b64_tr_b16 v[58:59], v232 offset:20160\n"
"v_mfma_f32_16x16x32_bf16 v[96:99], v[28:31], a[204:207], v[96:99]\n"
"s_add_u32 s50, s50, 32768\n"
"ds_read_b64_tr_b16 v[60:61], v232 offset:24768\n"
"ds_read_b64_tr_b16 v[62:63], v232 offset:28352\n"
"v_mfma_f32_16x16x32_bf16 v[100:103], v[28:31], a[236:239], v[100:103]\n"
"s_cmp_lt_u32 s50, 163840\n"
"s_cselect_b32 s50, s50, 0\n"
"v_cvt_pk_bf16_f32 v242, v156, v157\n"
"v_mfma_f32_16x16x32_bf16 v[100:103], v[24:27], a[232:235], v[100:103]\n"
"buffer_load_dwordx4 v214, s[24:27], s49 offen offset:2048 lds\n"
"v_mfma_f32_16x16x32_bf16 v[100:103], v[20:23], a[228:231], v[100:103]\n"
"s_waitcnt lgkmcnt(0)\n"
"v_add_u32 v232, s50, v228\n"
"v_cvt_pk_bf16_f32 v243, v158, v159\n"
"v_mfma_f32_16x16x32_bf16 v[100:103], v[16:19], a[224:227], v[100:103]\n"
"s_waitcnt vmcnt(19)\n"
"s_barrier\n"
"v_mfma_f32_16x16x32_bf16 v[104:107], v[32:35], a[192:195], v[104:107]\n"
"ds_read_b64_tr_b16 v[0:1], v232 offset:0\n"
"buffer_load_dwordx4 v215, s[24:27], s49 offen offset:3072 lds\n"
"v_mfma_f32_16x16x32_bf16 v[104:107], v[36:39], a[196:199], v[104:107]\n"
"ds_read_b64_tr_b16 v[2:3], v232 offset:3584\n"
"buffer_store_dwordx4 v[240:243], v229, s[20:23], s53 offen offset:1024 sc1\n"
"v_mfma_f32_16x16x32_bf16 v[104:107], v[40:43], a[200:203], v[104:107]\n"
"s_mov_b32 m0, s54\n"
"ds_read_b64_tr_b16 v[4:5], v232 offset:8192\n"
"v_mfma_f32_16x16x32_bf16 v[104:107], v[44:47], a[204:207], v[104:107]\n"
"ds_read_b64_tr_b16 v[6:7], v232 offset:11776\n"
"ds_read_b64_tr_b16 v[8:9], v232 offset:16384\n"
"v_mfma_f32_16x16x32_bf16 v[108:111], v[44:47], a[236:239], v[108:111]\n"
"v_mfma_f32_16x16x32_bf16 v[108:111], v[40:43], a[232:235], v[108:111]\n"
"ds_read_b64_tr_b16 v[10:11], v232 offset:19968\n"
"v_mfma_f32_16x16x32_bf16 v[108:111], v[36:39], a[228:231], v[108:111]\n"
"ds_read_b64_tr_b16 v[12:13], v232 offset:24576\n"
"v_mfma_f32_16x16x32_bf16 v[108:111], v[32:35], a[224:227], v[108:111]\n"
"ds_read_b64_tr_b16 v[14:15], v232 offset:28160\n"
"v_mfma_f32_16x16x32_bf16 v[112:115], v[48:51], a[192:195], v[112:115]\n"
"ds_read_b64_tr_b16 v[16:17], v232 offset:32\n"
"v_mfma_f32_16x16x32_bf16 v[112:115], v[52:55], a[196:199], v[112:115]\n"
"ds_read_b64_tr_b16 v[18:19], v232 offset:3616\n"
"v_mfma_f32_16x16x32_bf16 v[112:115], v[56:59], a[200:203], v[112:115]\n"
"ds_read_b64_tr_b16 v[20:21], v232 offset:8224\n"
"v_mfma_f32_16x16x32_bf16 v[112:115], v[60:63], a[204:207], v[112:115]\n"
"ds_read_b64_tr_b16 v[22:23], v232 offset:11808\n"
"ds_read_b64_tr_b16 v[24:25], v232 offset:16416\n"
"v_mfma_f32_16x16x32_bf16 v[116:119], v[60:63], a[236:239], v[116:119]\n"
"v_mfma_f32_16x16x32_bf16 v[116:119], v[56:59], a[232:235], v[116:119]\n"
"ds_read_b64_tr_b16 v[26:27], v232 offset:20000\n"
"v_mfma_f32_16x16x32_bf16 v[116:119], v[52:55], a[228:231], v[116:119]\n"
"ds_read_b64_tr_b16 v[28:29], v232 offset:24608\n"
"v_mfma_f32_16x16x32_bf16 v[116:119], v[48:51], a[224:227], v[116:119]\n"
"s_waitcnt lgkmcnt(7)\n"
"ds_read_b64_tr_b16 v[30:31], v232 offset:28192\n"
"v_mov_b32 v233, 1\n"
"s_mov_b64 exec, 1\n"
"buffer_store_dword v233, v231, s[20:23], s9 offen offset:0 sc1\n"
"s_mov_b64 exec, -1\n"
"s_nop 3\n"
"v_mfma_f32_16x16x32_bf16 v[64:67], v[0:3], a[208:211], v[64:67]\n"
"ds_read_b64_tr_b16 v[32:33], v232 offset:64\n"
"v_mfma_f32_16x16x32_bf16 v[64:67], v[4:7], a[212:215], v[64:67]\n"
"ds_read_b64_tr_b16 v[34:35], v232 offset:3648\n"
"v_mfma_f32_16x16x32_bf16 v[64:67], v[8:11], a[216:219], v[64:67]\n"
"ds_read_b64_tr_b16 v[36:37], v232 offset:8256\n"
"v_bfe_u32 v233, v227, 6, 2\n"
"v_mfma_f32_16x16x32_bf16 v[64:67], v[12:15], a[220:223], v[64:67]\n"
"ds_read_b64_tr_b16 v[38:39], v232 offset:11840\n"
"ds_read_b64_tr_b16 v[40:41], v232 offset:16448\n"
"v_mfma_f32_16x16x32_bf16 v[68:71], v[12:15], a[252:255], v[68:71]\n"
"v_mul_u32_u24 v220, 896, v233\n"
"v_cvt_pk_bf16_f32 v244, v160, v161\n"
"v_mfma_f32_16x16x32_bf16 v[68:71], v[8:11], a[248:251], v[68:71]\n"
"ds_read_b64_tr_b16 v[42:43], v232 offset:20032\n"
"v_lshrrev_b32 v233, 8, v227\n"
"v_mfma_f32_16x16x32_bf16 v[68:71], v[4:7], a[244:247], v[68:71]\n"
"ds_read_b64_tr_b16 v[44:45], v232 offset:24640\n"
"v_cvt_pk_bf16_f32 v245, v162, v163\n"
"v_mfma_f32_16x16x32_bf16 v[68:71], v[0:3], a[240:243], v[68:71]\n"
"s_waitcnt lgkmcnt(7)\n"
"ds_read_b64_tr_b16 v[46:47], v232 offset:28224\n"
"v_lshlrev_b32 v226, 5, v233\n"
"v_mfma_f32_16x16x32_bf16 v[72:75], v[16:19], a[208:211], v[72:75]\n"
"ds_read_b64_tr_b16 v[48:49], v232 offset:96\n"
"v_bfe_u32 v234, v227, 4, 2\n"
"v_mfma_f32_16x16x32_bf16 v[72:75], v[20:23], a[212:215], v[72:75]\n"
"ds_read_b64_tr_b16 v[50:51], v232 offset:3680\n"
"v_cvt_pk_bf16_f32 v246, v164, v165\n"
"v_mfma_f32_16x16x32_bf16 v[72:75], v[24:27], a[216:219], v[72:75]\n"
"ds_read_b64_tr_b16 v[52:53], v232 offset:8288\n"
"v_lshl_add_u32 v226, v234, 3, v226\n"
"v_mfma_f32_16x16x32_bf16 v[72:75], v[28:31], a[220:223], v[72:75]\n"
"ds_read_b64_tr_b16 v[54:55], v232 offset:11872\n"
"ds_read_b64_tr_b16 v[56:57], v232 offset:16480\n"
"v_mfma_f32_16x16x32_bf16 v[76:79], v[28:31], a[252:255], v[76:79]\n"
"v_cvt_pk_bf16_f32 v247, v166, v167\n"
"v_add_u32 v220, v220, v226\n"
"v_mfma_f32_16x16x32_bf16 v[76:79], v[24:27], a[248:251], v[76:79]\n"
"ds_read_b64_tr_b16 v[58:59], v232 offset:20064\n"
"v_cmp_eq_u32 vcc, 3, v233\n"
"v_mfma_f32_16x16x32_bf16 v[76:79], v[20:23], a[244:247], v[76:79]\n"
"ds_read_b64_tr_b16 v[60:61], v232 offset:24672\n"
"v_mov_b32 v234, 0x40000000\n"
"v_mfma_f32_16x16x32_bf16 v[76:79], v[16:19], a[240:243], v[76:79]\n"
"s_waitcnt lgkmcnt(7)\n"
"ds_read_b64_tr_b16 v[62:63], v232 offset:28256\n"
"v_mfma_f32_16x16x32_bf16 v[80:83], v[32:35], a[208:211], v[80:83]\n"
"ds_read_b64_tr_b16 v[0:1], v232 offset:128\n"
"buffer_store_dwordx4 v[244:247], v229, s[20:23], s53 offen offset:1280 sc1\n"
"v_mfma_f32_16x16x32_bf16 v[80:83], v[36:39], a[212:215], v[80:83]\n"
"ds_read_b64_tr_b16 v[2:3], v232 offset:3712\n"
"v_cndmask_b32 v234, 0, v234, vcc\n"
"v_mfma_f32_16x16x32_bf16 v[80:83], v[40:43], a[216:219], v[80:83]\n"
"ds_read_b64_tr_b16 v[4:5], v232 offset:8320\n"
"v_add_u32 v222, v234, v227\n"
"v_mfma_f32_16x16x32_bf16 v[80:83], v[44:47], a[220:223], v[80:83]\n"
"ds_read_b64_tr_b16 v[6:7], v232 offset:11904\n"
"ds_read_b64_tr_b16 v[8:9], v232 offset:16512\n"
"v_mfma_f32_16x16x32_bf16 v[84:87], v[44:47], a[252:255], v[84:87]\n"
"v_add_u32 v221, v234, v220\n"
"v_add_u32 v221, 128, v221\n"
"v_mfma_f32_16x16x32_bf16 v[84:87], v[40:43], a[248:251], v[84:87]\n"
"ds_read_b64_tr_b16 v[10:11], v232 offset:20096\n"
"v_mfma_f32_16x16x32_bf16 v[84:87], v[36:39], a[244:247], v[84:87]\n"
"ds_read_b64_tr_b16 v[12:13], v232 offset:24704\n"
"v_bfe_u32 v233, v227, 4, 4\n"
"v_mfma_f32_16x16x32_bf16 v[84:87], v[32:35], a[240:243], v[84:87]\n"
"s_waitcnt lgkmcnt(7)\n"
"ds_read_b64_tr_b16 v[14:15], v232 offset:28288\n"
"v_lshlrev_b32 v224, 11, v233\n"
"v_mfma_f32_16x16x32_bf16 v[88:91], v[48:51], a[208:211], v[88:91]\n"
"ds_read_b64_tr_b16 v[16:17], v232 offset:160\n"
"v_mfma_f32_16x16x32_bf16 v[88:91], v[52:55], a[212:215], v[88:91]\n"
"ds_read_b64_tr_b16 v[18:19], v232 offset:3744\n"
"v_cmp_eq_u32 vcc, s7, v233\n"
"v_mfma_f32_16x16x32_bf16 v[88:91], v[56:59], a[216:219], v[88:91]\n"
"ds_read_b64_tr_b16 v[20:21], v232 offset:8352\n"
"v_cndmask_b32 v225, 0, 3, vcc\n"
"v_mfma_f32_16x16x32_bf16 v[88:91], v[60:63], a[220:223], v[88:91]\n"
"ds_read_b64_tr_b16 v[22:23], v232 offset:11936\n"
"ds_read_b64_tr_b16 v[24:25], v232 offset:16544\n"
"v_mfma_f32_16x16x32_bf16 v[92:95], v[60:63], a[252:255], v[92:95]\n"
"buffer_load_dwordx2 a[128:129], v220, s[60:63], s4 offen offset:0\n"
"v_mfma_f32_16x16x32_bf16 v[92:95], v[56:59], a[248:251], v[92:95]\n"
"ds_read_b64_tr_b16 v[26:27], v232 offset:20128\n"
"v_cvt_pk_bf16_f32 v248, v168, v169\n"
"v_mfma_f32_16x16x32_bf16 v[92:95], v[52:55], a[244:247], v[92:95]\n"
"ds_read_b64_tr_b16 v[28:29], v232 offset:24736\n"
"v_cvt_pk_bf16_f32 v249, v170, v171\n"
"v_mfma_f32_16x16x32_bf16 v[92:95], v[48:51], a[240:243], v[92:95]\n"
"s_waitcnt lgkmcnt(7)\n"
"ds_read_b64_tr_b16 v[30:31], v232 offset:28320\n"
"v_mfma_f32_16x16x32_bf16 v[96:99], v[0:3], a[208:211], v[96:99]\n"
"ds_read_b64_tr_b16 v[32:33], v232 offset:192\n"
"ds_read_b64_tr_b16 v[34:35], v232 offset:3776\n"
"v_mfma_f32_16x16x32_bf16 v[96:99], v[4:7], a[212:215], v[96:99]\n"
"ds_read_b64_tr_b16 v[36:37], v232 offset:8384\n"
"ds_read_b64_tr_b16 v[38:39], v232 offset:11968\n"
"v_mfma_f32_16x16x32_bf16 v[96:99], v[8:11], a[216:219], v[96:99]\n"
"s_add_u32 s54, s50, s47\n"
"ds_read_b64_tr_b16 v[40:41], v232 offset:16576\n"
"ds_read_b64_tr_b16 v[42:43], v232 offset:20160\n"
"v_mfma_f32_16x16x32_bf16 v[96:99], v[12:15], a[220:223], v[96:99]\n"
"s_add_u32 s50, s50, 32768\n"
"ds_read_b64_tr_b16 v[44:45], v232 offset:24768\n"
"ds_read_b64_tr_b16 v[46:47], v232 offset:28352\n"
"v_mfma_f32_16x16x32_bf16 v[100:103], v[12:15], a[252:255], v[100:103]\n"
"s_cmp_lt_u32 s50, 163840\n"
"s_cselect_b32 s50, s50, 0\n"
"v_cvt_pk_bf16_f32 v250, v172, v173\n"
"v_mfma_f32_16x16x32_bf16 v[100:103], v[8:11], a[248:251], v[100:103]\n"
"buffer_load_dwordx2 a[130:131], v220, s[60:63], s5 offen offset:0\n"
"v_mfma_f32_16x16x32_bf16 v[100:103], v[4:7], a[244:247], v[100:103]\n"
"s_waitcnt lgkmcnt(0)\n"
"v_add_u32 v232, s50, v228\n"
"v_cvt_pk_bf16_f32 v251, v174, v175\n"
"v_mfma_f32_16x16x32_bf16 v[100:103], v[0:3], a[240:243], v[100:103]\n"
"s_waitcnt vmcnt(26)\n"
"s_barrier\n"
"v_mfma_f32_16x16x32_bf16 v[104:107], v[16:19], a[208:211], v[104:107]\n"
"ds_read_b64_tr_b16 v[48:49], v232 offset:0\n"
"buffer_load_dwordx2 a[132:133], v226, s[96:99], s6 offen offset:0\n"
"v_mfma_f32_16x16x32_bf16 v[104:107], v[20:23], a[212:215], v[104:107]\n"
"ds_read_b64_tr_b16 v[50:51], v232 offset:3584\n"
"buffer_load_dwordx2 a[134:135], v221, s[60:63], s4 offen offset:0\n"
"v_mfma_f32_16x16x32_bf16 v[104:107], v[24:27], a[216:219], v[104:107]\n"
"ds_read_b64_tr_b16 v[52:53], v232 offset:8192\n"
"buffer_load_dwordx2 a[136:137], v221, s[60:63], s5 offen offset:0\n"
"v_mfma_f32_16x16x32_bf16 v[104:107], v[28:31], a[220:223], v[104:107]\n"
"s_mov_b32 m0, s54\n"
"ds_read_b64_tr_b16 v[54:55], v232 offset:11776\n"
"ds_read_b64_tr_b16 v[56:57], v232 offset:16384\n"
"v_mfma_f32_16x16x32_bf16 v[108:111], v[28:31], a[252:255], v[108:111]\n"
"buffer_load_dwordx2 a[138:139], v226, s[96:99], s6 offen offset:128\n"
"v_mfma_f32_16x16x32_bf16 v[108:111], v[24:27], a[248:251], v[108:111]\n"
"ds_read_b64_tr_b16 v[58:59], v232 offset:19968\n"
"buffer_store_dwordx4 v[248:251], v229, s[20:23], s53 offen offset:1536 sc1\n"
"v_mfma_f32_16x16x32_bf16 v[108:111], v[20:23], a[244:247], v[108:111]\n"
"ds_read_b64_tr_b16 v[60:61], v232 offset:24576\n"
"v_bfe_u32 v208, v227, 4, 4\n"
"v_mfma_f32_16x16x32_bf16 v[108:111], v[16:19], a[240:243], v[108:111]\n"
"ds_read_b64_tr_b16 v[62:63], v232 offset:28160\n"
"v_lshrrev_b32 v209, 8, v227\n"
"v_mfma_f32_16x16x32_bf16 v[112:115], v[32:35], a[208:211], v[112:115]\n"
"ds_read_b64_tr_b16 v[0:1], v232 offset:32\n"
"v_mov_b32 v214, 0\n"
"v_mfma_f32_16x16x32_bf16 v[112:115], v[36:39], a[212:215], v[112:115]\n"
"ds_read_b64_tr_b16 v[2:3], v232 offset:3616\n"
"v_mov_b32 v215, 0\n"
"v_mfma_f32_16x16x32_bf16 v[112:115], v[40:43], a[216:219], v[112:115]\n"
"ds_read_b64_tr_b16 v[4:5], v232 offset:8224\n"
"v_mov_b32 v218, 0\n"
"v_mfma_f32_16x16x32_bf16 v[112:115], v[44:47], a[220:223], v[112:115]\n"
"ds_read_b64_tr_b16 v[6:7], v232 offset:11808\n"
"ds_read_b64_tr_b16 v[8:9], v232 offset:16416\n"
"v_mfma_f32_16x16x32_bf16 v[116:119], v[44:47], a[252:255], v[116:119]\n"
"v_mov_b32 v219, 0\n"
"v_and_b32 v210, 1, v208\n"
"v_mfma_f32_16x16x32_bf16 v[116:119], v[40:43], a[248:251], v[116:119]\n"
"ds_read_b64_tr_b16 v[10:11], v232 offset:20000\n"
"v_lshlrev_b32 v210, 4, v210\n"
"v_mfma_f32_16x16x32_bf16 v[116:119], v[36:39], a[244:247], v[116:119]\n"
"ds_read_b64_tr_b16 v[12:13], v232 offset:24608\n"
"v_mov_b32 v211, 0x3f80\n"
"v_mfma_f32_16x16x32_bf16 v[116:119], v[32:35], a[240:243], v[116:119]\n"
"s_waitcnt lgkmcnt(7)\n"
"ds_read_b64_tr_b16 v[14:15], v232 offset:28192\n"
"v_lshlrev_b32 v211, v210, v211\n"
"v_lshrrev_b32 v210, 1, v208\n"
"v_lshlrev_b32 v212, 1, v209\n"
"v_cmp_eq_u32 vcc, v210, v212\n"
"s_nop 1\n"
"v_cndmask_b32 v216, 0, v211, vcc\n"
"v_add_u32 v212, 1, v212\n"
"v_cmp_eq_u32 vcc, v210, v212\n"
"s_nop 1\n"
"v_cndmask_b32 v217, 0, v211, vcc\n"
"v_mfma_f32_16x16x32_bf16 v[120:123], v[48:51], v[176:179], 0\n"
"ds_read_b64_tr_b16 v[16:17], v232 offset:64\n"
"v_mfma_f32_16x16x32_bf16 v[120:123], v[52:55], v[180:183], v[120:123]\n"
"ds_read_b64_tr_b16 v[18:19], v232 offset:3648\n"
"v_mfma_f32_16x16x32_bf16 v[120:123], v[56:59], v[184:187], v[120:123]\n"
"ds_read_b64_tr_b16 v[20:21], v232 offset:8256\n"
"v_mfma_f32_16x16x32_bf16 v[120:123], v[60:63], v[188:191], v[120:123]\n"
"ds_read_b64_tr_b16 v[22:23], v232 offset:11840\n"
"ds_read_b64_tr_b16 v[24:25], v232 offset:16448\n"
"v_mfma_f32_16x16x32_bf16 v[124:127], v[60:63], v[204:207], 0\n"
"v_cvt_pk_bf16_f32 v240, v64, v65\n"
"v_mfma_f32_16x16x32_bf16 v[124:127], v[56:59], v[200:203], v[124:127]\n"
"ds_read_b64_tr_b16 v[26:27], v232 offset:20032\n"
"v_mfma_f32_16x16x32_bf16 v[124:127], v[52:55], v[196:199], v[124:127]\n"
"ds_read_b64_tr_b16 v[28:29], v232 offset:24640\n"
"v_cvt_pk_bf16_f32 v241, v66, v67\n"
"v_mfma_f32_16x16x32_bf16 v[124:127], v[48:51], v[192:195], v[124:127]\n"
"s_waitcnt lgkmcnt(7)\n"
"ds_read_b64_tr_b16 v[30:31], v232 offset:28224\n"
"v_mfma_f32_16x16x32_bf16 v[128:131], v[0:3], v[176:179], 0\n"
"ds_read_b64_tr_b16 v[32:33], v232 offset:96\n"
"v_cvt_pk_bf16_f32 v242, v68, v69\n"
"v_mfma_f32_16x16x32_bf16 v[128:131], v[4:7], v[180:183], v[128:131]\n"
"ds_read_b64_tr_b16 v[34:35], v232 offset:3680\n"
"v_mfma_f32_16x16x32_bf16 v[128:131], v[8:11], v[184:187], v[128:131]\n"
"ds_read_b64_tr_b16 v[36:37], v232 offset:8288\n"
"v_cvt_pk_bf16_f32 v243, v70, v71\n"
"v_mfma_f32_16x16x32_bf16 v[128:131], v[12:15], v[188:191], v[128:131]\n"
"ds_read_b64_tr_b16 v[38:39], v232 offset:11872\n"
"ds_read_b64_tr_b16 v[40:41], v232 offset:16480\n"
"v_mfma_f32_16x16x32_bf16 v[132:135], v[12:15], v[204:207], 0\n"
"s_add_u32 s53, s46, 3584\n"
"v_mfma_f32_16x16x32_bf16 v[132:135], v[8:11], v[200:203], v[132:135]\n"
"ds_read_b64_tr_b16 v[42:43], v232 offset:20064\n"
"v_cvt_pk_bf16_f32 v244, v72, v73\n"
"v_mfma_f32_16x16x32_bf16 v[132:135], v[4:7], v[196:199], v[132:135]\n"
"ds_read_b64_tr_b16 v[44:45], v232 offset:24672\n"
"v_mfma_f32_16x16x32_bf16 v[132:135], v[0:3], v[192:195], v[132:135]\n"
"s_waitcnt lgkmcnt(7)\n"
"ds_read_b64_tr_b16 v[46:47], v232 offset:28256\n"
"v_cvt_pk_bf16_f32 v245, v74, v75\n"
"v_mfma_f32_16x16x32_bf16 v[136:139], v[16:19], v[176:179], 0\n"
"ds_read_b64_tr_b16 v[48:49], v232 offset:128\n"
"v_mfma_f32_16x16x32_bf16 v[136:139], v[20:23], v[180:183], v[136:139]\n"
"ds_read_b64_tr_b16 v[50:51], v232 offset:3712\n"
"v_cvt_pk_bf16_f32 v246, v76, v77\n"
"v_mfma_f32_16x16x32_bf16 v[136:139], v[24:27], v[184:187], v[136:139]\n"
"ds_read_b64_tr_b16 v[52:53], v232 offset:8320\n"
"buffer_store_dwordx4 v[240:243], v229, s[20:23], s53 offen offset:0 sc1\n"
"v_mfma_f32_16x16x32_bf16 v[136:139], v[28:31], v[188:191], v[136:139]\n"
"ds_read_b64_tr_b16 v[54:55], v232 offset:11904\n"
"ds_read_b64_tr_b16 v[56:57], v232 offset:16512\n"
"v_mfma_f32_16x16x32_bf16 v[140:143], v[28:31], v[204:207], 0\n"
"v_cvt_pk_bf16_f32 v247, v78, v79\n"
"v_mfma_f32_16x16x32_bf16 v[140:143], v[24:27], v[200:203], v[140:143]\n"
"ds_read_b64_tr_b16 v[58:59], v232 offset:20096\n"
"v_mfma_f32_16x16x32_bf16 v[140:143], v[20:23], v[196:199], v[140:143]\n"
"ds_read_b64_tr_b16 v[60:61], v232 offset:24704\n"
"v_cvt_pk_bf16_f32 v248, v80, v81\n"
"v_mfma_f32_16x16x32_bf16 v[140:143], v[16:19], v[192:195], v[140:143]\n"
"s_waitcnt lgkmcnt(7)\n"
"ds_read_b64_tr_b16 v[62:63], v232 offset:28288\n"
"v_mfma_f32_16x16x32_bf16 v[144:147], v[32:35], v[176:179], 0\n"
"ds_read_b64_tr_b16 v[0:1], v232 offset:160\n"
"v_cvt_pk_bf16_f32 v249, v82, v83\n"
"v_mfma_f32_16x16x32_bf16 v[144:147], v[36:39], v[180:183], v[144:147]\n"
"ds_read_b64_tr_b16 v[2:3], v232 offset:3744\n"
"buffer_store_dwordx4 v[244:247], v229, s[20:23], s53 offen offset:256 sc1\n"
"v_mfma_f32_16x16x32_bf16 v[144:147], v[40:43], v[184:187], v[144:147]\n"
"ds_read_b64_tr_b16 v[4:5], v232 offset:8352\n"
"v_cvt_pk_bf16_f32 v250, v84, v85\n"
"v_mfma_f32_16x16x32_bf16 v[144:147], v[44:47], v[188:191], v[144:147]\n"
"ds_read_b64_tr_b16 v[6:7], v232 offset:11936\n"
"ds_read_b64_tr_b16 v[8:9], v232 offset:16544\n"
"v_mfma_f32_16x16x32_bf16 v[148:151], v[44:47], v[204:207], 0\n"
"v_cvt_pk_bf16_f32 v251, v86, v87\n"
"v_mfma_f32_16x16x32_bf16 v[148:151], v[40:43], v[200:203], v[148:151]\n"
"ds_read_b64_tr_b16 v[10:11], v232 offset:20128\n"
"v_mfma_f32_16x16x32_bf16 v[148:151], v[36:39], v[196:199], v[148:151]\n"
"ds_read_b64_tr_b16 v[12:13], v232 offset:24736\n"
"v_mfma_f32_16x16x32_bf16 v[148:151], v[32:35], v[192:195], v[148:151]\n"
"s_waitcnt lgkmcnt(7)\n"
"ds_read_b64_tr_b16 v[14:15], v232 offset:28320\n"
"v_cvt_pk_bf16_f32 v252, v88, v89\n"
"v_mfma_f32_16x16x32_bf16 v[152:155], v[48:51], v[176:179], 0\n"
"ds_read_b64_tr_b16 v[16:17], v232 offset:192\n"
"ds_read_b64_tr_b16 v[18:19], v232 offset:3776\n"
"v_mfma_f32_16x16x32_bf16 v[152:155], v[52:55], v[180:183], v[152:155]\n"
"ds_read_b64_tr_b16 v[20:21], v232 offset:8384\n"
"ds_read_b64_tr_b16 v[22:23], v232 offset:11968\n"
"v_mfma_f32_16x16x32_bf16 v[152:155], v[56:59], v[184:187], v[152:155]\n"
"s_add_u32 s54, s50, s47\n"
"ds_read_b64_tr_b16 v[24:25], v232 offset:16576\n"
"ds_read_b64_tr_b16 v[26:27], v232 offset:20160\n"
"v_mfma_f32_16x16x32_bf16 v[152:155], v[60:63], v[188:191], v[152:155]\n"
"s_add_u32 s50, s50, 32768\n"
"ds_read_b64_tr_b16 v[28:29], v232 offset:24768\n"
"ds_read_b64_tr_b16 v[30:31], v232 offset:28352\n"
"v_mfma_f32_16x16x32_bf16 v[156:159], v[60:63], v[204:207], 0\n"
"s_cmp_lt_u32 s50, 163840\n"
"s_cselect_b32 s50, s50, 0\n"
"v_cvt_pk_bf16_f32 v253, v90, v91\n"
"v_mfma_f32_16x16x32_bf16 v[156:159], v[56:59], v[200:203], v[156:159]\n"
"buffer_store_dwordx4 v[248:251], v229, s[20:23], s53 offen offset:512 sc1\n"
"v_mfma_f32_16x16x32_bf16 v[156:159], v[52:55], v[196:199], v[156:159]\n"
"s_waitcnt lgkmcnt(0)\n"
"v_add_u32 v232, s50, v228\n"
"v_cvt_pk_bf16_f32 v254, v92, v93\n"
"v_mfma_f32_16x16x32_bf16 v[156:159], v[48:51], v[192:195], v[156:159]\n"
"s_waitcnt vmcnt(4)\n"
"s_barrier\n"
"v_mfma_f32_16x16x32_bf16 v[160:163], v[0:3], v[176:179], 0\n"
"s_mov_b32 m0, s54\n"
"ds_read_b64_tr_b16 v[32:33], v232 offset:0\n"
"v_cvt_pk_bf16_f32 v255, v94, v95\n"
"v_mfma_f32_16x16x32_bf16 v[160:163], v[4:7], v[180:183], v[160:163]\n"
"ds_read_b64_tr_b16 v[34:35], v232 offset:3584\n"
"v_cvt_pk_bf16_f32 v240, v96, v97\n"
"v_mfma_f32_16x16x32_bf16 v[160:163], v[8:11], v[184:187], v[160:163]\n"
"ds_read_b64_tr_b16 v[36:37], v232 offset:8192\n"
"v_cvt_pk_bf16_f32 v241, v98, v99\n"
"v_mfma_f32_16x16x32_bf16 v[160:163], v[12:15], v[188:191], v[160:163]\n"
"ds_read_b64_tr_b16 v[38:39], v232 offset:11776\n"
"ds_read_b64_tr_b16 v[40:41], v232 offset:16384\n"
"v_mfma_f32_16x16x32_bf16 v[164:167], v[12:15], v[204:207], 0\n"
"buffer_store_dwordx4 v[252:255], v229, s[20:23], s53 offen offset:768 sc1\n"
"v_mfma_f32_16x16x32_bf16 v[164:167], v[8:11], v[200:203], v[164:167]\n"
"ds_read_b64_tr_b16 v[42:43], v232 offset:19968\n"
"v_cvt_pk_bf16_f32 v242, v100, v101\n"
"v_mfma_f32_16x16x32_bf16 v[164:167], v[4:7], v[196:199], v[164:167]\n"
"ds_read_b64_tr_b16 v[44:45], v232 offset:24576\n"
"v_cvt_pk_bf16_f32 v243, v102, v103\n"
"v_mfma_f32_16x16x32_bf16 v[164:167], v[0:3], v[192:195], v[164:167]\n"
"ds_read_b64_tr_b16 v[46:47], v232 offset:28160\n"
"v_mfma_f32_16x16x32_bf16 v[168:171], v[16:19], v[176:179], 0\n"
"ds_read_b64_tr_b16 v[48:49], v232 offset:32\n"
"v_mfma_f32_16x16x32_bf16 v[168:171], v[20:23], v[180:183], v[168:171]\n"
"ds_read_b64_tr_b16 v[50:51], v232 offset:3616\n"
"v_mfma_f32_16x16x32_bf16 v[168:171], v[24:27], v[184:187], v[168:171]\n"
"ds_read_b64_tr_b16 v[52:53], v232 offset:8224\n"
"v_mfma_f32_16x16x32_bf16 v[168:171], v[28:31], v[188:191], v[168:171]\n"
"ds_read_b64_tr_b16 v[54:55], v232 offset:11808\n"
"ds_read_b64_tr_b16 v[56:57], v232 offset:16416\n"
"v_mfma_f32_16x16x32_bf16 v[172:175], v[28:31], v[204:207], 0\n"
"buffer_store_dwordx4 v[240:243], v229, s[20:23], s53 offen offset:1024 sc1\n"
"v_mfma_f32_16x16x32_bf16 v[172:175], v[24:27], v[200:203], v[172:175]\n"
"ds_read_b64_tr_b16 v[58:59], v232 offset:20000\n"
"v_mfma_f32_16x16x32_bf16 v[172:175], v[20:23], v[196:199], v[172:175]\n"
"ds_read_b64_tr_b16 v[60:61], v232 offset:24608\n"
"v_mfma_f32_16x16x32_bf16 v[172:175], v[16:19], v[192:195], v[172:175]\n"
"s_waitcnt lgkmcnt(7)\n"
"ds_read_b64_tr_b16 v[62:63], v232 offset:28192\n"
"v_mfma_f32_16x16x32_bf16 v[120:123], v[32:35], a[192:195], v[120:123]\n"
"ds_read_b64_tr_b16 v[0:1], v232 offset:64\n"
"v_mfma_f32_16x16x32_bf16 v[120:123], v[36:39], a[196:199], v[120:123]\n"
"ds_read_b64_tr_b16 v[2:3], v232 offset:3648\n"
"v_mfma_f32_16x16x32_bf16 v[120:123], v[40:43], a[200:203], v[120:123]\n"
"ds_read_b64_tr_b16 v[4:5], v232 offset:8256\n"
"v_accvgpr_read_b32 v194, a132\n"
"v_mfma_f32_16x16x32_bf16 v[120:123], v[44:47], a[204:207], v[120:123]\n"
"ds_read_b64_tr_b16 v[6:7], v232 offset:11840\n"
"ds_read_b64_tr_b16 v[8:9], v232 offset:16448\n"
"v_mfma_f32_16x16x32_bf16 v[124:127], v[44:47], a[236:239], v[124:127]\n"
"v_lshlrev_b32 v192, 16, v194\n"
"v_and_b32 v193, 0xffff0000, v194\n"
"v_mfma_f32_16x16x32_bf16 v[124:127], v[40:43], a[232:235], v[124:127]\n"
"ds_read_b64_tr_b16 v[10:11], v232 offset:20032\n"
"v_cvt_pk_bf16_f32 v244, v104, v105\n"
"v_mfma_f32_16x16x32_bf16 v[124:127], v[36:39], a[228:231], v[124:127]\n"
"ds_read_b64_tr_b16 v[12:13], v232 offset:24640\n"
"v_accvgpr_read_b32 v177, a128\n"
"v_mfma_f32_16x16x32_bf16 v[124:127], v[32:35], a[224:227], v[124:127]\n"
"s_waitcnt lgkmcnt(7)\n"
"ds_read_b64_tr_b16 v[14:15], v232 offset:28224\n"
"v_lshlrev_b32 v176, 16, v177\n"
"v_mfma_f32_16x16x32_bf16 v[128:131], v[48:51], a[192:195], v[128:131]\n"
"ds_read_b64_tr_b16 v[16:17], v232 offset:96\n"
"v_cvt_pk_bf16_f32 v245, v106, v107\n"
"v_mfma_f32_16x16x32_bf16 v[128:131], v[52:55], a[196:199], v[128:131]\n"
"ds_read_b64_tr_b16 v[18:19], v232 offset:3680\n"
"v_and_b32 v177, 0xffff0000, v177\n"
"v_mfma_f32_16x16x32_bf16 v[128:131], v[56:59], a[200:203], v[128:131]\n"
"ds_read_b64_tr_b16 v[20:21], v232 offset:8288\n"
"v_add_f32 v176, v176, v192\n"
"v_mfma_f32_16x16x32_bf16 v[128:131], v[60:63], a[204:207], v[128:131]\n"
"ds_read_b64_tr_b16 v[22:23], v232 offset:11872\n"
"ds_read_b64_tr_b16 v[24:25], v232 offset:16480\n"
"v_mfma_f32_16x16x32_bf16 v[132:135], v[60:63], a[236:239], v[132:135]\n"
"v_cvt_pk_bf16_f32 v246, v108, v109\n"
"v_add_f32 v177, v177, v193\n"
"v_mfma_f32_16x16x32_bf16 v[132:135], v[56:59], a[232:235], v[132:135]\n"
"ds_read_b64_tr_b16 v[26:27], v232 offset:20064\n"
"v_accvgpr_read_b32 v181, a130\n"
"v_mfma_f32_16x16x32_bf16 v[132:135], v[52:55], a[228:231], v[132:135]\n"
"ds_read_b64_tr_b16 v[28:29], v232 offset:24672\n"
"v_cvt_pk_bf16_f32 v247, v110, v111\n"
"v_mfma_f32_16x16x32_bf16 v[132:135], v[48:51], a[224:227], v[132:135]\n"
"s_waitcnt lgkmcnt(7)\n"
"ds_read_b64_tr_b16 v[30:31], v232 offset:28256\n"
"v_lshlrev_b32 v180, 16, v181\n"
"v_mfma_f32_16x16x32_bf16 v[136:139], v[0:3], a[192:195], v[136:139]\n"
"ds_read_b64_tr_b16 v[32:33], v232 offset:128\n"
"buffer_store_dwordx4 v[244:247], v229, s[20:23], s53 offen offset:1280 sc1\n"
"v_mfma_f32_16x16x32_bf16 v[136:139], v[4:7], a[196:199], v[136:139]\n"
"ds_read_b64_tr_b16 v[34:35], v232 offset:3712\n"
"v_and_b32 v181, 0xffff0000, v181\n"
"v_mfma_f32_16x16x32_bf16 v[136:139], v[8:11], a[200:203], v[136:139]\n"
"ds_read_b64_tr_b16 v[36:37], v232 offset:8320\n"
"v_add_f32 v180, v180, v192\n"
"v_mfma_f32_16x16x32_bf16 v[136:139], v[12:15], a[204:207], v[136:139]\n"
"ds_read_b64_tr_b16 v[38:39], v232 offset:11904\n"
"ds_read_b64_tr_b16 v[40:41], v232 offset:16512\n"
"v_mfma_f32_16x16x32_bf16 v[140:143], v[12:15], a[236:239], v[140:143]\n"
"v_add_f32 v181, v181, v193\n"
"v_accvgpr_read_b32 v194, a133\n"
"v_mfma_f32_16x16x32_bf16 v[140:143], v[8:11], a[232:235], v[140:143]\n"
"ds_read_b64_tr_b16 v[42:43], v232 offset:20096\n"
"v_lshlrev_b32 v192, 16, v194\n"
"v_mfma_f32_16x16x32_bf16 v[140:143], v[4:7], a[228:231], v[140:143]\n"
"ds_read_b64_tr_b16 v[44:45], v232 offset:24704\n"
"v_cvt_pk_bf16_f32 v248, v112, v113\n"
"v_mfma_f32_16x16x32_bf16 v[140:143], v[0:3], a[224:227], v[140:143]\n"
"s_waitcnt lgkmcnt(7)\n"
"ds_read_b64_tr_b16 v[46:47], v232 offset:28288\n"
"v_and_b32 v193, 0xffff0000, v194\n"
"v_mfma_f32_16x16x32_bf16 v[144:147], v[16:19], a[192:195], v[144:147]\n"
"ds_read_b64_tr_b16 v[48:49], v232 offset:160\n"
"v_accvgpr_read_b32 v179, a129\n"
"v_mfma_f32_16x16x32_bf16 v[144:147], v[20:23], a[196:199], v[144:147]\n"
"ds_read_b64_tr_b16 v[50:51], v232 offset:3744\n"
"v_cvt_pk_bf16_f32 v249, v114, v115\n"
"v_mfma_f32_16x16x32_bf16 v[144:147], v[24:27], a[200:203], v[144:147]\n"
"ds_read_b64_tr_b16 v[52:53], v232 offset:8352\n"
"v_lshlrev_b32 v178, 16, v179\n"
"v_mfma_f32_16x16x32_bf16 v[144:147], v[28:31], a[204:207], v[144:147]\n"
"ds_read_b64_tr_b16 v[54:55], v232 offset:11936\n"
"ds_read_b64_tr_b16 v[56:57], v232 offset:16544\n"
"v_mfma_f32_16x16x32_bf16 v[148:151], v[28:31], a[236:239], v[148:151]\n"
"v_and_b32 v179, 0xffff0000, v179\n"
"v_cvt_pk_bf16_f32 v250, v116, v117\n"
"v_mfma_f32_16x16x32_bf16 v[148:151], v[24:27], a[232:235], v[148:151]\n"
"ds_read_b64_tr_b16 v[58:59], v232 offset:20128\n"
"v_add_f32 v178, v178, v192\n"
"v_mfma_f32_16x16x32_bf16 v[148:151], v[20:23], a[228:231], v[148:151]\n"
"ds_read_b64_tr_b16 v[60:61], v232 offset:24736\n"
"v_add_f32 v179, v179, v193\n"
"v_mfma_f32_16x16x32_bf16 v[148:151], v[16:19], a[224:227], v[148:151]\n"
"s_waitcnt lgkmcnt(7)\n"
"ds_read_b64_tr_b16 v[62:63], v232 offset:28320\n"
"v_cvt_pk_bf16_f32 v251, v118, v119\n"
"v_mfma_f32_16x16x32_bf16 v[152:155], v[32:35], a[192:195], v[152:155]\n"
"ds_read_b64_tr_b16 v[0:1], v232 offset:192\n"
"ds_read_b64_tr_b16 v[2:3], v232 offset:3776\n"
"v_mfma_f32_16x16x32_bf16 v[152:155], v[36:39], a[196:199], v[152:155]\n"
"ds_read_b64_tr_b16 v[4:5], v232 offset:8384\n"
"ds_read_b64_tr_b16 v[6:7], v232 offset:11968\n"
"v_mfma_f32_16x16x32_bf16 v[152:155], v[40:43], a[200:203], v[152:155]\n"
"s_add_u32 s54, s50, s47\n"
"ds_read_b64_tr_b16 v[8:9], v232 offset:16576\n"
"ds_read_b64_tr_b16 v[10:11], v232 offset:20160\n"
"v_mfma_f32_16x16x32_bf16 v[152:155], v[44:47], a[204:207], v[152:155]\n"
"s_add_u32 s50, s50, 32768\n"
"ds_read_b64_tr_b16 v[12:13], v232 offset:24768\n"
"ds_read_b64_tr_b16 v[14:15], v232 offset:28352\n"
"v_mfma_f32_16x16x32_bf16 v[156:159], v[44:47], a[236:239], v[156:159]\n"
"s_cmp_lt_u32 s50, 163840\n"
"s_cselect_b32 s50, s50, 0\n"
"v_accvgpr_read_b32 v183, a131\n"
"v_mfma_f32_16x16x32_bf16 v[156:159], v[40:43], a[232:235], v[156:159]\n"
"buffer_store_dwordx4 v[248:251], v229, s[20:23], s53 offen offset:1536 sc1\n"
"v_mfma_f32_16x16x32_bf16 v[156:159], v[36:39], a[228:231], v[156:159]\n"
"s_waitcnt lgkmcnt(0)\n"
"v_add_u32 v232, s50, v228\n"
"v_lshlrev_b32 v182, 16, v183\n"
"v_mfma_f32_16x16x32_bf16 v[156:159], v[32:35], a[224:227], v[156:159]\n"
"s_waitcnt vmcnt(7)\n"
"s_barrier\n"
"v_mfma_f32_16x16x32_bf16 v[160:163], v[48:51], a[192:195], v[160:163]\n"
"s_mov_b32 m0, s54\n"
"ds_read_b64_tr_b16 v[16:17], v232 offset:0\n"
"v_and_b32 v183, 0xffff0000, v183\n"
"v_mfma_f32_16x16x32_bf16 v[160:163], v[52:55], a[196:199], v[160:163]\n"
"ds_read_b64_tr_b16 v[18:19], v232 offset:3584\n"
"v_add_f32 v182, v182, v192\n"
"v_mfma_f32_16x16x32_bf16 v[160:163], v[56:59], a[200:203], v[160:163]\n"
"ds_read_b64_tr_b16 v[20:21], v232 offset:8192\n"
"v_add_f32 v183, v183, v193\n"
"v_mfma_f32_16x16x32_bf16 v[160:163], v[60:63], a[204:207], v[160:163]\n"
"ds_read_b64_tr_b16 v[22:23], v232 offset:11776\n"
"ds_read_b64_tr_b16 v[24:25], v232 offset:16384\n"
"v_mfma_f32_16x16x32_bf16 v[164:167], v[60:63], a[236:239], v[164:167]\n"
"v_accvgpr_read_b32 v194, a138\n"
"v_lshlrev_b32 v192, 16, v194\n"
"v_mfma_f32_16x16x32_bf16 v[164:167], v[56:59], a[232:235], v[164:167]\n"
"ds_read_b64_tr_b16 v[26:27], v232 offset:19968\n"
"v_and_b32 v193, 0xffff0000, v194\n"
"v_mfma_f32_16x16x32_bf16 v[164:167], v[52:55], a[228:231], v[164:167]\n"
"ds_read_b64_tr_b16 v[28:29], v232 offset:24576\n"
"v_accvgpr_read_b32 v185, a134\n"
"v_mfma_f32_16x16x32_bf16 v[164:167], v[48:51], a[224:227], v[164:167]\n"
"ds_read_b64_tr_b16 v[30:31], v232 offset:28160\n"
"v_lshlrev_b32 v184, 16, v185\n"
"v_mfma_f32_16x16x32_bf16 v[168:171], v[0:3], a[192:195], v[168:171]\n"
"ds_read_b64_tr_b16 v[32:33], v232 offset:32\n"
"v_and_b32 v185, 0xffff0000, v185\n"
"v_mfma_f32_16x16x32_bf16 v[168:171], v[4:7], a[196:199], v[168:171]\n"
"ds_read_b64_tr_b16 v[34:35], v232 offset:3616\n"
"v_add_f32 v184, v184, v192\n"
"v_mfma_f32_16x16x32_bf16 v[168:171], v[8:11], a[200:203], v[168:171]\n"
"ds_read_b64_tr_b16 v[36:37], v232 offset:8224\n"
"v_add_f32 v185, v185, v193\n"
"v_mfma_f32_16x16x32_bf16 v[168:171], v[12:15], a[204:207], v[168:171]\n"
"ds_read_b64_tr_b16 v[38:39], v232 offset:11808\n"
"ds_read_b64_tr_b16 v[40:41], v232 offset:16416\n"
"v_mfma_f32_16x16x32_bf16 v[172:175], v[12:15], a[236:239], v[172:175]\n"
"v_accvgpr_read_b32 v189, a136\n"
"v_lshlrev_b32 v188, 16, v189\n"
"v_mfma_f32_16x16x32_bf16 v[172:175], v[8:11], a[232:235], v[172:175]\n"
"ds_read_b64_tr_b16 v[42:43], v232 offset:20000\n"
"v_and_b32 v189, 0xffff0000, v189\n"
"v_mfma_f32_16x16x32_bf16 v[172:175], v[4:7], a[228:231], v[172:175]\n"
"ds_read_b64_tr_b16 v[44:45], v232 offset:24608\n"
"v_add_f32 v188, v188, v192\n"
"v_mfma_f32_16x16x32_bf16 v[172:175], v[0:3], a[224:227], v[172:175]\n"
"s_waitcnt lgkmcnt(7)\n"
"ds_read_b64_tr_b16 v[46:47], v232 offset:28192\n"
"v_add_f32 v189, v189, v193\n"
"v_accvgpr_read_b32 v194, a139\n"
"v_lshlrev_b32 v192, 16, v194\n"
"v_and_b32 v193, 0xffff0000, v194\n"
"v_accvgpr_read_b32 v187, a135\n"
"v_lshlrev_b32 v186, 16, v187\n"
"v_and_b32 v187, 0xffff0000, v187\n"
"v_add_f32 v186, v186, v192\n"
"v_add_f32 v187, v187, v193\n"
"v_accvgpr_read_b32 v191, a137\n"
"v_lshlrev_b32 v190, 16, v191\n"
"v_and_b32 v191, 0xffff0000, v191\n"
"v_add_f32 v190, v190, v192\n"
"v_add_f32 v191, v191, v193\n"
"v_mov_b32 v233, 2\n"
"s_mov_b64 exec, 1\n"
"buffer_store_dword v233, v231, s[20:23], s9 offen offset:0 sc1\n"
"s_mov_b64 exec, -1\n"
"s_nop 3\n"
"v_mfma_f32_16x16x32_bf16 v[120:123], v[16:19], a[208:211], v[120:123]\n"
"ds_read_b64_tr_b16 v[48:49], v232 offset:64\n"
"v_mfma_f32_16x16x32_bf16 v[120:123], v[20:23], a[212:215], v[120:123]\n"
"ds_read_b64_tr_b16 v[50:51], v232 offset:3648\n"
"v_mfma_f32_16x16x32_bf16 v[120:123], v[24:27], a[216:219], v[120:123]\n"
"ds_read_b64_tr_b16 v[52:53], v232 offset:8256\n"
"v_mfma_f32_16x16x32_bf16 v[120:123], v[28:31], a[220:223], v[120:123]\n"
"ds_read_b64_tr_b16 v[54:55], v232 offset:11840\n"
"ds_read_b64_tr_b16 v[56:57], v232 offset:16448\n"
"v_mfma_f32_16x16x32_bf16 v[124:127], v[28:31], a[252:255], v[124:127]\n"
"v_mfma_f32_16x16x32_bf16 v[124:127], v[24:27], a[248:251], v[124:127]\n"
"ds_read_b64_tr_b16 v[58:59], v232 offset:20032\n"
"v_mfma_f32_16x16x32_bf16 v[124:127], v[20:23], a[244:247], v[124:127]\n"
"ds_read_b64_tr_b16 v[60:61], v232 offset:24640\n"
"v_mfma_f32_16x16x32_bf16 v[124:127], v[16:19], a[240:243], v[124:127]\n"
"s_waitcnt lgkmcnt(7)\n"
"ds_read_b64_tr_b16 v[62:63], v232 offset:28224\n"
"v_mfma_f32_16x16x32_bf16 v[128:131], v[32:35], a[208:211], v[128:131]\n"
"ds_read_b64_tr_b16 v[0:1], v232 offset:96\n"
"v_mfma_f32_16x16x32_bf16 v[128:131], v[36:39], a[212:215], v[128:131]\n"
"ds_read_b64_tr_b16 v[2:3], v232 offset:3680\n"
"v_mfma_f32_16x16x32_bf16 v[128:131], v[40:43], a[216:219], v[128:131]\n"
"ds_read_b64_tr_b16 v[4:5], v232 offset:8288\n"
"v_mfma_f32_16x16x32_bf16 v[128:131], v[44:47], a[220:223], v[128:131]\n"
"s_add_u32 s53, s46, 5376\n"
"ds_read_b64_tr_b16 v[6:7], v232 offset:11872\n"
"ds_read_b64_tr_b16 v[8:9], v232 offset:16480\n"
"v_mfma_f32_16x16x32_bf16 v[132:135], v[44:47], a[252:255], v[132:135]\n"
"v_cvt_pk_bf16_f32 v240, v120, v121\n"
"v_cvt_pk_bf16_f32 v241, v122, v123\n"
"v_mfma_f32_16x16x32_bf16 v[132:135], v[40:43], a[248:251], v[132:135]\n"
"ds_read_b64_tr_b16 v[10:11], v232 offset:20064\n"
"v_cvt_pk_bf16_f32 v242, v124, v125\n"
"v_mfma_f32_16x16x32_bf16 v[132:135], v[36:39], a[244:247], v[132:135]\n"
"ds_read_b64_tr_b16 v[12:13], v232 offset:24672\n"
"v_cvt_pk_bf16_f32 v243, v126, v127\n"
"v_mfma_f32_16x16x32_bf16 v[132:135], v[32:35], a[240:243], v[132:135]\n"
"s_waitcnt lgkmcnt(7)\n"
"ds_read_b64_tr_b16 v[14:15], v232 offset:28256\n"
"buffer_store_dwordx4 v[240:243], v229, s[20:23], s53 offen offset:0 sc1\n"
"v_mfma_f32_16x16x32_bf16 v[136:139], v[48:51], a[208:211], v[136:139]\n"
"ds_read_b64_tr_b16 v[16:17], v232 offset:128\n"
"v_mfma_f32_16x16x32_bf16 v[136:139], v[52:55], a[212:215], v[136:139]\n"
"ds_read_b64_tr_b16 v[18:19], v232 offset:3712\n"
"v_mfma_f32_16x16x32_bf16 v[136:139], v[56:59], a[216:219], v[136:139]\n"
"ds_read_b64_tr_b16 v[20:21], v232 offset:8320\n"
"v_mfma_f32_16x16x32_bf16 v[136:139], v[60:63], a[220:223], v[136:139]\n"
"ds_read_b64_tr_b16 v[22:23], v232 offset:11904\n"
"ds_read_b64_tr_b16 v[24:25], v232 offset:16512\n"
"v_mfma_f32_16x16x32_bf16 v[140:143], v[60:63], a[252:255], v[140:143]\n"
"v_cvt_pk_bf16_f32 v244, v128, v129\n"
"v_cvt_pk_bf16_f32 v245, v130, v131\n"
"v_mfma_f32_16x16x32_bf16 v[140:143], v[56:59], a[248:251], v[140:143]\n"
"ds_read_b64_tr_b16 v[26:27], v232 offset:20096\n"
"v_cvt_pk_bf16_f32 v246, v132, v133\n"
"v_mfma_f32_16x16x32_bf16 v[140:143], v[52:55], a[244:247], v[140:143]\n"
"ds_read_b64_tr_b16 v[28:29], v232 offset:24704\n"
"v_cvt_pk_bf16_f32 v247, v134, v135\n"
"v_mfma_f32_16x16x32_bf16 v[140:143], v[48:51], a[240:243], v[140:143]\n"
"s_waitcnt lgkmcnt(7)\n"
"ds_read_b64_tr_b16 v[30:31], v232 offset:28288\n"
"buffer_store_dwordx4 v[244:247], v229, s[20:23], s53 offen offset:256 sc1\n"
"v_mfma_f32_16x16x32_bf16 v[144:147], v[0:3], a[208:211], v[144:147]\n"
"ds_read_b64_tr_b16 v[32:33], v232 offset:160\n"
"v_mfma_f32_16x16x32_bf16 v[144:147], v[4:7], a[212:215], v[144:147]\n"
"ds_read_b64_tr_b16 v[34:35], v232 offset:3744\n"
"v_mfma_f32_16x16x32_bf16 v[144:147], v[8:11], a[216:219], v[144:147]\n"
"ds_read_b64_tr_b16 v[36:37], v232 offset:8352\n"
"v_mfma_f32_16x16x32_bf16 v[144:147], v[12:15], a[220:223], v[144:147]\n"
"ds_read_b64_tr_b16 v[38:39], v232 offset:11936\n"
"ds_read_b64_tr_b16 v[40:41], v232 offset:16544\n"
"v_mfma_f32_16x16x32_bf16 v[148:151], v[12:15], a[252:255], v[148:151]\n"
"v_cvt_pk_bf16_f32 v248, v136, v137\n"
"v_cvt_pk_bf16_f32 v249, v138, v139\n"
"v_mfma_f32_16x16x32_bf16 v[148:151], v[8:11], a[248:251], v[148:151]\n"
"ds_read_b64_tr_b16 v[42:43], v232 offset:20128\n"
"v_cvt_pk_bf16_f32 v250, v140, v141\n"
"v_mfma_f32_16x16x32_bf16 v[148:151], v[4:7], a[244:247], v[148:151]\n"
"ds_read_b64_tr_b16 v[44:45], v232 offset:24736\n"
"v_cvt_pk_bf16_f32 v251, v142, v143\n"
"v_mfma_f32_16x16x32_bf16 v[148:151], v[0:3], a[240:243], v[148:151]\n"
"s_waitcnt lgkmcnt(7)\n"
"ds_read_b64_tr_b16 v[46:47], v232 offset:28320\n"
"buffer_store_dwordx4 v[248:251], v229, s[20:23], s53 offen offset:512 sc1\n"
"v_mfma_f32_16x16x32_bf16 v[152:155], v[16:19], a[208:211], v[152:155]\n"
"ds_read_b64_tr_b16 v[48:49], v232 offset:192\n"
"ds_read_b64_tr_b16 v[50:51], v232 offset:3776\n"
"v_mfma_f32_16x16x32_bf16 v[152:155], v[20:23], a[212:215], v[152:155]\n"
"ds_read_b64_tr_b16 v[52:53], v232 offset:8384\n"
"ds_read_b64_tr_b16 v[54:55], v232 offset:11968\n"
"v_mfma_f32_16x16x32_bf16 v[152:155], v[24:27], a[216:219], v[152:155]\n"
"s_add_u32 s54, s50, s47\n"
"ds_read_b64_tr_b16 v[56:57], v232 offset:16576\n"
"ds_read_b64_tr_b16 v[58:59], v232 offset:20160\n"
"v_mfma_f32_16x16x32_bf16 v[152:155], v[28:31], a[220:223], v[152:155]\n"
"s_add_u32 s50, s50, 32768\n"
"ds_read_b64_tr_b16 v[60:61], v232 offset:24768\n"
"ds_read_b64_tr_b16 v[62:63], v232 offset:28352\n"
"v_mfma_f32_16x16x32_bf16 v[156:159], v[28:31], a[252:255], v[156:159]\n"
"s_cmp_lt_u32 s50, 163840\n"
"s_cselect_b32 s50, s50, 0\n"
"v_cvt_pk_bf16_f32 v252, v144, v145\n"
"v_mfma_f32_16x16x32_bf16 v[156:159], v[24:27], a[248:251], v[156:159]\n"
"s_mov_b32 m0, s54\n"
"v_cvt_pk_bf16_f32 v253, v146, v147\n"
"v_cvt_pk_bf16_f32 v254, v148, v149\n"
"v_mfma_f32_16x16x32_bf16 v[156:159], v[20:23], a[244:247], v[156:159]\n"
"v_cvt_pk_bf16_f32 v255, v150, v151\n"
"v_mfma_f32_16x16x32_bf16 v[156:159], v[16:19], a[240:243], v[156:159]\n"
"s_waitcnt lgkmcnt(8)\n"
"buffer_store_dwordx4 v[252:255], v229, s[20:23], s53 offen offset:768 sc1\n"
"v_mfma_f32_16x16x32_bf16 v[160:163], v[32:35], a[208:211], v[160:163]\n"
"v_mfma_f32_16x16x32_bf16 v[160:163], v[36:39], a[212:215], v[160:163]\n"
"v_mfma_f32_16x16x32_bf16 v[160:163], v[40:43], a[216:219], v[160:163]\n"
"buffer_load_dword v239, v224, s[20:23], s10 offen offset:0 sc1\n"
"v_mfma_f32_16x16x32_bf16 v[160:163], v[44:47], a[220:223], v[160:163]\n"
"v_cvt_pk_bf16_f32 v240, v152, v153\n"
"v_cvt_pk_bf16_f32 v241, v154, v155\n"
"v_mfma_f32_16x16x32_bf16 v[164:167], v[44:47], a[252:255], v[164:167]\n"
"v_cvt_pk_bf16_f32 v242, v156, v157\n"
"v_cvt_pk_bf16_f32 v243, v158, v159\n"
"v_mfma_f32_16x16x32_bf16 v[164:167], v[40:43], a[248:251], v[164:167]\n"
"buffer_store_dwordx4 v[240:243], v229, s[20:23], s53 offen offset:1024 sc1\n"
"v_mfma_f32_16x16x32_bf16 v[164:167], v[36:39], a[244:247], v[164:167]\n"
"v_mfma_f32_16x16x32_bf16 v[164:167], v[32:35], a[240:243], v[164:167]\n"
"s_waitcnt lgkmcnt(0)\n"
"v_mfma_f32_16x16x32_bf16 v[168:171], v[48:51], a[208:211], v[168:171]\n"
"v_mfma_f32_16x16x32_bf16 v[168:171], v[52:55], a[212:215], v[168:171]\n"
"v_mfma_f32_16x16x32_bf16 v[168:171], v[56:59], a[216:219], v[168:171]\n"
"v_mfma_f32_16x16x32_bf16 v[168:171], v[60:63], a[220:223], v[168:171]\n"
"v_cvt_pk_bf16_f32 v244, v160, v161\n"
"v_cvt_pk_bf16_f32 v245, v162, v163\n"
"v_mfma_f32_16x16x32_bf16 v[172:175], v[60:63], a[252:255], v[172:175]\n"
"v_cvt_pk_bf16_f32 v246, v164, v165\n"
"v_cvt_pk_bf16_f32 v247, v166, v167\n"
"v_mfma_f32_16x16x32_bf16 v[172:175], v[56:59], a[248:251], v[172:175]\n"
"buffer_store_dwordx4 v[244:247], v229, s[20:23], s53 offen offset:1280 sc1\n"
"v_mfma_f32_16x16x32_bf16 v[172:175], v[52:55], a[244:247], v[172:175]\n"
"v_mfma_f32_16x16x32_bf16 v[172:175], v[48:51], a[240:243], v[172:175]\n"
"v_cvt_pk_bf16_f32 v248, v168, v169\n"
"v_cvt_pk_bf16_f32 v249, v170, v171\n"
"s_nop 5\n"
"v_cvt_pk_bf16_f32 v250, v172, v173\n"
"v_cvt_pk_bf16_f32 v251, v174, v175\n"
"buffer_store_dwordx4 v[248:251], v229, s[20:23], s53 offen offset:1536 sc1\n"
"s_waitcnt vmcnt(9)\n"
"v_mov_b32 v233, 3\n"
"s_mov_b64 exec, 1\n"
"buffer_store_dword v233, v231, s[20:23], s9 offen offset:0 sc1\n"
"s_mov_b64 exec, -1\n"
"s_nop 3\n"
"s_barrier\n"
"s_waitcnt vmcnt(4)\n"
"v_max_u32 v239, v239, v225\n"
"v_cmp_lt_u32 vcc, s33, v239\n"
"s_nop 0\n"
"s_cmp_eq_u64 vcc, -1\n"
"s_cbranch_scc0 L_ylate%=\n"
"s_add_u32 s54, s8, 0\n"
"buffer_load_dwordx4 a[0:3], v227, s[20:23], s54 offen offset:0 sc1\n"
"buffer_load_dwordx4 a[4:7], v222, s[20:23], s54 offen offset:1024 sc1\n"
"s_add_u32 s54, s8, 1835008\n"
"buffer_load_dwordx4 a[8:11], v227, s[20:23], s54 offen offset:0 sc1\n"
"buffer_load_dwordx4 a[12:15], v222, s[20:23], s54 offen offset:1024 sc1\n"
"s_add_u32 s54, s8, 3670016\n"
"buffer_load_dwordx4 a[16:19], v227, s[20:23], s54 offen offset:0 sc1\n"
"buffer_load_dwordx4 a[20:23], v222, s[20:23], s54 offen offset:1024 sc1\n"
"s_add_u32 s54, s8, 5505024\n"
"buffer_load_dwordx4 a[24:27], v227, s[20:23], s54 offen offset:0 sc1\n"
"buffer_load_dwordx4 a[28:31], v222, s[20:23], s54 offen offset:1024 sc1\n"
"s_add_u32 s54, s8, 7340032\n"
"buffer_load_dwordx4 a[32:35], v227, s[20:23], s54 offen offset:0 sc1\n"
"buffer_load_dwordx4 a[36:39], v222, s[20:23], s54 offen offset:1024 sc1\n"
"s_add_u32 s54, s8, 9175040\n"
"buffer_load_dwordx4 a[40:43], v227, s[20:23], s54 offen offset:0 sc1\n"
"buffer_load_dwordx4 a[44:47], v222, s[20:23], s54 offen offset:1024 sc1\n"
"s_add_u32 s54, s8, 11010048\n"
"buffer_load_dwordx4 a[48:51], v227, s[20:23], s54 offen offset:0 sc1\n"
"buffer_load_dwordx4 a[52:55], v222, s[20:23], s54 offen offset:1024 sc1\n"
"s_add_u32 s54, s8, 12845056\n"
"buffer_load_dwordx4 a[56:59], v227, s[20:23], s54 offen offset:0 sc1\n"
"buffer_load_dwordx4 a[60:63], v222, s[20:23], s54 offen offset:1024 sc1\n"
"s_waitcnt vmcnt(16)\n"
"v_mov_b32 v233, 4\n"
"s_mov_b64 exec, 1\n"
"buffer_store_dword v233, v231, s[20:23], s9 offen offset:0 sc1\n"
"s_mov_b64 exec, -1\n"
"s_nop 3\n"
"s_add_u32 s54, s8, 14680064\n"
"buffer_load_dwordx4 a[64:67], v227, s[20:23], s54 offen offset:0 sc1\n"
"buffer_load_dwordx4 a[68:71], v222, s[20:23], s54 offen offset:1024 sc1\n"
"s_add_u32 s54, s8, 16515072\n"
"buffer_load_dwordx4 a[72:75], v227, s[20:23], s54 offen offset:0 sc1\n"
"buffer_load_dwordx4 a[76:79], v222, s[20:23], s54 offen offset:1024 sc1\n"
"s_add_u32 s54, s8, 18350080\n"
"buffer_load_dwordx4 a[80:83], v227, s[20:23], s54 offen offset:0 sc1\n"
"buffer_load_dwordx4 a[84:87], v222, s[20:23], s54 offen offset:1024 sc1\n"
"s_add_u32 s54, s8, 20185088\n"
"buffer_load_dwordx4 a[88:91], v227, s[20:23], s54 offen offset:0 sc1\n"
"buffer_load_dwordx4 a[92:95], v222, s[20:23], s54 offen offset:1024 sc1\n"
"s_add_u32 s54, s8, 22020096\n"
"buffer_load_dwordx4 a[96:99], v227, s[20:23], s54 offen offset:0 sc1\n"
"buffer_load_dwordx4 a[100:103], v222, s[20:23], s54 offen offset:1024 sc1\n"
"s_add_u32 s54, s8, 23855104\n"
"buffer_load_dwordx4 a[104:107], v227, s[20:23], s54 offen offset:0 sc1\n"
"buffer_load_dwordx4 a[108:111], v222, s[20:23], s54 offen offset:1024 sc1\n"
"s_add_u32 s54, s8, 25690112\n"
"buffer_load_dwordx4 a[112:115], v227, s[20:23], s54 offen offset:0 sc1\n"
"buffer_load_dwordx4 a[116:119], v222, s[20:23], s54 offen offset:1024 sc1\n"
"s_add_u32 s54, s8, 27525120\n"
"buffer_load_dwordx4 a[120:123], v227, s[20:23], s54 offen offset:0 sc1\n"
"buffer_load_dwordx4 a[124:127], v222, s[20:23], s54 offen offset:1024 sc1\n"
"s_branch L_ysum%=\n"
"L_ylate%=:\n"
"s_waitcnt vmcnt(0)\n"
"v_mov_b32 v233, 4\n"
"s_mov_b64 exec, 1\n"
"buffer_store_dword v233, v231, s[20:23], s9 offen offset:0 sc1\n"
"s_mov_b64 exec, -1\n"
"s_nop 3\n"
"s_mov_b32 s55, 0\n"
"L_yw%=_chk:\n"
"v_max_u32 v239, v239, v225\n"
"v_cmp_lt_u32 vcc, s33, v239\n"
"s_nop 0\n"
"s_cmp_eq_u64 vcc, -1\n"
"s_cbranch_scc1 L_yw%=_ok\n"
"s_sleep 3\n"
"buffer_load_dword v239, v224, s[20:23], s10 offen offset:0 sc1\n"
"s_add_u32 s55, s55, 1\n"
"s_waitcnt vmcnt(0)\n"
"s_cmp_lt_u32 s55, 500000\n"
"s_cbranch_scc1 L_yw%=_chk\n"
"L_yw%=_ok:\n"
"s_add_u32 s54, s8, 0\n"
"buffer_load_dwordx4 a[0:3], v227, s[20:23], s54 offen offset:0 sc1\n"
"buffer_load_dwordx4 a[4:7], v222, s[20:23], s54 offen offset:1024 sc1\n"
"s_add_u32 s54, s8, 1835008\n"
"buffer_load_dwordx4 a[8:11], v227, s[20:23], s54 offen offset:0 sc1\n"
"buffer_load_dwordx4 a[12:15], v222, s[20:23], s54 offen offset:1024 sc1\n"
"s_add_u32 s54, s8, 3670016\n"
"buffer_load_dwordx4 a[16:19], v227, s[20:23], s54 offen offset:0 sc1\n"
"buffer_load_dwordx4 a[20:23], v222, s[20:23], s54 offen offset:1024 sc1\n"
"s_add_u32 s54, s8, 5505024\n"
"buffer_load_dwordx4 a[24:27], v227, s[20:23], s54 offen offset:0 sc1\n"
"buffer_load_dwordx4 a[28:31], v222, s[20:23], s54 offen offset:1024 sc1\n"
"s_add_u32 s54, s8, 7340032\n"
"buffer_load_dwordx4 a[32:35], v227, s[20:23], s54 offen offset:0 sc1\n"
"buffer_load_dwordx4 a[36:39], v222, s[20:23], s54 offen offset:1024 sc1\n"
"s_add_u32 s54, s8, 9175040\n"
"buffer_load_dwordx4 a[40:43], v227, s[20:23], s54 offen offset:0 sc1\n"
"buffer_load_dwordx4 a[44:47], v222, s[20:23], s54 offen offset:1024 sc1\n"
"s_add_u32 s54, s8, 11010048\n"
"buffer_load_dwordx4 a[48:51], v227, s[20:23], s54 offen offset:0 sc1\n"
"buffer_load_dwordx4 a[52:55], v222, s[20:23], s54 offen offset:1024 sc1\n"
"s_add_u32 s54, s8, 12845056\n"
"buffer_load_dwordx4 a[56:59], v227, s[20:23], s54 offen offset:0 sc1\n"
"buffer_load_dwordx4 a[60:63], v222, s[20:23], s54 offen offset:1024 sc1\n"
"s_add_u32 s54, s8, 14680064\n"
"buffer_load_dwordx4 a[64:67], v227, s[20:23], s54 offen offset:0 sc1\n"
"buffer_load_dwordx4 a[68:71], v222, s[20:23], s54 offen offset:1024 sc1\n"
"s_add_u32 s54, s8, 16515072\n"
"buffer_load_dwordx4 a[72:75], v227, s[20:23], s54 offen offset:0 sc1\n"
"buffer_load_dwordx4 a[76:79], v222, s[20:23], s54 offen offset:1024 sc1\n"
"s_add_u32 s54, s8, 18350080\n"
"buffer_load_dwordx4 a[80:83], v227, s[20:23], s54 offen offset:0 sc1\n"
"buffer_load_dwordx4 a[84:87], v222, s[20:23], s54 offen offset:1024 sc1\n"
"s_add_u32 s54, s8, 20185088\n"
"buffer_load_dwordx4 a[88:91], v227, s[20:23], s54 offen offset:0 sc1\n"
"buffer_load_dwordx4 a[92:95], v222, s[20:23], s54 offen offset:1024 sc1\n"
"s_add_u32 s54, s8, 22020096\n"
"buffer_load_dwordx4 a[96:99], v227, s[20:23], s54 offen offset:0 sc1\n"
"buffer_load_dwordx4 a[100:103], v222, s[20:23], s54 offen offset:1024 sc1\n"
"s_add_u32 s54, s8, 23855104\n"
"buffer_load_dwordx4 a[104:107], v227, s[20:23], s54 offen offset:0 sc1\n"
"buffer_load_dwordx4 a[108:111], v222, s[20:23], s54 offen offset:1024 sc1\n"
"s_add_u32 s54, s8, 25690112\n"
"buffer_load_dwordx4 a[112:115], v227, s[20:23], s54 offen offset:0 sc1\n"
"buffer_load_dwordx4 a[116:119], v222, s[20:23], s54 offen offset:1024 sc1\n"
"s_add_u32 s54, s8, 27525120\n"
"buffer_load_dwordx4 a[120:123], v227, s[20:23], s54 offen offset:0 sc1\n"
"buffer_load_dwordx4 a[124:127], v222, s[20:23], s54 offen offset:1024 sc1\n"
"L_ysum%=:\n"
"s_waitcnt vmcnt(31)\n"
"v_mfma_f32_16x16x32_bf16 v[176:179], v[216:219], a[0:3], v[176:179]\n"
"v_mfma_f32_16x16x32_bf16 v[180:183], v[214:217], a[0:3], v[180:183]\n"
"s_waitcnt vmcnt(30)\n"
"v_mfma_f32_16x16x32_bf16 v[184:187], v[216:219], a[4:7], v[184:187]\n"
"v_mfma_f32_16x16x32_bf16 v[188:191], v[214:217], a[4:7], v[188:191]\n"
"s_waitcnt vmcnt(29)\n"
"v_mfma_f32_16x16x32_bf16 v[176:179], v[216:219], a[8:11], v[176:179]\n"
"v_mfma_f32_16x16x32_bf16 v[180:183], v[214:217], a[8:11], v[180:183]\n"
"s_waitcnt vmcnt(28)\n"
"v_mfma_f32_16x16x32_bf16 v[184:187], v[216:219], a[12:15], v[184:187]\n"
"v_mfma_f32_16x16x32_bf16 v[188:191], v[214:217], a[12:15], v[188:191]\n"
"s_waitcnt vmcnt(27)\n"
"v_mfma_f32_16x16x32_bf16 v[176:179], v[216:219], a[16:19], v[176:179]\n"
"v_mfma_f32_16x16x32_bf16 v[180:183], v[214:217], a[16:19], v[180:183]\n"
"s_waitcnt vmcnt(26)\n"
"v_mfma_f32_16x16x32_bf16 v[184:187], v[216:219], a[20:23], v[184:187]\n"
"v_mfma_f32_16x16x32_bf16 v[188:191], v[214:217], a[20:23], v[188:191]\n"
"s_waitcnt vmcnt(25)\n"
"v_mfma_f32_16x16x32_bf16 v[176:179], v[216:219], a[24:27], v[176:179]\n"
"v_mfma_f32_16x16x32_bf16 v[180:183], v[214:217], a[24:27], v[180:183]\n"
"s_waitcnt vmcnt(24)\n"
"v_mfma_f32_16x16x32_bf16 v[184:187], v[216:219], a[28:31], v[184:187]\n"
"v_mfma_f32_16x16x32_bf16 v[188:191], v[214:217], a[28:31], v[188:191]\n"
"s_waitcnt vmcnt(23)\n"
"v_mfma_f32_16x16x32_bf16 v[176:179], v[216:219], a[32:35], v[176:179]\n"
"v_mfma_f32_16x16x32_bf16 v[180:183], v[214:217], a[32:35], v[180:183]\n"
"s_waitcnt vmcnt(22)\n"
"v_mfma_f32_16x16x32_bf16 v[184:187], v[216:219], a[36:39], v[184:187]\n"
"v_mfma_f32_16x16x32_bf16 v[188:191], v[214:217], a[36:39], v[188:191]\n"
"s_waitcnt vmcnt(21)\n"
"v_mfma_f32_16x16x32_bf16 v[176:179], v[216:219], a[40:43], v[176:179]\n"
"v_mfma_f32_16x16x32_bf16 v[180:183], v[214:217], a[40:43], v[180:183]\n"
"s_waitcnt vmcnt(20)\n"
"v_mfma_f32_16x16x32_bf16 v[184:187], v[216:219], a[44:47], v[184:187]\n"
"v_mfma_f32_16x16x32_bf16 v[188:191], v[214:217], a[44:47], v[188:191]\n"
"s_waitcnt vmcnt(19)\n"
"v_mfma_f32_16x16x32_bf16 v[176:179], v[216:219], a[48:51], v[176:179]\n"
"v_mfma_f32_16x16x32_bf16 v[180:183], v[214:217], a[48:51], v[180:183]\n"
"s_waitcnt vmcnt(18)\n"
"v_mfma_f32_16x16x32_bf16 v[184:187], v[216:219], a[52:55], v[184:187]\n"
"v_mfma_f32_16x16x32_bf16 v[188:191], v[214:217], a[52:55], v[188:191]\n"
"s_waitcnt vmcnt(17)\n"
"v_mfma_f32_16x16x32_bf16 v[176:179], v[216:219], a[56:59], v[176:179]\n"
"v_mfma_f32_16x16x32_bf16 v[180:183], v[214:217], a[56:59], v[180:183]\n"
"s_waitcnt vmcnt(16)\n"
"v_mfma_f32_16x16x32_bf16 v[184:187], v[216:219], a[60:63], v[184:187]\n"
"v_mfma_f32_16x16x32_bf16 v[188:191], v[214:217], a[60:63], v[188:191]\n"
"s_waitcnt vmcnt(15)\n"
"v_mfma_f32_16x16x32_bf16 v[176:179], v[216:219], a[64:67], v[176:179]\n"
"v_mfma_f32_16x16x32_bf16 v[180:183], v[214:217], a[64:67], v[180:183]\n"
"s_waitcnt vmcnt(14)\n"
"v_mfma_f32_16x16x32_bf16 v[184:187], v[216:219], a[68:71], v[184:187]\n"
"v_mfma_f32_16x16x32_bf16 v[188:191], v[214:217], a[68:71], v[188:191]\n"
"s_waitcnt vmcnt(13)\n"
"v_mfma_f32_16x16x32_bf16 v[176:179], v[216:219], a[72:75], v[176:179]\n"
"v_mfma_f32_16x16x32_bf16 v[180:183], v[214:217], a[72:75], v[180:183]\n"
"s_waitcnt vmcnt(12)\n"
"v_mfma_f32_16x16x32_bf16 v[184:187], v[216:219], a[76:79], v[184:187]\n"
"v_mfma_f32_16x16x32_bf16 v[188:191], v[214:217], a[76:79], v[188:191]\n"
"s_waitcnt vmcnt(11)\n"
"v_mfma_f32_16x16x32_bf16 v[176:179], v[216:219], a[80:83], v[176:179]\n"
"v_mfma_f32_16x16x32_bf16 v[180:183], v[214:217], a[80:83], v[180:183]\n"
"s_waitcnt vmcnt(10)\n"
"v_mfma_f32_16x16x32_bf16 v[184:187], v[216:219], a[84:87], v[184:187]\n"
"v_mfma_f32_16x16x32_bf16 v[188:191], v[214:217], a[84:87], v[188:191]\n"
"s_waitcnt vmcnt(9)\n"
"v_mfma_f32_16x16x32_bf16 v[176:179], v[216:219], a[88:91], v[176:179]\n"
"v_mfma_f32_16x16x32_bf16 v[180:183], v[214:217], a[88:91], v[180:183]\n"
"s_waitcnt vmcnt(8)\n"
"v_mfma_f32_16x16x32_bf16 v[184:187], v[216:219], a[92:95], v[184:187]\n"
"v_mfma_f32_16x16x32_bf16 v[188:191], v[214:217], a[92:95], v[188:191]\n"
"s_waitcnt vmcnt(7)\n"
"v_mfma_f32_16x16x32_bf16 v[176:179], v[216:219], a[96:99], v[176:179]\n"
"v_mfma_f32_16x16x32_bf16 v[180:183], v[214:217], a[96:99], v[180:183]\n"
"s_waitcnt vmcnt(6)\n"
"v_mfma_f32_16x16x32_bf16 v[184:187], v[216:219], a[100:103], v[184:187]\n"
"v_mfma_f32_16x16x32_bf16 v[188:191], v[214:217], a[100:103], v[188:191]\n"
"s_waitcnt vmcnt(5)\n"
"v_mfma_f32_16x16x32_bf16 v[176:179], v[216:219], a[104:107], v[176:179]\n"
"v_mfma_f32_16x16x32_bf16 v[180:183], v[214:217], a[104:107], v[180:183]\n"
"s_waitcnt vmcnt(4)\n"
"v_mfma_f32_16x16x32_bf16 v[184:187], v[216:219], a[108:111], v[184:187]\n"
"v_mfma_f32_16x16x32_bf16 v[188:191], v[214:217], a[108:111], v[188:191]\n"
"s_waitcnt vmcnt(3)\n"
"v_mfma_f32_16x16x32_bf16 v[176:179], v[216:219], a[112:115], v[176:179]\n"
"v_mfma_f32_16x16x32_bf16 v[180:183], v[214:217], a[112:115], v[180:183]\n"
"s_waitcnt vmcnt(2)\n"
"v_mfma_f32_16x16x32_bf16 v[184:187], v[216:219], a[116:119], v[184:187]\n"
"v_mfma_f32_16x16x32_bf16 v[188:191], v[214:217], a[116:119], v[188:191]\n"
"s_waitcnt vmcnt(1)\n"
"v_mfma_f32_16x16x32_bf16 v[176:179], v[216:219], a[120:123], v[176:179]\n"
"v_mfma_f32_16x16x32_bf16 v[180:183], v[214:217], a[120:123], v[180:183]\n"
"s_waitcnt vmcnt(0)\n"
"v_mfma_f32_16x16x32_bf16 v[184:187], v[216:219], a[124:127], v[184:187]\n"
"v_mfma_f32_16x16x32_bf16 v[188:191], v[214:217], a[124:127], v[188:191]\n"
"s_nop 3\n"
"v_cvt_pk_bf16_f32 v48, v176, v177\n"
"v_cvt_pk_bf16_f32 v49, v178, v179\n"
"buffer_store_dwordx2 v[48:49], v220, s[88:91], s4 offen offset:0\n"
"v_cvt_pk_bf16_f32 v50, v180, v181\n"
"v_cvt_pk_bf16_f32 v51, v182, v183\n"
"buffer_store_dwordx2 v[50:51], v220, s[88:91], s5 offen offset:0\n"
"v_cvt_pk_bf16_f32 v52, v184, v185\n"
"v_cvt_pk_bf16_f32 v53, v186, v187\n"
"buffer_store_dwordx2 v[52:53], v221, s[88:91], s4 offen offset:0\n"
"v_cvt_pk_bf16_f32 v54, v188, v189\n"
"v_cvt_pk_bf16_f32 v55, v190, v191\n"
"buffer_store_dwordx2 v[54:55], v221, s[88:91], s5 offen offset:0\n"
"s_branch L_end%=\n"
"L_half%=:\n"
"s_waitcnt lgkmcnt(0)\n"
"s_nop 1\n"
"v_mfma_f32_16x16x32_bf16 v[68:71], v[0:3], a[132:135], v[100:103]\n"
"s_mov_b32 s58, 0\n"
"ds_read_b128 v[32:35], v232 offset:8192\n"
"v_readlane_b32 s49, v224, s48\n"
"v_mfma_f32_16x16x32_bf16 v[68:71], v[4:7], a[140:143], v[68:71]\n"
"s_cmp_ge_u32 s48, s42\n"
"s_cbranch_scc1 L_sw5%=_go\n"
"L_sw5%=_ret:\n"
"ds_read_b128 v[36:39], v232 offset:9216\n"
"v_mfma_f32_16x16x32_bf16 v[68:71], v[8:11], a[148:151], v[68:71]\n"
"ds_read_b128 v[40:43], v232 offset:10240\n"
"v_add_f32 v233, v233, v235\n"
"v_mfma_f32_16x16x32_bf16 v[68:71], v[12:15], a[156:159], v[68:71]\n"
"s_add_u32 s48, s48, 1\n"
"ds_read_b128 v[44:47], v232 offset:11264\n"
"v_add_f32 v234, v234, v236\n"
"v_mfma_f32_16x16x32_bf16 v[68:71], v[16:19], a[164:167], v[68:71]\n"
"ds_read_b128 v[48:51], v232 offset:12288\n"
"v_cvt_pk_bf16_f32 v128, v144, v145\n"
"v_mfma_f32_16x16x32_bf16 v[68:71], v[20:23], a[172:175], v[68:71]\n"
"ds_read_b128 v[52:55], v232 offset:13312\n"
"v_cvt_pk_bf16_f32 v129, v146, v147\n"
"v_mfma_f32_16x16x32_bf16 v[68:71], v[24:27], a[180:183], v[68:71]\n"
"ds_read_b128 v[56:59], v232 offset:14336\n"
"v_cvt_pk_bf16_f32 v136, v148, v149\n"
"v_mfma_f32_16x16x32_bf16 v[68:71], v[28:31], a[188:191], v[68:71]\n"
"ds_read_b128 v[60:63], v232 offset:15360\n"
"v_cvt_pk_bf16_f32 v137, v150, v151\n"
"s_waitcnt lgkmcnt(7)\n"
"v_mfma_f32_16x16x32_bf16 v[76:79], v[32:35], a[132:135], v[108:111]\n"
"ds_read_b128 v[0:3], v232 offset:16384\n"
"v_cvt_pk_bf16_f32 v130, v152, v153\n"
"s_waitcnt lgkmcnt(7)\n"
"v_mfma_f32_16x16x32_bf16 v[76:79], v[36:39], a[140:143], v[76:79]\n"
"ds_read_b128 v[4:7], v232 offset:17408\n"
"buffer_load_dwordx4 v208, s[24:27], s49 offen offset:0 sc1 lds\n"
"s_waitcnt lgkmcnt(7)\n"
"v_mfma_f32_16x16x32_bf16 v[76:79], v[40:43], a[148:151], v[76:79]\n"
"ds_read_b128 v[8:11], v232 offset:18432\n"
"v_cvt_pk_bf16_f32 v131, v154, v155\n"
"s_waitcnt lgkmcnt(7)\n"
"v_mfma_f32_16x16x32_bf16 v[76:79], v[44:47], a[156:159], v[76:79]\n"
"ds_read_b128 v[12:15], v232 offset:19456\n"
"v_cvt_pk_bf16_f32 v138, v156, v157\n"
"s_waitcnt lgkmcnt(6)\n"
"v_mfma_f32_16x16x32_bf16 v[76:79], v[48:51], a[164:167], v[76:79]\n"
"ds_read_b128 v[16:19], v232 offset:20480\n"
"buffer_load_dwordx4 v209, s[24:27], s49 offen offset:1024 sc1 lds\n"
"v_mfma_f32_16x16x32_bf16 v[76:79], v[52:55], a[172:175], v[76:79]\n"
"ds_read_b128 v[20:23], v232 offset:21504\n"
"v_cvt_pk_bf16_f32 v139, v158, v159\n"
"s_waitcnt lgkmcnt(6)\n"
"v_mfma_f32_16x16x32_bf16 v[76:79], v[56:59], a[180:183], v[76:79]\n"
"ds_read_b128 v[24:27], v232 offset:22528\n"
"v_cvt_pk_bf16_f32 v132, v160, v161\n"
"v_mfma_f32_16x16x32_bf16 v[76:79], v[60:63], a[188:191], v[76:79]\n"
"ds_read_b128 v[28:31], v232 offset:23552\n"
"buffer_load_dwordx4 v210, s[24:27], s49 offen offset:2048 sc1 lds\n"
"s_waitcnt lgkmcnt(6)\n"
"v_mfma_f32_16x16x32_bf16 v[84:87], v[0:3], a[132:135], v[116:119]\n"
"ds_read_b128 v[32:35], v232 offset:24576\n"
"v_cvt_pk_bf16_f32 v133, v162, v163\n"
"v_mfma_f32_16x16x32_bf16 v[84:87], v[4:7], a[140:143], v[84:87]\n"
"ds_read_b128 v[36:39], v232 offset:25600\n"
"v_cvt_pk_bf16_f32 v140, v164, v165\n"
"s_waitcnt lgkmcnt(7)\n"
"v_mfma_f32_16x16x32_bf16 v[84:87], v[8:11], a[148:151], v[84:87]\n"
"ds_read_b128 v[40:43], v232 offset:26624\n"
"buffer_load_dwordx4 v211, s[24:27], s49 offen offset:3072 sc1 lds\n"
"s_waitcnt lgkmcnt(7)\n"
"v_mfma_f32_16x16x32_bf16 v[84:87], v[12:15], a[156:159], v[84:87]\n"
"s_add_u32 m0, m0, 4096\n"
"ds_read_b128 v[44:47], v232 offset:27648\n"
"v_cvt_pk_bf16_f32 v141, v166, v167\n"
"s_waitcnt lgkmcnt(6)\n"
"v_mfma_f32_16x16x32_bf16 v[84:87], v[16:19], a[164:167], v[84:87]\n"
"ds_read_b128 v[48:51], v232 offset:28672\n"
"v_cvt_pk_bf16_f32 v134, v168, v169\n"
"v_mfma_f32_16x16x32_bf16 v[84:87], v[20:23], a[172:175], v[84:87]\n"
"ds_read_b128 v[52:55], v232 offset:29696\n"
"buffer_load_dwordx4 v212, s[24:27], s49 offen offset:0 sc1 lds\n"
"s_waitcnt lgkmcnt(6)\n"
"v_mfma_f32_16x16x32_bf16 v[84:87], v[24:27], a[180:183], v[84:87]\n"
"ds_read_b128 v[56:59], v232 offset:30720\n"
"v_cvt_pk_bf16_f32 v135, v170, v171\n"
"v_mfma_f32_16x16x32_bf16 v[84:87], v[28:31], a[188:191], v[84:87]\n"
"ds_read_b128 v[60:63], v232 offset:31744\n"
"v_cvt_pk_bf16_f32 v142, v172, v173\n"
"s_waitcnt lgkmcnt(6)\n"
"v_mfma_f32_16x16x32_bf16 v[92:95], v[32:35], a[132:135], v[124:127]\n"
"s_add_u32 s54, s50, s47\n"
"s_add_u32 s50, s50, 32768\n"
"v_cvt_pk_bf16_f32 v143, v174, v175\n"
"v_mfma_f32_16x16x32_bf16 v[92:95], v[36:39], a[140:143], v[92:95]\n"
"s_cmp_lt_u32 s50, 163840\n"
"s_cselect_b32 s50, s50, 0\n"
"s_waitcnt lgkmcnt(4)\n"
"v_mfma_f32_16x16x32_bf16 v[92:95], v[40:43], a[148:151], v[92:95]\n"
"v_add_u32 v232, s50, v227\n"
"v_mfma_f32_16x16x32_bf16 v[92:95], v[44:47], a[156:159], v[92:95]\n"
"s_waitcnt lgkmcnt(0)\n"
"s_waitcnt vmcnt(21)\n"
"s_barrier\n"
"v_mfma_f32_16x16x32_bf16 v[92:95], v[48:51], a[164:167], v[92:95]\n"
"ds_read_b128 v[0:3], v232 offset:0\n"
"ds_read_b128 v[4:7], v232 offset:1024\n"
"v_mfma_f32_16x16x32_bf16 v[92:95], v[52:55], a[172:175], v[92:95]\n"
"ds_read_b128 v[8:11], v232 offset:2048\n"
"ds_read_b128 v[12:15], v232 offset:3072\n"
"v_mfma_f32_16x16x32_bf16 v[92:95], v[56:59], a[180:183], v[92:95]\n"
"ds_read_b128 v[16:19], v232 offset:4096\n"
"ds_read_b128 v[20:23], v232 offset:5120\n"
"v_mfma_f32_16x16x32_bf16 v[92:95], v[60:63], a[188:191], v[92:95]\n"
"ds_read_b128 v[24:27], v232 offset:6144\n"
"ds_read_b128 v[28:31], v232 offset:7168\n"
"buffer_load_dwordx4 v213, s[24:27], s49 offen offset:1024 sc1 lds\n"
"buffer_load_dwordx4 v214, s[24:27], s49 offen offset:2048 sc1 lds\n"
"buffer_load_dwordx4 v215, s[24:27], s49 offen offset:3072 sc1 lds\n"
"s_mov_b32 m0, s54\n"
"s_waitcnt lgkmcnt(7)\n"
"v_mfma_f32_16x16x32_bf16 a[0:3], v[0:3], v[128:131], a[0:3]\n"
"ds_read_b128 v[32:35], v232 offset:8192\n"
"v_mov_b32 v235, 0\n"
"s_waitcnt lgkmcnt(7)\n"
"v_mfma_f32_16x16x32_bf16 a[0:3], v[4:7], v[132:135], a[0:3]\n"
"ds_read_b128 v[36:39], v232 offset:9216\n"
"v_readlane_b32 s49, v224, s48\n"
"v_mfma_f32_16x16x32_bf16 a[4:7], v[4:7], v[140:143], a[4:7]\n"
"s_cmp_ge_u32 s48, s42\n"
"s_cbranch_scc1 L_sw6%=_go\n"
"L_sw6%=_ret:\n"
"v_mfma_f32_16x16x32_bf16 a[4:7], v[0:3], v[136:139], a[4:7]\n"
"ds_read_b128 v[40:43], v232 offset:10240\n"
"v_mov_b32 v64, 0xff800000\n"
"s_waitcnt lgkmcnt(7)\n"
"v_mfma_f32_16x16x32_bf16 a[8:11], v[8:11], v[128:131], a[8:11]\n"
"s_add_u32 s48, s48, 1\n"
"ds_read_b128 v[44:47], v232 offset:11264\n"
"v_mov_b32 v65, 0xff800000\n"
"v_mfma_f32_16x16x32_bf16 a[8:11], v[12:15], v[132:135], a[8:11]\n"
"v_exp_f32 v148, v68\n"
"v_mfma_f32_16x16x32_bf16 a[12:15], v[12:15], v[140:143], a[12:15]\n"
"ds_read_b128 v[48:51], v232 offset:12288\n"
"v_mov_b32 v66, 0xff800000\n"
"v_mfma_f32_16x16x32_bf16 a[12:15], v[8:11], v[136:139], a[12:15]\n"
"s_waitcnt lgkmcnt(5)\n"
"ds_read_b128 v[52:55], v232 offset:13312\n"
"v_mov_b32 v67, 0xff800000\n"
"v_mfma_f32_16x16x32_bf16 a[16:19], v[16:19], v[128:131], a[16:19]\n"
"buffer_load_dwordx4 v208, s[24:27], s49 offen offset:0 sc1 lds\n"
"v_mfma_f32_16x16x32_bf16 a[16:19], v[20:23], v[132:135], a[16:19]\n"
"ds_read_b128 v[56:59], v232 offset:14336\n"
"v_mfma_f32_16x16x32_bf16 a[20:23], v[20:23], v[140:143], a[20:23]\n"
"ds_read_b128 v[60:63], v232 offset:15360\n"
"v_mfma_f32_16x16x32_bf16 a[20:23], v[16:19], v[136:139], a[20:23]\n"
"buffer_load_dwordx4 v209, s[24:27], s49 offen offset:1024 sc1 lds\n"
"v_mfma_f32_16x16x32_bf16 a[24:27], v[24:27], v[128:131], a[24:27]\n"
"v_exp_f32 v149, v69\n"
"v_mfma_f32_16x16x32_bf16 a[24:27], v[28:31], v[132:135], a[24:27]\n"
"v_exp_f32 v150, v70\n"
"v_mfma_f32_16x16x32_bf16 a[28:31], v[28:31], v[140:143], a[28:31]\n"
"v_exp_f32 v151, v71\n"
"v_mfma_f32_16x16x32_bf16 a[28:31], v[24:27], v[136:139], a[28:31]\n"
"s_waitcnt lgkmcnt(4)\n"
"v_add_f32 v236, v148, v149\n"
"v_add_f32 v236, v236, v150\n"
"v_mfma_f32_16x16x32_bf16 a[32:35], v[32:35], v[128:131], a[32:35]\n"
"ds_read_b128 v[0:3], v232 offset:16384\n"
"v_add_f32 v236, v236, v151\n"
"v_mfma_f32_16x16x32_bf16 a[32:35], v[36:39], v[132:135], a[32:35]\n"
"ds_read_b128 v[4:7], v232 offset:17408\n"
"v_mov_b32 v72, 0xff800000\n"
"v_mfma_f32_16x16x32_bf16 a[36:39], v[36:39], v[140:143], a[36:39]\n"
"buffer_load_dwordx4 v210, s[24:27], s49 offen offset:2048 sc1 lds\n"
"v_mfma_f32_16x16x32_bf16 a[36:39], v[32:35], v[136:139], a[36:39]\n"
"ds_read_b128 v[8:11], v232 offset:18432\n"
"v_mov_b32 v73, 0xff800000\n"
"v_mfma_f32_16x16x32_bf16 a[40:43], v[40:43], v[128:131], a[40:43]\n"
"ds_read_b128 v[12:15], v232 offset:19456\n"
"v_mov_b32 v74, 0xff800000\n"
"v_mfma_f32_16x16x32_bf16 a[40:43], v[44:47], v[132:135], a[40:43]\n"
"buffer_load_dwordx4 v211, s[24:27], s49 offen offset:3072 sc1 lds\n"
"v_mfma_f32_16x16x32_bf16 a[44:47], v[44:47], v[140:143], a[44:47]\n"
"ds_read_b128 v[16:19], v232 offset:20480\n"
"v_mov_b32 v75, 0xff800000\n"
"v_mfma_f32_16x16x32_bf16 a[44:47], v[40:43], v[136:139], a[44:47]\n"
"s_waitcnt lgkmcnt(5)\n"
"ds_read_b128 v[20:23], v232 offset:21504\n"
"v_mov_b32 v80, 0xff800000\n"
"v_mfma_f32_16x16x32_bf16 a[48:51], v[48:51], v[128:131], a[48:51]\n"
"s_add_u32 m0, m0, 4096\n"
"v_exp_f32 v156, v76\n"
"v_mfma_f32_16x16x32_bf16 a[48:51], v[52:55], v[132:135], a[48:51]\n"
"ds_read_b128 v[24:27], v232 offset:22528\n"
"v_add_f32 v236, v236, v156\n"
"v_mfma_f32_16x16x32_bf16 a[52:55], v[52:55], v[140:143], a[52:55]\n"
"ds_read_b128 v[28:31], v232 offset:23552\n"
"v_mov_b32 v81, 0xff800000\n"
"v_mfma_f32_16x16x32_bf16 a[52:55], v[48:51], v[136:139], a[52:55]\n"
"buffer_load_dwordx4 v212, s[24:27], s49 offen offset:0 sc1 lds\n"
"v_mfma_f32_16x16x32_bf16 a[56:59], v[56:59], v[128:131], a[56:59]\n"
"v_exp_f32 v157, v77\n"
"v_mfma_f32_16x16x32_bf16 a[56:59], v[60:63], v[132:135], a[56:59]\n"
"v_exp_f32 v158, v78\n"
"v_mfma_f32_16x16x32_bf16 a[60:63], v[60:63], v[140:143], a[60:63]\n"
"v_exp_f32 v159, v79\n"
"v_mfma_f32_16x16x32_bf16 a[60:63], v[56:59], v[136:139], a[60:63]\n"
"s_waitcnt lgkmcnt(4)\n"
"v_exp_f32 v164, v84\n"
"v_mfma_f32_16x16x32_bf16 a[64:67], v[0:3], v[128:131], a[64:67]\n"
"ds_read_b128 v[32:35], v232 offset:24576\n"
"v_add_f32 v236, v236, v157\n"
"v_mfma_f32_16x16x32_bf16 a[64:67], v[4:7], v[132:135], a[64:67]\n"
"ds_read_b128 v[36:39], v232 offset:25600\n"
"v_add_f32 v236, v236, v158\n"
"v_mfma_f32_16x16x32_bf16 a[68:71], v[4:7], v[140:143], a[68:71]\n"
"ds_read_b128 v[40:43], v232 offset:26624\n"
"v_add_f32 v236, v236, v159\n"
"v_mfma_f32_16x16x32_bf16 a[68:71], v[0:3], v[136:139], a[68:71]\n"
"ds_read_b128 v[44:47], v232 offset:27648\n"
"buffer_load_dwordx4 v213, s[24:27], s49 offen offset:1024 sc1 lds\n"
"v_mfma_f32_16x16x32_bf16 a[72:75], v[8:11], v[128:131], a[72:75]\n"
"ds_read_b128 v[48:51], v232 offset:28672\n"
"v_mov_b32 v82, 0xff800000\n"
"v_mfma_f32_16x16x32_bf16 a[72:75], v[12:15], v[132:135], a[72:75]\n"
"ds_read_b128 v[52:55], v232 offset:29696\n"
"v_mov_b32 v83, 0xff800000\n"
"v_mfma_f32_16x16x32_bf16 a[76:79], v[12:15], v[140:143], a[76:79]\n"
"ds_read_b128 v[56:59], v232 offset:30720\n"
"v_add_f32 v236, v236, v164\n"
"v_mfma_f32_16x16x32_bf16 a[76:79], v[8:11], v[136:139], a[76:79]\n"
"s_waitcnt lgkmcnt(7)\n"
"ds_read_b128 v[60:63], v232 offset:31744\n"
"v_mov_b32 v88, 0xff800000\n"
"v_mfma_f32_16x16x32_bf16 a[80:83], v[16:19], v[128:131], a[80:83]\n"
"buffer_load_dwordx4 v214, s[24:27], s49 offen offset:2048 sc1 lds\n"
"v_mfma_f32_16x16x32_bf16 a[80:83], v[20:23], v[132:135], a[80:83]\n"
"buffer_load_dwordx4 v215, s[24:27], s49 offen offset:3072 sc1 lds\n"
"v_mfma_f32_16x16x32_bf16 a[84:87], v[20:23], v[140:143], a[84:87]\n"
"s_add_u32 s54, s50, s47\n"
"v_exp_f32 v165, v85\n"
"v_mfma_f32_16x16x32_bf16 a[84:87], v[16:19], v[136:139], a[84:87]\n"
"s_add_u32 s50, s50, 32768\n"
"v_exp_f32 v166, v86\n"
"v_mfma_f32_16x16x32_bf16 a[88:91], v[24:27], v[128:131], a[88:91]\n"
"s_cmp_lt_u32 s50, 163840\n"
"s_cselect_b32 s50, s50, 0\n"
"v_add_f32 v236, v236, v165\n"
"v_mfma_f32_16x16x32_bf16 a[88:91], v[28:31], v[132:135], a[88:91]\n"
"s_mov_b32 m0, s54\n"
"v_exp_f32 v167, v87\n"
"v_mfma_f32_16x16x32_bf16 a[92:95], v[28:31], v[140:143], a[92:95]\n"
"s_waitcnt lgkmcnt(0)\n"
"v_add_u32 v232, s50, v227\n"
"v_add_f32 v236, v236, v166\n"
"v_mfma_f32_16x16x32_bf16 a[92:95], v[24:27], v[136:139], a[92:95]\n"
"s_waitcnt vmcnt(24)\n"
"s_barrier\n"
"v_mfma_f32_16x16x32_bf16 a[96:99], v[32:35], v[128:131], a[96:99]\n"
"ds_read_b128 v[0:3], v232 offset:0\n"
"v_add_f32 v236, v236, v167\n"
"v_mfma_f32_16x16x32_bf16 a[96:99], v[36:39], v[132:135], a[96:99]\n"
"ds_read_b128 v[4:7], v232 offset:1024\n"
"v_mov_b32 v89, 0xff800000\n"
"v_mfma_f32_16x16x32_bf16 a[100:103], v[36:39], v[140:143], a[100:103]\n"
"v_exp_f32 v172, v92\n"
"v_mfma_f32_16x16x32_bf16 a[100:103], v[32:35], v[136:139], a[100:103]\n"
"ds_read_b128 v[8:11], v232 offset:2048\n"
"v_mov_b32 v90, 0xff800000\n"
"v_mfma_f32_16x16x32_bf16 a[104:107], v[40:43], v[128:131], a[104:107]\n"
"ds_read_b128 v[12:15], v232 offset:3072\n"
"v_mov_b32 v91, 0xff800000\n"
"v_mfma_f32_16x16x32_bf16 a[104:107], v[44:47], v[132:135], a[104:107]\n"
"v_exp_f32 v173, v93\n"
"v_mfma_f32_16x16x32_bf16 a[108:111], v[44:47], v[140:143], a[108:111]\n"
"ds_read_b128 v[16:19], v232 offset:4096\n"
"v_add_f32 v236, v236, v172\n"
"v_mfma_f32_16x16x32_bf16 a[108:111], v[40:43], v[136:139], a[108:111]\n"
"ds_read_b128 v[20:23], v232 offset:5120\n"
"v_add_f32 v236, v236, v173\n"
"v_mfma_f32_16x16x32_bf16 a[112:115], v[48:51], v[128:131], a[112:115]\n"
"v_exp_f32 v174, v94\n"
"v_mfma_f32_16x16x32_bf16 a[112:115], v[52:55], v[132:135], a[112:115]\n"
"s_add_u32 s52, s52, 1\n"
"ds_read_b128 v[24:27], v232 offset:6144\n"
"v_add_f32 v236, v236, v174\n"
"v_mfma_f32_16x16x32_bf16 a[116:119], v[52:55], v[140:143], a[116:119]\n"
"s_sub_u32 s51, s51, 1\n"
"ds_read_b128 v[28:31], v232 offset:7168\n"
"v_mfma_f32_16x16x32_bf16 a[116:119], v[48:51], v[136:139], a[116:119]\n"
"v_exp_f32 v175, v95\n"
"v_mfma_f32_16x16x32_bf16 a[120:123], v[56:59], v[128:131], a[120:123]\n"
"v_add_f32 v236, v236, v175\n"
"v_cmp_lt_f32 vcc, s56, v236\n"
"v_mfma_f32_16x16x32_bf16 a[120:123], v[60:63], v[132:135], a[120:123]\n"
"v_mfma_f32_16x16x32_bf16 a[124:127], v[60:63], v[140:143], a[124:127]\n"
"v_mfma_f32_16x16x32_bf16 a[124:127], v[56:59], v[136:139], a[124:127]\n"
"s_waitcnt lgkmcnt(4)\n"
"s_waitcnt lgkmcnt(0)\n"
"s_branch L_tail%=\n"
"L_w13b%=:\n"
"s_waitcnt lgkmcnt(0)\n"
"v_mfma_f32_16x16x32_bf16 v[64:67], v[16:19], a[96:99], v[64:67]\n"
"s_add_u32 s55, s43, 37748736\n"
"ds_read_b64_tr_b16 v[48:49], v232 offset:128\n"
"ds_read_b64_tr_b16 v[50:51], v232 offset:3712\n"
"v_mfma_f32_16x16x32_bf16 v[64:67], v[20:23], a[104:107], v[64:67]\n"
"ds_read_b64_tr_b16 v[52:53], v232 offset:8320\n"
"ds_read_b64_tr_b16 v[54:55], v232 offset:11904\n"
"v_mfma_f32_16x16x32_bf16 v[68:71], v[20:23], a[108:111], v[68:71]\n"
"ds_read_b64_tr_b16 v[56:57], v232 offset:160\n"
"ds_read_b64_tr_b16 v[58:59], v232 offset:3744\n"
"v_mfma_f32_16x16x32_bf16 v[68:71], v[16:19], a[100:103], v[68:71]\n"
"ds_read_b64_tr_b16 v[60:61], v232 offset:8352\n"
"ds_read_b64_tr_b16 v[62:63], v232 offset:11936\n"
"v_mfma_f32_16x16x32_bf16 v[72:75], v[24:27], a[96:99], v[72:75]\n"
"ds_read_b64_tr_b16 v[0:1], v232 offset:192\n"
"ds_read_b64_tr_b16 v[2:3], v232 offset:3776\n"
"v_mfma_f32_16x16x32_bf16 v[72:75], v[28:31], a[104:107], v[72:75]\n"
"ds_read_b64_tr_b16 v[4:5], v232 offset:8384\n"
"ds_read_b64_tr_b16 v[6:7], v232 offset:11968\n"
"v_mfma_f32_16x16x32_bf16 v[76:79], v[28:31], a[108:111], v[76:79]\n"
"s_add_u32 s54, s50, s47\n"
"buffer_load_dwordx4 a[128:131], v227, s[20:23], s55 offen offset:0 sc1\n"
"v_mfma_f32_16x16x32_bf16 v[76:79], v[24:27], a[100:103], v[76:79]\n"
"s_add_u32 s50, s50, 32768\n"
"v_mfma_f32_16x16x32_bf16 v[80:83], v[32:35], a[96:99], v[80:83]\n"
"s_cmp_lt_u32 s50, 163840\n"
"s_cselect_b32 s50, s50, 0\n"
"v_mfma_f32_16x16x32_bf16 v[80:83], v[36:39], a[104:107], v[80:83]\n"
"buffer_load_dwordx4 a[132:135], v227, s[20:23], s55 offen offset:1024 sc1\n"
"v_mfma_f32_16x16x32_bf16 v[84:87], v[36:39], a[108:111], v[84:87]\n"
"buffer_load_dwordx4 a[136:139], v227, s[20:23], s55 offen offset:2048 sc1\n"
"v_mfma_f32_16x16x32_bf16 v[84:87], v[32:35], a[100:103], v[84:87]\n"
"buffer_load_dwordx4 a[140:143], v227, s[20:23], s55 offen offset:3072 sc1\n"
"v_mfma_f32_16x16x32_bf16 v[88:91], v[40:43], a[96:99], v[88:91]\n"
"s_add_u32 s55, s43, 37752832\n"
"v_mfma_f32_16x16x32_bf16 v[88:91], v[44:47], a[104:107], v[88:91]\n"
"buffer_load_dwordx4 a[144:147], v227, s[20:23], s55 offen offset:0 sc1\n"
"v_mfma_f32_16x16x32_bf16 v[92:95], v[44:47], a[108:111], v[92:95]\n"
"buffer_load_dwordx4 a[148:151], v227, s[20:23], s55 offen offset:1024 sc1\n"
"v_mfma_f32_16x16x32_bf16 v[92:95], v[40:43], a[100:103], v[92:95]\n"
"s_waitcnt lgkmcnt(4)\n"
"buffer_load_dwordx4 a[152:155], v227, s[20:23], s55 offen offset:2048 sc1\n"
"v_mfma_f32_16x16x32_bf16 v[96:99], v[48:51], a[96:99], v[96:99]\n"
"buffer_load_dwordx4 a[156:159], v227, s[20:23], s55 offen offset:3072 sc1\n"
"v_mfma_f32_16x16x32_bf16 v[96:99], v[52:55], a[104:107], v[96:99]\n"
"s_add_u32 s55, s43, 37756928\n"
"v_mfma_f32_16x16x32_bf16 v[100:103], v[52:55], a[108:111], v[100:103]\n"
"buffer_load_dwordx4 a[160:163], v227, s[20:23], s55 offen offset:0 sc1\n"
"v_mfma_f32_16x16x32_bf16 v[100:103], v[48:51], a[100:103], v[100:103]\n"
"buffer_load_dwordx4 a[164:167], v227, s[20:23], s55 offen offset:1024 sc1\n"
"v_mfma_f32_16x16x32_bf16 v[104:107], v[56:59], a[96:99], v[104:107]\n"
"buffer_load_dwordx4 a[168:171], v227, s[20:23], s55 offen offset:2048 sc1\n"
"v_mfma_f32_16x16x32_bf16 v[104:107], v[60:63], a[104:107], v[104:107]\n"
"buffer_load_dwordx4 a[172:175], v227, s[20:23], s55 offen offset:3072 sc1\n"
"v_mfma_f32_16x16x32_bf16 v[108:111], v[60:63], a[108:111], v[108:111]\n"
"s_add_u32 s55, s43, 37761024\n"
"v_mfma_f32_16x16x32_bf16 v[108:111], v[56:59], a[100:103], v[108:111]\n"
"s_waitcnt lgkmcnt(0)\n"
"buffer_load_dwordx4 a[176:179], v227, s[20:23], s55 offen offset:0 sc1\n"
"v_mfma_f32_16x16x32_bf16 v[112:115], v[0:3], a[96:99], v[112:115]\n"
"buffer_load_dwordx4 a[180:183], v227, s[20:23], s55 offen offset:1024 sc1\n"
"v_mfma_f32_16x16x32_bf16 v[112:115], v[4:7], a[104:107], v[112:115]\n"
"buffer_load_dwordx4 a[184:187], v227, s[20:23], s55 offen offset:2048 sc1\n"
"v_mfma_f32_16x16x32_bf16 v[116:119], v[4:7], a[108:111], v[116:119]\n"
"buffer_load_dwordx4 a[188:191], v227, s[20:23], s55 offen offset:3072 sc1\n"
"v_mfma_f32_16x16x32_bf16 v[116:119], v[0:3], a[100:103], v[116:119]\n"
"s_nop 7\n"
"s_branch L_w1end%=\n"
"L_b1k%=:\n"
"s_cmp_eq_u32 s80, 5\n"
"s_cbranch_scc1 L_b2xk%=\n"
"s_cmp_eq_u32 s80, 4\n"
"s_cbranch_scc1 L_b3k%=\n"
"v_add_u32 v228, 4096, v227\n"
"global_load_dwordx3 v[224:226], v227, s[18:19] offset:0\n"
"global_load_dwordx3 v[228:230], v228, s[16:17] offset:0\n"
"global_load_dwordx4 v[184:187], v227, s[16:17] offset:-4096\n"
"global_load_dwordx4 v[188:191], v227, s[16:17] offset:-3072\n"
"s_cmp_lg_u32 s47, 0\n"
"s_cbranch_scc1 L_b1sk%=\n"
"v_readlane_b32 s54, v180, s79\n"
"s_nop 0\n"
"s_add_u32 s54, s54, 1\n"
"v_mov_b32 v240, s54\n"
"s_mov_b64 exec, 1\n"
"buffer_store_dword v240, v231, s[20:23], s78 offen offset:0 sc1\n"
"s_mov_b64 exec, -1\n"
"s_nop 3\n"
"L_b1sk%=:\n"
"s_branch L_b1rk%=\n"
"L_b2xk%=:\n"
"v_lshrrev_b32 v250, 24, v225\n"
"v_mad_u32_u24 v224, v225, s7, v224\n"
"global_load_dwordx2 v[248:249], v250, %0\n"
"v_sub_u32 v226, v226, v224\n"
"global_load_dwordx4 v[192:195], v227, s[16:17] offset:-2048\n"
"global_load_dwordx4 v[196:199], v227, s[16:17] offset:-1024\n"
"global_load_dwordx4 v[208:211], v227, s[16:17] offset:0\n"
"s_branch L_b1rk%=\n"
"L_b3k%=:\n"
"global_load_dwordx4 v[212:215], v227, s[16:17] offset:1024\n"
"global_load_dwordx4 v[216:219], v227, s[16:17] offset:2048\n"
"global_load_dwordx3 v[220:222], v227, s[16:17] offset:3072\n"
"v_add_co_u32 v224, vcc, v224, v248\n"
"s_nop 1\n"
"v_addc_co_u32 v225, vcc, 0, v249, vcc\n"
"v_and_b32 v225, 0xffff, v225\n"
"s_branch L_b1rk%=\n"
"L_b1v%=:\n"
"s_cmp_eq_u32 s80, 5\n"
"s_cbranch_scc1 L_b2xv%=\n"
"s_cmp_eq_u32 s80, 4\n"
"s_cbranch_scc1 L_b3v%=\n"
"v_add_u32 v228, 4096, v227\n"
"global_load_dwordx3 v[224:226], v227, s[18:19] offset:0\n"
"global_load_dwordx3 v[228:230], v228, s[16:17] offset:0\n"
"global_load_dwordx4 v[184:187], v227, s[16:17] offset:-4096\n"
"global_load_dwordx4 v[188:191], v227, s[16:17] offset:-3072\n"
"s_cmp_lg_u32 s47, 0\n"
"s_cbranch_scc1 L_b1sv%=\n"
"v_readlane_b32 s54, v180, s79\n"
"s_nop 0\n"
"s_add_u32 s54, s54, 1\n"
"v_mov_b32 v240, s54\n"
"s_mov_b64 exec, 1\n"
"buffer_store_dword v240, v231, s[20:23], s78 offen offset:0 sc1\n"
"s_mov_b64 exec, -1\n"
"s_nop 3\n"
"L_b1sv%=:\n"
"s_branch L_b1rv%=\n"
"L_b2xv%=:\n"
"v_lshrrev_b32 v250, 24, v225\n"
"v_mad_u32_u24 v224, v225, s7, v224\n"
"global_load_dwordx2 v[248:249], v250, %0\n"
"v_sub_u32 v226, v226, v224\n"
"global_load_dwordx4 v[192:195], v227, s[16:17] offset:-2048\n"
"global_load_dwordx4 v[196:199], v227, s[16:17] offset:-1024\n"
"global_load_dwordx4 v[208:211], v227, s[16:17] offset:0\n"
"s_branch L_b1rv%=\n"
"L_b3v%=:\n"
"global_load_dwordx4 v[212:215], v227, s[16:17] offset:1024\n"
"global_load_dwordx4 v[216:219], v227, s[16:17] offset:2048\n"
"global_load_dwordx3 v[220:222], v227, s[16:17] offset:3072\n"
"v_add_co_u32 v224, vcc, v224, v248\n"
"s_nop 1\n"
"v_addc_co_u32 v225, vcc, 0, v249, vcc\n"
"v_and_b32 v225, 0xffff, v225\n"
"s_branch L_b1rv%=\n"
"L_sw0%=_go:\n"
"v_mov_b32 v208, v216\n"
"v_mov_b32 v209, v217\n"
"v_mov_b32 v210, v218\n"
"v_mov_b32 v211, v219\n"
"v_mov_b32 v212, v220\n"
"v_mov_b32 v213, v221\n"
"v_mov_b32 v214, v222\n"
"v_mov_b32 v215, 0x7f000000\n"
"v_readlane_b32 s24, v224, s48\n"
"v_readlane_b32 s25, v225, s48\n"
"v_readlane_b32 s26, v226, s48\n"
"s_mov_b32 s49, 0\n"
"s_nop 4\n"
"s_branch L_sw0%=_ret\n"
"L_sw1%=_go:\n"
"v_mov_b32 v208, v216\n"
"v_mov_b32 v209, v217\n"
"v_mov_b32 v210, v218\n"
"v_mov_b32 v211, v219\n"
"v_mov_b32 v212, v220\n"
"v_mov_b32 v213, v221\n"
"v_mov_b32 v214, v222\n"
"v_mov_b32 v215, 0x7f000000\n"
"v_readlane_b32 s24, v224, s48\n"
"v_readlane_b32 s25, v225, s48\n"
"v_readlane_b32 s26, v226, s48\n"
"s_mov_b32 s49, 0\n"
"s_nop 4\n"
"s_branch L_sw1%=_ret\n"
"L_sw2%=_go:\n"
"v_mov_b32 v208, v216\n"
"v_mov_b32 v209, v217\n"
"v_mov_b32 v210, v218\n"
"v_mov_b32 v211, v219\n"
"v_mov_b32 v212, v220\n"
"v_mov_b32 v213, v221\n"
"v_mov_b32 v214, v222\n"
"v_mov_b32 v215, 0x7f000000\n"
"v_readlane_b32 s24, v224, s48\n"
"v_readlane_b32 s25, v225, s48\n"
"v_readlane_b32 s26, v226, s48\n"
"s_mov_b32 s49, 0\n"
"s_nop 4\n"
"s_branch L_sw2%=_ret\n"
"L_sw3%=_go:\n"
"v_mov_b32 v208, v216\n"
"v_mov_b32 v209, v217\n"
"v_mov_b32 v210, v218\n"
"v_mov_b32 v211, v219\n"
"v_mov_b32 v212, v220\n"
"v_mov_b32 v213, v221\n"
"v_mov_b32 v214, v222\n"
"v_mov_b32 v215, 0x7f000000\n"
"v_readlane_b32 s24, v224, s48\n"
"v_readlane_b32 s25, v225, s48\n"
"v_readlane_b32 s26, v226, s48\n"
"s_mov_b32 s49, 0\n"
"s_nop 4\n"
"s_branch L_sw3%=_ret\n"
"L_sw4%=_go:\n"
"v_mov_b32 v208, v216\n"
"v_mov_b32 v209, v217\n"
"v_mov_b32 v210, v218\n"
"v_mov_b32 v211, v219\n"
"v_mov_b32 v212, v220\n"
"v_mov_b32 v213, v221\n"
"v_mov_b32 v214, v222\n"
"v_mov_b32 v215, 0x7f000000\n"
"v_readlane_b32 s24, v224, s48\n"
"v_readlane_b32 s25, v225, s48\n"
"v_readlane_b32 s26, v226, s48\n"
"s_mov_b32 s49, 0\n"
"s_nop 4\n"
"s_branch L_sw4%=_ret\n"
"L_sw5%=_go:\n"
"v_mov_b32 v208, v216\n"
"v_mov_b32 v209, v217\n"
"v_mov_b32 v210, v218\n"
"v_mov_b32 v211, v219\n"
"v_mov_b32 v212, v220\n"
"v_mov_b32 v213, v221\n"
"v_mov_b32 v214, v222\n"
"v_mov_b32 v215, 0x7f000000\n"
"v_readlane_b32 s24, v224, s48\n"
"v_readlane_b32 s25, v225, s48\n"
"v_readlane_b32 s26, v226, s48\n"
"s_mov_b32 s49, 0\n"
"s_nop 4\n"
"s_branch L_sw5%=_ret\n"
"L_sw6%=_go:\n"
"v_mov_b32 v208, v216\n"
"v_mov_b32 v209, v217\n"
"v_mov_b32 v210, v218\n"
"v_mov_b32 v211, v219\n"
"v_mov_b32 v212, v220\n"
"v_mov_b32 v213, v221\n"
"v_mov_b32 v214, v222\n"
"v_mov_b32 v215, 0x7f000000\n"
"v_readlane_b32 s24, v224, s48\n"
"v_readlane_b32 s25, v225, s48\n"
"v_readlane_b32 s26, v226, s48\n"
"s_mov_b32 s49, 0\n"
"s_nop 4\n"
"s_branch L_sw6%=_ret\n"
"L_resc%=:\n"
"s_nop 9\n"
"v_max3_f32 v242, v64, v65, v66\n"
"v_max3_f32 v242, v242, v67, v72\n"
"v_max3_f32 v242, v242, v73, v74\n"
"v_max3_f32 v242, v242, v75, v80\n"
"v_max3_f32 v242, v242, v81, v82\n"
"v_max3_f32 v242, v242, v83, v88\n"
"v_max3_f32 v242, v242, v89, v90\n"
"v_max_f32 v242, v242, v91\n"
"v_mov_b32 v244, v242\n"
"s_nop 1\n"
"v_permlane32_swap_b32 v244, v242\n"
"v_max_f32 v242, v242, v244\n"
"v_mov_b32 v244, v242\n"
"s_nop 1\n"
"v_permlane16_swap_b32 v244, v242\n"
"v_max_f32 v242, v242, v244\n"
"v_max_f32 v242, s58, v242\n"
"v_sub_f32 v237, v237, v242\n"
"v_max_f32 v246, 0, v242\n"
"v_exp_f32 v246, -v246\n"
"s_nop 0\n"
"v_mul_f32 v233, v233, v246\n"
"v_sub_f32 v144, v64, v242\n"
"v_sub_f32 v145, v65, v242\n"
"v_sub_f32 v146, v66, v242\n"
"v_sub_f32 v147, v67, v242\n"
"v_exp_f32 v144, v144\n"
"v_exp_f32 v145, v145\n"
"v_exp_f32 v146, v146\n"
"v_exp_f32 v147, v147\n"
"v_sub_f32 v152, v72, v242\n"
"v_sub_f32 v153, v73, v242\n"
"v_sub_f32 v154, v74, v242\n"
"v_sub_f32 v155, v75, v242\n"
"v_exp_f32 v152, v152\n"
"v_exp_f32 v153, v153\n"
"v_exp_f32 v154, v154\n"
"v_exp_f32 v155, v155\n"
"v_sub_f32 v160, v80, v242\n"
"v_sub_f32 v161, v81, v242\n"
"v_sub_f32 v162, v82, v242\n"
"v_sub_f32 v163, v83, v242\n"
"v_exp_f32 v160, v160\n"
"v_exp_f32 v161, v161\n"
"v_exp_f32 v162, v162\n"
"v_exp_f32 v163, v163\n"
"v_sub_f32 v168, v88, v242\n"
"v_sub_f32 v169, v89, v242\n"
"v_sub_f32 v170, v90, v242\n"
"v_sub_f32 v171, v91, v242\n"
"v_exp_f32 v168, v168\n"
"v_exp_f32 v169, v169\n"
"v_exp_f32 v170, v170\n"
"v_exp_f32 v171, v171\n"
"v_add_f32 v235, v144, v145\n"
"v_add_f32 v235, v235, v146\n"
"v_add_f32 v235, v235, v147\n"
"v_add_f32 v235, v235, v152\n"
"v_add_f32 v235, v235, v153\n"
"v_add_f32 v235, v235, v154\n"
"v_add_f32 v235, v235, v155\n"
"v_add_f32 v235, v235, v160\n"
"v_add_f32 v235, v235, v161\n"
"v_add_f32 v235, v235, v162\n"
"v_add_f32 v235, v235, v163\n"
"v_add_f32 v235, v235, v168\n"
"v_add_f32 v235, v235, v169\n"
"v_add_f32 v235, v235, v170\n"
"v_add_f32 v235, v235, v171\n"
"v_accvgpr_read_b32 v248, a0\n"
"v_accvgpr_read_b32 v249, a1\n"
"v_accvgpr_read_b32 v250, a2\n"
"v_accvgpr_read_b32 v251, a3\n"
"v_mul_f32 v248, v248, v246\n"
"v_mul_f32 v249, v249, v246\n"
"v_mul_f32 v250, v250, v246\n"
"v_mul_f32 v251, v251, v246\n"
"v_accvgpr_write_b32 a0, v248\n"
"v_accvgpr_write_b32 a1, v249\n"
"v_accvgpr_write_b32 a2, v250\n"
"v_accvgpr_write_b32 a3, v251\n"
"v_accvgpr_read_b32 v248, a8\n"
"v_accvgpr_read_b32 v249, a9\n"
"v_accvgpr_read_b32 v250, a10\n"
"v_accvgpr_read_b32 v251, a11\n"
"v_mul_f32 v248, v248, v246\n"
"v_mul_f32 v249, v249, v246\n"
"v_mul_f32 v250, v250, v246\n"
"v_mul_f32 v251, v251, v246\n"
"v_accvgpr_write_b32 a8, v248\n"
"v_accvgpr_write_b32 a9, v249\n"
"v_accvgpr_write_b32 a10, v250\n"
"v_accvgpr_write_b32 a11, v251\n"
"v_accvgpr_read_b32 v248, a16\n"
"v_accvgpr_read_b32 v249, a17\n"
"v_accvgpr_read_b32 v250, a18\n"
"v_accvgpr_read_b32 v251, a19\n"
"v_mul_f32 v248, v248, v246\n"
"v_mul_f32 v249, v249, v246\n"
"v_mul_f32 v250, v250, v246\n"
"v_mul_f32 v251, v251, v246\n"
"v_accvgpr_write_b32 a16, v248\n"
"v_accvgpr_write_b32 a17, v249\n"
"v_accvgpr_write_b32 a18, v250\n"
"v_accvgpr_write_b32 a19, v251\n"
"v_accvgpr_read_b32 v248, a24\n"
"v_accvgpr_read_b32 v249, a25\n"
"v_accvgpr_read_b32 v250, a26\n"
"v_accvgpr_read_b32 v251, a27\n"
"v_mul_f32 v248, v248, v246\n"
"v_mul_f32 v249, v249, v246\n"
"v_mul_f32 v250, v250, v246\n"
"v_mul_f32 v251, v251, v246\n"
"v_accvgpr_write_b32 a24, v248\n"
"v_accvgpr_write_b32 a25, v249\n"
"v_accvgpr_write_b32 a26, v250\n"
"v_accvgpr_write_b32 a27, v251\n"
"v_accvgpr_read_b32 v248, a32\n"
"v_accvgpr_read_b32 v249, a33\n"
"v_accvgpr_read_b32 v250, a34\n"
"v_accvgpr_read_b32 v251, a35\n"
"v_mul_f32 v248, v248, v246\n"
"v_mul_f32 v249, v249, v246\n"
"v_mul_f32 v250, v250, v246\n"
"v_mul_f32 v251, v251, v246\n"
"v_accvgpr_write_b32 a32, v248\n"
"v_accvgpr_write_b32 a33, v249\n"
"v_accvgpr_write_b32 a34, v250\n"
"v_accvgpr_write_b32 a35, v251\n"
"v_accvgpr_read_b32 v248, a40\n"
"v_accvgpr_read_b32 v249, a41\n"
"v_accvgpr_read_b32 v250, a42\n"
"v_accvgpr_read_b32 v251, a43\n"
"v_mul_f32 v248, v248, v246\n"
"v_mul_f32 v249, v249, v246\n"
"v_mul_f32 v250, v250, v246\n"
"v_mul_f32 v251, v251, v246\n"
"v_accvgpr_write_b32 a40, v248\n"
"v_accvgpr_write_b32 a41, v249\n"
"v_accvgpr_write_b32 a42, v250\n"
"v_accvgpr_write_b32 a43, v251\n"
"v_accvgpr_read_b32 v248, a48\n"
"v_accvgpr_read_b32 v249, a49\n"
"v_accvgpr_read_b32 v250, a50\n"
"v_accvgpr_read_b32 v251, a51\n"
"v_mul_f32 v248, v248, v246\n"
"v_mul_f32 v249, v249, v246\n"
"v_mul_f32 v250, v250, v246\n"
"v_mul_f32 v251, v251, v246\n"
"v_accvgpr_write_b32 a48, v248\n"
"v_accvgpr_write_b32 a49, v249\n"
"v_accvgpr_write_b32 a50, v250\n"
"v_accvgpr_write_b32 a51, v251\n"
"v_accvgpr_read_b32 v248, a56\n"
"v_accvgpr_read_b32 v249, a57\n"
"v_accvgpr_read_b32 v250, a58\n"
"v_accvgpr_read_b32 v251, a59\n"
"v_mul_f32 v248, v248, v246\n"
"v_mul_f32 v249, v249, v246\n"
"v_mul_f32 v250, v250, v246\n"
"v_mul_f32 v251, v251, v246\n"
"v_accvgpr_write_b32 a56, v248\n"
"v_accvgpr_write_b32 a57, v249\n"
"v_accvgpr_write_b32 a58, v250\n"
"v_accvgpr_write_b32 a59, v251\n"
"v_accvgpr_read_b32 v248, a64\n"
"v_accvgpr_read_b32 v249, a65\n"
"v_accvgpr_read_b32 v250, a66\n"
"v_accvgpr_read_b32 v251, a67\n"
"v_mul_f32 v248, v248, v246\n"
"v_mul_f32 v249, v249, v246\n"
"v_mul_f32 v250, v250, v246\n"
"v_mul_f32 v251, v251, v246\n"
"v_accvgpr_write_b32 a64, v248\n"
"v_accvgpr_write_b32 a65, v249\n"
"v_accvgpr_write_b32 a66, v250\n"
"v_accvgpr_write_b32 a67, v251\n"
"v_accvgpr_read_b32 v248, a72\n"
"v_accvgpr_read_b32 v249, a73\n"
"v_accvgpr_read_b32 v250, a74\n"
"v_accvgpr_read_b32 v251, a75\n"
"v_mul_f32 v248, v248, v246\n"
"v_mul_f32 v249, v249, v246\n"
"v_mul_f32 v250, v250, v246\n"
"v_mul_f32 v251, v251, v246\n"
"v_accvgpr_write_b32 a72, v248\n"
"v_accvgpr_write_b32 a73, v249\n"
"v_accvgpr_write_b32 a74, v250\n"
"v_accvgpr_write_b32 a75, v251\n"
"v_accvgpr_read_b32 v248, a80\n"
"v_accvgpr_read_b32 v249, a81\n"
"v_accvgpr_read_b32 v250, a82\n"
"v_accvgpr_read_b32 v251, a83\n"
"v_mul_f32 v248, v248, v246\n"
"v_mul_f32 v249, v249, v246\n"
"v_mul_f32 v250, v250, v246\n"
"v_mul_f32 v251, v251, v246\n"
"v_accvgpr_write_b32 a80, v248\n"
"v_accvgpr_write_b32 a81, v249\n"
"v_accvgpr_write_b32 a82, v250\n"
"v_accvgpr_write_b32 a83, v251\n"
"v_accvgpr_read_b32 v248, a88\n"
"v_accvgpr_read_b32 v249, a89\n"
"v_accvgpr_read_b32 v250, a90\n"
"v_accvgpr_read_b32 v251, a91\n"
"v_mul_f32 v248, v248, v246\n"
"v_mul_f32 v249, v249, v246\n"
"v_mul_f32 v250, v250, v246\n"
"v_mul_f32 v251, v251, v246\n"
"v_accvgpr_write_b32 a88, v248\n"
"v_accvgpr_write_b32 a89, v249\n"
"v_accvgpr_write_b32 a90, v250\n"
"v_accvgpr_write_b32 a91, v251\n"
"v_accvgpr_read_b32 v248, a96\n"
"v_accvgpr_read_b32 v249, a97\n"
"v_accvgpr_read_b32 v250, a98\n"
"v_accvgpr_read_b32 v251, a99\n"
"v_mul_f32 v248, v248, v246\n"
"v_mul_f32 v249, v249, v246\n"
"v_mul_f32 v250, v250, v246\n"
"v_mul_f32 v251, v251, v246\n"
"v_accvgpr_write_b32 a96, v248\n"
"v_accvgpr_write_b32 a97, v249\n"
"v_accvgpr_write_b32 a98, v250\n"
"v_accvgpr_write_b32 a99, v251\n"
"v_accvgpr_read_b32 v248, a104\n"
"v_accvgpr_read_b32 v249, a105\n"
"v_accvgpr_read_b32 v250, a106\n"
"v_accvgpr_read_b32 v251, a107\n"
"v_mul_f32 v248, v248, v246\n"
"v_mul_f32 v249, v249, v246\n"
"v_mul_f32 v250, v250, v246\n"
"v_mul_f32 v251, v251, v246\n"
"v_accvgpr_write_b32 a104, v248\n"
"v_accvgpr_write_b32 a105, v249\n"
"v_accvgpr_write_b32 a106, v250\n"
"v_accvgpr_write_b32 a107, v251\n"
"v_accvgpr_read_b32 v248, a112\n"
"v_accvgpr_read_b32 v249, a113\n"
"v_accvgpr_read_b32 v250, a114\n"
"v_accvgpr_read_b32 v251, a115\n"
"v_mul_f32 v248, v248, v246\n"
"v_mul_f32 v249, v249, v246\n"
"v_mul_f32 v250, v250, v246\n"
"v_mul_f32 v251, v251, v246\n"
"v_accvgpr_write_b32 a112, v248\n"
"v_accvgpr_write_b32 a113, v249\n"
"v_accvgpr_write_b32 a114, v250\n"
"v_accvgpr_write_b32 a115, v251\n"
"v_accvgpr_read_b32 v248, a120\n"
"v_accvgpr_read_b32 v249, a121\n"
"v_accvgpr_read_b32 v250, a122\n"
"v_accvgpr_read_b32 v251, a123\n"
"v_mul_f32 v248, v248, v246\n"
"v_mul_f32 v249, v249, v246\n"
"v_mul_f32 v250, v250, v246\n"
"v_mul_f32 v251, v251, v246\n"
"v_accvgpr_write_b32 a120, v248\n"
"v_accvgpr_write_b32 a121, v249\n"
"v_accvgpr_write_b32 a122, v250\n"
"v_accvgpr_write_b32 a123, v251\n"
"v_max3_f32 v243, v68, v69, v70\n"
"v_max3_f32 v243, v243, v71, v76\n"
"v_max3_f32 v243, v243, v77, v78\n"
"v_max3_f32 v243, v243, v79, v84\n"
"v_max3_f32 v243, v243, v85, v86\n"
"v_max3_f32 v243, v243, v87, v92\n"
"v_max3_f32 v243, v243, v93, v94\n"
"v_max_f32 v243, v243, v95\n"
"v_mov_b32 v244, v243\n"
"s_nop 1\n"
"v_permlane32_swap_b32 v244, v243\n"
"v_max_f32 v243, v243, v244\n"
"v_mov_b32 v244, v243\n"
"s_nop 1\n"
"v_permlane16_swap_b32 v244, v243\n"
"v_max_f32 v243, v243, v244\n"
"v_max_f32 v243, s58, v243\n"
"v_sub_f32 v238, v238, v243\n"
"v_max_f32 v247, 0, v243\n"
"v_exp_f32 v247, -v247\n"
"s_nop 0\n"
"v_mul_f32 v234, v234, v247\n"
"v_sub_f32 v148, v68, v243\n"
"v_sub_f32 v149, v69, v243\n"
"v_sub_f32 v150, v70, v243\n"
"v_sub_f32 v151, v71, v243\n"
"v_exp_f32 v148, v148\n"
"v_exp_f32 v149, v149\n"
"v_exp_f32 v150, v150\n"
"v_exp_f32 v151, v151\n"
"v_sub_f32 v156, v76, v243\n"
"v_sub_f32 v157, v77, v243\n"
"v_sub_f32 v158, v78, v243\n"
"v_sub_f32 v159, v79, v243\n"
"v_exp_f32 v156, v156\n"
"v_exp_f32 v157, v157\n"
"v_exp_f32 v158, v158\n"
"v_exp_f32 v159, v159\n"
"v_sub_f32 v164, v84, v243\n"
"v_sub_f32 v165, v85, v243\n"
"v_sub_f32 v166, v86, v243\n"
"v_sub_f32 v167, v87, v243\n"
"v_exp_f32 v164, v164\n"
"v_exp_f32 v165, v165\n"
"v_exp_f32 v166, v166\n"
"v_exp_f32 v167, v167\n"
"v_sub_f32 v172, v92, v243\n"
"v_sub_f32 v173, v93, v243\n"
"v_sub_f32 v174, v94, v243\n"
"v_sub_f32 v175, v95, v243\n"
"v_exp_f32 v172, v172\n"
"v_exp_f32 v173, v173\n"
"v_exp_f32 v174, v174\n"
"v_exp_f32 v175, v175\n"
"v_add_f32 v236, v148, v149\n"
"v_add_f32 v236, v236, v150\n"
"v_add_f32 v236, v236, v151\n"
"v_add_f32 v236, v236, v156\n"
"v_add_f32 v236, v236, v157\n"
"v_add_f32 v236, v236, v158\n"
"v_add_f32 v236, v236, v159\n"
"v_add_f32 v236, v236, v164\n"
"v_add_f32 v236, v236, v165\n"
"v_add_f32 v236, v236, v166\n"
"v_add_f32 v236, v236, v167\n"
"v_add_f32 v236, v236, v172\n"
"v_add_f32 v236, v236, v173\n"
"v_add_f32 v236, v236, v174\n"
"v_add_f32 v236, v236, v175\n"
"v_accvgpr_read_b32 v248, a4\n"
"v_accvgpr_read_b32 v249, a5\n"
"v_accvgpr_read_b32 v250, a6\n"
"v_accvgpr_read_b32 v251, a7\n"
"v_mul_f32 v248, v248, v247\n"
"v_mul_f32 v249, v249, v247\n"
"v_mul_f32 v250, v250, v247\n"
"v_mul_f32 v251, v251, v247\n"
"v_accvgpr_write_b32 a4, v248\n"
"v_accvgpr_write_b32 a5, v249\n"
"v_accvgpr_write_b32 a6, v250\n"
"v_accvgpr_write_b32 a7, v251\n"
"v_accvgpr_read_b32 v248, a12\n"
"v_accvgpr_read_b32 v249, a13\n"
"v_accvgpr_read_b32 v250, a14\n"
"v_accvgpr_read_b32 v251, a15\n"
"v_mul_f32 v248, v248, v247\n"
"v_mul_f32 v249, v249, v247\n"
"v_mul_f32 v250, v250, v247\n"
"v_mul_f32 v251, v251, v247\n"
"v_accvgpr_write_b32 a12, v248\n"
"v_accvgpr_write_b32 a13, v249\n"
"v_accvgpr_write_b32 a14, v250\n"
"v_accvgpr_write_b32 a15, v251\n"
"v_accvgpr_read_b32 v248, a20\n"
"v_accvgpr_read_b32 v249, a21\n"
"v_accvgpr_read_b32 v250, a22\n"
"v_accvgpr_read_b32 v251, a23\n"
"v_mul_f32 v248, v248, v247\n"
"v_mul_f32 v249, v249, v247\n"
"v_mul_f32 v250, v250, v247\n"
"v_mul_f32 v251, v251, v247\n"
"v_accvgpr_write_b32 a20, v248\n"
"v_accvgpr_write_b32 a21, v249\n"
"v_accvgpr_write_b32 a22, v250\n"
"v_accvgpr_write_b32 a23, v251\n"
"v_accvgpr_read_b32 v248, a28\n"
"v_accvgpr_read_b32 v249, a29\n"
"v_accvgpr_read_b32 v250, a30\n"
"v_accvgpr_read_b32 v251, a31\n"
"v_mul_f32 v248, v248, v247\n"
"v_mul_f32 v249, v249, v247\n"
"v_mul_f32 v250, v250, v247\n"
"v_mul_f32 v251, v251, v247\n"
"v_accvgpr_write_b32 a28, v248\n"
"v_accvgpr_write_b32 a29, v249\n"
"v_accvgpr_write_b32 a30, v250\n"
"v_accvgpr_write_b32 a31, v251\n"
"v_accvgpr_read_b32 v248, a36\n"
"v_accvgpr_read_b32 v249, a37\n"
"v_accvgpr_read_b32 v250, a38\n"
"v_accvgpr_read_b32 v251, a39\n"
"v_mul_f32 v248, v248, v247\n"
"v_mul_f32 v249, v249, v247\n"
"v_mul_f32 v250, v250, v247\n"
"v_mul_f32 v251, v251, v247\n"
"v_accvgpr_write_b32 a36, v248\n"
"v_accvgpr_write_b32 a37, v249\n"
"v_accvgpr_write_b32 a38, v250\n"
"v_accvgpr_write_b32 a39, v251\n"
"v_accvgpr_read_b32 v248, a44\n"
"v_accvgpr_read_b32 v249, a45\n"
"v_accvgpr_read_b32 v250, a46\n"
"v_accvgpr_read_b32 v251, a47\n"
"v_mul_f32 v248, v248, v247\n"
"v_mul_f32 v249, v249, v247\n"
"v_mul_f32 v250, v250, v247\n"
"v_mul_f32 v251, v251, v247\n"
"v_accvgpr_write_b32 a44, v248\n"
"v_accvgpr_write_b32 a45, v249\n"
"v_accvgpr_write_b32 a46, v250\n"
"v_accvgpr_write_b32 a47, v251\n"
"v_accvgpr_read_b32 v248, a52\n"
"v_accvgpr_read_b32 v249, a53\n"
"v_accvgpr_read_b32 v250, a54\n"
"v_accvgpr_read_b32 v251, a55\n"
"v_mul_f32 v248, v248, v247\n"
"v_mul_f32 v249, v249, v247\n"
"v_mul_f32 v250, v250, v247\n"
"v_mul_f32 v251, v251, v247\n"
"v_accvgpr_write_b32 a52, v248\n"
"v_accvgpr_write_b32 a53, v249\n"
"v_accvgpr_write_b32 a54, v250\n"
"v_accvgpr_write_b32 a55, v251\n"
"v_accvgpr_read_b32 v248, a60\n"
"v_accvgpr_read_b32 v249, a61\n"
"v_accvgpr_read_b32 v250, a62\n"
"v_accvgpr_read_b32 v251, a63\n"
"v_mul_f32 v248, v248, v247\n"
"v_mul_f32 v249, v249, v247\n"
"v_mul_f32 v250, v250, v247\n"
"v_mul_f32 v251, v251, v247\n"
"v_accvgpr_write_b32 a60, v248\n"
"v_accvgpr_write_b32 a61, v249\n"
"v_accvgpr_write_b32 a62, v250\n"
"v_accvgpr_write_b32 a63, v251\n"
"v_accvgpr_read_b32 v248, a68\n"
"v_accvgpr_read_b32 v249, a69\n"
"v_accvgpr_read_b32 v250, a70\n"
"v_accvgpr_read_b32 v251, a71\n"
"v_mul_f32 v248, v248, v247\n"
"v_mul_f32 v249, v249, v247\n"
"v_mul_f32 v250, v250, v247\n"
"v_mul_f32 v251, v251, v247\n"
"v_accvgpr_write_b32 a68, v248\n"
"v_accvgpr_write_b32 a69, v249\n"
"v_accvgpr_write_b32 a70, v250\n"
"v_accvgpr_write_b32 a71, v251\n"
"v_accvgpr_read_b32 v248, a76\n"
"v_accvgpr_read_b32 v249, a77\n"
"v_accvgpr_read_b32 v250, a78\n"
"v_accvgpr_read_b32 v251, a79\n"
"v_mul_f32 v248, v248, v247\n"
"v_mul_f32 v249, v249, v247\n"
"v_mul_f32 v250, v250, v247\n"
"v_mul_f32 v251, v251, v247\n"
"v_accvgpr_write_b32 a76, v248\n"
"v_accvgpr_write_b32 a77, v249\n"
"v_accvgpr_write_b32 a78, v250\n"
"v_accvgpr_write_b32 a79, v251\n"
"v_accvgpr_read_b32 v248, a84\n"
"v_accvgpr_read_b32 v249, a85\n"
"v_accvgpr_read_b32 v250, a86\n"
"v_accvgpr_read_b32 v251, a87\n"
"v_mul_f32 v248, v248, v247\n"
"v_mul_f32 v249, v249, v247\n"
"v_mul_f32 v250, v250, v247\n"
"v_mul_f32 v251, v251, v247\n"
"v_accvgpr_write_b32 a84, v248\n"
"v_accvgpr_write_b32 a85, v249\n"
"v_accvgpr_write_b32 a86, v250\n"
"v_accvgpr_write_b32 a87, v251\n"
"v_accvgpr_read_b32 v248, a92\n"
"v_accvgpr_read_b32 v249, a93\n"
"v_accvgpr_read_b32 v250, a94\n"
"v_accvgpr_read_b32 v251, a95\n"
"v_mul_f32 v248, v248, v247\n"
"v_mul_f32 v249, v249, v247\n"
"v_mul_f32 v250, v250, v247\n"
"v_mul_f32 v251, v251, v247\n"
"v_accvgpr_write_b32 a92, v248\n"
"v_accvgpr_write_b32 a93, v249\n"
"v_accvgpr_write_b32 a94, v250\n"
"v_accvgpr_write_b32 a95, v251\n"
"v_accvgpr_read_b32 v248, a100\n"
"v_accvgpr_read_b32 v249, a101\n"
"v_accvgpr_read_b32 v250, a102\n"
"v_accvgpr_read_b32 v251, a103\n"
"v_mul_f32 v248, v248, v247\n"
"v_mul_f32 v249, v249, v247\n"
"v_mul_f32 v250, v250, v247\n"
"v_mul_f32 v251, v251, v247\n"
"v_accvgpr_write_b32 a100, v248\n"
"v_accvgpr_write_b32 a101, v249\n"
"v_accvgpr_write_b32 a102, v250\n"
"v_accvgpr_write_b32 a103, v251\n"
"v_accvgpr_read_b32 v248, a108\n"
"v_accvgpr_read_b32 v249, a109\n"
"v_accvgpr_read_b32 v250, a110\n"
"v_accvgpr_read_b32 v251, a111\n"
"v_mul_f32 v248, v248, v247\n"
"v_mul_f32 v249, v249, v247\n"
"v_mul_f32 v250, v250, v247\n"
"v_mul_f32 v251, v251, v247\n"
"v_accvgpr_write_b32 a108, v248\n"
"v_accvgpr_write_b32 a109, v249\n"
"v_accvgpr_write_b32 a110, v250\n"
"v_accvgpr_write_b32 a111, v251\n"
"v_accvgpr_read_b32 v248, a116\n"
"v_accvgpr_read_b32 v249, a117\n"
"v_accvgpr_read_b32 v250, a118\n"
"v_accvgpr_read_b32 v251, a119\n"
"v_mul_f32 v248, v248, v247\n"
"v_mul_f32 v249, v249, v247\n"
"v_mul_f32 v250, v250, v247\n"
"v_mul_f32 v251, v251, v247\n"
"v_accvgpr_write_b32 a116, v248\n"
"v_accvgpr_write_b32 a117, v249\n"
"v_accvgpr_write_b32 a118, v250\n"
"v_accvgpr_write_b32 a119, v251\n"
"v_accvgpr_read_b32 v248, a124\n"
"v_accvgpr_read_b32 v249, a125\n"
"v_accvgpr_read_b32 v250, a126\n"
"v_accvgpr_read_b32 v251, a127\n"
"v_mul_f32 v248, v248, v247\n"
"v_mul_f32 v249, v249, v247\n"
"v_mul_f32 v250, v250, v247\n"
"v_mul_f32 v251, v251, v247\n"
"v_accvgpr_write_b32 a124, v248\n"
"v_accvgpr_write_b32 a125, v249\n"
"v_accvgpr_write_b32 a126, v250\n"
"v_accvgpr_write_b32 a127, v251\n"
"s_cmp_eq_u32 s52, 0\n"
"s_cselect_b32 s54, -1, 0\n"
"s_cmp_eq_u32 s52, 1\n"
"s_cselect_b32 s55, -1, 0\n"
"v_and_b32 v240, s54, v229\n"
"v_and_b32 v241, s55, v230\n"
"v_or_b32 v240, v240, v241\n"
"v_mov_b32 v241, 0xff800000\n"
"v_bfe_i32 v244, v240, 0, 1\n"
"v_bfe_i32 v245, v240, 1, 1\n"
"v_bfe_i32 v246, v240, 2, 1\n"
"v_bfe_i32 v247, v240, 3, 1\n"
"v_bfi_b32 v96, v244, v241, v237\n"
"v_bfi_b32 v97, v245, v241, v237\n"
"v_bfi_b32 v98, v246, v241, v237\n"
"v_bfi_b32 v99, v247, v241, v237\n"
"v_bfe_i32 v244, v240, 4, 1\n"
"v_bfe_i32 v245, v240, 5, 1\n"
"v_bfe_i32 v246, v240, 6, 1\n"
"v_bfe_i32 v247, v240, 7, 1\n"
"v_bfi_b32 v100, v244, v241, v238\n"
"v_bfi_b32 v101, v245, v241, v238\n"
"v_bfi_b32 v102, v246, v241, v238\n"
"v_bfi_b32 v103, v247, v241, v238\n"
"v_bfe_i32 v244, v240, 8, 1\n"
"v_bfe_i32 v245, v240, 9, 1\n"
"v_bfe_i32 v246, v240, 10, 1\n"
"v_bfe_i32 v247, v240, 11, 1\n"
"v_bfi_b32 v104, v244, v241, v237\n"
"v_bfi_b32 v105, v245, v241, v237\n"
"v_bfi_b32 v106, v246, v241, v237\n"
"v_bfi_b32 v107, v247, v241, v237\n"
"v_bfe_i32 v244, v240, 12, 1\n"
"v_bfe_i32 v245, v240, 13, 1\n"
"v_bfe_i32 v246, v240, 14, 1\n"
"v_bfe_i32 v247, v240, 15, 1\n"
"v_bfi_b32 v108, v244, v241, v238\n"
"v_bfi_b32 v109, v245, v241, v238\n"
"v_bfi_b32 v110, v246, v241, v238\n"
"v_bfi_b32 v111, v247, v241, v238\n"
"v_bfe_i32 v244, v240, 16, 1\n"
"v_bfe_i32 v245, v240, 17, 1\n"
"v_bfe_i32 v246, v240, 18, 1\n"
"v_bfe_i32 v247, v240, 19, 1\n"
"v_bfi_b32 v112, v244, v241, v237\n"
"v_bfi_b32 v113, v245, v241, v237\n"
"v_bfi_b32 v114, v246, v241, v237\n"
"v_bfi_b32 v115, v247, v241, v237\n"
"v_bfe_i32 v244, v240, 20, 1\n"
"v_bfe_i32 v245, v240, 21, 1\n"
"v_bfe_i32 v246, v240, 22, 1\n"
"v_bfe_i32 v247, v240, 23, 1\n"
"v_bfi_b32 v116, v244, v241, v238\n"
"v_bfi_b32 v117, v245, v241, v238\n"
"v_bfi_b32 v118, v246, v241, v238\n"
"v_bfi_b32 v119, v247, v241, v238\n"
"v_bfe_i32 v244, v240, 24, 1\n"
"v_bfe_i32 v245, v240, 25, 1\n"
"v_bfe_i32 v246, v240, 26, 1\n"
"v_bfe_i32 v247, v240, 27, 1\n"
"v_bfi_b32 v120, v244, v241, v237\n"
"v_bfi_b32 v121, v245, v241, v237\n"
"v_bfi_b32 v122, v246, v241, v237\n"
"v_bfi_b32 v123, v247, v241, v237\n"
"v_bfe_i32 v244, v240, 28, 1\n"
"v_bfe_i32 v245, v240, 29, 1\n"
"v_bfe_i32 v246, v240, 30, 1\n"
"v_bfe_i32 v247, v240, 31, 1\n"
"v_bfi_b32 v124, v244, v241, v238\n"
"v_bfi_b32 v125, v245, v241, v238\n"
"v_bfi_b32 v126, v246, v241, v238\n"
"v_bfi_b32 v127, v247, v241, v238\n"
"s_cmp_eq_u32 s52, 1\n"
"s_cbranch_scc1 L_half%=\n"
"s_branch L_ret%=\n"
"L_mask%=:\n"
"v_mov_b32 v241, 0xff800000\n"
"s_cmp_eq_u32 s52, 1\n"
"s_cbranch_scc1 L_mask1%=\n"
"v_bfe_i32 v244, v229, 0, 1\n"
"v_bfe_i32 v245, v229, 1, 1\n"
"v_bfe_i32 v246, v229, 2, 1\n"
"v_bfe_i32 v247, v229, 3, 1\n"
"v_bfi_b32 v96, v244, v241, v237\n"
"v_bfi_b32 v97, v245, v241, v237\n"
"v_bfi_b32 v98, v246, v241, v237\n"
"v_bfi_b32 v99, v247, v241, v237\n"
"v_bfe_i32 v244, v229, 8, 1\n"
"v_bfe_i32 v245, v229, 9, 1\n"
"v_bfe_i32 v246, v229, 10, 1\n"
"v_bfe_i32 v247, v229, 11, 1\n"
"v_bfi_b32 v104, v244, v241, v237\n"
"v_bfi_b32 v105, v245, v241, v237\n"
"v_bfi_b32 v106, v246, v241, v237\n"
"v_bfi_b32 v107, v247, v241, v237\n"
"v_bfe_i32 v244, v229, 16, 1\n"
"v_bfe_i32 v245, v229, 17, 1\n"
"v_bfe_i32 v246, v229, 18, 1\n"
"v_bfe_i32 v247, v229, 19, 1\n"
"v_bfi_b32 v112, v244, v241, v237\n"
"v_bfi_b32 v113, v245, v241, v237\n"
"v_bfi_b32 v114, v246, v241, v237\n"
"v_bfi_b32 v115, v247, v241, v237\n"
"v_bfe_i32 v244, v229, 24, 1\n"
"v_bfe_i32 v245, v229, 25, 1\n"
"v_bfe_i32 v246, v229, 26, 1\n"
"v_bfe_i32 v247, v229, 27, 1\n"
"v_bfi_b32 v120, v244, v241, v237\n"
"v_bfi_b32 v121, v245, v241, v237\n"
"v_bfi_b32 v122, v246, v241, v237\n"
"v_bfi_b32 v123, v247, v241, v237\n"
"s_nop 1\n"
"s_branch L_ret%=\n"
"L_mask1%=:\n"
"v_bfe_i32 v244, v230, 4, 1\n"
"v_bfe_i32 v245, v230, 5, 1\n"
"v_bfe_i32 v246, v230, 6, 1\n"
"v_bfe_i32 v247, v230, 7, 1\n"
"v_bfi_b32 v100, v244, v241, v238\n"
"v_bfi_b32 v101, v245, v241, v238\n"
"v_bfi_b32 v102, v246, v241, v238\n"
"v_bfi_b32 v103, v247, v241, v238\n"
"v_bfe_i32 v244, v230, 12, 1\n"
"v_bfe_i32 v245, v230, 13, 1\n"
"v_bfe_i32 v246, v230, 14, 1\n"
"v_bfe_i32 v247, v230, 15, 1\n"
"v_bfi_b32 v108, v244, v241, v238\n"
"v_bfi_b32 v109, v245, v241, v238\n"
"v_bfi_b32 v110, v246, v241, v238\n"
"v_bfi_b32 v111, v247, v241, v238\n"
"v_bfe_i32 v244, v230, 20, 1\n"
"v_bfe_i32 v245, v230, 21, 1\n"
"v_bfe_i32 v246, v230, 22, 1\n"
"v_bfe_i32 v247, v230, 23, 1\n"
"v_bfi_b32 v116, v244, v241, v238\n"
"v_bfi_b32 v117, v245, v241, v238\n"
"v_bfi_b32 v118, v246, v241, v238\n"
"v_bfi_b32 v119, v247, v241, v238\n"
"v_bfe_i32 v244, v230, 28, 1\n"
"v_bfe_i32 v245, v230, 29, 1\n"
"v_bfe_i32 v246, v230, 30, 1\n"
"v_bfe_i32 v247, v230, 31, 1\n"
"v_bfi_b32 v124, v244, v241, v238\n"
"v_bfi_b32 v125, v245, v241, v238\n"
"v_bfi_b32 v126, v246, v241, v238\n"
"v_bfi_b32 v127, v247, v241, v238\n"
"s_branch L_half%=\n"
"L_eq%=:\n"
"s_nop 9\n"
"v_min_f32 v240, v237, v248\n"
"v_sub_f32 v241, v240, v237\n"
"v_sub_f32 v242, v240, v248\n"
"v_exp_f32 v241, v241\n"
"v_exp_f32 v242, v242\n"
"v_mul_f32 v233, v233, v241\n"
"v_mul_f32 v249, v249, v242\n"
"v_accvgpr_read_b32 v80, a0\n"
"v_accvgpr_read_b32 v81, a1\n"
"v_accvgpr_read_b32 v82, a2\n"
"v_accvgpr_read_b32 v83, a3\n"
"v_mul_f32 v80, v80, v241\n"
"v_mul_f32 v81, v81, v241\n"
"v_mul_f32 v82, v82, v241\n"
"v_mul_f32 v83, v83, v241\n"
"v_accvgpr_write_b32 a0, v80\n"
"v_accvgpr_write_b32 a1, v81\n"
"v_accvgpr_write_b32 a2, v82\n"
"v_accvgpr_write_b32 a3, v83\n"
"v_accvgpr_read_b32 v80, a8\n"
"v_accvgpr_read_b32 v81, a9\n"
"v_accvgpr_read_b32 v82, a10\n"
"v_accvgpr_read_b32 v83, a11\n"
"v_mul_f32 v80, v80, v241\n"
"v_mul_f32 v81, v81, v241\n"
"v_mul_f32 v82, v82, v241\n"
"v_mul_f32 v83, v83, v241\n"
"v_accvgpr_write_b32 a8, v80\n"
"v_accvgpr_write_b32 a9, v81\n"
"v_accvgpr_write_b32 a10, v82\n"
"v_accvgpr_write_b32 a11, v83\n"
"v_accvgpr_read_b32 v80, a16\n"
"v_accvgpr_read_b32 v81, a17\n"
"v_accvgpr_read_b32 v82, a18\n"
"v_accvgpr_read_b32 v83, a19\n"
"v_mul_f32 v80, v80, v241\n"
"v_mul_f32 v81, v81, v241\n"
"v_mul_f32 v82, v82, v241\n"
"v_mul_f32 v83, v83, v241\n"
"v_accvgpr_write_b32 a16, v80\n"
"v_accvgpr_write_b32 a17, v81\n"
"v_accvgpr_write_b32 a18, v82\n"
"v_accvgpr_write_b32 a19, v83\n"
"v_accvgpr_read_b32 v80, a24\n"
"v_accvgpr_read_b32 v81, a25\n"
"v_accvgpr_read_b32 v82, a26\n"
"v_accvgpr_read_b32 v83, a27\n"
"v_mul_f32 v80, v80, v241\n"
"v_mul_f32 v81, v81, v241\n"
"v_mul_f32 v82, v82, v241\n"
"v_mul_f32 v83, v83, v241\n"
"v_accvgpr_write_b32 a24, v80\n"
"v_accvgpr_write_b32 a25, v81\n"
"v_accvgpr_write_b32 a26, v82\n"
"v_accvgpr_write_b32 a27, v83\n"
"v_accvgpr_read_b32 v80, a32\n"
"v_accvgpr_read_b32 v81, a33\n"
"v_accvgpr_read_b32 v82, a34\n"
"v_accvgpr_read_b32 v83, a35\n"
"v_mul_f32 v80, v80, v241\n"
"v_mul_f32 v81, v81, v241\n"
"v_mul_f32 v82, v82, v241\n"
"v_mul_f32 v83, v83, v241\n"
"v_accvgpr_write_b32 a32, v80\n"
"v_accvgpr_write_b32 a33, v81\n"
"v_accvgpr_write_b32 a34, v82\n"
"v_accvgpr_write_b32 a35, v83\n"
"v_accvgpr_read_b32 v80, a40\n"
"v_accvgpr_read_b32 v81, a41\n"
"v_accvgpr_read_b32 v82, a42\n"
"v_accvgpr_read_b32 v83, a43\n"
"v_mul_f32 v80, v80, v241\n"
"v_mul_f32 v81, v81, v241\n"
"v_mul_f32 v82, v82, v241\n"
"v_mul_f32 v83, v83, v241\n"
"v_accvgpr_write_b32 a40, v80\n"
"v_accvgpr_write_b32 a41, v81\n"
"v_accvgpr_write_b32 a42, v82\n"
"v_accvgpr_write_b32 a43, v83\n"
"v_accvgpr_read_b32 v80, a48\n"
"v_accvgpr_read_b32 v81, a49\n"
"v_accvgpr_read_b32 v82, a50\n"
"v_accvgpr_read_b32 v83, a51\n"
"v_mul_f32 v80, v80, v241\n"
"v_mul_f32 v81, v81, v241\n"
"v_mul_f32 v82, v82, v241\n"
"v_mul_f32 v83, v83, v241\n"
"v_accvgpr_write_b32 a48, v80\n"
"v_accvgpr_write_b32 a49, v81\n"
"v_accvgpr_write_b32 a50, v82\n"
"v_accvgpr_write_b32 a51, v83\n"
"v_accvgpr_read_b32 v80, a56\n"
"v_accvgpr_read_b32 v81, a57\n"
"v_accvgpr_read_b32 v82, a58\n"
"v_accvgpr_read_b32 v83, a59\n"
"v_mul_f32 v80, v80, v241\n"
"v_mul_f32 v81, v81, v241\n"
"v_mul_f32 v82, v82, v241\n"
"v_mul_f32 v83, v83, v241\n"
"v_accvgpr_write_b32 a56, v80\n"
"v_accvgpr_write_b32 a57, v81\n"
"v_accvgpr_write_b32 a58, v82\n"
"v_accvgpr_write_b32 a59, v83\n"
"v_accvgpr_read_b32 v80, a64\n"
"v_accvgpr_read_b32 v81, a65\n"
"v_accvgpr_read_b32 v82, a66\n"
"v_accvgpr_read_b32 v83, a67\n"
"v_mul_f32 v80, v80, v241\n"
"v_mul_f32 v81, v81, v241\n"
"v_mul_f32 v82, v82, v241\n"
"v_mul_f32 v83, v83, v241\n"
"v_accvgpr_write_b32 a64, v80\n"
"v_accvgpr_write_b32 a65, v81\n"
"v_accvgpr_write_b32 a66, v82\n"
"v_accvgpr_write_b32 a67, v83\n"
"v_accvgpr_read_b32 v80, a72\n"
"v_accvgpr_read_b32 v81, a73\n"
"v_accvgpr_read_b32 v82, a74\n"
"v_accvgpr_read_b32 v83, a75\n"
"v_mul_f32 v80, v80, v241\n"
"v_mul_f32 v81, v81, v241\n"
"v_mul_f32 v82, v82, v241\n"
"v_mul_f32 v83, v83, v241\n"
"v_accvgpr_write_b32 a72, v80\n"
"v_accvgpr_write_b32 a73, v81\n"
"v_accvgpr_write_b32 a74, v82\n"
"v_accvgpr_write_b32 a75, v83\n"
"v_accvgpr_read_b32 v80, a80\n"
"v_accvgpr_read_b32 v81, a81\n"
"v_accvgpr_read_b32 v82, a82\n"
"v_accvgpr_read_b32 v83, a83\n"
"v_mul_f32 v80, v80, v241\n"
"v_mul_f32 v81, v81, v241\n"
"v_mul_f32 v82, v82, v241\n"
"v_mul_f32 v83, v83, v241\n"
"v_accvgpr_write_b32 a80, v80\n"
"v_accvgpr_write_b32 a81, v81\n"
"v_accvgpr_write_b32 a82, v82\n"
"v_accvgpr_write_b32 a83, v83\n"
"v_accvgpr_read_b32 v80, a88\n"
"v_accvgpr_read_b32 v81, a89\n"
"v_accvgpr_read_b32 v82, a90\n"
"v_accvgpr_read_b32 v83, a91\n"
"v_mul_f32 v80, v80, v241\n"
"v_mul_f32 v81, v81, v241\n"
"v_mul_f32 v82, v82, v241\n"
"v_mul_f32 v83, v83, v241\n"
"v_accvgpr_write_b32 a88, v80\n"
"v_accvgpr_write_b32 a89, v81\n"
"v_accvgpr_write_b32 a90, v82\n"
"v_accvgpr_write_b32 a91, v83\n"
"v_accvgpr_read_b32 v80, a96\n"
"v_accvgpr_read_b32 v81, a97\n"
"v_accvgpr_read_b32 v82, a98\n"
"v_accvgpr_read_b32 v83, a99\n"
"v_mul_f32 v80, v80, v241\n"
"v_mul_f32 v81, v81, v241\n"
"v_mul_f32 v82, v82, v241\n"
"v_mul_f32 v83, v83, v241\n"
"v_accvgpr_write_b32 a96, v80\n"
"v_accvgpr_write_b32 a97, v81\n"
"v_accvgpr_write_b32 a98, v82\n"
"v_accvgpr_write_b32 a99, v83\n"
"v_accvgpr_read_b32 v80, a104\n"
"v_accvgpr_read_b32 v81, a105\n"
"v_accvgpr_read_b32 v82, a106\n"
"v_accvgpr_read_b32 v83, a107\n"
"v_mul_f32 v80, v80, v241\n"
"v_mul_f32 v81, v81, v241\n"
"v_mul_f32 v82, v82, v241\n"
"v_mul_f32 v83, v83, v241\n"
"v_accvgpr_write_b32 a104, v80\n"
"v_accvgpr_write_b32 a105, v81\n"
"v_accvgpr_write_b32 a106, v82\n"
"v_accvgpr_write_b32 a107, v83\n"
"v_accvgpr_read_b32 v80, a112\n"
"v_accvgpr_read_b32 v81, a113\n"
"v_accvgpr_read_b32 v82, a114\n"
"v_accvgpr_read_b32 v83, a115\n"
"v_mul_f32 v80, v80, v241\n"
"v_mul_f32 v81, v81, v241\n"
"v_mul_f32 v82, v82, v241\n"
"v_mul_f32 v83, v83, v241\n"
"v_accvgpr_write_b32 a112, v80\n"
"v_accvgpr_write_b32 a113, v81\n"
"v_accvgpr_write_b32 a114, v82\n"
"v_accvgpr_write_b32 a115, v83\n"
"v_accvgpr_read_b32 v80, a120\n"
"v_accvgpr_read_b32 v81, a121\n"
"v_accvgpr_read_b32 v82, a122\n"
"v_accvgpr_read_b32 v83, a123\n"
"v_mul_f32 v80, v80, v241\n"
"v_mul_f32 v81, v81, v241\n"
"v_mul_f32 v82, v82, v241\n"
"v_mul_f32 v83, v83, v241\n"
"v_accvgpr_write_b32 a120, v80\n"
"v_accvgpr_write_b32 a121, v81\n"
"v_accvgpr_write_b32 a122, v82\n"
"v_accvgpr_write_b32 a123, v83\n"
"v_accvgpr_read_b32 v84, a128\n"
"v_lshlrev_b32 v85, 16, v84\n"
"v_and_b32 v84, 0xffff0000, v84\n"
"v_mul_f32 v85, v85, v242\n"
"v_mul_f32 v84, v84, v242\n"
"v_cvt_pk_bf16_f32 v84, v85, v84\n"
"v_accvgpr_write_b32 a128, v84\n"
"v_accvgpr_read_b32 v84, a129\n"
"v_lshlrev_b32 v85, 16, v84\n"
"v_and_b32 v84, 0xffff0000, v84\n"
"v_mul_f32 v85, v85, v242\n"
"v_mul_f32 v84, v84, v242\n"
"v_cvt_pk_bf16_f32 v84, v85, v84\n"
"v_accvgpr_write_b32 a129, v84\n"
"v_accvgpr_read_b32 v84, a130\n"
"v_lshlrev_b32 v85, 16, v84\n"
"v_and_b32 v84, 0xffff0000, v84\n"
"v_mul_f32 v85, v85, v242\n"
"v_mul_f32 v84, v84, v242\n"
"v_cvt_pk_bf16_f32 v84, v85, v84\n"
"v_accvgpr_write_b32 a130, v84\n"
"v_accvgpr_read_b32 v84, a131\n"
"v_lshlrev_b32 v85, 16, v84\n"
"v_and_b32 v84, 0xffff0000, v84\n"
"v_mul_f32 v85, v85, v242\n"
"v_mul_f32 v84, v84, v242\n"
"v_cvt_pk_bf16_f32 v84, v85, v84\n"
"v_accvgpr_write_b32 a131, v84\n"
"v_accvgpr_read_b32 v84, a132\n"
"v_lshlrev_b32 v85, 16, v84\n"
"v_and_b32 v84, 0xffff0000, v84\n"
"v_mul_f32 v85, v85, v242\n"
"v_mul_f32 v84, v84, v242\n"
"v_cvt_pk_bf16_f32 v84, v85, v84\n"
"v_accvgpr_write_b32 a132, v84\n"
"v_accvgpr_read_b32 v84, a133\n"
"v_lshlrev_b32 v85, 16, v84\n"
"v_and_b32 v84, 0xffff0000, v84\n"
"v_mul_f32 v85, v85, v242\n"
"v_mul_f32 v84, v84, v242\n"
"v_cvt_pk_bf16_f32 v84, v85, v84\n"
"v_accvgpr_write_b32 a133, v84\n"
"v_accvgpr_read_b32 v84, a134\n"
"v_lshlrev_b32 v85, 16, v84\n"
"v_and_b32 v84, 0xffff0000, v84\n"
"v_mul_f32 v85, v85, v242\n"
"v_mul_f32 v84, v84, v242\n"
"v_cvt_pk_bf16_f32 v84, v85, v84\n"
"v_accvgpr_write_b32 a134, v84\n"
"v_accvgpr_read_b32 v84, a135\n"
"v_lshlrev_b32 v85, 16, v84\n"
"v_and_b32 v84, 0xffff0000, v84\n"
"v_mul_f32 v85, v85, v242\n"
"v_mul_f32 v84, v84, v242\n"
"v_cvt_pk_bf16_f32 v84, v85, v84\n"
"v_accvgpr_write_b32 a135, v84\n"
"v_accvgpr_read_b32 v84, a136\n"
"v_lshlrev_b32 v85, 16, v84\n"
"v_and_b32 v84, 0xffff0000, v84\n"
"v_mul_f32 v85, v85, v242\n"
"v_mul_f32 v84, v84, v242\n"
"v_cvt_pk_bf16_f32 v84, v85, v84\n"
"v_accvgpr_write_b32 a136, v84\n"
"v_accvgpr_read_b32 v84, a137\n"
"v_lshlrev_b32 v85, 16, v84\n"
"v_and_b32 v84, 0xffff0000, v84\n"
"v_mul_f32 v85, v85, v242\n"
"v_mul_f32 v84, v84, v242\n"
"v_cvt_pk_bf16_f32 v84, v85, v84\n"
"v_accvgpr_write_b32 a137, v84\n"
"v_accvgpr_read_b32 v84, a138\n"
"v_lshlrev_b32 v85, 16, v84\n"
"v_and_b32 v84, 0xffff0000, v84\n"
"v_mul_f32 v85, v85, v242\n"
"v_mul_f32 v84, v84, v242\n"
"v_cvt_pk_bf16_f32 v84, v85, v84\n"
"v_accvgpr_write_b32 a138, v84\n"
"v_accvgpr_read_b32 v84, a139\n"
"v_lshlrev_b32 v85, 16, v84\n"
"v_and_b32 v84, 0xffff0000, v84\n"
"v_mul_f32 v85, v85, v242\n"
"v_mul_f32 v84, v84, v242\n"
"v_cvt_pk_bf16_f32 v84, v85, v84\n"
"v_accvgpr_write_b32 a139, v84\n"
"v_accvgpr_read_b32 v84, a140\n"
"v_lshlrev_b32 v85, 16, v84\n"
"v_and_b32 v84, 0xffff0000, v84\n"
"v_mul_f32 v85, v85, v242\n"
"v_mul_f32 v84, v84, v242\n"
"v_cvt_pk_bf16_f32 v84, v85, v84\n"
"v_accvgpr_write_b32 a140, v84\n"
"v_accvgpr_read_b32 v84, a141\n"
"v_lshlrev_b32 v85, 16, v84\n"
"v_and_b32 v84, 0xffff0000, v84\n"
"v_mul_f32 v85, v85, v242\n"
"v_mul_f32 v84, v84, v242\n"
"v_cvt_pk_bf16_f32 v84, v85, v84\n"
"v_accvgpr_write_b32 a141, v84\n"
"v_accvgpr_read_b32 v84, a142\n"
"v_lshlrev_b32 v85, 16, v84\n"
"v_and_b32 v84, 0xffff0000, v84\n"
"v_mul_f32 v85, v85, v242\n"
"v_mul_f32 v84, v84, v242\n"
"v_cvt_pk_bf16_f32 v84, v85, v84\n"
"v_accvgpr_write_b32 a142, v84\n"
"v_accvgpr_read_b32 v84, a143\n"
"v_lshlrev_b32 v85, 16, v84\n"
"v_and_b32 v84, 0xffff0000, v84\n"
"v_mul_f32 v85, v85, v242\n"
"v_mul_f32 v84, v84, v242\n"
"v_cvt_pk_bf16_f32 v84, v85, v84\n"
"v_accvgpr_write_b32 a143, v84\n"
"v_accvgpr_read_b32 v84, a144\n"
"v_lshlrev_b32 v85, 16, v84\n"
"v_and_b32 v84, 0xffff0000, v84\n"
"v_mul_f32 v85, v85, v242\n"
"v_mul_f32 v84, v84, v242\n"
"v_cvt_pk_bf16_f32 v84, v85, v84\n"
"v_accvgpr_write_b32 a144, v84\n"
"v_accvgpr_read_b32 v84, a145\n"
"v_lshlrev_b32 v85, 16, v84\n"
"v_and_b32 v84, 0xffff0000, v84\n"
"v_mul_f32 v85, v85, v242\n"
"v_mul_f32 v84, v84, v242\n"
"v_cvt_pk_bf16_f32 v84, v85, v84\n"
"v_accvgpr_write_b32 a145, v84\n"
"v_accvgpr_read_b32 v84, a146\n"
"v_lshlrev_b32 v85, 16, v84\n"
"v_and_b32 v84, 0xffff0000, v84\n"
"v_mul_f32 v85, v85, v242\n"
"v_mul_f32 v84, v84, v242\n"
"v_cvt_pk_bf16_f32 v84, v85, v84\n"
"v_accvgpr_write_b32 a146, v84\n"
"v_accvgpr_read_b32 v84, a147\n"
"v_lshlrev_b32 v85, 16, v84\n"
"v_and_b32 v84, 0xffff0000, v84\n"
"v_mul_f32 v85, v85, v242\n"
"v_mul_f32 v84, v84, v242\n"
"v_cvt_pk_bf16_f32 v84, v85, v84\n"
"v_accvgpr_write_b32 a147, v84\n"
"v_accvgpr_read_b32 v84, a148\n"
"v_lshlrev_b32 v85, 16, v84\n"
"v_and_b32 v84, 0xffff0000, v84\n"
"v_mul_f32 v85, v85, v242\n"
"v_mul_f32 v84, v84, v242\n"
"v_cvt_pk_bf16_f32 v84, v85, v84\n"
"v_accvgpr_write_b32 a148, v84\n"
"v_accvgpr_read_b32 v84, a149\n"
"v_lshlrev_b32 v85, 16, v84\n"
"v_and_b32 v84, 0xffff0000, v84\n"
"v_mul_f32 v85, v85, v242\n"
"v_mul_f32 v84, v84, v242\n"
"v_cvt_pk_bf16_f32 v84, v85, v84\n"
"v_accvgpr_write_b32 a149, v84\n"
"v_accvgpr_read_b32 v84, a150\n"
"v_lshlrev_b32 v85, 16, v84\n"
"v_and_b32 v84, 0xffff0000, v84\n"
"v_mul_f32 v85, v85, v242\n"
"v_mul_f32 v84, v84, v242\n"
"v_cvt_pk_bf16_f32 v84, v85, v84\n"
"v_accvgpr_write_b32 a150, v84\n"
"v_accvgpr_read_b32 v84, a151\n"
"v_lshlrev_b32 v85, 16, v84\n"
"v_and_b32 v84, 0xffff0000, v84\n"
"v_mul_f32 v85, v85, v242\n"
"v_mul_f32 v84, v84, v242\n"
"v_cvt_pk_bf16_f32 v84, v85, v84\n"
"v_accvgpr_write_b32 a151, v84\n"
"v_accvgpr_read_b32 v84, a152\n"
"v_lshlrev_b32 v85, 16, v84\n"
"v_and_b32 v84, 0xffff0000, v84\n"
"v_mul_f32 v85, v85, v242\n"
"v_mul_f32 v84, v84, v242\n"
"v_cvt_pk_bf16_f32 v84, v85, v84\n"
"v_accvgpr_write_b32 a152, v84\n"
"v_accvgpr_read_b32 v84, a153\n"
"v_lshlrev_b32 v85, 16, v84\n"
"v_and_b32 v84, 0xffff0000, v84\n"
"v_mul_f32 v85, v85, v242\n"
"v_mul_f32 v84, v84, v242\n"
"v_cvt_pk_bf16_f32 v84, v85, v84\n"
"v_accvgpr_write_b32 a153, v84\n"
"v_accvgpr_read_b32 v84, a154\n"
"v_lshlrev_b32 v85, 16, v84\n"
"v_and_b32 v84, 0xffff0000, v84\n"
"v_mul_f32 v85, v85, v242\n"
"v_mul_f32 v84, v84, v242\n"
"v_cvt_pk_bf16_f32 v84, v85, v84\n"
"v_accvgpr_write_b32 a154, v84\n"
"v_accvgpr_read_b32 v84, a155\n"
"v_lshlrev_b32 v85, 16, v84\n"
"v_and_b32 v84, 0xffff0000, v84\n"
"v_mul_f32 v85, v85, v242\n"
"v_mul_f32 v84, v84, v242\n"
"v_cvt_pk_bf16_f32 v84, v85, v84\n"
"v_accvgpr_write_b32 a155, v84\n"
"v_accvgpr_read_b32 v84, a156\n"
"v_lshlrev_b32 v85, 16, v84\n"
"v_and_b32 v84, 0xffff0000, v84\n"
"v_mul_f32 v85, v85, v242\n"
"v_mul_f32 v84, v84, v242\n"
"v_cvt_pk_bf16_f32 v84, v85, v84\n"
"v_accvgpr_write_b32 a156, v84\n"
"v_accvgpr_read_b32 v84, a157\n"
"v_lshlrev_b32 v85, 16, v84\n"
"v_and_b32 v84, 0xffff0000, v84\n"
"v_mul_f32 v85, v85, v242\n"
"v_mul_f32 v84, v84, v242\n"
"v_cvt_pk_bf16_f32 v84, v85, v84\n"
"v_accvgpr_write_b32 a157, v84\n"
"v_accvgpr_read_b32 v84, a158\n"
"v_lshlrev_b32 v85, 16, v84\n"
"v_and_b32 v84, 0xffff0000, v84\n"
"v_mul_f32 v85, v85, v242\n"
"v_mul_f32 v84, v84, v242\n"
"v_cvt_pk_bf16_f32 v84, v85, v84\n"
"v_accvgpr_write_b32 a158, v84\n"
"v_accvgpr_read_b32 v84, a159\n"
"v_lshlrev_b32 v85, 16, v84\n"
"v_and_b32 v84, 0xffff0000, v84\n"
"v_mul_f32 v85, v85, v242\n"
"v_mul_f32 v84, v84, v242\n"
"v_cvt_pk_bf16_f32 v84, v85, v84\n"
"v_accvgpr_write_b32 a159, v84\n"
"v_min_f32 v240, v238, v250\n"
"v_sub_f32 v241, v240, v238\n"
"v_sub_f32 v242, v240, v250\n"
"v_exp_f32 v241, v241\n"
"v_exp_f32 v242, v242\n"
"v_mul_f32 v234, v234, v241\n"
"v_mul_f32 v251, v251, v242\n"
"v_accvgpr_read_b32 v80, a4\n"
"v_accvgpr_read_b32 v81, a5\n"
"v_accvgpr_read_b32 v82, a6\n"
"v_accvgpr_read_b32 v83, a7\n"
"v_mul_f32 v80, v80, v241\n"
"v_mul_f32 v81, v81, v241\n"
"v_mul_f32 v82, v82, v241\n"
"v_mul_f32 v83, v83, v241\n"
"v_accvgpr_write_b32 a4, v80\n"
"v_accvgpr_write_b32 a5, v81\n"
"v_accvgpr_write_b32 a6, v82\n"
"v_accvgpr_write_b32 a7, v83\n"
"v_accvgpr_read_b32 v80, a12\n"
"v_accvgpr_read_b32 v81, a13\n"
"v_accvgpr_read_b32 v82, a14\n"
"v_accvgpr_read_b32 v83, a15\n"
"v_mul_f32 v80, v80, v241\n"
"v_mul_f32 v81, v81, v241\n"
"v_mul_f32 v82, v82, v241\n"
"v_mul_f32 v83, v83, v241\n"
"v_accvgpr_write_b32 a12, v80\n"
"v_accvgpr_write_b32 a13, v81\n"
"v_accvgpr_write_b32 a14, v82\n"
"v_accvgpr_write_b32 a15, v83\n"
"v_accvgpr_read_b32 v80, a20\n"
"v_accvgpr_read_b32 v81, a21\n"
"v_accvgpr_read_b32 v82, a22\n"
"v_accvgpr_read_b32 v83, a23\n"
"v_mul_f32 v80, v80, v241\n"
"v_mul_f32 v81, v81, v241\n"
"v_mul_f32 v82, v82, v241\n"
"v_mul_f32 v83, v83, v241\n"
"v_accvgpr_write_b32 a20, v80\n"
"v_accvgpr_write_b32 a21, v81\n"
"v_accvgpr_write_b32 a22, v82\n"
"v_accvgpr_write_b32 a23, v83\n"
"v_accvgpr_read_b32 v80, a28\n"
"v_accvgpr_read_b32 v81, a29\n"
"v_accvgpr_read_b32 v82, a30\n"
"v_accvgpr_read_b32 v83, a31\n"
"v_mul_f32 v80, v80, v241\n"
"v_mul_f32 v81, v81, v241\n"
"v_mul_f32 v82, v82, v241\n"
"v_mul_f32 v83, v83, v241\n"
"v_accvgpr_write_b32 a28, v80\n"
"v_accvgpr_write_b32 a29, v81\n"
"v_accvgpr_write_b32 a30, v82\n"
"v_accvgpr_write_b32 a31, v83\n"
"v_accvgpr_read_b32 v80, a36\n"
"v_accvgpr_read_b32 v81, a37\n"
"v_accvgpr_read_b32 v82, a38\n"
"v_accvgpr_read_b32 v83, a39\n"
"v_mul_f32 v80, v80, v241\n"
"v_mul_f32 v81, v81, v241\n"
"v_mul_f32 v82, v82, v241\n"
"v_mul_f32 v83, v83, v241\n"
"v_accvgpr_write_b32 a36, v80\n"
"v_accvgpr_write_b32 a37, v81\n"
"v_accvgpr_write_b32 a38, v82\n"
"v_accvgpr_write_b32 a39, v83\n"
"v_accvgpr_read_b32 v80, a44\n"
"v_accvgpr_read_b32 v81, a45\n"
"v_accvgpr_read_b32 v82, a46\n"
"v_accvgpr_read_b32 v83, a47\n"
"v_mul_f32 v80, v80, v241\n"
"v_mul_f32 v81, v81, v241\n"
"v_mul_f32 v82, v82, v241\n"
"v_mul_f32 v83, v83, v241\n"
"v_accvgpr_write_b32 a44, v80\n"
"v_accvgpr_write_b32 a45, v81\n"
"v_accvgpr_write_b32 a46, v82\n"
"v_accvgpr_write_b32 a47, v83\n"
"v_accvgpr_read_b32 v80, a52\n"
"v_accvgpr_read_b32 v81, a53\n"
"v_accvgpr_read_b32 v82, a54\n"
"v_accvgpr_read_b32 v83, a55\n"
"v_mul_f32 v80, v80, v241\n"
"v_mul_f32 v81, v81, v241\n"
"v_mul_f32 v82, v82, v241\n"
"v_mul_f32 v83, v83, v241\n"
"v_accvgpr_write_b32 a52, v80\n"
"v_accvgpr_write_b32 a53, v81\n"
"v_accvgpr_write_b32 a54, v82\n"
"v_accvgpr_write_b32 a55, v83\n"
"v_accvgpr_read_b32 v80, a60\n"
"v_accvgpr_read_b32 v81, a61\n"
"v_accvgpr_read_b32 v82, a62\n"
"v_accvgpr_read_b32 v83, a63\n"
"v_mul_f32 v80, v80, v241\n"
"v_mul_f32 v81, v81, v241\n"
"v_mul_f32 v82, v82, v241\n"
"v_mul_f32 v83, v83, v241\n"
"v_accvgpr_write_b32 a60, v80\n"
"v_accvgpr_write_b32 a61, v81\n"
"v_accvgpr_write_b32 a62, v82\n"
"v_accvgpr_write_b32 a63, v83\n"
"v_accvgpr_read_b32 v80, a68\n"
"v_accvgpr_read_b32 v81, a69\n"
"v_accvgpr_read_b32 v82, a70\n"
"v_accvgpr_read_b32 v83, a71\n"
"v_mul_f32 v80, v80, v241\n"
"v_mul_f32 v81, v81, v241\n"
"v_mul_f32 v82, v82, v241\n"
"v_mul_f32 v83, v83, v241\n"
"v_accvgpr_write_b32 a68, v80\n"
"v_accvgpr_write_b32 a69, v81\n"
"v_accvgpr_write_b32 a70, v82\n"
"v_accvgpr_write_b32 a71, v83\n"
"v_accvgpr_read_b32 v80, a76\n"
"v_accvgpr_read_b32 v81, a77\n"
"v_accvgpr_read_b32 v82, a78\n"
"v_accvgpr_read_b32 v83, a79\n"
"v_mul_f32 v80, v80, v241\n"
"v_mul_f32 v81, v81, v241\n"
"v_mul_f32 v82, v82, v241\n"
"v_mul_f32 v83, v83, v241\n"
"v_accvgpr_write_b32 a76, v80\n"
"v_accvgpr_write_b32 a77, v81\n"
"v_accvgpr_write_b32 a78, v82\n"
"v_accvgpr_write_b32 a79, v83\n"
"v_accvgpr_read_b32 v80, a84\n"
"v_accvgpr_read_b32 v81, a85\n"
"v_accvgpr_read_b32 v82, a86\n"
"v_accvgpr_read_b32 v83, a87\n"
"v_mul_f32 v80, v80, v241\n"
"v_mul_f32 v81, v81, v241\n"
"v_mul_f32 v82, v82, v241\n"
"v_mul_f32 v83, v83, v241\n"
"v_accvgpr_write_b32 a84, v80\n"
"v_accvgpr_write_b32 a85, v81\n"
"v_accvgpr_write_b32 a86, v82\n"
"v_accvgpr_write_b32 a87, v83\n"
"v_accvgpr_read_b32 v80, a92\n"
"v_accvgpr_read_b32 v81, a93\n"
"v_accvgpr_read_b32 v82, a94\n"
"v_accvgpr_read_b32 v83, a95\n"
"v_mul_f32 v80, v80, v241\n"
"v_mul_f32 v81, v81, v241\n"
"v_mul_f32 v82, v82, v241\n"
"v_mul_f32 v83, v83, v241\n"
"v_accvgpr_write_b32 a92, v80\n"
"v_accvgpr_write_b32 a93, v81\n"
"v_accvgpr_write_b32 a94, v82\n"
"v_accvgpr_write_b32 a95, v83\n"
"v_accvgpr_read_b32 v80, a100\n"
"v_accvgpr_read_b32 v81, a101\n"
"v_accvgpr_read_b32 v82, a102\n"
"v_accvgpr_read_b32 v83, a103\n"
"v_mul_f32 v80, v80, v241\n"
"v_mul_f32 v81, v81, v241\n"
"v_mul_f32 v82, v82, v241\n"
"v_mul_f32 v83, v83, v241\n"
"v_accvgpr_write_b32 a100, v80\n"
"v_accvgpr_write_b32 a101, v81\n"
"v_accvgpr_write_b32 a102, v82\n"
"v_accvgpr_write_b32 a103, v83\n"
"v_accvgpr_read_b32 v80, a108\n"
"v_accvgpr_read_b32 v81, a109\n"
"v_accvgpr_read_b32 v82, a110\n"
"v_accvgpr_read_b32 v83, a111\n"
"v_mul_f32 v80, v80, v241\n"
"v_mul_f32 v81, v81, v241\n"
"v_mul_f32 v82, v82, v241\n"
"v_mul_f32 v83, v83, v241\n"
"v_accvgpr_write_b32 a108, v80\n"
"v_accvgpr_write_b32 a109, v81\n"
"v_accvgpr_write_b32 a110, v82\n"
"v_accvgpr_write_b32 a111, v83\n"
"v_accvgpr_read_b32 v80, a116\n"
"v_accvgpr_read_b32 v81, a117\n"
"v_accvgpr_read_b32 v82, a118\n"
"v_accvgpr_read_b32 v83, a119\n"
"v_mul_f32 v80, v80, v241\n"
"v_mul_f32 v81, v81, v241\n"
"v_mul_f32 v82, v82, v241\n"
"v_mul_f32 v83, v83, v241\n"
"v_accvgpr_write_b32 a116, v80\n"
"v_accvgpr_write_b32 a117, v81\n"
"v_accvgpr_write_b32 a118, v82\n"
"v_accvgpr_write_b32 a119, v83\n"
"v_accvgpr_read_b32 v80, a124\n"
"v_accvgpr_read_b32 v81, a125\n"
"v_accvgpr_read_b32 v82, a126\n"
"v_accvgpr_read_b32 v83, a127\n"
"v_mul_f32 v80, v80, v241\n"
"v_mul_f32 v81, v81, v241\n"
"v_mul_f32 v82, v82, v241\n"
"v_mul_f32 v83, v83, v241\n"
"v_accvgpr_write_b32 a124, v80\n"
"v_accvgpr_write_b32 a125, v81\n"
"v_accvgpr_write_b32 a126, v82\n"
"v_accvgpr_write_b32 a127, v83\n"
"v_accvgpr_read_b32 v84, a160\n"
"v_lshlrev_b32 v85, 16, v84\n"
"v_and_b32 v84, 0xffff0000, v84\n"
"v_mul_f32 v85, v85, v242\n"
"v_mul_f32 v84, v84, v242\n"
"v_cvt_pk_bf16_f32 v84, v85, v84\n"
"v_accvgpr_write_b32 a160, v84\n"
"v_accvgpr_read_b32 v84, a161\n"
"v_lshlrev_b32 v85, 16, v84\n"
"v_and_b32 v84, 0xffff0000, v84\n"
"v_mul_f32 v85, v85, v242\n"
"v_mul_f32 v84, v84, v242\n"
"v_cvt_pk_bf16_f32 v84, v85, v84\n"
"v_accvgpr_write_b32 a161, v84\n"
"v_accvgpr_read_b32 v84, a162\n"
"v_lshlrev_b32 v85, 16, v84\n"
"v_and_b32 v84, 0xffff0000, v84\n"
"v_mul_f32 v85, v85, v242\n"
"v_mul_f32 v84, v84, v242\n"
"v_cvt_pk_bf16_f32 v84, v85, v84\n"
"v_accvgpr_write_b32 a162, v84\n"
"v_accvgpr_read_b32 v84, a163\n"
"v_lshlrev_b32 v85, 16, v84\n"
"v_and_b32 v84, 0xffff0000, v84\n"
"v_mul_f32 v85, v85, v242\n"
"v_mul_f32 v84, v84, v242\n"
"v_cvt_pk_bf16_f32 v84, v85, v84\n"
"v_accvgpr_write_b32 a163, v84\n"
"v_accvgpr_read_b32 v84, a164\n"
"v_lshlrev_b32 v85, 16, v84\n"
"v_and_b32 v84, 0xffff0000, v84\n"
"v_mul_f32 v85, v85, v242\n"
"v_mul_f32 v84, v84, v242\n"
"v_cvt_pk_bf16_f32 v84, v85, v84\n"
"v_accvgpr_write_b32 a164, v84\n"
"v_accvgpr_read_b32 v84, a165\n"
"v_lshlrev_b32 v85, 16, v84\n"
"v_and_b32 v84, 0xffff0000, v84\n"
"v_mul_f32 v85, v85, v242\n"
"v_mul_f32 v84, v84, v242\n"
"v_cvt_pk_bf16_f32 v84, v85, v84\n"
"v_accvgpr_write_b32 a165, v84\n"
"v_accvgpr_read_b32 v84, a166\n"
"v_lshlrev_b32 v85, 16, v84\n"
"v_and_b32 v84, 0xffff0000, v84\n"
"v_mul_f32 v85, v85, v242\n"
"v_mul_f32 v84, v84, v242\n"
"v_cvt_pk_bf16_f32 v84, v85, v84\n"
"v_accvgpr_write_b32 a166, v84\n"
"v_accvgpr_read_b32 v84, a167\n"
"v_lshlrev_b32 v85, 16, v84\n"
"v_and_b32 v84, 0xffff0000, v84\n"
"v_mul_f32 v85, v85, v242\n"
"v_mul_f32 v84, v84, v242\n"
"v_cvt_pk_bf16_f32 v84, v85, v84\n"
"v_accvgpr_write_b32 a167, v84\n"
"v_accvgpr_read_b32 v84, a168\n"
"v_lshlrev_b32 v85, 16, v84\n"
"v_and_b32 v84, 0xffff0000, v84\n"
"v_mul_f32 v85, v85, v242\n"
"v_mul_f32 v84, v84, v242\n"
"v_cvt_pk_bf16_f32 v84, v85, v84\n"
"v_accvgpr_write_b32 a168, v84\n"
"v_accvgpr_read_b32 v84, a169\n"
"v_lshlrev_b32 v85, 16, v84\n"
"v_and_b32 v84, 0xffff0000, v84\n"
"v_mul_f32 v85, v85, v242\n"
"v_mul_f32 v84, v84, v242\n"
"v_cvt_pk_bf16_f32 v84, v85, v84\n"
"v_accvgpr_write_b32 a169, v84\n"
"v_accvgpr_read_b32 v84, a170\n"
"v_lshlrev_b32 v85, 16, v84\n"
"v_and_b32 v84, 0xffff0000, v84\n"
"v_mul_f32 v85, v85, v242\n"
"v_mul_f32 v84, v84, v242\n"
"v_cvt_pk_bf16_f32 v84, v85, v84\n"
"v_accvgpr_write_b32 a170, v84\n"
"v_accvgpr_read_b32 v84, a171\n"
"v_lshlrev_b32 v85, 16, v84\n"
"v_and_b32 v84, 0xffff0000, v84\n"
"v_mul_f32 v85, v85, v242\n"
"v_mul_f32 v84, v84, v242\n"
"v_cvt_pk_bf16_f32 v84, v85, v84\n"
"v_accvgpr_write_b32 a171, v84\n"
"v_accvgpr_read_b32 v84, a172\n"
"v_lshlrev_b32 v85, 16, v84\n"
"v_and_b32 v84, 0xffff0000, v84\n"
"v_mul_f32 v85, v85, v242\n"
"v_mul_f32 v84, v84, v242\n"
"v_cvt_pk_bf16_f32 v84, v85, v84\n"
"v_accvgpr_write_b32 a172, v84\n"
"v_accvgpr_read_b32 v84, a173\n"
"v_lshlrev_b32 v85, 16, v84\n"
"v_and_b32 v84, 0xffff0000, v84\n"
"v_mul_f32 v85, v85, v242\n"
"v_mul_f32 v84, v84, v242\n"
"v_cvt_pk_bf16_f32 v84, v85, v84\n"
"v_accvgpr_write_b32 a173, v84\n"
"v_accvgpr_read_b32 v84, a174\n"
"v_lshlrev_b32 v85, 16, v84\n"
"v_and_b32 v84, 0xffff0000, v84\n"
"v_mul_f32 v85, v85, v242\n"
"v_mul_f32 v84, v84, v242\n"
"v_cvt_pk_bf16_f32 v84, v85, v84\n"
"v_accvgpr_write_b32 a174, v84\n"
"v_accvgpr_read_b32 v84, a175\n"
"v_lshlrev_b32 v85, 16, v84\n"
"v_and_b32 v84, 0xffff0000, v84\n"
"v_mul_f32 v85, v85, v242\n"
"v_mul_f32 v84, v84, v242\n"
"v_cvt_pk_bf16_f32 v84, v85, v84\n"
"v_accvgpr_write_b32 a175, v84\n"
"v_accvgpr_read_b32 v84, a176\n"
"v_lshlrev_b32 v85, 16, v84\n"
"v_and_b32 v84, 0xffff0000, v84\n"
"v_mul_f32 v85, v85, v242\n"
"v_mul_f32 v84, v84, v242\n"
"v_cvt_pk_bf16_f32 v84, v85, v84\n"
"v_accvgpr_write_b32 a176, v84\n"
"v_accvgpr_read_b32 v84, a177\n"
"v_lshlrev_b32 v85, 16, v84\n"
"v_and_b32 v84, 0xffff0000, v84\n"
"v_mul_f32 v85, v85, v242\n"
"v_mul_f32 v84, v84, v242\n"
"v_cvt_pk_bf16_f32 v84, v85, v84\n"
"v_accvgpr_write_b32 a177, v84\n"
"v_accvgpr_read_b32 v84, a178\n"
"v_lshlrev_b32 v85, 16, v84\n"
"v_and_b32 v84, 0xffff0000, v84\n"
"v_mul_f32 v85, v85, v242\n"
"v_mul_f32 v84, v84, v242\n"
"v_cvt_pk_bf16_f32 v84, v85, v84\n"
"v_accvgpr_write_b32 a178, v84\n"
"v_accvgpr_read_b32 v84, a179\n"
"v_lshlrev_b32 v85, 16, v84\n"
"v_and_b32 v84, 0xffff0000, v84\n"
"v_mul_f32 v85, v85, v242\n"
"v_mul_f32 v84, v84, v242\n"
"v_cvt_pk_bf16_f32 v84, v85, v84\n"
"v_accvgpr_write_b32 a179, v84\n"
"v_accvgpr_read_b32 v84, a180\n"
"v_lshlrev_b32 v85, 16, v84\n"
"v_and_b32 v84, 0xffff0000, v84\n"
"v_mul_f32 v85, v85, v242\n"
"v_mul_f32 v84, v84, v242\n"
"v_cvt_pk_bf16_f32 v84, v85, v84\n"
"v_accvgpr_write_b32 a180, v84\n"
"v_accvgpr_read_b32 v84, a181\n"
"v_lshlrev_b32 v85, 16, v84\n"
"v_and_b32 v84, 0xffff0000, v84\n"
"v_mul_f32 v85, v85, v242\n"
"v_mul_f32 v84, v84, v242\n"
"v_cvt_pk_bf16_f32 v84, v85, v84\n"
"v_accvgpr_write_b32 a181, v84\n"
"v_accvgpr_read_b32 v84, a182\n"
"v_lshlrev_b32 v85, 16, v84\n"
"v_and_b32 v84, 0xffff0000, v84\n"
"v_mul_f32 v85, v85, v242\n"
"v_mul_f32 v84, v84, v242\n"
"v_cvt_pk_bf16_f32 v84, v85, v84\n"
"v_accvgpr_write_b32 a182, v84\n"
"v_accvgpr_read_b32 v84, a183\n"
"v_lshlrev_b32 v85, 16, v84\n"
"v_and_b32 v84, 0xffff0000, v84\n"
"v_mul_f32 v85, v85, v242\n"
"v_mul_f32 v84, v84, v242\n"
"v_cvt_pk_bf16_f32 v84, v85, v84\n"
"v_accvgpr_write_b32 a183, v84\n"
"v_accvgpr_read_b32 v84, a184\n"
"v_lshlrev_b32 v85, 16, v84\n"
"v_and_b32 v84, 0xffff0000, v84\n"
"v_mul_f32 v85, v85, v242\n"
"v_mul_f32 v84, v84, v242\n"
"v_cvt_pk_bf16_f32 v84, v85, v84\n"
"v_accvgpr_write_b32 a184, v84\n"
"v_accvgpr_read_b32 v84, a185\n"
"v_lshlrev_b32 v85, 16, v84\n"
"v_and_b32 v84, 0xffff0000, v84\n"
"v_mul_f32 v85, v85, v242\n"
"v_mul_f32 v84, v84, v242\n"
"v_cvt_pk_bf16_f32 v84, v85, v84\n"
"v_accvgpr_write_b32 a185, v84\n"
"v_accvgpr_read_b32 v84, a186\n"
"v_lshlrev_b32 v85, 16, v84\n"
"v_and_b32 v84, 0xffff0000, v84\n"
"v_mul_f32 v85, v85, v242\n"
"v_mul_f32 v84, v84, v242\n"
"v_cvt_pk_bf16_f32 v84, v85, v84\n"
"v_accvgpr_write_b32 a186, v84\n"
"v_accvgpr_read_b32 v84, a187\n"
"v_lshlrev_b32 v85, 16, v84\n"
"v_and_b32 v84, 0xffff0000, v84\n"
"v_mul_f32 v85, v85, v242\n"
"v_mul_f32 v84, v84, v242\n"
"v_cvt_pk_bf16_f32 v84, v85, v84\n"
"v_accvgpr_write_b32 a187, v84\n"
"v_accvgpr_read_b32 v84, a188\n"
"v_lshlrev_b32 v85, 16, v84\n"
"v_and_b32 v84, 0xffff0000, v84\n"
"v_mul_f32 v85, v85, v242\n"
"v_mul_f32 v84, v84, v242\n"
"v_cvt_pk_bf16_f32 v84, v85, v84\n"
"v_accvgpr_write_b32 a188, v84\n"
"v_accvgpr_read_b32 v84, a189\n"
"v_lshlrev_b32 v85, 16, v84\n"
"v_and_b32 v84, 0xffff0000, v84\n"
"v_mul_f32 v85, v85, v242\n"
"v_mul_f32 v84, v84, v242\n"
"v_cvt_pk_bf16_f32 v84, v85, v84\n"
"v_accvgpr_write_b32 a189, v84\n"
"v_accvgpr_read_b32 v84, a190\n"
"v_lshlrev_b32 v85, 16, v84\n"
"v_and_b32 v84, 0xffff0000, v84\n"
"v_mul_f32 v85, v85, v242\n"
"v_mul_f32 v84, v84, v242\n"
"v_cvt_pk_bf16_f32 v84, v85, v84\n"
"v_accvgpr_write_b32 a190, v84\n"
"v_accvgpr_read_b32 v84, a191\n"
"v_lshlrev_b32 v85, 16, v84\n"
"v_and_b32 v84, 0xffff0000, v84\n"
"v_mul_f32 v85, v85, v242\n"
"v_mul_f32 v84, v84, v242\n"
"v_cvt_pk_bf16_f32 v84, v85, v84\n"
"v_accvgpr_write_b32 a191, v84\n"
"s_nop 1\n"
"s_branch L_eqr%=\n"
"L_m0%=:\n"
"s_cmp_eq_u32 s52, 0\n"
"s_cselect_b32 s54, -1, 0\n"
"s_cmp_eq_u32 s52, 1\n"
"s_cselect_b32 s55, -1, 0\n"
"v_and_b32 v240, s54, v229\n"
"v_and_b32 v241, s55, v230\n"
"v_or_b32 v240, v240, v241\n"
"v_mov_b32 v241, 0xff800000\n"
"v_bfe_i32 v244, v240, 0, 1\n"
"v_bfe_i32 v245, v240, 1, 1\n"
"v_bfe_i32 v246, v240, 2, 1\n"
"v_bfe_i32 v247, v240, 3, 1\n"
"v_bfi_b32 v96, v244, v241, v237\n"
"v_bfi_b32 v97, v245, v241, v237\n"
"v_bfi_b32 v98, v246, v241, v237\n"
"v_bfi_b32 v99, v247, v241, v237\n"
"v_bfe_i32 v244, v240, 4, 1\n"
"v_bfe_i32 v245, v240, 5, 1\n"
"v_bfe_i32 v246, v240, 6, 1\n"
"v_bfe_i32 v247, v240, 7, 1\n"
"v_bfi_b32 v100, v244, v241, v238\n"
"v_bfi_b32 v101, v245, v241, v238\n"
"v_bfi_b32 v102, v246, v241, v238\n"
"v_bfi_b32 v103, v247, v241, v238\n"
"v_bfe_i32 v244, v240, 8, 1\n"
"v_bfe_i32 v245, v240, 9, 1\n"
"v_bfe_i32 v246, v240, 10, 1\n"
"v_bfe_i32 v247, v240, 11, 1\n"
"v_bfi_b32 v104, v244, v241, v237\n"
"v_bfi_b32 v105, v245, v241, v237\n"
"v_bfi_b32 v106, v246, v241, v237\n"
"v_bfi_b32 v107, v247, v241, v237\n"
"v_bfe_i32 v244, v240, 12, 1\n"
"v_bfe_i32 v245, v240, 13, 1\n"
"v_bfe_i32 v246, v240, 14, 1\n"
"v_bfe_i32 v247, v240, 15, 1\n"
"v_bfi_b32 v108, v244, v241, v238\n"
"v_bfi_b32 v109, v245, v241, v238\n"
"v_bfi_b32 v110, v246, v241, v238\n"
"v_bfi_b32 v111, v247, v241, v238\n"
"v_bfe_i32 v244, v240, 16, 1\n"
"v_bfe_i32 v245, v240, 17, 1\n"
"v_bfe_i32 v246, v240, 18, 1\n"
"v_bfe_i32 v247, v240, 19, 1\n"
"v_bfi_b32 v112, v244, v241, v237\n"
"v_bfi_b32 v113, v245, v241, v237\n"
"v_bfi_b32 v114, v246, v241, v237\n"
"v_bfi_b32 v115, v247, v241, v237\n"
"v_bfe_i32 v244, v240, 20, 1\n"
"v_bfe_i32 v245, v240, 21, 1\n"
"v_bfe_i32 v246, v240, 22, 1\n"
"v_bfe_i32 v247, v240, 23, 1\n"
"v_bfi_b32 v116, v244, v241, v238\n"
"v_bfi_b32 v117, v245, v241, v238\n"
"v_bfi_b32 v118, v246, v241, v238\n"
"v_bfi_b32 v119, v247, v241, v238\n"
"v_bfe_i32 v244, v240, 24, 1\n"
"v_bfe_i32 v245, v240, 25, 1\n"
"v_bfe_i32 v246, v240, 26, 1\n"
"v_bfe_i32 v247, v240, 27, 1\n"
"v_bfi_b32 v120, v244, v241, v237\n"
"v_bfi_b32 v121, v245, v241, v237\n"
"v_bfi_b32 v122, v246, v241, v237\n"
"v_bfi_b32 v123, v247, v241, v237\n"
"v_bfe_i32 v244, v240, 28, 1\n"
"v_bfe_i32 v245, v240, 29, 1\n"
"v_bfe_i32 v246, v240, 30, 1\n"
"v_bfe_i32 v247, v240, 31, 1\n"
"v_bfi_b32 v124, v244, v241, v238\n"
"v_bfi_b32 v125, v245, v241, v238\n"
"v_bfi_b32 v126, v246, v241, v238\n"
"v_bfi_b32 v127, v247, v241, v238\n"
"s_nop 1\n"
"s_branch L_m0r%=\n"
"L_end%=:\n"
"L_fused_code_end:\n"
    :: "s"(ka), "{v[160:167]}"(kv1), "{v[168:175]}"(kv2), "{v176}"(kv3[0]), "{v177}"(kv3[1]), "{v202}"(lnx), "{v252}"(lnb), "{v223}"(stv), "{v227}"(l16),
       "{v231}"(vz), "{s[16:17]}"(TAB), "{s[64:71]}"(ks1), "{s[72:79]}"(ks2), "{s33}"(w), "{s34}"(bid), "{s7}"(h),
       "{s[36:43]}"(rt), "{s[24:27]}"(p0), "{s[12:15]}"(p1), "{s[20:21]}"(p2), "{s[60:61]}"(p3), "{s[88:89]}"(p4), "{s[96:97]}"(p5), "{s[28:29]}"(p6), "{s92}"(dm), "{v254}"(pw0), "{v253}"(pc0) : "v0","v1","v2","v3","v4","v5","v6","v7","v8","v9","v10","v11","v12","v13","v14","v15","v16","v17","v18","v19","v20","v21","v22","v23","v24","v25","v26","v27","v28","v29","v30","v31","v32","v33","v34","v35","v36","v37","v38","v39","v40","v41","v42","v43","v44","v45","v46","v47","v48","v49","v50","v51","v52","v53","v54","v55","v56","v57","v58","v59","v60","v61","v62","v63","v64","v65","v66","v67","v68","v69","v70","v71","v72","v73","v74","v75","v76","v77","v78","v79","v80","v81","v82","v83","v84","v85","v86","v87","v88","v89","v90","v91","v92","v93","v94","v95","v96","v97","v98","v99","v100","v101","v102","v103","v104","v105","v106","v107","v108","v109","v110","v111","v112","v113","v114","v115","v116","v117","v118","v119","v120","v121","v122","v123","v124","v125","v126","v127","v128","v129","v130","v131","v132","v133","v134","v135","v136","v137","v138","v139","v140","v141","v142","v143","v144","v145","v146","v147","v148","v149","v150","v151","v152","v153","v154","v155","v156","v157","v158","v159","v178","v179","v180","v181","v182","v183","v184","v185","v186","v187","v188","v189","v190","v191","v192","v193","v194","v195","v196","v197","v198","v199","v200","v201","v203","v204","v205","v206","v207","v208","v209","v210","v211","v212","v213","v214","v215","v216","v217","v218","v219","v220","v221","v222","v224","v225","v226","v228","v229","v230","v232","v233","v234","v235","v236","v237","v238","v239","v240","v241","v242","v243","v244","v245","v246","v247","v248","v249","v250","v251","v255","a0","a1","a2","a3","a4","a5","a6","a7","a8","a9","a10","a11","a12","a13","a14","a15","a16","a17","a18","a19","a20","a21","a22","a23","a24","a25","a26","a27","a28","a29","a30","a31","a32","a33","a34","a35","a36","a37","a38","a39","a40","a41","a42","a43","a44","a45","a46","a47","a48","a49","a50","a51","a52","a53","a54","a55","a56","a57","a58","a59","a60","a61","a62","a63","a64","a65","a66","a67","a68","a69","a70","a71","a72","a73","a74","a75","a76","a77","a78","a79","a80","a81","a82","a83","a84","a85","a86","a87","a88","a89","a90","a91","a92","a93","a94","a95","a96","a97","a98","a99","a100","a101","a102","a103","a104","a105","a106","a107","a108","a109","a110","a111","a112","a113","a114","a115","a116","a117","a118","a119","a120","a121","a122","a123","a124","a125","a126","a127","a128","a129","a130","a131","a132","a133","a134","a135","a136","a137","a138","a139","a140","a141","a142","a143","a144","a145","a146","a147","a148","a149","a150","a151","a152","a153","a154","a155","a156","a157","a158","a159","a160","a161","a162","a163","a164","a165","a166","a167","a168","a169","a170","a171","a172","a173","a174","a175","a176","a177","a178","a179","a180","a181","a182","a183","a184","a185","a186","a187","a188","a189","a190","a191","a192","a193","a194","a195","a196","a197","a198","a199","a200","a201","a202","a203","a204","a205","a206","a207","a208","a209","a210","a211","a212","a213","a214","a215","a216","a217","a218","a219","a220","a221","a222","a223","a224","a225","a226","a227","a228","a229","a230","a231","a232","a233","a234","a235","a236","a237","a238","a239","a240","a241","a242","a243","a244","a245","a246","a247","a248","a249","a250","a251","a252","a253","a254","a255","s4","s5","s6","s8","s9","s10","s11","s18","s19","s22","s23","s30","s31","s35","s44","s45","s46","s47","s48","s49","s50","s51","s52","s53","s54","s55","s56","s57","s58","s59","s62","s63","s80","s81","s82","s83","s84","s85","s86","s87","s90","s91","s93","s94","s95","s98","s99","vcc","memory","scc");
}

#define DBG_PTR nullptr
extern "C" void kernel_launch(void* const* d_in, const int* in_sizes, int n_in, void* d_out, int out_size, void* d_ws, size_t ws_size, hipStream_t stream) {
  (void)hipFuncSetAttribute((const void*)fused_block, hipFuncAttributeMaxDynamicSharedMemorySize, 163840);
  fused_block<<<256, 256, 163840, stream>>>((const u16*)d_in[0], d_in[1], d_in[2], d_in[3], d_in[4], d_in[5], d_in[7], d_in[8], d_in[9],
                                            (const u16*)d_in[10], (u16*)d_out, (char*)d_ws, DBG_PTR, nullptr);
}
```
